```python
import torch

def _softmax(x, axis=-1):
    return torch.softmax(x, dim=axis)
def _gelu(x):
    return torch.nn.functional.gelu(x, approximate="tanh")
def _silu(x):
    return torch.nn.functional.silu(x)
def _sigmoid(x):
    return torch.sigmoid(x)
def _layernorm(x, axis=-1, eps=1e-5):
    m = x.mean(dim=axis, keepdim=True)
    v = x.var(dim=axis, keepdim=True, unbiased=False)
    return (x - m) / torch.sqrt(v + eps)
def _scatter_add(tgt, idx, src, axis=0):
    return tgt.clone().index_add_(axis, idx, src)
def _segment_sum(src, seg, n):
    out = torch.zeros((n,) + src.shape[1:], dtype=src.dtype)
    return out.index_add_(0, seg, src)
def _pad(x, pw):
    flat = tuple(v for pair in reversed(pw) for v in pair)
    return torch.nn.functional.pad(x, flat)

kernel_name = 'concat_heads__gqa_block_cached__gqa_block_cached__near_ridge'

def setup_inputs():

    _w = (lambda a: (lambda t: t.to(torch.bfloat16) if t.is_floating_point() else t)(torch.as_tensor(a)))
    def _A_setup():
        B, Sq, D = 16, 128, 448
        Hq, Hkv, Sk, Dh = 16, 8, 512, 256
        assert Hq % Hkv == 0, (Hq, Hkv)
        return {
            "x": _w((lambda *s: torch.randn(*s, dtype=torch.bfloat16) * 0.8)(B, Sq, D)),
            "k_cache": _w((lambda *s: torch.randn(*s, dtype=torch.bfloat16) * 0.8)(B, Hkv, Sk, Dh)),
            "v_cache": _w((lambda *s: torch.randn(*s, dtype=torch.bfloat16) * 0.05)(B, Hkv, Sk, Dh)),
            "wq": _w((lambda *s: torch.randn(*s, dtype=torch.bfloat16) * 0.8)(D, Hq * Dh) * (D ** -0.5)),
            "wo": _w((lambda *s: torch.randn(*s, dtype=torch.bfloat16) * 0.05)(Hq * Dh, D) * ((Hq * Dh) ** -0.5)),
        }
    _A_in = _A_setup()

    def _B_setup():
        B, Sq, D = 16, 128, 448
        Hq, Hkv, Sk, Dh = 16, 8, 512, 256
        assert Hq % Hkv == 0, (Hq, Hkv)
        return {
            "x": _w((lambda *s: torch.randn(*s, dtype=torch.bfloat16) * 0.8)(B, Sq, D)),
            "k_cache": _w((lambda *s: torch.randn(*s, dtype=torch.bfloat16) * 0.8)(B, Hkv, Sk, Dh)),
            "v_cache": _w((lambda *s: torch.randn(*s, dtype=torch.bfloat16) * 0.05)(B, Hkv, Sk, Dh)),
            "wq": _w((lambda *s: torch.randn(*s, dtype=torch.bfloat16) * 0.8)(D, Hq * Dh) * (D ** -0.5)),
            "wo": _w((lambda *s: torch.randn(*s, dtype=torch.bfloat16) * 0.05)(Hq * Dh, D) * ((Hq * Dh) ** -0.5)),
        }
    _B_in = _B_setup()

    return {
        "A_x": _A_in["x"],
        "A_k_cache": _A_in["k_cache"],
        "A_v_cache": _A_in["v_cache"],
        "A_wq": _A_in["wq"],
        "A_wo": _A_in["wo"],
        "B_x": _B_in["x"],
        "B_k_cache": _B_in["k_cache"],
        "B_v_cache": _B_in["v_cache"],
        "B_wq": _B_in["wq"],
        "B_wo": _B_in["wo"],
    }

def reference(A_x, A_k_cache, A_v_cache, A_wq, A_wo, B_x, B_k_cache, B_v_cache, B_wq, B_wo):
    def _A_ref(x, k_cache, v_cache, wq, wo):
        B, Sq, D = x.shape
        Hq, Dh = 16, 256
        Hkv = k_cache.shape[1]
        rep = Hq // Hkv
        q = (lambda x, *d: x.permute(*d))((x @ wq).reshape(B, Sq, Hq, Dh), 0, 2, 1, 3)
        k = (lambda x, n, axis: torch.repeat_interleave(x, n, dim=axis))(k_cache, rep, axis=1)
        v = (lambda x, n, axis: torch.repeat_interleave(x, n, dim=axis))(v_cache, rep, axis=1)
        scores = torch.einsum("bhqd,bhkd->bhqk", q, k) * (Dh ** -0.5)
        p = _softmax(scores, axis=-1)
        o = torch.einsum("bhqk,bhkd->bhqd", p, v)
        return (lambda x, *d: x.permute(*d))(o, 0, 2, 1, 3).reshape(B, Sq, Hq * Dh) @ wo

    def _B_ref(x, k_cache, v_cache, wq, wo):
        B, Sq, D = x.shape
        Hq, Dh = 16, 256
        Hkv = k_cache.shape[1]
        rep = Hq // Hkv
        q = (lambda x, *d: x.permute(*d))((x @ wq).reshape(B, Sq, Hq, Dh), 0, 2, 1, 3)
        k = (lambda x, n, axis: torch.repeat_interleave(x, n, dim=axis))(k_cache, rep, axis=1)
        v = (lambda x, n, axis: torch.repeat_interleave(x, n, dim=axis))(v_cache, rep, axis=1)
        scores = torch.einsum("bhqd,bhkd->bhqk", q, k) * (Dh ** -0.5)
        p = _softmax(scores, axis=-1)
        o = torch.einsum("bhqk,bhkd->bhqd", p, v)
        return (lambda x, *d: x.permute(*d))(o, 0, 2, 1, 3).reshape(B, Sq, Hq * Dh) @ wo

    a = _A_ref(A_x, A_k_cache, A_v_cache, A_wq, A_wo)
    b = _B_ref(B_x, B_k_cache, B_v_cache, B_wq, B_wo)
    return torch.concatenate([a, b], axis=-1)
```

```cpp
#include <hip/hip_runtime.h>
#include <hip/hip_bf16.h>
typedef __hip_bfloat16 bf16;
__device__ __forceinline__ float b2f(bf16 x) { return __bfloat162float(x); }
__device__ __forceinline__ bf16 f2b(float x) { return __float2bfloat16(x); }

__global__ void nq(const bf16* x, const bf16* wq, bf16* q) {
    int n = blockIdx.x * 256 + threadIdx.x;
    int m = blockIdx.y;
    __shared__ float xs[448];
    for (int k = threadIdx.x; k < 448; k += 256) xs[k] = b2f(x[m * 448 + k]);
    __syncthreads();
    float acc = 0.f;
    for (int k = 0; k < 448; ++k) acc += xs[k] * b2f(wq[k * 4096 + n]);
    q[(size_t)m * 4096 + n] = f2b(acc);
}
__global__ void nattn(const bf16* q, const bf16* kc, const bf16* vc, bf16* o) {
    int row = blockIdx.x, h = blockIdx.y, b = blockIdx.z;
    int g = h >> 1;
    __shared__ float qs[256];
    __shared__ float ps[512];
    __shared__ float red[256];
    int t = threadIdx.x;
    qs[t] = b2f(q[((size_t)(b * 128 + row)) * 4096 + h * 256 + t]);
    __syncthreads();
    const bf16* K = kc + ((size_t)(b * 8 + g)) * 512 * 256;
    const bf16* V = vc + ((size_t)(b * 8 + g)) * 512 * 256;
    float s[2];
    for (int i = 0; i < 2; ++i) {
        int key = t + 256 * i;
        float acc = 0.f;
        for (int d = 0; d < 256; ++d) acc += qs[d] * b2f(K[key * 256 + d]);
        s[i] = b2f(f2b(acc)) * 0.0625f;
    }
    red[t] = fmaxf(s[0], s[1]);
    __syncthreads();
    for (int o2 = 128; o2 > 0; o2 >>= 1) { if (t < o2) red[t] = fmaxf(red[t], red[t + o2]); __syncthreads(); }
    float m = red[0];
    __syncthreads();
    float e0 = expf(s[0] - m), e1 = expf(s[1] - m);
    red[t] = e0 + e1;
    __syncthreads();
    for (int o2 = 128; o2 > 0; o2 >>= 1) { if (t < o2) red[t] += red[t + o2]; __syncthreads(); }
    float l = red[0];
    ps[t] = b2f(f2b(e0 / l)); ps[t + 256] = b2f(f2b(e1 / l));
    __syncthreads();
    float acc = 0.f;
    for (int key = 0; key < 512; ++key) acc += ps[key] * b2f(V[key * 256 + t]);
    o[((size_t)(b * 128 + row)) * 4096 + h * 256 + t] = f2b(acc);
}
__global__ void no(const bf16* o, const bf16* wo, bf16* out, int br) {
    int m = blockIdx.x;
    int n = threadIdx.x;
    __shared__ float os[4096];
    for (int k = threadIdx.x; k < 4096; k += 448) os[k] = b2f(o[(size_t)m * 4096 + k]);
    __syncthreads();
    float acc = 0.f;
    for (int k = 0; k < 4096; ++k) acc += os[k] * b2f(wo[k * 448 + n]);
    out[(size_t)m * 896 + br * 448 + n] = f2b(acc);
}
static void naive_launch(void* const* d_in, void* d_out, void* d_ws, hipStream_t stream) {
    for (int br = 0; br < 2; ++br) {
        const bf16* x = (const bf16*)d_in[5 * br + 0];
        const bf16* kc = (const bf16*)d_in[5 * br + 1];
        const bf16* vc = (const bf16*)d_in[5 * br + 2];
        const bf16* wq = (const bf16*)d_in[5 * br + 3];
        const bf16* wo = (const bf16*)d_in[5 * br + 4];
        bf16* q = (bf16*)d_ws + (size_t)br * 2 * 2048 * 4096;
        bf16* o = q + (size_t)2048 * 4096;
        nq<<<dim3(16, 2048), 256, 0, stream>>>(x, wq, q);
        nattn<<<dim3(128, 16, 16), 256, 0, stream>>>(q, kc, vc, o);
        no<<<2048, 448, 0, stream>>>(o, wo, (bf16*)d_out, br);
    }
}
extern "C" void kernel_launch(void* const* d_in, const int* in_sizes, int n_in, void* d_out, int out_size, void* d_ws, size_t ws_size, hipStream_t stream) {
    naive_launch(d_in, d_out, d_ws, stream);
}
```

```cpp
#include <hip/hip_runtime.h>
#include <hip/hip_bf16.h>

__global__ __launch_bounds__(256, 1) void fused_kernel(const char* xA, const char* kA, const char* vA, const char* wqA, const char* woA,
                                                       const char* xB, const char* kB, const char* vB, const char* wqB, const char* woB,
                                                       char* ws, char* out) {
  __shared__ __attribute__((aligned(16))) char lds[163840];
  unsigned bid = blockIdx.x;

  unsigned xcd = bid & 7, j = bid >> 3, br = xcd >> 2, g = 2 * (xcd & 3) + (j & 1), b = j >> 1, bb = br * 16 + b;
  const char* x = (br ? xB : xA) + (size_t)b * (128 * 896);
  const char* k = (br ? kB : kA) + (size_t)(b * 8 + g) * 262144;
  const char* v = (br ? vB : vA) + (size_t)(b * 8 + g) * 262144;
  const char* wq = (br ? wqB : wqA) + (size_t)g * 1024;
  const char* wo = (br ? woB : woA) + (size_t)g * (512 * 896);
  char* wsp = ws + (size_t)bb * 1855488;
  char* op = out + (size_t)(b * 128) * 1792 + br * 896;
  unsigned tid = threadIdx.x;
  asm volatile(
"v_lshrrev_b32 v8, 6, %[tid]\n"
"v_and_b32 v2, 63, %[tid]\n"
"s_mov_b64 s[32:33], %[x]\n"
"s_and_b32 s33, s33, 0xffff\n"
"s_mov_b32 s34, 114688\n"
"s_mov_b32 s35, 0x00020000\n"
"v_readfirstlane_b32 s60, v8\n"
"s_mov_b64 s[36:37], %[wq]\n"
"s_and_b32 s37, s37, 0xffff\n"
"s_mov_b32 s38, 3670016\n"
"s_mov_b32 s39, 0x00020000\n"
"s_lshl_b32 s81, %[g], 10\n"
"s_sub_u32 s38, s38, s81\n"
"v_bfe_u32 v3, v2, 2, 3\n"
"v_lshrrev_b32 v4, 5, v2\n"
"v_and_b32 v5, 3, v2\n"
"v_bfe_i32 v11, v2, 4, 1\n"
"v_lshlrev_b32 v9, 6, v4\n"
"v_lshl_add_u32 v7, v5, 4, v9\n"
"v_and_b32 v10, 48, v11\n"
"v_xor_b32 v6, v7, v10\n"
"s_lshl_b32 s81, s60, 5\n"
"s_lshr_b32 s79, %[g], 1\n"
"s_lshl_b32 s79, s79, 8\n"
"v_add_u32 v8, s81, v3\n"
"v_mul_u32_u24 v8, 896, v8\n"
"v_add_u32 v243, v8, v6\n"
"s_lshl_b32 s71, s60, 12\n"
"s_mov_b32 s64, 6144\n"
"s_mov_b32 s65, 12288\n"
"s_mov_b32 s66, 18432\n"
"s_add_u32 m0, s71, 0\n"
"s_mov_b32 s67, 64512\n"
"s_mov_b32 s68, 129024\n"
"s_mov_b32 s69, 193536\n"
"v_add_u32 v242, s79, v243\n"
"s_lshl_b32 s80, s79, 12\n"
"buffer_load_dwordx4 v242, s[32:35], 0 offen offset:0  lds\n"
"buffer_load_dwordx4 v242, s[32:35], s64 offen offset:1024  lds\n"
"buffer_load_dwordx4 v242, s[32:35], s65 offen offset:2048  lds\n"
"buffer_load_dwordx4 v242, s[32:35], s66 offen offset:3072  lds\n"
"s_and_b32 s81, s60, 1\n"
"s_lshl_b32 s85, s81, 13\n"
"s_lshl_b32 s82, s81, 9\n"
"v_lshl_add_u32 v255, v3, 13, v7\n"
"v_add_u32 v255, s82, v255\n"
"v_lshrrev_b32 v247, 3, %[tid]\n"
"s_mul_i32 s82, %[b], 28\n"
"v_add_u32 v247, s82, v247\n"
"v_lshlrev_b32 v247, 13, v247\n"
"v_and_b32 v8, 7, v2\n"
"v_lshl_add_u32 v247, v8, 7, v247\n"
"s_getpc_b64 s[56:57]\n"
"L_PC:\n"
"s_and_b32 s57, s57, 0xffff\n"
"s_mov_b32 s58, L_END-L_PC\n"
"s_mov_b32 s59, 0x00020000\n"
"s_and_b32 s82, %[g], 1\n"
"s_lshl1_add_u32 s76, %[b], s82\n"
"s_lshl_b32 s82, s76, 12\n"
"v_lshl_add_u32 v246, %[tid], 4, s82\n"
"s_cmp_ge_u32 s60, 2\n"
"s_cbranch_scc1 L_ROLEB\n"
"s_mov_b32 exec_lo, 0x33333333\n"
"s_mov_b32 exec_hi, 0x33333333\n"
"buffer_load_dword v248, v247, s[36:39], 0 offen\n"
"s_mov_b64 exec, -1\n"
"v_add_u32 v242, s80, v255\n"
"v_xor_b32 v244, 32, v242\n"
"s_add_u32 m0, s85, 81920\n"
"s_nop 0\n"
"buffer_load_dwordx4 v242, s[36:39], 0 offen offset:0  lds\n"
"buffer_load_dwordx4 v242, s[36:39], s67 offen offset:1024  lds\n"
"buffer_load_dwordx4 v244, s[36:39], s68 offen offset:2048  lds\n"
"buffer_load_dwordx4 v244, s[36:39], s69 offen offset:3072  lds\n"
"s_add_u32 s78, s80, 262144\n"
"v_add_u32 v242, s78, v255\n"
"v_xor_b32 v244, 32, v242\n"
"s_add_u32 m0, s85, 86016\n"
"s_nop 0\n"
"buffer_load_dwordx4 v242, s[36:39], 0 offen offset:0  lds\n"
"buffer_load_dwordx4 v242, s[36:39], s67 offen offset:1024  lds\n"
"buffer_load_dwordx4 v244, s[36:39], s68 offen offset:2048  lds\n"
"buffer_load_dwordx4 v244, s[36:39], s69 offen offset:3072  lds\n"
"s_mov_b32 s83, 0\n"
"buffer_load_dword v248, v246, s[56:59], s83 offen\n"
"s_mov_b64 s[40:41], %[k]\n"
"s_and_b32 s41, s41, 0xffff\n"
"s_mov_b32 s42, 262144\n"
"s_mov_b32 s43, 0x00020000\n"
"s_mov_b64 s[44:45], %[v]\n"
"s_and_b32 s45, s45, 0xffff\n"
"s_mov_b32 s46, 262144\n"
"s_mov_b32 s47, 0x00020000\n"
"s_mov_b64 s[48:49], %[wo]\n"
"s_and_b32 s49, s49, 0xffff\n"
"s_mov_b32 s50, 458752\n"
"s_mov_b32 s51, 0x00020000\n"
"s_mov_b64 s[52:53], %[ws]\n"
"s_and_b32 s53, s53, 0xffff\n"
"s_mov_b32 s54, 1855488\n"
"s_mov_b32 s55, 0x00020000\n"
"s_mov_b32 s83, 1835008\n"
"buffer_load_dwordx2 v[64:65], off, s[52:55], s83 sc1\n"
"s_mov_b32 s61, 3072\n"
"s_mov_b32 s62, 6144\n"
"s_mov_b32 s63, 9216\n"
"s_lshr_b32 s81, s85, 5\n"
"s_lshl_b32 s82, s60, 7\n"
"v_lshl_add_u32 v241, v3, 9, v6\n"
"v_add_u32 v245, s81, v241\n"
"v_add_u32 v241, s82, v241\n"
"v_bfe_u32 v8, v2, 3, 1\n"
"v_and_b32 v9, 7, v2\n"
"v_lshlrev_b32 v9, 6, v9\n"
"v_lshl_add_u32 v8, v8, 10, v9\n"
"v_and_b32 v9, 2, v11\n"
"v_add_u32 v9, v9, v4\n"
"v_bfe_i32 v10, v2, 2, 1\n"
"v_and_b32 v10, 3, v10\n"
"v_xor_b32 v10, v10, v9\n"
"v_lshl_add_u32 v232, v10, 4, v8\n"
"v_add_u32 v233, 65536, v232\n"
"v_add_u32 v234, 131072, v232\n"
"s_lshr_b32 s81, s60, 1\n"
"s_lshl_b32 s82, s81, 13\n"
"v_add_u32 v254, s82, v232\n"
"v_lshlrev_b32 v8, 3, v5\n"
"v_bfe_u32 v10, v2, 2, 2\n"
"v_lshl_add_u32 v10, v10, 6, v8\n"
"v_lshl_add_u32 v10, v9, 10, v10\n"
"v_and_b32 v9, 32, v11\n"
"s_add_u32 s82, s85, 49152\n"
"v_add3_u32 v250, v10, v9, s82\n"
"v_xor_b32 v251, 32, v250\n"
"v_add_u32 v252, 65536, v250\n"
"v_add_u32 v253, 65536, v251\n"
"v_and_b32 v9, 48, v11\n"
"v_xor_b32 v8, v8, v9\n"
"v_lshl_add_u32 v8, v3, 6, v8\n"
"v_lshl_add_u32 v235, v4, 10, v8\n"
"v_xor_b32 v236, 32, v235\n"
"v_add_u32 v237, 65536, v235\n"
"v_add_u32 v238, 65536, v236\n"
"v_add_u32 v239, 131072, v235\n"
"v_add_u32 v240, 131072, v236\n"
"v_and_b32 v249, 127, %[tid]\n"
"v_lshlrev_b32 v249, 7, v249\n"
"s_mul_i32 s87, %[b], 28672\n"
"s_lshl_b32 s81, s81, 14\n"
"s_add_u32 s87, s87, s81\n"
"s_mov_b32 s70, 0x3db8aa3b\n"
"s_add_u32 s78, s80, 128\n"
"v_add_u32 v242, s78, v255\n"
"v_xor_b32 v244, 32, v242\n"
"s_add_u32 m0, s85, 98304\n"
"s_nop 0\n"
"buffer_load_dwordx4 v242, s[36:39], 0 offen offset:0  lds\n"
"buffer_load_dwordx4 v242, s[36:39], s67 offen offset:1024  lds\n"
"buffer_load_dwordx4 v244, s[36:39], s68 offen offset:2048  lds\n"
"buffer_load_dwordx4 v244, s[36:39], s69 offen offset:3072  lds\n"
"s_add_u32 s78, s80, 262272\n"
"v_add_u32 v242, s78, v255\n"
"v_xor_b32 v244, 32, v242\n"
"s_add_u32 m0, s85, 102400\n"
"s_nop 0\n"
"buffer_load_dwordx4 v242, s[36:39], 0 offen offset:0  lds\n"
"buffer_load_dwordx4 v242, s[36:39], s67 offen offset:1024  lds\n"
"buffer_load_dwordx4 v244, s[36:39], s68 offen offset:2048  lds\n"
"buffer_load_dwordx4 v244, s[36:39], s69 offen offset:3072  lds\n"
"s_add_u32 s79, s79, 128\n"
"s_sub_u32 s78, s79, 896\n"
"s_min_u32 s79, s79, s78\n"
"v_add_u32 v242, s79, v243\n"
"s_add_u32 m0, s71, 16384\n"
"s_nop 0\n"
"buffer_load_dwordx4 v242, s[32:35], 0 offen offset:0  lds\n"
"buffer_load_dwordx4 v242, s[32:35], s64 offen offset:1024  lds\n"
"buffer_load_dwordx4 v242, s[32:35], s65 offen offset:2048  lds\n"
"buffer_load_dwordx4 v242, s[32:35], s66 offen offset:3072  lds\n"
"s_add_u32 s80, s80, 524288\n"
"s_sub_u32 s78, s80, 3670016\n"
"s_min_u32 s80, s80, s78\n"
"v_add_u32 v242, s80, v255\n"
"v_xor_b32 v244, 32, v242\n"
"s_add_u32 m0, s85, 114688\n"
"s_nop 0\n"
"buffer_load_dwordx4 v242, s[36:39], 0 offen offset:0  lds\n"
"buffer_load_dwordx4 v242, s[36:39], s67 offen offset:1024  lds\n"
"buffer_load_dwordx4 v244, s[36:39], s68 offen offset:2048  lds\n"
"buffer_load_dwordx4 v244, s[36:39], s69 offen offset:3072  lds\n"
"s_add_u32 s78, s80, 262144\n"
"v_add_u32 v242, s78, v255\n"
"v_xor_b32 v244, 32, v242\n"
"s_add_u32 m0, s85, 118784\n"
"s_nop 0\n"
"buffer_load_dwordx4 v242, s[36:39], 0 offen offset:0  lds\n"
"buffer_load_dwordx4 v242, s[36:39], s67 offen offset:1024  lds\n"
"buffer_load_dwordx4 v244, s[36:39], s68 offen offset:2048  lds\n"
"buffer_load_dwordx4 v244, s[36:39], s69 offen offset:3072  lds\n"
"s_waitcnt vmcnt(22)\n"
"s_barrier\n"
"ds_read_b128 a[0:3], v254 offset:0\n"
"ds_read_b128 a[4:7], v254 offset:2048\n"
"ds_read_b128 a[8:11], v254 offset:4096\n"
"ds_read_b128 a[12:15], v254 offset:6144\n"
"ds_read_b64_tr_b16 a[232:233], v250 offset:32768\n"
"ds_read_b64_tr_b16 a[234:235], v250 offset:33024\n"
"ds_read_b128 a[16:19], v254 offset:512\n"
"ds_read_b128 a[20:23], v254 offset:2560\n"
"ds_read_b128 a[24:27], v254 offset:4608\n"
"ds_read_b128 a[28:31], v254 offset:6656\n"
"ds_read_b64_tr_b16 a[236:237], v250 offset:36864\n"
"ds_read_b64_tr_b16 a[238:239], v250 offset:37120\n"
"ds_read_b64_tr_b16 a[240:241], v251 offset:32768\n"
"ds_read_b64_tr_b16 a[242:243], v251 offset:33024\n"
"s_waitcnt lgkmcnt(10)\n"
"ds_read_b64_tr_b16 a[244:245], v251 offset:36864\n"
"ds_read_b64_tr_b16 a[246:247], v251 offset:37120\n"
"s_waitcnt lgkmcnt(4)\n"
"v_mfma_f32_16x16x32_bf16 v[104:107], a[232:235], a[0:3], 0\n"
"ds_read_b64_tr_b16 a[248:249], v250 offset:33280\n"
"v_mfma_f32_16x16x32_bf16 v[104:107], a[236:239], a[16:19], v[104:107]\n"
"s_add_u32 s78, s80, 128\n"
"ds_read_b64_tr_b16 a[250:251], v250 offset:33536\n"
"v_add_u32 v242, s78, v255\n"
"v_xor_b32 v244, 32, v242\n"
"s_add_u32 m0, s85, 49152\n"
"v_mfma_f32_16x16x32_bf16 v[112:115], a[232:235], a[4:7], 0\n"
"buffer_load_dwordx4 v242, s[36:39], 0 offen offset:0  lds\n"
"v_mfma_f32_16x16x32_bf16 v[112:115], a[236:239], a[20:23], v[112:115]\n"
"v_mfma_f32_16x16x32_bf16 v[120:123], a[232:235], a[8:11], 0\n"
"ds_read_b64_tr_b16 a[252:253], v250 offset:37376\n"
"v_mfma_f32_16x16x32_bf16 v[120:123], a[236:239], a[24:27], v[120:123]\n"
"ds_read_b64_tr_b16 a[254:255], v250 offset:37632\n"
"v_mfma_f32_16x16x32_bf16 v[128:131], a[232:235], a[12:15], 0\n"
"buffer_load_dwordx4 v242, s[36:39], s67 offen offset:1024  lds\n"
"v_mfma_f32_16x16x32_bf16 v[128:131], a[236:239], a[28:31], v[128:131]\n"
"s_waitcnt lgkmcnt(4)\n"
"v_mfma_f32_16x16x32_bf16 v[132:135], a[240:243], a[12:15], 0\n"
"buffer_load_dwordx4 v244, s[36:39], s68 offen offset:2048  lds\n"
"ds_read_b64_tr_b16 a[232:233], v251 offset:33280\n"
"v_mfma_f32_16x16x32_bf16 v[132:135], a[244:247], a[28:31], v[132:135]\n"
"ds_read_b64_tr_b16 a[234:235], v251 offset:33536\n"
"v_mfma_f32_16x16x32_bf16 v[124:127], a[240:243], a[8:11], 0\n"
"v_mfma_f32_16x16x32_bf16 v[124:127], a[244:247], a[24:27], v[124:127]\n"
"buffer_load_dwordx4 v244, s[36:39], s69 offen offset:3072  lds\n"
"v_mfma_f32_16x16x32_bf16 v[116:119], a[240:243], a[4:7], 0\n"
"ds_read_b64_tr_b16 a[236:237], v251 offset:37376\n"
"v_mfma_f32_16x16x32_bf16 v[116:119], a[244:247], a[20:23], v[116:119]\n"
"s_add_u32 s78, s80, 262272\n"
"ds_read_b64_tr_b16 a[238:239], v251 offset:37632\n"
"v_add_u32 v242, s78, v255\n"
"v_xor_b32 v244, 32, v242\n"
"s_add_u32 m0, s85, 53248\n"
"v_mfma_f32_16x16x32_bf16 v[108:111], a[240:243], a[0:3], 0\n"
"buffer_load_dwordx4 v242, s[36:39], 0 offen offset:0  lds\n"
"v_mfma_f32_16x16x32_bf16 v[108:111], a[244:247], a[16:19], v[108:111]\n"
"s_waitcnt lgkmcnt(4)\n"
"v_mfma_f32_16x16x32_bf16 v[136:139], a[248:251], a[0:3], 0\n"
"s_waitcnt vmcnt(17)\n"
"s_barrier\n"
"ds_read_b64_tr_b16 a[240:241], v250 offset:49152\n"
"v_mfma_f32_16x16x32_bf16 v[136:139], a[252:255], a[16:19], v[136:139]\n"
"ds_read_b64_tr_b16 a[242:243], v250 offset:49408\n"
"v_mfma_f32_16x16x32_bf16 v[144:147], a[248:251], a[4:7], 0\n"
"buffer_load_dwordx4 v242, s[36:39], s67 offen offset:1024  lds\n"
"v_mfma_f32_16x16x32_bf16 v[144:147], a[252:255], a[20:23], v[144:147]\n"
"v_mfma_f32_16x16x32_bf16 v[152:155], a[248:251], a[8:11], 0\n"
"buffer_load_dwordx4 v244, s[36:39], s68 offen offset:2048  lds\n"
"ds_read_b64_tr_b16 a[244:245], v250 offset:53248\n"
"v_mfma_f32_16x16x32_bf16 v[152:155], a[252:255], a[24:27], v[152:155]\n"
"ds_read_b64_tr_b16 a[246:247], v250 offset:53504\n"
"v_mfma_f32_16x16x32_bf16 v[160:163], a[248:251], a[12:15], 0\n"
"s_add_u32 s80, s80, 524288\n"
"v_mfma_f32_16x16x32_bf16 v[160:163], a[252:255], a[28:31], v[160:163]\n"
"s_waitcnt lgkmcnt(4)\n"
"buffer_load_dwordx4 v244, s[36:39], s69 offen offset:3072  lds\n"
"v_mfma_f32_16x16x32_bf16 v[164:167], a[232:235], a[12:15], 0\n"
"s_sub_u32 s78, s80, 3670016\n"
"ds_read_b64_tr_b16 a[248:249], v251 offset:49152\n"
"v_mfma_f32_16x16x32_bf16 v[164:167], a[236:239], a[28:31], v[164:167]\n"
"s_min_u32 s80, s80, s78\n"
"ds_read_b64_tr_b16 a[250:251], v251 offset:49408\n"
"v_add_u32 v242, s80, v255\n"
"v_xor_b32 v244, 32, v242\n"
"s_add_u32 m0, s85, 65536\n"
"v_mfma_f32_16x16x32_bf16 v[156:159], a[232:235], a[8:11], 0\n"
"buffer_load_dwordx4 v242, s[36:39], 0 offen offset:0  lds\n"
"v_mfma_f32_16x16x32_bf16 v[156:159], a[236:239], a[24:27], v[156:159]\n"
"v_mfma_f32_16x16x32_bf16 v[148:151], a[232:235], a[4:7], 0\n"
"ds_read_b64_tr_b16 a[252:253], v251 offset:53248\n"
"v_mfma_f32_16x16x32_bf16 v[148:151], a[236:239], a[20:23], v[148:151]\n"
"ds_read_b64_tr_b16 a[254:255], v251 offset:53504\n"
"v_mfma_f32_16x16x32_bf16 v[140:143], a[232:235], a[0:3], 0\n"
"buffer_load_dwordx4 v242, s[36:39], s67 offen offset:1024  lds\n"
"v_mfma_f32_16x16x32_bf16 v[140:143], a[236:239], a[16:19], v[140:143]\n"
"s_waitcnt lgkmcnt(4)\n"
"v_mfma_f32_16x16x32_bf16 v[168:171], a[240:243], a[0:3], 0\n"
"buffer_load_dwordx4 v244, s[36:39], s68 offen offset:2048  lds\n"
"ds_read_b64_tr_b16 a[232:233], v250 offset:49664\n"
"v_mfma_f32_16x16x32_bf16 v[168:171], a[244:247], a[16:19], v[168:171]\n"
"ds_read_b64_tr_b16 a[234:235], v250 offset:49920\n"
"v_mfma_f32_16x16x32_bf16 v[176:179], a[240:243], a[4:7], 0\n"
"v_mfma_f32_16x16x32_bf16 v[176:179], a[244:247], a[20:23], v[176:179]\n"
"buffer_load_dwordx4 v244, s[36:39], s69 offen offset:3072  lds\n"
"v_mfma_f32_16x16x32_bf16 v[184:187], a[240:243], a[8:11], 0\n"
"ds_read_b64_tr_b16 a[236:237], v250 offset:53760\n"
"v_mfma_f32_16x16x32_bf16 v[184:187], a[244:247], a[24:27], v[184:187]\n"
"s_add_u32 s78, s80, 262144\n"
"ds_read_b64_tr_b16 a[238:239], v250 offset:54016\n"
"v_add_u32 v242, s78, v255\n"
"v_xor_b32 v244, 32, v242\n"
"s_add_u32 m0, s85, 69632\n"
"v_mfma_f32_16x16x32_bf16 v[192:195], a[240:243], a[12:15], 0\n"
"buffer_load_dwordx4 v242, s[36:39], 0 offen offset:0  lds\n"
"v_mfma_f32_16x16x32_bf16 v[192:195], a[244:247], a[28:31], v[192:195]\n"
"s_waitcnt lgkmcnt(4)\n"
"v_mfma_f32_16x16x32_bf16 v[196:199], a[248:251], a[12:15], 0\n"
"ds_read_b64_tr_b16 a[240:241], v251 offset:49664\n"
"v_mfma_f32_16x16x32_bf16 v[196:199], a[252:255], a[28:31], v[196:199]\n"
"ds_read_b64_tr_b16 a[242:243], v251 offset:49920\n"
"v_mfma_f32_16x16x32_bf16 v[188:191], a[248:251], a[8:11], 0\n"
"buffer_load_dwordx4 v242, s[36:39], s67 offen offset:1024  lds\n"
"v_mfma_f32_16x16x32_bf16 v[188:191], a[252:255], a[24:27], v[188:191]\n"
"v_mfma_f32_16x16x32_bf16 v[180:183], a[248:251], a[4:7], 0\n"
"buffer_load_dwordx4 v244, s[36:39], s68 offen offset:2048  lds\n"
"ds_read_b64_tr_b16 a[244:245], v251 offset:53760\n"
"v_mfma_f32_16x16x32_bf16 v[180:183], a[252:255], a[20:23], v[180:183]\n"
"s_add_u32 s79, s79, 128\n"
"ds_read_b64_tr_b16 a[246:247], v251 offset:54016\n"
"v_mfma_f32_16x16x32_bf16 v[172:175], a[248:251], a[0:3], 0\n"
"s_sub_u32 s78, s79, 896\n"
"v_mfma_f32_16x16x32_bf16 v[172:175], a[252:255], a[16:19], v[172:175]\n"
"s_waitcnt lgkmcnt(4)\n"
"buffer_load_dwordx4 v244, s[36:39], s69 offen offset:3072  lds\n"
"v_mfma_f32_16x16x32_bf16 v[200:203], a[232:235], a[0:3], 0\n"
"s_waitcnt vmcnt(16)\n"
"s_barrier\n"
"ds_read_b64_tr_b16 a[248:249], v252 offset:0\n"
"v_mfma_f32_16x16x32_bf16 v[200:203], a[236:239], a[16:19], v[200:203]\n"
"s_min_u32 s79, s79, s78\n"
"ds_read_b64_tr_b16 a[250:251], v252 offset:256\n"
"v_add_u32 v242, s79, v243\n"
"s_add_u32 m0, s71, 32768\n"
"v_mfma_f32_16x16x32_bf16 v[208:211], a[232:235], a[4:7], 0\n"
"buffer_load_dwordx4 v242, s[32:35], 0 offen offset:0  lds\n"
"ds_read_b128 a[32:35], v254 offset:16384\n"
"ds_read_b128 a[36:39], v254 offset:18432\n"
"v_mfma_f32_16x16x32_bf16 v[208:211], a[236:239], a[20:23], v[208:211]\n"
"ds_read_b128 a[40:43], v254 offset:20480\n"
"ds_read_b128 a[44:47], v254 offset:22528\n"
"v_mfma_f32_16x16x32_bf16 v[216:219], a[232:235], a[8:11], 0\n"
"ds_read_b64_tr_b16 a[252:253], v252 offset:4096\n"
"v_mfma_f32_16x16x32_bf16 v[216:219], a[236:239], a[24:27], v[216:219]\n"
"buffer_load_dwordx4 v242, s[32:35], s64 offen offset:1024  lds\n"
"ds_read_b64_tr_b16 a[254:255], v252 offset:4352\n"
"v_mfma_f32_16x16x32_bf16 v[224:227], a[232:235], a[12:15], 0\n"
"ds_read_b128 a[48:51], v254 offset:16896\n"
"ds_read_b128 a[52:55], v254 offset:18944\n"
"v_mfma_f32_16x16x32_bf16 v[224:227], a[236:239], a[28:31], v[224:227]\n"
"s_waitcnt lgkmcnt(10)\n"
"ds_read_b128 a[56:59], v254 offset:20992\n"
"ds_read_b128 a[60:63], v254 offset:23040\n"
"v_mfma_f32_16x16x32_bf16 v[228:231], a[240:243], a[12:15], 0\n"
"buffer_load_dwordx4 v242, s[32:35], s65 offen offset:2048  lds\n"
"ds_read_b64_tr_b16 a[232:233], v253 offset:0\n"
"v_mfma_f32_16x16x32_bf16 v[228:231], a[244:247], a[28:31], v[228:231]\n"
"ds_read_b64_tr_b16 a[234:235], v253 offset:256\n"
"v_mfma_f32_16x16x32_bf16 v[220:223], a[240:243], a[8:11], 0\n"
"v_mfma_f32_16x16x32_bf16 v[220:223], a[244:247], a[24:27], v[220:223]\n"
"s_waitcnt lgkmcnt(12)\n"
"buffer_load_dwordx4 v242, s[32:35], s66 offen offset:3072  lds\n"
"v_mfma_f32_16x16x32_bf16 v[212:215], a[240:243], a[4:7], 0\n"
"ds_read_b64_tr_b16 a[236:237], v253 offset:4096\n"
"v_mfma_f32_16x16x32_bf16 v[212:215], a[244:247], a[20:23], v[212:215]\n"
"s_add_u32 s78, s80, 128\n"
"ds_read_b64_tr_b16 a[238:239], v253 offset:4352\n"
"v_add_u32 v242, s78, v255\n"
"v_xor_b32 v244, 32, v242\n"
"s_add_u32 m0, s85, 81920\n"
"v_mfma_f32_16x16x32_bf16 v[204:207], a[240:243], a[0:3], 0\n"
"buffer_load_dwordx4 v242, s[36:39], 0 offen offset:0  lds\n"
"v_mfma_f32_16x16x32_bf16 v[204:207], a[244:247], a[16:19], v[204:207]\n"
"s_waitcnt lgkmcnt(4)\n"
"v_mfma_f32_16x16x32_bf16 v[104:107], a[248:251], a[32:35], v[104:107]\n"
"ds_read_b64_tr_b16 a[240:241], v252 offset:512\n"
"v_mfma_f32_16x16x32_bf16 v[104:107], a[252:255], a[48:51], v[104:107]\n"
"ds_read_b64_tr_b16 a[242:243], v252 offset:768\n"
"v_mfma_f32_16x16x32_bf16 v[112:115], a[248:251], a[36:39], v[112:115]\n"
"buffer_load_dwordx4 v242, s[36:39], s67 offen offset:1024  lds\n"
"v_mfma_f32_16x16x32_bf16 v[112:115], a[252:255], a[52:55], v[112:115]\n"
"v_mfma_f32_16x16x32_bf16 v[120:123], a[248:251], a[40:43], v[120:123]\n"
"buffer_load_dwordx4 v244, s[36:39], s68 offen offset:2048  lds\n"
"ds_read_b64_tr_b16 a[244:245], v252 offset:4608\n"
"v_mfma_f32_16x16x32_bf16 v[120:123], a[252:255], a[56:59], v[120:123]\n"
"ds_read_b64_tr_b16 a[246:247], v252 offset:4864\n"
"v_mfma_f32_16x16x32_bf16 v[128:131], a[248:251], a[44:47], v[128:131]\n"
"v_mfma_f32_16x16x32_bf16 v[128:131], a[252:255], a[60:63], v[128:131]\n"
"s_waitcnt lgkmcnt(4)\n"
"buffer_load_dwordx4 v244, s[36:39], s69 offen offset:3072  lds\n"
"v_mfma_f32_16x16x32_bf16 v[132:135], a[232:235], a[44:47], v[132:135]\n"
"ds_read_b64_tr_b16 a[248:249], v253 offset:512\n"
"v_mfma_f32_16x16x32_bf16 v[132:135], a[236:239], a[60:63], v[132:135]\n"
"s_add_u32 s78, s80, 262272\n"
"ds_read_b64_tr_b16 a[250:251], v253 offset:768\n"
"v_add_u32 v242, s78, v255\n"
"v_xor_b32 v244, 32, v242\n"
"s_add_u32 m0, s85, 86016\n"
"v_mfma_f32_16x16x32_bf16 v[124:127], a[232:235], a[40:43], v[124:127]\n"
"buffer_load_dwordx4 v242, s[36:39], 0 offen offset:0  lds\n"
"v_mfma_f32_16x16x32_bf16 v[124:127], a[236:239], a[56:59], v[124:127]\n"
"v_mfma_f32_16x16x32_bf16 v[116:119], a[232:235], a[36:39], v[116:119]\n"
"ds_read_b64_tr_b16 a[252:253], v253 offset:4608\n"
"v_mfma_f32_16x16x32_bf16 v[116:119], a[236:239], a[52:55], v[116:119]\n"
"ds_read_b64_tr_b16 a[254:255], v253 offset:4864\n"
"v_mfma_f32_16x16x32_bf16 v[108:111], a[232:235], a[32:35], v[108:111]\n"
"buffer_load_dwordx4 v242, s[36:39], s67 offen offset:1024  lds\n"
"v_mfma_f32_16x16x32_bf16 v[108:111], a[236:239], a[48:51], v[108:111]\n"
"s_waitcnt lgkmcnt(4)\n"
"v_mfma_f32_16x16x32_bf16 v[136:139], a[240:243], a[32:35], v[136:139]\n"
"s_waitcnt vmcnt(18)\n"
"s_barrier\n"
"ds_read_b64_tr_b16 a[232:233], v250 offset:0\n"
"v_mfma_f32_16x16x32_bf16 v[136:139], a[244:247], a[48:51], v[136:139]\n"
"buffer_load_dwordx4 v244, s[36:39], s68 offen offset:2048  lds\n"
"ds_read_b64_tr_b16 a[234:235], v250 offset:256\n"
"v_mfma_f32_16x16x32_bf16 v[144:147], a[240:243], a[36:39], v[144:147]\n"
"v_mfma_f32_16x16x32_bf16 v[144:147], a[244:247], a[52:55], v[144:147]\n"
"s_add_u32 s79, s79, 128\n"
"buffer_load_dwordx4 v244, s[36:39], s69 offen offset:3072  lds\n"
"v_mfma_f32_16x16x32_bf16 v[152:155], a[240:243], a[40:43], v[152:155]\n"
"s_sub_u32 s78, s79, 896\n"
"ds_read_b64_tr_b16 a[236:237], v250 offset:4096\n"
"v_mfma_f32_16x16x32_bf16 v[152:155], a[244:247], a[56:59], v[152:155]\n"
"s_min_u32 s79, s79, s78\n"
"ds_read_b64_tr_b16 a[238:239], v250 offset:4352\n"
"v_add_u32 v242, s79, v243\n"
"s_add_u32 m0, s71, 0\n"
"v_mfma_f32_16x16x32_bf16 v[160:163], a[240:243], a[44:47], v[160:163]\n"
"buffer_load_dwordx4 v242, s[32:35], 0 offen offset:0  lds\n"
"v_mfma_f32_16x16x32_bf16 v[160:163], a[244:247], a[60:63], v[160:163]\n"
"s_waitcnt lgkmcnt(4)\n"
"v_mfma_f32_16x16x32_bf16 v[164:167], a[248:251], a[44:47], v[164:167]\n"
"ds_read_b64_tr_b16 a[240:241], v251 offset:0\n"
"v_mfma_f32_16x16x32_bf16 v[164:167], a[252:255], a[60:63], v[164:167]\n"
"ds_read_b64_tr_b16 a[242:243], v251 offset:256\n"
"v_mfma_f32_16x16x32_bf16 v[156:159], a[248:251], a[40:43], v[156:159]\n"
"buffer_load_dwordx4 v242, s[32:35], s64 offen offset:1024  lds\n"
"v_mfma_f32_16x16x32_bf16 v[156:159], a[252:255], a[56:59], v[156:159]\n"
"v_mfma_f32_16x16x32_bf16 v[148:151], a[248:251], a[36:39], v[148:151]\n"
"buffer_load_dwordx4 v242, s[32:35], s65 offen offset:2048  lds\n"
"ds_read_b64_tr_b16 a[244:245], v251 offset:4096\n"
"v_mfma_f32_16x16x32_bf16 v[148:151], a[252:255], a[52:55], v[148:151]\n"
"ds_read_b64_tr_b16 a[246:247], v251 offset:4352\n"
"v_mfma_f32_16x16x32_bf16 v[140:143], a[248:251], a[32:35], v[140:143]\n"
"s_add_u32 s80, s80, 524288\n"
"v_mfma_f32_16x16x32_bf16 v[140:143], a[252:255], a[48:51], v[140:143]\n"
"s_waitcnt lgkmcnt(4)\n"
"buffer_load_dwordx4 v242, s[32:35], s66 offen offset:3072  lds\n"
"v_mfma_f32_16x16x32_bf16 v[168:171], a[232:235], a[32:35], v[168:171]\n"
"s_sub_u32 s78, s80, 3670016\n"
"ds_read_b64_tr_b16 a[248:249], v250 offset:512\n"
"v_mfma_f32_16x16x32_bf16 v[168:171], a[236:239], a[48:51], v[168:171]\n"
"s_min_u32 s80, s80, s78\n"
"ds_read_b64_tr_b16 a[250:251], v250 offset:768\n"
"v_add_u32 v242, s80, v255\n"
"v_xor_b32 v244, 32, v242\n"
"s_add_u32 m0, s85, 98304\n"
"v_mfma_f32_16x16x32_bf16 v[176:179], a[232:235], a[36:39], v[176:179]\n"
"buffer_load_dwordx4 v242, s[36:39], 0 offen offset:0  lds\n"
"v_mfma_f32_16x16x32_bf16 v[176:179], a[236:239], a[52:55], v[176:179]\n"
"v_mfma_f32_16x16x32_bf16 v[184:187], a[232:235], a[40:43], v[184:187]\n"
"ds_read_b64_tr_b16 a[252:253], v250 offset:4608\n"
"v_mfma_f32_16x16x32_bf16 v[184:187], a[236:239], a[56:59], v[184:187]\n"
"ds_read_b64_tr_b16 a[254:255], v250 offset:4864\n"
"v_mfma_f32_16x16x32_bf16 v[192:195], a[232:235], a[44:47], v[192:195]\n"
"buffer_load_dwordx4 v242, s[36:39], s67 offen offset:1024  lds\n"
"v_mfma_f32_16x16x32_bf16 v[192:195], a[236:239], a[60:63], v[192:195]\n"
"s_waitcnt lgkmcnt(4)\n"
"v_mfma_f32_16x16x32_bf16 v[196:199], a[240:243], a[44:47], v[196:199]\n"
"buffer_load_dwordx4 v244, s[36:39], s68 offen offset:2048  lds\n"
"ds_read_b64_tr_b16 a[232:233], v251 offset:512\n"
"v_mfma_f32_16x16x32_bf16 v[196:199], a[244:247], a[60:63], v[196:199]\n"
"ds_read_b64_tr_b16 a[234:235], v251 offset:768\n"
"v_mfma_f32_16x16x32_bf16 v[188:191], a[240:243], a[40:43], v[188:191]\n"
"v_mfma_f32_16x16x32_bf16 v[188:191], a[244:247], a[56:59], v[188:191]\n"
"buffer_load_dwordx4 v244, s[36:39], s69 offen offset:3072  lds\n"
"v_mfma_f32_16x16x32_bf16 v[180:183], a[240:243], a[36:39], v[180:183]\n"
"ds_read_b64_tr_b16 a[236:237], v251 offset:4608\n"
"v_mfma_f32_16x16x32_bf16 v[180:183], a[244:247], a[52:55], v[180:183]\n"
"s_add_u32 s78, s80, 262144\n"
"ds_read_b64_tr_b16 a[238:239], v251 offset:4864\n"
"v_add_u32 v242, s78, v255\n"
"v_xor_b32 v244, 32, v242\n"
"s_add_u32 m0, s85, 102400\n"
"v_mfma_f32_16x16x32_bf16 v[172:175], a[240:243], a[32:35], v[172:175]\n"
"buffer_load_dwordx4 v242, s[36:39], 0 offen offset:0  lds\n"
"v_mfma_f32_16x16x32_bf16 v[172:175], a[244:247], a[48:51], v[172:175]\n"
"s_waitcnt lgkmcnt(4)\n"
"v_mfma_f32_16x16x32_bf16 v[200:203], a[248:251], a[32:35], v[200:203]\n"
"s_waitcnt vmcnt(17)\n"
"s_barrier\n"
"ds_read_b64_tr_b16 a[240:241], v250 offset:16384\n"
"v_mfma_f32_16x16x32_bf16 v[200:203], a[252:255], a[48:51], v[200:203]\n"
"buffer_load_dwordx4 v242, s[36:39], s67 offen offset:1024  lds\n"
"ds_read_b64_tr_b16 a[242:243], v250 offset:16640\n"
"v_mfma_f32_16x16x32_bf16 v[208:211], a[248:251], a[36:39], v[208:211]\n"
"ds_read_b128 a[64:67], v254 offset:32768\n"
"ds_read_b128 a[68:71], v254 offset:34816\n"
"v_mfma_f32_16x16x32_bf16 v[208:211], a[252:255], a[52:55], v[208:211]\n"
"ds_read_b128 a[72:75], v254 offset:36864\n"
"ds_read_b128 a[76:79], v254 offset:38912\n"
"v_mfma_f32_16x16x32_bf16 v[216:219], a[248:251], a[40:43], v[216:219]\n"
"buffer_load_dwordx4 v244, s[36:39], s68 offen offset:2048  lds\n"
"ds_read_b64_tr_b16 a[244:245], v250 offset:20480\n"
"v_mfma_f32_16x16x32_bf16 v[216:219], a[252:255], a[56:59], v[216:219]\n"
"ds_read_b64_tr_b16 a[246:247], v250 offset:20736\n"
"v_mfma_f32_16x16x32_bf16 v[224:227], a[248:251], a[44:47], v[224:227]\n"
"ds_read_b128 a[80:83], v254 offset:33280\n"
"ds_read_b128 a[84:87], v254 offset:35328\n"
"v_mfma_f32_16x16x32_bf16 v[224:227], a[252:255], a[60:63], v[224:227]\n"
"s_waitcnt lgkmcnt(10)\n"
"ds_read_b128 a[88:91], v254 offset:37376\n"
"ds_read_b128 a[92:95], v254 offset:39424\n"
"v_mfma_f32_16x16x32_bf16 v[228:231], a[232:235], a[44:47], v[228:231]\n"
"buffer_load_dwordx4 v244, s[36:39], s69 offen offset:3072  lds\n"
"ds_read_b64_tr_b16 a[248:249], v251 offset:16384\n"
"v_mfma_f32_16x16x32_bf16 v[228:231], a[236:239], a[60:63], v[228:231]\n"
"s_add_u32 s78, s80, 128\n"
"ds_read_b64_tr_b16 a[250:251], v251 offset:16640\n"
"v_add_u32 v242, s78, v255\n"
"v_xor_b32 v244, 32, v242\n"
"s_add_u32 m0, s85, 114688\n"
"v_mfma_f32_16x16x32_bf16 v[220:223], a[232:235], a[40:43], v[220:223]\n"
"buffer_load_dwordx4 v242, s[36:39], 0 offen offset:0  lds\n"
"v_mfma_f32_16x16x32_bf16 v[220:223], a[236:239], a[56:59], v[220:223]\n"
"s_waitcnt lgkmcnt(12)\n"
"v_mfma_f32_16x16x32_bf16 v[212:215], a[232:235], a[36:39], v[212:215]\n"
"ds_read_b64_tr_b16 a[252:253], v251 offset:20480\n"
"v_mfma_f32_16x16x32_bf16 v[212:215], a[236:239], a[52:55], v[212:215]\n"
"ds_read_b64_tr_b16 a[254:255], v251 offset:20736\n"
"v_mfma_f32_16x16x32_bf16 v[204:207], a[232:235], a[32:35], v[204:207]\n"
"buffer_load_dwordx4 v242, s[36:39], s67 offen offset:1024  lds\n"
"v_mfma_f32_16x16x32_bf16 v[204:207], a[236:239], a[48:51], v[204:207]\n"
"s_waitcnt lgkmcnt(4)\n"
"v_mfma_f32_16x16x32_bf16 v[104:107], a[240:243], a[64:67], v[104:107]\n"
"buffer_load_dwordx4 v244, s[36:39], s68 offen offset:2048  lds\n"
"ds_read_b64_tr_b16 a[232:233], v250 offset:16896\n"
"v_mfma_f32_16x16x32_bf16 v[104:107], a[244:247], a[80:83], v[104:107]\n"
"ds_read_b64_tr_b16 a[234:235], v250 offset:17152\n"
"v_mfma_f32_16x16x32_bf16 v[112:115], a[240:243], a[68:71], v[112:115]\n"
"v_mfma_f32_16x16x32_bf16 v[112:115], a[244:247], a[84:87], v[112:115]\n"
"buffer_load_dwordx4 v244, s[36:39], s69 offen offset:3072  lds\n"
"v_mfma_f32_16x16x32_bf16 v[120:123], a[240:243], a[72:75], v[120:123]\n"
"ds_read_b64_tr_b16 a[236:237], v250 offset:20992\n"
"v_mfma_f32_16x16x32_bf16 v[120:123], a[244:247], a[88:91], v[120:123]\n"
"s_add_u32 s78, s80, 262272\n"
"ds_read_b64_tr_b16 a[238:239], v250 offset:21248\n"
"v_add_u32 v242, s78, v255\n"
"v_xor_b32 v244, 32, v242\n"
"s_add_u32 m0, s85, 118784\n"
"v_mfma_f32_16x16x32_bf16 v[128:131], a[240:243], a[76:79], v[128:131]\n"
"buffer_load_dwordx4 v242, s[36:39], 0 offen offset:0  lds\n"
"v_mfma_f32_16x16x32_bf16 v[128:131], a[244:247], a[92:95], v[128:131]\n"
"s_waitcnt lgkmcnt(4)\n"
"v_mfma_f32_16x16x32_bf16 v[132:135], a[248:251], a[76:79], v[132:135]\n"
"ds_read_b64_tr_b16 a[240:241], v251 offset:16896\n"
"v_mfma_f32_16x16x32_bf16 v[132:135], a[252:255], a[92:95], v[132:135]\n"
"ds_read_b64_tr_b16 a[242:243], v251 offset:17152\n"
"v_mfma_f32_16x16x32_bf16 v[124:127], a[248:251], a[72:75], v[124:127]\n"
"buffer_load_dwordx4 v242, s[36:39], s67 offen offset:1024  lds\n"
"v_mfma_f32_16x16x32_bf16 v[124:127], a[252:255], a[88:91], v[124:127]\n"
"v_mfma_f32_16x16x32_bf16 v[116:119], a[248:251], a[68:71], v[116:119]\n"
"buffer_load_dwordx4 v244, s[36:39], s68 offen offset:2048  lds\n"
"ds_read_b64_tr_b16 a[244:245], v251 offset:20992\n"
"v_mfma_f32_16x16x32_bf16 v[116:119], a[252:255], a[84:87], v[116:119]\n"
"s_add_u32 s79, s79, 128\n"
"ds_read_b64_tr_b16 a[246:247], v251 offset:21248\n"
"v_mfma_f32_16x16x32_bf16 v[108:111], a[248:251], a[64:67], v[108:111]\n"
"s_sub_u32 s78, s79, 896\n"
"v_mfma_f32_16x16x32_bf16 v[108:111], a[252:255], a[80:83], v[108:111]\n"
"s_waitcnt lgkmcnt(4)\n"
"buffer_load_dwordx4 v244, s[36:39], s69 offen offset:3072  lds\n"
"v_mfma_f32_16x16x32_bf16 v[136:139], a[232:235], a[64:67], v[136:139]\n"
"s_waitcnt vmcnt(20)\n"
"s_barrier\n"
"ds_read_b64_tr_b16 a[248:249], v250 offset:32768\n"
"v_mfma_f32_16x16x32_bf16 v[136:139], a[236:239], a[80:83], v[136:139]\n"
"s_min_u32 s79, s79, s78\n"
"ds_read_b64_tr_b16 a[250:251], v250 offset:33024\n"
"v_add_u32 v242, s79, v243\n"
"s_add_u32 m0, s71, 16384\n"
"v_mfma_f32_16x16x32_bf16 v[144:147], a[232:235], a[68:71], v[144:147]\n"
"buffer_load_dwordx4 v242, s[32:35], 0 offen offset:0  lds\n"
"v_mfma_f32_16x16x32_bf16 v[144:147], a[236:239], a[84:87], v[144:147]\n"
"v_mfma_f32_16x16x32_bf16 v[152:155], a[232:235], a[72:75], v[152:155]\n"
"ds_read_b64_tr_b16 a[252:253], v250 offset:36864\n"
"v_mfma_f32_16x16x32_bf16 v[152:155], a[236:239], a[88:91], v[152:155]\n"
"ds_read_b64_tr_b16 a[254:255], v250 offset:37120\n"
"v_mfma_f32_16x16x32_bf16 v[160:163], a[232:235], a[76:79], v[160:163]\n"
"buffer_load_dwordx4 v242, s[32:35], s64 offen offset:1024  lds\n"
"v_mfma_f32_16x16x32_bf16 v[160:163], a[236:239], a[92:95], v[160:163]\n"
"s_waitcnt lgkmcnt(4)\n"
"v_mfma_f32_16x16x32_bf16 v[164:167], a[240:243], a[76:79], v[164:167]\n"
"buffer_load_dwordx4 v242, s[32:35], s65 offen offset:2048  lds\n"
"ds_read_b64_tr_b16 a[232:233], v251 offset:32768\n"
"v_mfma_f32_16x16x32_bf16 v[164:167], a[244:247], a[92:95], v[164:167]\n"
"ds_read_b64_tr_b16 a[234:235], v251 offset:33024\n"
"v_mfma_f32_16x16x32_bf16 v[156:159], a[240:243], a[72:75], v[156:159]\n"
"v_mfma_f32_16x16x32_bf16 v[156:159], a[244:247], a[88:91], v[156:159]\n"
"s_add_u32 s80, s80, 524288\n"
"buffer_load_dwordx4 v242, s[32:35], s66 offen offset:3072  lds\n"
"v_mfma_f32_16x16x32_bf16 v[148:151], a[240:243], a[68:71], v[148:151]\n"
"s_sub_u32 s78, s80, 3670016\n"
"ds_read_b64_tr_b16 a[236:237], v251 offset:36864\n"
"v_mfma_f32_16x16x32_bf16 v[148:151], a[244:247], a[84:87], v[148:151]\n"
"s_min_u32 s80, s80, s78\n"
"ds_read_b64_tr_b16 a[238:239], v251 offset:37120\n"
"v_add_u32 v242, s80, v255\n"
"v_xor_b32 v244, 32, v242\n"
"s_add_u32 m0, s85, 49152\n"
"v_mfma_f32_16x16x32_bf16 v[140:143], a[240:243], a[64:67], v[140:143]\n"
"buffer_load_dwordx4 v242, s[36:39], 0 offen offset:0  lds\n"
"v_mfma_f32_16x16x32_bf16 v[140:143], a[244:247], a[80:83], v[140:143]\n"
"s_waitcnt lgkmcnt(4)\n"
"v_mfma_f32_16x16x32_bf16 v[168:171], a[248:251], a[64:67], v[168:171]\n"
"ds_read_b64_tr_b16 a[240:241], v250 offset:33280\n"
"s_mov_b32 exec_lo, 0xcccccccc\n"
"s_mov_b32 exec_hi, 0xcccccccc\n"
"buffer_load_dword v248, v247, s[36:39], 0 offen\n"
"s_mov_b64 exec, -1\n"
"v_mfma_f32_16x16x32_bf16 v[168:171], a[252:255], a[80:83], v[168:171]\n"
"ds_read_b64_tr_b16 a[242:243], v250 offset:33536\n"
"v_mfma_f32_16x16x32_bf16 v[176:179], a[248:251], a[68:71], v[176:179]\n"
"buffer_load_dwordx4 v242, s[36:39], s67 offen offset:1024  lds\n"
"v_mfma_f32_16x16x32_bf16 v[176:179], a[252:255], a[84:87], v[176:179]\n"
"v_mfma_f32_16x16x32_bf16 v[184:187], a[248:251], a[72:75], v[184:187]\n"
"buffer_load_dwordx4 v244, s[36:39], s68 offen offset:2048  lds\n"
"ds_read_b64_tr_b16 a[244:245], v250 offset:37376\n"
"v_mfma_f32_16x16x32_bf16 v[184:187], a[252:255], a[88:91], v[184:187]\n"
"ds_read_b64_tr_b16 a[246:247], v250 offset:37632\n"
"v_mfma_f32_16x16x32_bf16 v[192:195], a[248:251], a[76:79], v[192:195]\n"
"v_mfma_f32_16x16x32_bf16 v[192:195], a[252:255], a[92:95], v[192:195]\n"
"s_waitcnt lgkmcnt(4)\n"
"buffer_load_dwordx4 v244, s[36:39], s69 offen offset:3072  lds\n"
"v_mfma_f32_16x16x32_bf16 v[196:199], a[232:235], a[76:79], v[196:199]\n"
"ds_read_b64_tr_b16 a[248:249], v251 offset:33280\n"
"v_mfma_f32_16x16x32_bf16 v[196:199], a[236:239], a[92:95], v[196:199]\n"
"s_add_u32 s78, s80, 262144\n"
"ds_read_b64_tr_b16 a[250:251], v251 offset:33536\n"
"v_add_u32 v242, s78, v255\n"
"v_xor_b32 v244, 32, v242\n"
"s_add_u32 m0, s85, 53248\n"
"v_mfma_f32_16x16x32_bf16 v[188:191], a[232:235], a[72:75], v[188:191]\n"
"buffer_load_dwordx4 v242, s[36:39], 0 offen offset:0  lds\n"
"v_mfma_f32_16x16x32_bf16 v[188:191], a[236:239], a[88:91], v[188:191]\n"
"v_mfma_f32_16x16x32_bf16 v[180:183], a[232:235], a[68:71], v[180:183]\n"
"ds_read_b64_tr_b16 a[252:253], v251 offset:37376\n"
"v_mfma_f32_16x16x32_bf16 v[180:183], a[236:239], a[84:87], v[180:183]\n"
"ds_read_b64_tr_b16 a[254:255], v251 offset:37632\n"
"v_mfma_f32_16x16x32_bf16 v[172:175], a[232:235], a[64:67], v[172:175]\n"
"buffer_load_dwordx4 v242, s[36:39], s67 offen offset:1024  lds\n"
"v_mfma_f32_16x16x32_bf16 v[172:175], a[236:239], a[80:83], v[172:175]\n"
"s_waitcnt lgkmcnt(4)\n"
"v_mfma_f32_16x16x32_bf16 v[200:203], a[240:243], a[64:67], v[200:203]\n"
"s_waitcnt vmcnt(19)\n"
"s_barrier\n"
"ds_read_b64_tr_b16 a[232:233], v250 offset:49152\n"
"v_mfma_f32_16x16x32_bf16 v[200:203], a[244:247], a[80:83], v[200:203]\n"
"buffer_load_dwordx4 v244, s[36:39], s68 offen offset:2048  lds\n"
"ds_read_b64_tr_b16 a[234:235], v250 offset:49408\n"
"v_mfma_f32_16x16x32_bf16 v[208:211], a[240:243], a[68:71], v[208:211]\n"
"ds_read_b128 a[96:99], v254 offset:0\n"
"ds_read_b128 a[100:103], v254 offset:2048\n"
"v_mfma_f32_16x16x32_bf16 v[208:211], a[244:247], a[84:87], v[208:211]\n"
"ds_read_b128 a[104:107], v254 offset:4096\n"
"ds_read_b128 a[108:111], v254 offset:6144\n"
"v_mfma_f32_16x16x32_bf16 v[216:219], a[240:243], a[72:75], v[216:219]\n"
"buffer_load_dwordx4 v244, s[36:39], s69 offen offset:3072  lds\n"
"ds_read_b64_tr_b16 a[236:237], v250 offset:53248\n"
"v_mfma_f32_16x16x32_bf16 v[216:219], a[244:247], a[88:91], v[216:219]\n"
"s_add_u32 s78, s80, 128\n"
"ds_read_b64_tr_b16 a[238:239], v250 offset:53504\n"
"v_add_u32 v242, s78, v255\n"
"v_xor_b32 v244, 32, v242\n"
"s_add_u32 m0, s85, 65536\n"
"v_mfma_f32_16x16x32_bf16 v[224:227], a[240:243], a[76:79], v[224:227]\n"
"buffer_load_dwordx4 v242, s[36:39], 0 offen offset:0  lds\n"
"ds_read_b128 a[112:115], v254 offset:512\n"
"ds_read_b128 a[116:119], v254 offset:2560\n"
"v_mfma_f32_16x16x32_bf16 v[224:227], a[244:247], a[92:95], v[224:227]\n"
"s_waitcnt lgkmcnt(10)\n"
"ds_read_b128 a[120:123], v254 offset:4608\n"
"ds_read_b128 a[124:127], v254 offset:6656\n"
"v_mfma_f32_16x16x32_bf16 v[228:231], a[248:251], a[76:79], v[228:231]\n"
"ds_read_b64_tr_b16 a[240:241], v251 offset:49152\n"
"v_mfma_f32_16x16x32_bf16 v[228:231], a[252:255], a[92:95], v[228:231]\n"
"ds_read_b64_tr_b16 a[242:243], v251 offset:49408\n"
"v_mfma_f32_16x16x32_bf16 v[220:223], a[248:251], a[72:75], v[220:223]\n"
"buffer_load_dwordx4 v242, s[36:39], s67 offen offset:1024  lds\n"
"v_mfma_f32_16x16x32_bf16 v[220:223], a[252:255], a[88:91], v[220:223]\n"
"s_waitcnt lgkmcnt(12)\n"
"v_mfma_f32_16x16x32_bf16 v[212:215], a[248:251], a[68:71], v[212:215]\n"
"buffer_load_dwordx4 v244, s[36:39], s68 offen offset:2048  lds\n"
"ds_read_b64_tr_b16 a[244:245], v251 offset:53248\n"
"v_mfma_f32_16x16x32_bf16 v[212:215], a[252:255], a[84:87], v[212:215]\n"
"ds_read_b64_tr_b16 a[246:247], v251 offset:53504\n"
"v_mfma_f32_16x16x32_bf16 v[204:207], a[248:251], a[64:67], v[204:207]\n"
"v_mfma_f32_16x16x32_bf16 v[204:207], a[252:255], a[80:83], v[204:207]\n"
"s_waitcnt lgkmcnt(4)\n"
"buffer_load_dwordx4 v244, s[36:39], s69 offen offset:3072  lds\n"
"v_mfma_f32_16x16x32_bf16 v[104:107], a[232:235], a[96:99], v[104:107]\n"
"ds_read_b64_tr_b16 a[248:249], v250 offset:49664\n"
"v_mfma_f32_16x16x32_bf16 v[104:107], a[236:239], a[112:115], v[104:107]\n"
"s_add_u32 s78, s80, 262272\n"
"ds_read_b64_tr_b16 a[250:251], v250 offset:49920\n"
"v_add_u32 v242, s78, v255\n"
"v_xor_b32 v244, 32, v242\n"
"s_add_u32 m0, s85, 69632\n"
"v_mfma_f32_16x16x32_bf16 v[112:115], a[232:235], a[100:103], v[112:115]\n"
"buffer_load_dwordx4 v242, s[36:39], 0 offen offset:0  lds\n"
"v_mfma_f32_16x16x32_bf16 v[112:115], a[236:239], a[116:119], v[112:115]\n"
"v_mfma_f32_16x16x32_bf16 v[120:123], a[232:235], a[104:107], v[120:123]\n"
"ds_read_b64_tr_b16 a[252:253], v250 offset:53760\n"
"v_mfma_f32_16x16x32_bf16 v[120:123], a[236:239], a[120:123], v[120:123]\n"
"ds_read_b64_tr_b16 a[254:255], v250 offset:54016\n"
"v_mfma_f32_16x16x32_bf16 v[128:131], a[232:235], a[108:111], v[128:131]\n"
"buffer_load_dwordx4 v242, s[36:39], s67 offen offset:1024  lds\n"
"v_mfma_f32_16x16x32_bf16 v[128:131], a[236:239], a[124:127], v[128:131]\n"
"s_waitcnt lgkmcnt(4)\n"
"v_mfma_f32_16x16x32_bf16 v[132:135], a[240:243], a[108:111], v[132:135]\n"
"buffer_load_dwordx4 v244, s[36:39], s68 offen offset:2048  lds\n"
"ds_read_b64_tr_b16 a[232:233], v251 offset:49664\n"
"v_mfma_f32_16x16x32_bf16 v[132:135], a[244:247], a[124:127], v[132:135]\n"
"ds_read_b64_tr_b16 a[234:235], v251 offset:49920\n"
"v_mfma_f32_16x16x32_bf16 v[124:127], a[240:243], a[104:107], v[124:127]\n"
"v_mfma_f32_16x16x32_bf16 v[124:127], a[244:247], a[120:123], v[124:127]\n"
"s_add_u32 s79, s79, 128\n"
"buffer_load_dwordx4 v244, s[36:39], s69 offen offset:3072  lds\n"
"v_mfma_f32_16x16x32_bf16 v[116:119], a[240:243], a[100:103], v[116:119]\n"
"s_sub_u32 s78, s79, 896\n"
"ds_read_b64_tr_b16 a[236:237], v251 offset:53760\n"
"v_mfma_f32_16x16x32_bf16 v[116:119], a[244:247], a[116:119], v[116:119]\n"
"s_min_u32 s79, s79, s78\n"
"ds_read_b64_tr_b16 a[238:239], v251 offset:54016\n"
"v_add_u32 v242, s79, v243\n"
"s_add_u32 m0, s71, 32768\n"
"v_mfma_f32_16x16x32_bf16 v[108:111], a[240:243], a[96:99], v[108:111]\n"
"buffer_load_dwordx4 v242, s[32:35], 0 offen offset:0  lds\n"
"v_mfma_f32_16x16x32_bf16 v[108:111], a[244:247], a[112:115], v[108:111]\n"
"s_waitcnt lgkmcnt(4)\n"
"v_mfma_f32_16x16x32_bf16 v[136:139], a[248:251], a[96:99], v[136:139]\n"
"s_waitcnt vmcnt(22)\n"
"s_barrier\n"
"ds_read_b64_tr_b16 a[240:241], v252 offset:0\n"
"v_mfma_f32_16x16x32_bf16 v[136:139], a[252:255], a[112:115], v[136:139]\n"
"ds_read_b64_tr_b16 a[242:243], v252 offset:256\n"
"v_mfma_f32_16x16x32_bf16 v[144:147], a[248:251], a[100:103], v[144:147]\n"
"buffer_load_dwordx4 v242, s[32:35], s64 offen offset:1024  lds\n"
"v_mfma_f32_16x16x32_bf16 v[144:147], a[252:255], a[116:119], v[144:147]\n"
"v_mfma_f32_16x16x32_bf16 v[152:155], a[248:251], a[104:107], v[152:155]\n"
"buffer_load_dwordx4 v242, s[32:35], s65 offen offset:2048  lds\n"
"ds_read_b64_tr_b16 a[244:245], v252 offset:4096\n"
"v_mfma_f32_16x16x32_bf16 v[152:155], a[252:255], a[120:123], v[152:155]\n"
"ds_read_b64_tr_b16 a[246:247], v252 offset:4352\n"
"v_mfma_f32_16x16x32_bf16 v[160:163], a[248:251], a[108:111], v[160:163]\n"
"s_add_u32 s80, s80, 524288\n"
"v_mfma_f32_16x16x32_bf16 v[160:163], a[252:255], a[124:127], v[160:163]\n"
"s_waitcnt lgkmcnt(4)\n"
"buffer_load_dwordx4 v242, s[32:35], s66 offen offset:3072  lds\n"
"v_mfma_f32_16x16x32_bf16 v[164:167], a[232:235], a[108:111], v[164:167]\n"
"s_sub_u32 s78, s80, 3670016\n"
"ds_read_b64_tr_b16 a[248:249], v253 offset:0\n"
"v_mfma_f32_16x16x32_bf16 v[164:167], a[236:239], a[124:127], v[164:167]\n"
"s_min_u32 s80, s80, s78\n"
"ds_read_b64_tr_b16 a[250:251], v253 offset:256\n"
"v_add_u32 v242, s80, v255\n"
"v_xor_b32 v244, 32, v242\n"
"s_add_u32 m0, s85, 81920\n"
"v_mfma_f32_16x16x32_bf16 v[156:159], a[232:235], a[104:107], v[156:159]\n"
"buffer_load_dwordx4 v242, s[36:39], 0 offen offset:0  lds\n"
"v_mfma_f32_16x16x32_bf16 v[156:159], a[236:239], a[120:123], v[156:159]\n"
"v_mfma_f32_16x16x32_bf16 v[148:151], a[232:235], a[100:103], v[148:151]\n"
"ds_read_b64_tr_b16 a[252:253], v253 offset:4096\n"
"v_mfma_f32_16x16x32_bf16 v[148:151], a[236:239], a[116:119], v[148:151]\n"
"ds_read_b64_tr_b16 a[254:255], v253 offset:4352\n"
"v_mfma_f32_16x16x32_bf16 v[140:143], a[232:235], a[96:99], v[140:143]\n"
"buffer_load_dwordx4 v242, s[36:39], s67 offen offset:1024  lds\n"
"v_mfma_f32_16x16x32_bf16 v[140:143], a[236:239], a[112:115], v[140:143]\n"
"s_waitcnt lgkmcnt(4)\n"
"v_mfma_f32_16x16x32_bf16 v[168:171], a[240:243], a[96:99], v[168:171]\n"
"buffer_load_dwordx4 v244, s[36:39], s68 offen offset:2048  lds\n"
"ds_read_b64_tr_b16 a[232:233], v252 offset:512\n"
"v_mfma_f32_16x16x32_bf16 v[168:171], a[244:247], a[112:115], v[168:171]\n"
"ds_read_b64_tr_b16 a[234:235], v252 offset:768\n"
"v_mfma_f32_16x16x32_bf16 v[176:179], a[240:243], a[100:103], v[176:179]\n"
"v_mfma_f32_16x16x32_bf16 v[176:179], a[244:247], a[116:119], v[176:179]\n"
"buffer_load_dwordx4 v244, s[36:39], s69 offen offset:3072  lds\n"
"v_mfma_f32_16x16x32_bf16 v[184:187], a[240:243], a[104:107], v[184:187]\n"
"ds_read_b64_tr_b16 a[236:237], v252 offset:4608\n"
"v_mfma_f32_16x16x32_bf16 v[184:187], a[244:247], a[120:123], v[184:187]\n"
"s_add_u32 s78, s80, 262144\n"
"ds_read_b64_tr_b16 a[238:239], v252 offset:4864\n"
"v_add_u32 v242, s78, v255\n"
"v_xor_b32 v244, 32, v242\n"
"s_add_u32 m0, s85, 86016\n"
"v_mfma_f32_16x16x32_bf16 v[192:195], a[240:243], a[108:111], v[192:195]\n"
"buffer_load_dwordx4 v242, s[36:39], 0 offen offset:0  lds\n"
"v_mfma_f32_16x16x32_bf16 v[192:195], a[244:247], a[124:127], v[192:195]\n"
"s_waitcnt lgkmcnt(4)\n"
"v_mfma_f32_16x16x32_bf16 v[196:199], a[248:251], a[108:111], v[196:199]\n"
"ds_read_b64_tr_b16 a[240:241], v253 offset:512\n"
"v_mfma_f32_16x16x32_bf16 v[196:199], a[252:255], a[124:127], v[196:199]\n"
"ds_read_b64_tr_b16 a[242:243], v253 offset:768\n"
"v_mfma_f32_16x16x32_bf16 v[188:191], a[248:251], a[104:107], v[188:191]\n"
"buffer_load_dwordx4 v242, s[36:39], s67 offen offset:1024  lds\n"
"v_mfma_f32_16x16x32_bf16 v[188:191], a[252:255], a[120:123], v[188:191]\n"
"v_mfma_f32_16x16x32_bf16 v[180:183], a[248:251], a[100:103], v[180:183]\n"
"buffer_load_dwordx4 v244, s[36:39], s68 offen offset:2048  lds\n"
"ds_read_b64_tr_b16 a[244:245], v253 offset:4608\n"
"v_mfma_f32_16x16x32_bf16 v[180:183], a[252:255], a[116:119], v[180:183]\n"
"ds_read_b64_tr_b16 a[246:247], v253 offset:4864\n"
"v_mfma_f32_16x16x32_bf16 v[172:175], a[248:251], a[96:99], v[172:175]\n"
"v_mfma_f32_16x16x32_bf16 v[172:175], a[252:255], a[112:115], v[172:175]\n"
"s_waitcnt lgkmcnt(4)\n"
"buffer_load_dwordx4 v244, s[36:39], s69 offen offset:3072  lds\n"
"v_mfma_f32_16x16x32_bf16 v[200:203], a[232:235], a[96:99], v[200:203]\n"
"s_waitcnt vmcnt(20)\n"
"s_barrier\n"
"ds_read_b64_tr_b16 a[248:249], v250 offset:0\n"
"v_mfma_f32_16x16x32_bf16 v[200:203], a[236:239], a[112:115], v[200:203]\n"
"s_add_u32 s78, s80, 128\n"
"ds_read_b64_tr_b16 a[250:251], v250 offset:256\n"
"v_add_u32 v242, s78, v255\n"
"v_xor_b32 v244, 32, v242\n"
"s_add_u32 m0, s85, 98304\n"
"v_mfma_f32_16x16x32_bf16 v[208:211], a[232:235], a[100:103], v[208:211]\n"
"buffer_load_dwordx4 v242, s[36:39], 0 offen offset:0  lds\n"
"ds_read_b128 a[128:131], v254 offset:16384\n"
"ds_read_b128 a[132:135], v254 offset:18432\n"
"v_mfma_f32_16x16x32_bf16 v[208:211], a[236:239], a[116:119], v[208:211]\n"
"ds_read_b128 a[136:139], v254 offset:20480\n"
"ds_read_b128 a[140:143], v254 offset:22528\n"
"v_mfma_f32_16x16x32_bf16 v[216:219], a[232:235], a[104:107], v[216:219]\n"
"ds_read_b64_tr_b16 a[252:253], v250 offset:4096\n"
"v_mfma_f32_16x16x32_bf16 v[216:219], a[236:239], a[120:123], v[216:219]\n"
"buffer_load_dwordx4 v242, s[36:39], s67 offen offset:1024  lds\n"
"ds_read_b64_tr_b16 a[254:255], v250 offset:4352\n"
"v_mfma_f32_16x16x32_bf16 v[224:227], a[232:235], a[108:111], v[224:227]\n"
"ds_read_b128 a[144:147], v254 offset:16896\n"
"ds_read_b128 a[148:151], v254 offset:18944\n"
"v_mfma_f32_16x16x32_bf16 v[224:227], a[236:239], a[124:127], v[224:227]\n"
"s_waitcnt lgkmcnt(10)\n"
"ds_read_b128 a[152:155], v254 offset:20992\n"
"ds_read_b128 a[156:159], v254 offset:23040\n"
"v_mfma_f32_16x16x32_bf16 v[228:231], a[240:243], a[108:111], v[228:231]\n"
"buffer_load_dwordx4 v244, s[36:39], s68 offen offset:2048  lds\n"
"ds_read_b64_tr_b16 a[232:233], v251 offset:0\n"
"v_mfma_f32_16x16x32_bf16 v[228:231], a[244:247], a[124:127], v[228:231]\n"
"ds_read_b64_tr_b16 a[234:235], v251 offset:256\n"
"v_mfma_f32_16x16x32_bf16 v[220:223], a[240:243], a[104:107], v[220:223]\n"
"v_mfma_f32_16x16x32_bf16 v[220:223], a[244:247], a[120:123], v[220:223]\n"
"s_waitcnt lgkmcnt(12)\n"
"buffer_load_dwordx4 v244, s[36:39], s69 offen offset:3072  lds\n"
"v_mfma_f32_16x16x32_bf16 v[212:215], a[240:243], a[100:103], v[212:215]\n"
"ds_read_b64_tr_b16 a[236:237], v251 offset:4096\n"
"v_mfma_f32_16x16x32_bf16 v[212:215], a[244:247], a[116:119], v[212:215]\n"
"s_add_u32 s78, s80, 262272\n"
"ds_read_b64_tr_b16 a[238:239], v251 offset:4352\n"
"v_add_u32 v242, s78, v255\n"
"v_xor_b32 v244, 32, v242\n"
"s_add_u32 m0, s85, 102400\n"
"v_mfma_f32_16x16x32_bf16 v[204:207], a[240:243], a[96:99], v[204:207]\n"
"buffer_load_dwordx4 v242, s[36:39], 0 offen offset:0  lds\n"
"v_mfma_f32_16x16x32_bf16 v[204:207], a[244:247], a[112:115], v[204:207]\n"
"s_waitcnt lgkmcnt(4)\n"
"v_mfma_f32_16x16x32_bf16 v[104:107], a[248:251], a[128:131], v[104:107]\n"
"ds_read_b64_tr_b16 a[240:241], v250 offset:512\n"
"v_mfma_f32_16x16x32_bf16 v[104:107], a[252:255], a[144:147], v[104:107]\n"
"ds_read_b64_tr_b16 a[242:243], v250 offset:768\n"
"v_mfma_f32_16x16x32_bf16 v[112:115], a[248:251], a[132:135], v[112:115]\n"
"buffer_load_dwordx4 v242, s[36:39], s67 offen offset:1024  lds\n"
"v_mfma_f32_16x16x32_bf16 v[112:115], a[252:255], a[148:151], v[112:115]\n"
"v_mfma_f32_16x16x32_bf16 v[120:123], a[248:251], a[136:139], v[120:123]\n"
"buffer_load_dwordx4 v244, s[36:39], s68 offen offset:2048  lds\n"
"ds_read_b64_tr_b16 a[244:245], v250 offset:4608\n"
"v_mfma_f32_16x16x32_bf16 v[120:123], a[252:255], a[152:155], v[120:123]\n"
"ds_read_b64_tr_b16 a[246:247], v250 offset:4864\n"
"v_mfma_f32_16x16x32_bf16 v[128:131], a[248:251], a[140:143], v[128:131]\n"
"s_add_u32 s79, s79, 128\n"
"v_mfma_f32_16x16x32_bf16 v[128:131], a[252:255], a[156:159], v[128:131]\n"
"s_waitcnt lgkmcnt(4)\n"
"buffer_load_dwordx4 v244, s[36:39], s69 offen offset:3072  lds\n"
"v_mfma_f32_16x16x32_bf16 v[132:135], a[232:235], a[140:143], v[132:135]\n"
"s_sub_u32 s78, s79, 896\n"
"ds_read_b64_tr_b16 a[248:249], v251 offset:512\n"
"v_mfma_f32_16x16x32_bf16 v[132:135], a[236:239], a[156:159], v[132:135]\n"
"s_min_u32 s79, s79, s78\n"
"ds_read_b64_tr_b16 a[250:251], v251 offset:768\n"
"v_add_u32 v242, s79, v243\n"
"s_add_u32 m0, s71, 0\n"
"v_mfma_f32_16x16x32_bf16 v[124:127], a[232:235], a[136:139], v[124:127]\n"
"buffer_load_dwordx4 v242, s[32:35], 0 offen offset:0  lds\n"
"v_mfma_f32_16x16x32_bf16 v[124:127], a[236:239], a[152:155], v[124:127]\n"
"v_mfma_f32_16x16x32_bf16 v[116:119], a[232:235], a[132:135], v[116:119]\n"
"ds_read_b64_tr_b16 a[252:253], v251 offset:4608\n"
"v_mfma_f32_16x16x32_bf16 v[116:119], a[236:239], a[148:151], v[116:119]\n"
"ds_read_b64_tr_b16 a[254:255], v251 offset:4864\n"
"v_mfma_f32_16x16x32_bf16 v[108:111], a[232:235], a[128:131], v[108:111]\n"
"buffer_load_dwordx4 v242, s[32:35], s64 offen offset:1024  lds\n"
"v_mfma_f32_16x16x32_bf16 v[108:111], a[236:239], a[144:147], v[108:111]\n"
"s_waitcnt lgkmcnt(4)\n"
"v_mfma_f32_16x16x32_bf16 v[136:139], a[240:243], a[128:131], v[136:139]\n"
"s_waitcnt vmcnt(22)\n"
"s_barrier\n"
"ds_read_b64_tr_b16 a[232:233], v250 offset:16384\n"
"v_mfma_f32_16x16x32_bf16 v[136:139], a[244:247], a[144:147], v[136:139]\n"
"buffer_load_dwordx4 v242, s[32:35], s65 offen offset:2048  lds\n"
"ds_read_b64_tr_b16 a[234:235], v250 offset:16640\n"
"v_mfma_f32_16x16x32_bf16 v[144:147], a[240:243], a[132:135], v[144:147]\n"
"v_mfma_f32_16x16x32_bf16 v[144:147], a[244:247], a[148:151], v[144:147]\n"
"s_add_u32 s80, s80, 524288\n"
"buffer_load_dwordx4 v242, s[32:35], s66 offen offset:3072  lds\n"
"v_mfma_f32_16x16x32_bf16 v[152:155], a[240:243], a[136:139], v[152:155]\n"
"s_sub_u32 s78, s80, 3670016\n"
"ds_read_b64_tr_b16 a[236:237], v250 offset:20480\n"
"v_mfma_f32_16x16x32_bf16 v[152:155], a[244:247], a[152:155], v[152:155]\n"
"s_min_u32 s80, s80, s78\n"
"ds_read_b64_tr_b16 a[238:239], v250 offset:20736\n"
"v_add_u32 v242, s80, v255\n"
"v_xor_b32 v244, 32, v242\n"
"s_add_u32 m0, s85, 114688\n"
"v_mfma_f32_16x16x32_bf16 v[160:163], a[240:243], a[140:143], v[160:163]\n"
"buffer_load_dwordx4 v242, s[36:39], 0 offen offset:0  lds\n"
"v_mfma_f32_16x16x32_bf16 v[160:163], a[244:247], a[156:159], v[160:163]\n"
"s_waitcnt lgkmcnt(4)\n"
"v_mfma_f32_16x16x32_bf16 v[164:167], a[248:251], a[140:143], v[164:167]\n"
"ds_read_b64_tr_b16 a[240:241], v251 offset:16384\n"
"v_mfma_f32_16x16x32_bf16 v[164:167], a[252:255], a[156:159], v[164:167]\n"
"ds_read_b64_tr_b16 a[242:243], v251 offset:16640\n"
"v_mfma_f32_16x16x32_bf16 v[156:159], a[248:251], a[136:139], v[156:159]\n"
"buffer_load_dwordx4 v242, s[36:39], s67 offen offset:1024  lds\n"
"v_mfma_f32_16x16x32_bf16 v[156:159], a[252:255], a[152:155], v[156:159]\n"
"v_mfma_f32_16x16x32_bf16 v[148:151], a[248:251], a[132:135], v[148:151]\n"
"buffer_load_dwordx4 v244, s[36:39], s68 offen offset:2048  lds\n"
"ds_read_b64_tr_b16 a[244:245], v251 offset:20480\n"
"v_mfma_f32_16x16x32_bf16 v[148:151], a[252:255], a[148:151], v[148:151]\n"
"ds_read_b64_tr_b16 a[246:247], v251 offset:20736\n"
"v_mfma_f32_16x16x32_bf16 v[140:143], a[248:251], a[128:131], v[140:143]\n"
"v_mfma_f32_16x16x32_bf16 v[140:143], a[252:255], a[144:147], v[140:143]\n"
"s_waitcnt lgkmcnt(4)\n"
"buffer_load_dwordx4 v244, s[36:39], s69 offen offset:3072  lds\n"
"v_mfma_f32_16x16x32_bf16 v[168:171], a[232:235], a[128:131], v[168:171]\n"
"ds_read_b64_tr_b16 a[248:249], v250 offset:16896\n"
"v_mfma_f32_16x16x32_bf16 v[168:171], a[236:239], a[144:147], v[168:171]\n"
"s_add_u32 s78, s80, 262144\n"
"ds_read_b64_tr_b16 a[250:251], v250 offset:17152\n"
"v_add_u32 v242, s78, v255\n"
"v_xor_b32 v244, 32, v242\n"
"s_add_u32 m0, s85, 118784\n"
"v_mfma_f32_16x16x32_bf16 v[176:179], a[232:235], a[132:135], v[176:179]\n"
"buffer_load_dwordx4 v242, s[36:39], 0 offen offset:0  lds\n"
"v_mfma_f32_16x16x32_bf16 v[176:179], a[236:239], a[148:151], v[176:179]\n"
"v_mfma_f32_16x16x32_bf16 v[184:187], a[232:235], a[136:139], v[184:187]\n"
"ds_read_b64_tr_b16 a[252:253], v250 offset:20992\n"
"v_mfma_f32_16x16x32_bf16 v[184:187], a[236:239], a[152:155], v[184:187]\n"
"ds_read_b64_tr_b16 a[254:255], v250 offset:21248\n"
"v_mfma_f32_16x16x32_bf16 v[192:195], a[232:235], a[140:143], v[192:195]\n"
"buffer_load_dwordx4 v242, s[36:39], s67 offen offset:1024  lds\n"
"v_mfma_f32_16x16x32_bf16 v[192:195], a[236:239], a[156:159], v[192:195]\n"
"s_waitcnt lgkmcnt(4)\n"
"v_mfma_f32_16x16x32_bf16 v[196:199], a[240:243], a[140:143], v[196:199]\n"
"buffer_load_dwordx4 v244, s[36:39], s68 offen offset:2048  lds\n"
"ds_read_b64_tr_b16 a[232:233], v251 offset:16896\n"
"v_mfma_f32_16x16x32_bf16 v[196:199], a[244:247], a[156:159], v[196:199]\n"
"ds_read_b64_tr_b16 a[234:235], v251 offset:17152\n"
"v_mfma_f32_16x16x32_bf16 v[188:191], a[240:243], a[136:139], v[188:191]\n"
"v_mfma_f32_16x16x32_bf16 v[188:191], a[244:247], a[152:155], v[188:191]\n"
"buffer_load_dwordx4 v244, s[36:39], s69 offen offset:3072  lds\n"
"v_mfma_f32_16x16x32_bf16 v[180:183], a[240:243], a[132:135], v[180:183]\n"
"ds_read_b64_tr_b16 a[236:237], v251 offset:20992\n"
"v_mfma_f32_16x16x32_bf16 v[180:183], a[244:247], a[148:151], v[180:183]\n"
"ds_read_b64_tr_b16 a[238:239], v251 offset:21248\n"
"v_mfma_f32_16x16x32_bf16 v[172:175], a[240:243], a[128:131], v[172:175]\n"
"v_mfma_f32_16x16x32_bf16 v[172:175], a[244:247], a[144:147], v[172:175]\n"
"s_waitcnt lgkmcnt(4)\n"
"v_mfma_f32_16x16x32_bf16 v[200:203], a[248:251], a[128:131], v[200:203]\n"
"s_waitcnt vmcnt(20)\n"
"s_barrier\n"
"ds_read_b64_tr_b16 a[240:241], v250 offset:32768\n"
"v_mfma_f32_16x16x32_bf16 v[200:203], a[252:255], a[144:147], v[200:203]\n"
"s_add_u32 s78, s80, 128\n"
"ds_read_b64_tr_b16 a[242:243], v250 offset:33024\n"
"v_add_u32 v242, s78, v255\n"
"v_xor_b32 v244, 32, v242\n"
"s_add_u32 m0, s85, 49152\n"
"v_mfma_f32_16x16x32_bf16 v[208:211], a[248:251], a[132:135], v[208:211]\n"
"buffer_load_dwordx4 v242, s[36:39], 0 offen offset:0  lds\n"
"ds_read_b128 a[160:163], v254 offset:32768\n"
"ds_read_b128 a[164:167], v254 offset:34816\n"
"v_mfma_f32_16x16x32_bf16 v[208:211], a[252:255], a[148:151], v[208:211]\n"
"ds_read_b128 a[168:171], v254 offset:36864\n"
"ds_read_b128 a[172:175], v254 offset:38912\n"
"v_mfma_f32_16x16x32_bf16 v[216:219], a[248:251], a[136:139], v[216:219]\n"
"ds_read_b64_tr_b16 a[244:245], v250 offset:36864\n"
"v_mfma_f32_16x16x32_bf16 v[216:219], a[252:255], a[152:155], v[216:219]\n"
"buffer_load_dwordx4 v242, s[36:39], s67 offen offset:1024  lds\n"
"ds_read_b64_tr_b16 a[246:247], v250 offset:37120\n"
"v_mfma_f32_16x16x32_bf16 v[224:227], a[248:251], a[140:143], v[224:227]\n"
"ds_read_b128 a[176:179], v254 offset:33280\n"
"ds_read_b128 a[180:183], v254 offset:35328\n"
"v_mfma_f32_16x16x32_bf16 v[224:227], a[252:255], a[156:159], v[224:227]\n"
"s_waitcnt lgkmcnt(10)\n"
"ds_read_b128 a[184:187], v254 offset:37376\n"
"ds_read_b128 a[188:191], v254 offset:39424\n"
"v_mfma_f32_16x16x32_bf16 v[228:231], a[232:235], a[140:143], v[228:231]\n"
"buffer_load_dwordx4 v244, s[36:39], s68 offen offset:2048  lds\n"
"ds_read_b64_tr_b16 a[248:249], v251 offset:32768\n"
"v_mfma_f32_16x16x32_bf16 v[228:231], a[236:239], a[156:159], v[228:231]\n"
"ds_read_b64_tr_b16 a[250:251], v251 offset:33024\n"
"v_mfma_f32_16x16x32_bf16 v[220:223], a[232:235], a[136:139], v[220:223]\n"
"v_mfma_f32_16x16x32_bf16 v[220:223], a[236:239], a[152:155], v[220:223]\n"
"s_waitcnt lgkmcnt(12)\n"
"buffer_load_dwordx4 v244, s[36:39], s69 offen offset:3072  lds\n"
"v_mfma_f32_16x16x32_bf16 v[212:215], a[232:235], a[132:135], v[212:215]\n"
"ds_read_b64_tr_b16 a[252:253], v251 offset:36864\n"
"v_mfma_f32_16x16x32_bf16 v[212:215], a[236:239], a[148:151], v[212:215]\n"
"s_add_u32 s78, s80, 262272\n"
"ds_read_b64_tr_b16 a[254:255], v251 offset:37120\n"
"v_add_u32 v242, s78, v255\n"
"v_xor_b32 v244, 32, v242\n"
"s_add_u32 m0, s85, 53248\n"
"v_mfma_f32_16x16x32_bf16 v[204:207], a[232:235], a[128:131], v[204:207]\n"
"buffer_load_dwordx4 v242, s[36:39], 0 offen offset:0  lds\n"
"v_mfma_f32_16x16x32_bf16 v[204:207], a[236:239], a[144:147], v[204:207]\n"
"s_waitcnt lgkmcnt(4)\n"
"v_mfma_f32_16x16x32_bf16 v[104:107], a[240:243], a[160:163], v[104:107]\n"
"ds_read_b64_tr_b16 a[232:233], v250 offset:33280\n"
"v_mfma_f32_16x16x32_bf16 v[104:107], a[244:247], a[176:179], v[104:107]\n"
"ds_read_b64_tr_b16 a[234:235], v250 offset:33536\n"
"v_mfma_f32_16x16x32_bf16 v[112:115], a[240:243], a[164:167], v[112:115]\n"
"buffer_load_dwordx4 v242, s[36:39], s67 offen offset:1024  lds\n"
"v_mfma_f32_16x16x32_bf16 v[112:115], a[244:247], a[180:183], v[112:115]\n"
"v_mfma_f32_16x16x32_bf16 v[120:123], a[240:243], a[168:171], v[120:123]\n"
"buffer_load_dwordx4 v244, s[36:39], s68 offen offset:2048  lds\n"
"ds_read_b64_tr_b16 a[236:237], v250 offset:37376\n"
"v_mfma_f32_16x16x32_bf16 v[120:123], a[244:247], a[184:187], v[120:123]\n"
"ds_read_b64_tr_b16 a[238:239], v250 offset:37632\n"
"v_mfma_f32_16x16x32_bf16 v[128:131], a[240:243], a[172:175], v[128:131]\n"
"v_mfma_f32_16x16x32_bf16 v[128:131], a[244:247], a[188:191], v[128:131]\n"
"s_waitcnt lgkmcnt(4)\n"
"buffer_load_dwordx4 v244, s[36:39], s69 offen offset:3072  lds\n"
"v_mfma_f32_16x16x32_bf16 v[132:135], a[248:251], a[172:175], v[132:135]\n"
"ds_read_b64_tr_b16 a[240:241], v251 offset:33280\n"
"v_mfma_f32_16x16x32_bf16 v[132:135], a[252:255], a[188:191], v[132:135]\n"
"ds_read_b64_tr_b16 a[242:243], v251 offset:33536\n"
"v_mfma_f32_16x16x32_bf16 v[124:127], a[248:251], a[168:171], v[124:127]\n"
"v_mfma_f32_16x16x32_bf16 v[124:127], a[252:255], a[184:187], v[124:127]\n"
"v_mfma_f32_16x16x32_bf16 v[116:119], a[248:251], a[164:167], v[116:119]\n"
"s_add_u32 s80, s80, 524288\n"
"ds_read_b64_tr_b16 a[244:245], v251 offset:37376\n"
"v_mfma_f32_16x16x32_bf16 v[116:119], a[252:255], a[180:183], v[116:119]\n"
"s_sub_u32 s78, s80, 3670016\n"
"ds_read_b64_tr_b16 a[246:247], v251 offset:37632\n"
"v_mfma_f32_16x16x32_bf16 v[108:111], a[248:251], a[160:163], v[108:111]\n"
"s_min_u32 s80, s80, s78\n"
"v_mfma_f32_16x16x32_bf16 v[108:111], a[252:255], a[176:179], v[108:111]\n"
"s_waitcnt lgkmcnt(4)\n"
"v_mfma_f32_16x16x32_bf16 v[136:139], a[232:235], a[160:163], v[136:139]\n"
"s_waitcnt vmcnt(20)\n"
"s_barrier\n"
"ds_read_b64_tr_b16 a[248:249], v250 offset:49152\n"
"v_mfma_f32_16x16x32_bf16 v[136:139], a[236:239], a[176:179], v[136:139]\n"
"s_add_u32 s78, s80, 256\n"
"ds_read_b64_tr_b16 a[250:251], v250 offset:49408\n"
"v_add_u32 v242, s78, v255\n"
"v_xor_b32 v244, 32, v242\n"
"s_add_u32 m0, s85, 65536\n"
"v_mfma_f32_16x16x32_bf16 v[144:147], a[232:235], a[164:167], v[144:147]\n"
"buffer_load_dwordx4 v242, s[36:39], 0 offen offset:0  lds\n"
"v_mfma_f32_16x16x32_bf16 v[144:147], a[236:239], a[180:183], v[144:147]\n"
"v_mfma_f32_16x16x32_bf16 v[152:155], a[232:235], a[168:171], v[152:155]\n"
"ds_read_b64_tr_b16 a[252:253], v250 offset:53248\n"
"v_mfma_f32_16x16x32_bf16 v[152:155], a[236:239], a[184:187], v[152:155]\n"
"ds_read_b64_tr_b16 a[254:255], v250 offset:53504\n"
"v_mfma_f32_16x16x32_bf16 v[160:163], a[232:235], a[172:175], v[160:163]\n"
"buffer_load_dwordx4 v242, s[36:39], s67 offen offset:1024  lds\n"
"v_mfma_f32_16x16x32_bf16 v[160:163], a[236:239], a[188:191], v[160:163]\n"
"s_waitcnt lgkmcnt(4)\n"
"v_mfma_f32_16x16x32_bf16 v[164:167], a[240:243], a[172:175], v[164:167]\n"
"buffer_load_dwordx4 v244, s[36:39], s68 offen offset:2048  lds\n"
"ds_read_b64_tr_b16 a[232:233], v251 offset:49152\n"
"v_mfma_f32_16x16x32_bf16 v[164:167], a[244:247], a[188:191], v[164:167]\n"
"ds_read_b64_tr_b16 a[234:235], v251 offset:49408\n"
"v_mfma_f32_16x16x32_bf16 v[156:159], a[240:243], a[168:171], v[156:159]\n"
"v_mfma_f32_16x16x32_bf16 v[156:159], a[244:247], a[184:187], v[156:159]\n"
"buffer_load_dwordx4 v244, s[36:39], s69 offen offset:3072  lds\n"
"v_mfma_f32_16x16x32_bf16 v[148:151], a[240:243], a[164:167], v[148:151]\n"
"ds_read_b64_tr_b16 a[236:237], v251 offset:53248\n"
"v_mfma_f32_16x16x32_bf16 v[148:151], a[244:247], a[180:183], v[148:151]\n"
"s_add_u32 s78, s80, 262400\n"
"ds_read_b64_tr_b16 a[238:239], v251 offset:53504\n"
"v_add_u32 v242, s78, v255\n"
"v_xor_b32 v244, 32, v242\n"
"s_add_u32 m0, s85, 69632\n"
"v_mfma_f32_16x16x32_bf16 v[140:143], a[240:243], a[160:163], v[140:143]\n"
"buffer_load_dwordx4 v242, s[36:39], 0 offen offset:0  lds\n"
"v_mfma_f32_16x16x32_bf16 v[140:143], a[244:247], a[176:179], v[140:143]\n"
"s_waitcnt lgkmcnt(4)\n"
"v_mfma_f32_16x16x32_bf16 v[168:171], a[248:251], a[160:163], v[168:171]\n"
"ds_read_b64_tr_b16 a[240:241], v250 offset:49664\n"
"v_mfma_f32_16x16x32_bf16 v[168:171], a[252:255], a[176:179], v[168:171]\n"
"ds_read_b64_tr_b16 a[242:243], v250 offset:49920\n"
"v_mfma_f32_16x16x32_bf16 v[176:179], a[248:251], a[164:167], v[176:179]\n"
"buffer_load_dwordx4 v242, s[36:39], s67 offen offset:1024  lds\n"
"v_mfma_f32_16x16x32_bf16 v[176:179], a[252:255], a[180:183], v[176:179]\n"
"v_mfma_f32_16x16x32_bf16 v[184:187], a[248:251], a[168:171], v[184:187]\n"
"buffer_load_dwordx4 v244, s[36:39], s68 offen offset:2048  lds\n"
"ds_read_b64_tr_b16 a[244:245], v250 offset:53760\n"
"v_mfma_f32_16x16x32_bf16 v[184:187], a[252:255], a[184:187], v[184:187]\n"
"ds_read_b64_tr_b16 a[246:247], v250 offset:54016\n"
"v_mfma_f32_16x16x32_bf16 v[192:195], a[248:251], a[172:175], v[192:195]\n"
"v_mfma_f32_16x16x32_bf16 v[192:195], a[252:255], a[188:191], v[192:195]\n"
"s_waitcnt lgkmcnt(4)\n"
"buffer_load_dwordx4 v244, s[36:39], s69 offen offset:3072  lds\n"
"v_mfma_f32_16x16x32_bf16 v[196:199], a[232:235], a[172:175], v[196:199]\n"
"ds_read_b64_tr_b16 a[248:249], v251 offset:49664\n"
"v_mfma_f32_16x16x32_bf16 v[196:199], a[236:239], a[188:191], v[196:199]\n"
"ds_read_b64_tr_b16 a[250:251], v251 offset:49920\n"
"v_mfma_f32_16x16x32_bf16 v[188:191], a[232:235], a[168:171], v[188:191]\n"
"v_mfma_f32_16x16x32_bf16 v[188:191], a[236:239], a[184:187], v[188:191]\n"
"v_mfma_f32_16x16x32_bf16 v[180:183], a[232:235], a[164:167], v[180:183]\n"
"s_add_u32 s80, s80, 524288\n"
"ds_read_b64_tr_b16 a[252:253], v251 offset:53760\n"
"v_mfma_f32_16x16x32_bf16 v[180:183], a[236:239], a[180:183], v[180:183]\n"
"s_sub_u32 s78, s80, 3670016\n"
"ds_read_b64_tr_b16 a[254:255], v251 offset:54016\n"
"v_mfma_f32_16x16x32_bf16 v[172:175], a[232:235], a[160:163], v[172:175]\n"
"s_min_u32 s80, s80, s78\n"
"v_mfma_f32_16x16x32_bf16 v[172:175], a[236:239], a[176:179], v[172:175]\n"
"s_waitcnt lgkmcnt(4)\n"
"v_mfma_f32_16x16x32_bf16 v[200:203], a[240:243], a[160:163], v[200:203]\n"
"s_waitcnt vmcnt(16)\n"
"s_barrier\n"
"ds_read_b64_tr_b16 a[232:233], v252 offset:0\n"
"v_mfma_f32_16x16x32_bf16 v[200:203], a[244:247], a[176:179], v[200:203]\n"
"s_add_u32 s78, s80, 256\n"
"ds_read_b64_tr_b16 a[234:235], v252 offset:256\n"
"v_add_u32 v242, s78, v255\n"
"v_xor_b32 v244, 32, v242\n"
"s_add_u32 m0, s85, 81920\n"
"v_mfma_f32_16x16x32_bf16 v[208:211], a[240:243], a[164:167], v[208:211]\n"
"buffer_load_dwordx4 v242, s[36:39], 0 offen offset:0  lds\n"
"ds_read_b128 a[192:195], v254 offset:0\n"
"ds_read_b128 a[196:199], v254 offset:2048\n"
"v_mfma_f32_16x16x32_bf16 v[208:211], a[244:247], a[180:183], v[208:211]\n"
"ds_read_b128 a[200:203], v254 offset:4096\n"
"ds_read_b128 a[204:207], v254 offset:6144\n"
"v_mfma_f32_16x16x32_bf16 v[216:219], a[240:243], a[168:171], v[216:219]\n"
"ds_read_b64_tr_b16 a[236:237], v252 offset:4096\n"
"v_mfma_f32_16x16x32_bf16 v[216:219], a[244:247], a[184:187], v[216:219]\n"
"buffer_load_dwordx4 v242, s[36:39], s67 offen offset:1024  lds\n"
"ds_read_b64_tr_b16 a[238:239], v252 offset:4352\n"
"v_mfma_f32_16x16x32_bf16 v[224:227], a[240:243], a[172:175], v[224:227]\n"
"ds_read_b128 a[208:211], v254 offset:512\n"
"ds_read_b128 a[212:215], v254 offset:2560\n"
"v_mfma_f32_16x16x32_bf16 v[224:227], a[244:247], a[188:191], v[224:227]\n"
"s_waitcnt lgkmcnt(10)\n"
"ds_read_b128 a[216:219], v254 offset:4608\n"
"ds_read_b128 a[220:223], v254 offset:6656\n"
"v_mfma_f32_16x16x32_bf16 v[228:231], a[248:251], a[172:175], v[228:231]\n"
"buffer_load_dwordx4 v244, s[36:39], s68 offen offset:2048  lds\n"
"ds_read_b64_tr_b16 a[240:241], v253 offset:0\n"
"v_mfma_f32_16x16x32_bf16 v[228:231], a[252:255], a[188:191], v[228:231]\n"
"ds_read_b64_tr_b16 a[242:243], v253 offset:256\n"
"v_mfma_f32_16x16x32_bf16 v[220:223], a[248:251], a[168:171], v[220:223]\n"
"v_mfma_f32_16x16x32_bf16 v[220:223], a[252:255], a[184:187], v[220:223]\n"
"s_waitcnt lgkmcnt(12)\n"
"buffer_load_dwordx4 v244, s[36:39], s69 offen offset:3072  lds\n"
"v_mfma_f32_16x16x32_bf16 v[212:215], a[248:251], a[164:167], v[212:215]\n"
"ds_read_b64_tr_b16 a[244:245], v253 offset:4096\n"
"v_mfma_f32_16x16x32_bf16 v[212:215], a[252:255], a[180:183], v[212:215]\n"
"s_add_u32 s78, s80, 262400\n"
"ds_read_b64_tr_b16 a[246:247], v253 offset:4352\n"
"v_add_u32 v242, s78, v255\n"
"v_xor_b32 v244, 32, v242\n"
"s_add_u32 m0, s85, 86016\n"
"v_mfma_f32_16x16x32_bf16 v[204:207], a[248:251], a[160:163], v[204:207]\n"
"buffer_load_dwordx4 v242, s[36:39], 0 offen offset:0  lds\n"
"v_mfma_f32_16x16x32_bf16 v[204:207], a[252:255], a[176:179], v[204:207]\n"
"s_waitcnt lgkmcnt(4)\n"
"v_mfma_f32_16x16x32_bf16 v[104:107], a[232:235], a[192:195], v[104:107]\n"
"ds_read_b64_tr_b16 a[248:249], v252 offset:512\n"
"v_mfma_f32_16x16x32_bf16 v[104:107], a[236:239], a[208:211], v[104:107]\n"
"ds_read_b64_tr_b16 a[250:251], v252 offset:768\n"
"v_mfma_f32_16x16x32_bf16 v[112:115], a[232:235], a[196:199], v[112:115]\n"
"buffer_load_dwordx4 v242, s[36:39], s67 offen offset:1024  lds\n"
"v_mfma_f32_16x16x32_bf16 v[112:115], a[236:239], a[212:215], v[112:115]\n"
"v_mfma_f32_16x16x32_bf16 v[120:123], a[232:235], a[200:203], v[120:123]\n"
"buffer_load_dwordx4 v244, s[36:39], s68 offen offset:2048  lds\n"
"ds_read_b64_tr_b16 a[252:253], v252 offset:4608\n"
"v_mfma_f32_16x16x32_bf16 v[120:123], a[236:239], a[216:219], v[120:123]\n"
"ds_read_b64_tr_b16 a[254:255], v252 offset:4864\n"
"v_mfma_f32_16x16x32_bf16 v[128:131], a[232:235], a[204:207], v[128:131]\n"
"v_mfma_f32_16x16x32_bf16 v[128:131], a[236:239], a[220:223], v[128:131]\n"
"s_waitcnt lgkmcnt(4)\n"
"buffer_load_dwordx4 v244, s[36:39], s69 offen offset:3072  lds\n"
"v_mfma_f32_16x16x32_bf16 v[132:135], a[240:243], a[204:207], v[132:135]\n"
"ds_read_b64_tr_b16 a[232:233], v253 offset:512\n"
"v_mfma_f32_16x16x32_bf16 v[132:135], a[244:247], a[220:223], v[132:135]\n"
"ds_read_b64_tr_b16 a[234:235], v253 offset:768\n"
"v_mfma_f32_16x16x32_bf16 v[124:127], a[240:243], a[200:203], v[124:127]\n"
"v_mfma_f32_16x16x32_bf16 v[124:127], a[244:247], a[216:219], v[124:127]\n"
"v_mfma_f32_16x16x32_bf16 v[116:119], a[240:243], a[196:199], v[116:119]\n"
"s_add_u32 s80, s80, 524288\n"
"ds_read_b64_tr_b16 a[236:237], v253 offset:4608\n"
"v_mfma_f32_16x16x32_bf16 v[116:119], a[244:247], a[212:215], v[116:119]\n"
"s_sub_u32 s78, s80, 3670016\n"
"ds_read_b64_tr_b16 a[238:239], v253 offset:4864\n"
"v_mfma_f32_16x16x32_bf16 v[108:111], a[240:243], a[192:195], v[108:111]\n"
"s_min_u32 s80, s80, s78\n"
"v_mfma_f32_16x16x32_bf16 v[108:111], a[244:247], a[208:211], v[108:111]\n"
"s_waitcnt lgkmcnt(4)\n"
"v_mfma_f32_16x16x32_bf16 v[136:139], a[248:251], a[192:195], v[136:139]\n"
"s_waitcnt vmcnt(16)\n"
"s_barrier\n"
"ds_read_b64_tr_b16 a[240:241], v250 offset:0\n"
"v_mfma_f32_16x16x32_bf16 v[136:139], a[252:255], a[208:211], v[136:139]\n"
"s_add_u32 s78, s80, 256\n"
"ds_read_b64_tr_b16 a[242:243], v250 offset:256\n"
"v_add_u32 v242, s78, v255\n"
"v_xor_b32 v244, 32, v242\n"
"s_add_u32 m0, s85, 98304\n"
"v_mfma_f32_16x16x32_bf16 v[144:147], a[248:251], a[196:199], v[144:147]\n"
"buffer_load_dwordx4 v242, s[36:39], 0 offen offset:0  lds\n"
"v_mfma_f32_16x16x32_bf16 v[144:147], a[252:255], a[212:215], v[144:147]\n"
"v_mul_f32 v112, s70, v112\n"
"v_mul_f32 v113, s70, v113\n"
"v_mul_f32 v114, s70, v114\n"
"v_mfma_f32_16x16x32_bf16 v[152:155], a[248:251], a[200:203], v[152:155]\n"
"ds_read_b64_tr_b16 a[244:245], v250 offset:4096\n"
"v_mul_f32 v104, s70, v104\n"
"v_mfma_f32_16x16x32_bf16 v[152:155], a[252:255], a[216:219], v[152:155]\n"
"ds_read_b64_tr_b16 a[246:247], v250 offset:4352\n"
"v_mul_f32 v105, s70, v105\n"
"v_mfma_f32_16x16x32_bf16 v[160:163], a[248:251], a[204:207], v[160:163]\n"
"buffer_load_dwordx4 v242, s[36:39], s67 offen offset:1024  lds\n"
"v_mfma_f32_16x16x32_bf16 v[160:163], a[252:255], a[220:223], v[160:163]\n"
"s_waitcnt lgkmcnt(4)\n"
"v_mul_f32 v106, s70, v106\n"
"v_mul_f32 v107, s70, v107\n"
"v_mfma_f32_16x16x32_bf16 v[164:167], a[232:235], a[204:207], v[164:167]\n"
"buffer_load_dwordx4 v244, s[36:39], s68 offen offset:2048  lds\n"
"ds_read_b64_tr_b16 a[248:249], v251 offset:0\n"
"v_mfma_f32_16x16x32_bf16 v[164:167], a[236:239], a[220:223], v[164:167]\n"
"ds_read_b64_tr_b16 a[250:251], v251 offset:256\n"
"v_cvt_pk_bf16_f32 v104, v104, v105\n"
"v_mfma_f32_16x16x32_bf16 v[156:159], a[232:235], a[200:203], v[156:159]\n"
"v_cvt_pk_bf16_f32 v105, v106, v107\n"
"v_mul_f32 v108, s70, v108\n"
"v_mfma_f32_16x16x32_bf16 v[156:159], a[236:239], a[216:219], v[156:159]\n"
"buffer_load_dwordx4 v244, s[36:39], s69 offen offset:3072  lds\n"
"v_mfma_f32_16x16x32_bf16 v[148:151], a[232:235], a[196:199], v[148:151]\n"
"ds_read_b64_tr_b16 a[252:253], v251 offset:4096\n"
"v_mul_f32 v109, s70, v109\n"
"v_mfma_f32_16x16x32_bf16 v[148:151], a[236:239], a[212:215], v[148:151]\n"
"s_add_u32 s78, s80, 262400\n"
"ds_read_b64_tr_b16 a[254:255], v251 offset:4352\n"
"v_add_u32 v242, s78, v255\n"
"v_xor_b32 v244, 32, v242\n"
"s_add_u32 m0, s85, 102400\n"
"v_mfma_f32_16x16x32_bf16 v[140:143], a[232:235], a[192:195], v[140:143]\n"
"buffer_load_dwordx4 v242, s[36:39], 0 offen offset:0  lds\n"
"v_mfma_f32_16x16x32_bf16 v[140:143], a[236:239], a[208:211], v[140:143]\n"
"s_waitcnt lgkmcnt(4)\n"
"v_mul_f32 v110, s70, v110\n"
"v_mul_f32 v111, s70, v111\n"
"v_mfma_f32_16x16x32_bf16 v[168:171], a[240:243], a[192:195], v[168:171]\n"
"ds_read_b64_tr_b16 a[232:233], v250 offset:512\n"
"v_cvt_pk_bf16_f32 v106, v108, v109\n"
"v_mfma_f32_16x16x32_bf16 v[168:171], a[244:247], a[208:211], v[168:171]\n"
"ds_read_b64_tr_b16 a[234:235], v250 offset:768\n"
"v_cvt_pk_bf16_f32 v107, v110, v111\n"
"v_mfma_f32_16x16x32_bf16 v[176:179], a[240:243], a[196:199], v[176:179]\n"
"buffer_load_dwordx4 v242, s[36:39], s67 offen offset:1024  lds\n"
"v_mfma_f32_16x16x32_bf16 v[176:179], a[244:247], a[212:215], v[176:179]\n"
"v_permlane16_swap_b32 v104, v106\n"
"v_mfma_f32_16x16x32_bf16 v[184:187], a[240:243], a[200:203], v[184:187]\n"
"buffer_load_dwordx4 v244, s[36:39], s68 offen offset:2048  lds\n"
"ds_read_b64_tr_b16 a[236:237], v250 offset:4608\n"
"v_mfma_f32_16x16x32_bf16 v[184:187], a[244:247], a[216:219], v[184:187]\n"
"ds_read_b64_tr_b16 a[238:239], v250 offset:4864\n"
"v_mul_f32 v115, s70, v115\n"
"v_mfma_f32_16x16x32_bf16 v[192:195], a[240:243], a[204:207], v[192:195]\n"
"v_permlane16_swap_b32 v105, v107\n"
"v_mfma_f32_16x16x32_bf16 v[192:195], a[244:247], a[220:223], v[192:195]\n"
"s_waitcnt lgkmcnt(4)\n"
"buffer_load_dwordx4 v244, s[36:39], s69 offen offset:3072  lds\n"
"v_cvt_pk_bf16_f32 v112, v112, v113\n"
"v_mfma_f32_16x16x32_bf16 v[196:199], a[248:251], a[204:207], v[196:199]\n"
"ds_read_b64_tr_b16 a[240:241], v251 offset:512\n"
"v_cvt_pk_bf16_f32 v113, v114, v115\n"
"v_mfma_f32_16x16x32_bf16 v[196:199], a[252:255], a[220:223], v[196:199]\n"
"ds_read_b64_tr_b16 a[242:243], v251 offset:768\n"
"v_mul_f32 v116, s70, v116\n"
"v_mfma_f32_16x16x32_bf16 v[188:191], a[248:251], a[200:203], v[188:191]\n"
"v_mul_f32 v117, s70, v117\n"
"v_mul_f32 v118, s70, v118\n"
"v_mfma_f32_16x16x32_bf16 v[188:191], a[252:255], a[216:219], v[188:191]\n"
"v_mul_f32 v119, s70, v119\n"
"v_cvt_pk_bf16_f32 v114, v116, v117\n"
"v_mfma_f32_16x16x32_bf16 v[180:183], a[248:251], a[196:199], v[180:183]\n"
"s_add_u32 s80, s80, 524288\n"
"ds_read_b64_tr_b16 a[244:245], v251 offset:4608\n"
"v_cvt_pk_bf16_f32 v115, v118, v119\n"
"v_mfma_f32_16x16x32_bf16 v[180:183], a[252:255], a[212:215], v[180:183]\n"
"s_sub_u32 s78, s80, 3670016\n"
"ds_read_b64_tr_b16 a[246:247], v251 offset:4864\n"
"v_mul_f32 v120, s70, v120\n"
"v_mfma_f32_16x16x32_bf16 v[172:175], a[248:251], a[192:195], v[172:175]\n"
"s_min_u32 s80, s80, s78\n"
"v_permlane16_swap_b32 v112, v114\n"
"v_mfma_f32_16x16x32_bf16 v[172:175], a[252:255], a[208:211], v[172:175]\n"
"s_waitcnt lgkmcnt(4)\n"
"v_permlane16_swap_b32 v113, v115\n"
"v_mfma_f32_16x16x32_bf16 v[200:203], a[232:235], a[192:195], v[200:203]\n"
"s_waitcnt vmcnt(16)\n"
"s_barrier\n"
"ds_read_b64_tr_b16 a[248:249], v250 offset:16384\n"
"v_mfma_f32_16x16x32_bf16 v[200:203], a[236:239], a[208:211], v[200:203]\n"
"s_add_u32 s78, s80, 256\n"
"ds_read_b64_tr_b16 a[250:251], v250 offset:16640\n"
"v_add_u32 v242, s78, v255\n"
"v_xor_b32 v244, 32, v242\n"
"s_add_u32 m0, s85, 114688\n"
"v_mfma_f32_16x16x32_bf16 v[208:211], a[232:235], a[196:199], v[208:211]\n"
"buffer_load_dwordx4 v242, s[36:39], 0 offen offset:0  lds\n"
"v_mfma_f32_16x16x32_bf16 v[208:211], a[236:239], a[212:215], v[208:211]\n"
"v_mul_f32 v121, s70, v121\n"
"v_mul_f32 v122, s70, v122\n"
"v_mfma_f32_16x16x32_bf16 v[216:219], a[232:235], a[200:203], v[216:219]\n"
"ds_read_b64_tr_b16 a[252:253], v250 offset:20480\n"
"v_mul_f32 v123, s70, v123\n"
"v_mfma_f32_16x16x32_bf16 v[216:219], a[236:239], a[216:219], v[216:219]\n"
"ds_read_b64_tr_b16 a[254:255], v250 offset:20736\n"
"v_cvt_pk_bf16_f32 v120, v120, v121\n"
"v_mfma_f32_16x16x32_bf16 v[224:227], a[232:235], a[204:207], v[224:227]\n"
"buffer_load_dwordx4 v242, s[36:39], s67 offen offset:1024  lds\n"
"v_mfma_f32_16x16x32_bf16 v[224:227], a[236:239], a[220:223], v[224:227]\n"
"s_waitcnt lgkmcnt(4)\n"
"v_cvt_pk_bf16_f32 v121, v122, v123\n"
"v_mul_f32 v124, s70, v124\n"
"v_mfma_f32_16x16x32_bf16 v[228:231], a[240:243], a[204:207], v[228:231]\n"
"buffer_load_dwordx4 v244, s[36:39], s68 offen offset:2048  lds\n"
"ds_read_b64_tr_b16 a[232:233], v251 offset:16384\n"
"v_mfma_f32_16x16x32_bf16 v[228:231], a[244:247], a[220:223], v[228:231]\n"
"ds_read_b64_tr_b16 a[234:235], v251 offset:16640\n"
"v_mul_f32 v125, s70, v125\n"
"v_mfma_f32_16x16x32_bf16 v[220:223], a[240:243], a[200:203], v[220:223]\n"
"v_mul_f32 v126, s70, v126\n"
"v_mul_f32 v127, s70, v127\n"
"v_mfma_f32_16x16x32_bf16 v[220:223], a[244:247], a[216:219], v[220:223]\n"
"buffer_load_dwordx4 v244, s[36:39], s69 offen offset:3072  lds\n"
"v_cvt_pk_bf16_f32 v122, v124, v125\n"
"v_mfma_f32_16x16x32_bf16 v[212:215], a[240:243], a[196:199], v[212:215]\n"
"ds_read_b64_tr_b16 a[236:237], v251 offset:20480\n"
"v_cvt_pk_bf16_f32 v123, v126, v127\n"
"v_mfma_f32_16x16x32_bf16 v[212:215], a[244:247], a[212:215], v[212:215]\n"
"s_add_u32 s78, s80, 262400\n"
"ds_read_b64_tr_b16 a[238:239], v251 offset:20736\n"
"v_add_u32 v242, s78, v255\n"
"v_xor_b32 v244, 32, v242\n"
"s_add_u32 m0, s85, 118784\n"
"v_mfma_f32_16x16x32_bf16 v[204:207], a[240:243], a[192:195], v[204:207]\n"
"buffer_load_dwordx4 v242, s[36:39], 0 offen offset:0  lds\n"
"v_mfma_f32_16x16x32_bf16 v[204:207], a[244:247], a[208:211], v[204:207]\n"
"s_waitcnt lgkmcnt(4)\n"
"v_permlane16_swap_b32 v120, v122\n"
"v_mfma_f32_16x16x32_bf16 v[0:3], a[248:251], a[0:3], 0\n"
"ds_read_b64_tr_b16 a[240:241], v250 offset:16896\n"
"s_lshl2_add_u32 s84, s76, s60\n"
"s_lshl_b32 s84, s84, 7\n"
"v_mov_b32 v254, s84\n"
"v_mfma_f32_16x16x32_bf16 v[0:3], a[252:255], a[16:19], v[0:3]\n"
"ds_read_b64_tr_b16 a[242:243], v250 offset:17152\n"
"v_mul_f32 v128, s70, v128\n"
"v_mfma_f32_16x16x32_bf16 v[8:11], a[248:251], a[4:7], 0\n"
"buffer_load_dwordx4 v242, s[36:39], s67 offen offset:1024  lds\n"
"v_mfma_f32_16x16x32_bf16 v[8:11], a[252:255], a[20:23], v[8:11]\n"
"v_permlane16_swap_b32 v121, v123\n"
"v_mfma_f32_16x16x32_bf16 v[16:19], a[248:251], a[8:11], 0\n"
"buffer_load_dwordx4 v244, s[36:39], s68 offen offset:2048  lds\n"
"ds_read_b64_tr_b16 a[244:245], v250 offset:20992\n"
"v_mfma_f32_16x16x32_bf16 v[16:19], a[252:255], a[24:27], v[16:19]\n"
"ds_read_b64_tr_b16 a[246:247], v250 offset:21248\n"
"v_mul_f32 v129, s70, v129\n"
"v_mfma_f32_16x16x32_bf16 v[24:27], a[248:251], a[12:15], 0\n"
"v_mul_f32 v130, s70, v130\n"
"v_mul_f32 v131, s70, v131\n"
"v_mfma_f32_16x16x32_bf16 v[24:27], a[252:255], a[28:31], v[24:27]\n"
"s_waitcnt lgkmcnt(4)\n"
"buffer_load_dwordx4 v244, s[36:39], s69 offen offset:3072  lds\n"
"v_mfma_f32_16x16x32_bf16 v[28:31], a[232:235], a[12:15], 0\n"
"ds_read_b64_tr_b16 a[248:249], v251 offset:16896\n"
"v_cvt_pk_bf16_f32 v128, v128, v129\n"
"v_mfma_f32_16x16x32_bf16 v[28:31], a[236:239], a[28:31], v[28:31]\n"
"ds_read_b64_tr_b16 a[250:251], v251 offset:17152\n"
"v_cvt_pk_bf16_f32 v129, v130, v131\n"
"v_mfma_f32_16x16x32_bf16 v[20:23], a[232:235], a[8:11], 0\n"
"L_CW1:\n"
"s_mov_b32 s75, L_CW1-L_PC+3072\n"
"buffer_load_dword v248, v254, s[56:59], s75 offen\n"
"v_mfma_f32_16x16x32_bf16 v[20:23], a[236:239], a[24:27], v[20:23]\n"
"v_mul_f32 v132, s70, v132\n"
"v_mul_f32 v133, s70, v133\n"
"v_mfma_f32_16x16x32_bf16 v[12:15], a[232:235], a[4:7], 0\n"
"s_add_u32 s80, s80, 524288\n"
"ds_read_b64_tr_b16 a[252:253], v251 offset:20992\n"
"v_mul_f32 v134, s70, v134\n"
"v_mfma_f32_16x16x32_bf16 v[12:15], a[236:239], a[20:23], v[12:15]\n"
"s_sub_u32 s78, s80, 3670016\n"
"ds_read_b64_tr_b16 a[254:255], v251 offset:21248\n"
"v_mul_f32 v135, s70, v135\n"
"v_mfma_f32_16x16x32_bf16 v[4:7], a[232:235], a[0:3], 0\n"
"s_min_u32 s80, s80, s78\n"
"v_cvt_pk_bf16_f32 v130, v132, v133\n"
"v_cvt_pk_bf16_f32 v131, v134, v135\n"
"v_mfma_f32_16x16x32_bf16 v[4:7], a[236:239], a[16:19], v[4:7]\n"
"s_waitcnt lgkmcnt(4)\n"
"v_mul_f32 v136, s70, v136\n"
"v_mul_f32 v137, s70, v137\n"
"v_mul_f32 v138, s70, v138\n"
"v_mfma_f32_16x16x32_bf16 v[32:35], a[240:243], a[0:3], 0\n"
"s_waitcnt vmcnt(17)\n"
"s_barrier\n"
"ds_read_b64_tr_b16 a[232:233], v250 offset:32768\n"
"v_mfma_f32_16x16x32_bf16 v[32:35], a[244:247], a[16:19], v[32:35]\n"
"s_add_u32 s78, s80, 256\n"
"ds_read_b64_tr_b16 a[234:235], v250 offset:33024\n"
"v_add_u32 v242, s78, v255\n"
"v_xor_b32 v244, 32, v242\n"
"s_add_u32 m0, s85, 49152\n"
"v_mfma_f32_16x16x32_bf16 v[40:43], a[240:243], a[4:7], 0\n"
"buffer_load_dwordx4 v242, s[36:39], 0 offen offset:0  lds\n"
"v_mfma_f32_16x16x32_bf16 v[40:43], a[244:247], a[20:23], v[40:43]\n"
"v_permlane16_swap_b32 v128, v130\n"
"v_mfma_f32_16x16x32_bf16 v[48:51], a[240:243], a[8:11], 0\n"
"ds_read_b64_tr_b16 a[236:237], v250 offset:36864\n"
"v_mul_f32 v139, s70, v139\n"
"v_mfma_f32_16x16x32_bf16 v[48:51], a[244:247], a[24:27], v[48:51]\n"
"ds_read_b64_tr_b16 a[238:239], v250 offset:37120\n"
"v_cvt_pk_bf16_f32 v136, v136, v137\n"
"v_mfma_f32_16x16x32_bf16 v[56:59], a[240:243], a[12:15], 0\n"
"buffer_load_dwordx4 v242, s[36:39], s67 offen offset:1024  lds\n"
"v_mfma_f32_16x16x32_bf16 v[56:59], a[244:247], a[28:31], v[56:59]\n"
"s_waitcnt lgkmcnt(4)\n"
"v_permlane16_swap_b32 v129, v131\n"
"v_mfma_f32_16x16x32_bf16 v[60:63], a[248:251], a[12:15], 0\n"
"buffer_load_dwordx4 v244, s[36:39], s68 offen offset:2048  lds\n"
"ds_read_b64_tr_b16 a[240:241], v251 offset:32768\n"
"v_mfma_f32_16x16x32_bf16 v[60:63], a[252:255], a[28:31], v[60:63]\n"
"ds_read_b64_tr_b16 a[242:243], v251 offset:33024\n"
"v_cvt_pk_bf16_f32 v137, v138, v139\n"
"v_mfma_f32_16x16x32_bf16 v[52:55], a[248:251], a[8:11], 0\n"
"v_mul_f32 v140, s70, v140\n"
"v_mul_f32 v141, s70, v141\n"
"v_mfma_f32_16x16x32_bf16 v[52:55], a[252:255], a[24:27], v[52:55]\n"
"buffer_load_dwordx4 v244, s[36:39], s69 offen offset:3072  lds\n"
"v_mfma_f32_16x16x32_bf16 v[44:47], a[248:251], a[4:7], 0\n"
"ds_read_b64_tr_b16 a[244:245], v251 offset:36864\n"
"v_mul_f32 v142, s70, v142\n"
"v_mfma_f32_16x16x32_bf16 v[44:47], a[252:255], a[20:23], v[44:47]\n"
"s_add_u32 s78, s80, 262400\n"
"ds_read_b64_tr_b16 a[246:247], v251 offset:37120\n"
"v_add_u32 v242, s78, v255\n"
"v_xor_b32 v244, 32, v242\n"
"s_add_u32 m0, s85, 53248\n"
"v_mfma_f32_16x16x32_bf16 v[36:39], a[248:251], a[0:3], 0\n"
"buffer_load_dwordx4 v242, s[36:39], 0 offen offset:0  lds\n"
"v_mfma_f32_16x16x32_bf16 v[36:39], a[252:255], a[16:19], v[36:39]\n"
"s_waitcnt lgkmcnt(4)\n"
"v_mul_f32 v143, s70, v143\n"
"v_cvt_pk_bf16_f32 v138, v140, v141\n"
"v_mfma_f32_16x16x32_bf16 v[0:3], a[232:235], a[32:35], v[0:3]\n"
"ds_read_b64_tr_b16 a[248:249], v250 offset:33280\n"
"v_cvt_pk_bf16_f32 v139, v142, v143\n"
"v_mfma_f32_16x16x32_bf16 v[0:3], a[236:239], a[48:51], v[0:3]\n"
"ds_read_b64_tr_b16 a[250:251], v250 offset:33536\n"
"v_mul_f32 v144, s70, v144\n"
"v_mfma_f32_16x16x32_bf16 v[8:11], a[232:235], a[36:39], v[8:11]\n"
"buffer_load_dwordx4 v242, s[36:39], s67 offen offset:1024  lds\n"
"v_mfma_f32_16x16x32_bf16 v[8:11], a[236:239], a[52:55], v[8:11]\n"
"v_permlane16_swap_b32 v136, v138\n"
"v_mul_f32 v145, s70, v145\n"
"v_mfma_f32_16x16x32_bf16 v[16:19], a[232:235], a[40:43], v[16:19]\n"
"buffer_load_dwordx4 v244, s[36:39], s68 offen offset:2048  lds\n"
"ds_read_b64_tr_b16 a[252:253], v250 offset:37376\n"
"v_mfma_f32_16x16x32_bf16 v[16:19], a[236:239], a[56:59], v[16:19]\n"
"ds_read_b64_tr_b16 a[254:255], v250 offset:37632\n"
"v_mul_f32 v146, s70, v146\n"
"v_mfma_f32_16x16x32_bf16 v[24:27], a[232:235], a[44:47], v[24:27]\n"
"v_permlane16_swap_b32 v137, v139\n"
"v_mfma_f32_16x16x32_bf16 v[24:27], a[236:239], a[60:63], v[24:27]\n"
"s_waitcnt lgkmcnt(4)\n"
"buffer_load_dwordx4 v244, s[36:39], s69 offen offset:3072  lds\n"
"v_mfma_f32_16x16x32_bf16 v[28:31], a[240:243], a[44:47], v[28:31]\n"
"ds_read_b64_tr_b16 a[232:233], v251 offset:33280\n"
"v_mul_f32 v147, s70, v147\n"
"v_mfma_f32_16x16x32_bf16 v[28:31], a[244:247], a[60:63], v[28:31]\n"
"ds_read_b64_tr_b16 a[234:235], v251 offset:33536\n"
"v_cvt_pk_bf16_f32 v144, v144, v145\n"
"v_mfma_f32_16x16x32_bf16 v[20:23], a[240:243], a[40:43], v[20:23]\n"
"v_cvt_pk_bf16_f32 v145, v146, v147\n"
"v_mul_f32 v148, s70, v148\n"
"v_mfma_f32_16x16x32_bf16 v[20:23], a[244:247], a[56:59], v[20:23]\n"
"v_mul_f32 v149, s70, v149\n"
"v_mul_f32 v150, s70, v150\n"
"v_mfma_f32_16x16x32_bf16 v[12:15], a[240:243], a[36:39], v[12:15]\n"
"s_add_u32 s80, s80, 524288\n"
"ds_read_b64_tr_b16 a[236:237], v251 offset:37376\n"
"v_mul_f32 v151, s70, v151\n"
"v_mfma_f32_16x16x32_bf16 v[12:15], a[244:247], a[52:55], v[12:15]\n"
"s_sub_u32 s78, s80, 3670016\n"
"ds_read_b64_tr_b16 a[238:239], v251 offset:37632\n"
"v_cvt_pk_bf16_f32 v146, v148, v149\n"
"v_mfma_f32_16x16x32_bf16 v[4:7], a[240:243], a[32:35], v[4:7]\n"
"s_min_u32 s80, s80, s78\n"
"v_cvt_pk_bf16_f32 v147, v150, v151\n"
"v_mul_f32 v152, s70, v152\n"
"v_mfma_f32_16x16x32_bf16 v[4:7], a[244:247], a[48:51], v[4:7]\n"
"s_waitcnt lgkmcnt(4)\n"
"v_permlane16_swap_b32 v144, v146\n"
"v_mfma_f32_16x16x32_bf16 v[32:35], a[248:251], a[32:35], v[32:35]\n"
"s_waitcnt vmcnt(17)\n"
"s_barrier\n"
"ds_read_b64_tr_b16 a[240:241], v250 offset:49152\n"
"v_mfma_f32_16x16x32_bf16 v[32:35], a[252:255], a[48:51], v[32:35]\n"
"s_add_u32 s78, s80, 256\n"
"ds_read_b64_tr_b16 a[242:243], v250 offset:49408\n"
"v_add_u32 v242, s78, v255\n"
"v_xor_b32 v244, 32, v242\n"
"s_add_u32 m0, s85, 65536\n"
"v_mfma_f32_16x16x32_bf16 v[40:43], a[248:251], a[36:39], v[40:43]\n"
"buffer_load_dwordx4 v242, s[36:39], 0 offen offset:0  lds\n"
"v_mfma_f32_16x16x32_bf16 v[40:43], a[252:255], a[52:55], v[40:43]\n"
"v_permlane16_swap_b32 v145, v147\n"
"v_mfma_f32_16x16x32_bf16 v[48:51], a[248:251], a[40:43], v[48:51]\n"
"ds_read_b64_tr_b16 a[244:245], v250 offset:53248\n"
"v_mul_f32 v153, s70, v153\n"
"v_mfma_f32_16x16x32_bf16 v[48:51], a[252:255], a[56:59], v[48:51]\n"
"ds_read_b64_tr_b16 a[246:247], v250 offset:53504\n"
"v_mul_f32 v154, s70, v154\n"
"v_mfma_f32_16x16x32_bf16 v[56:59], a[248:251], a[44:47], v[56:59]\n"
"buffer_load_dwordx4 v242, s[36:39], s67 offen offset:1024  lds\n"
"v_mfma_f32_16x16x32_bf16 v[56:59], a[252:255], a[60:63], v[56:59]\n"
"s_waitcnt lgkmcnt(4)\n"
"v_mul_f32 v155, s70, v155\n"
"v_cvt_pk_bf16_f32 v152, v152, v153\n"
"v_cvt_pk_bf16_f32 v153, v154, v155\n"
"v_mfma_f32_16x16x32_bf16 v[60:63], a[232:235], a[44:47], v[60:63]\n"
"buffer_load_dwordx4 v244, s[36:39], s68 offen offset:2048  lds\n"
"ds_read_b64_tr_b16 a[248:249], v251 offset:49152\n"
"v_mfma_f32_16x16x32_bf16 v[60:63], a[236:239], a[60:63], v[60:63]\n"
"ds_read_b64_tr_b16 a[250:251], v251 offset:49408\n"
"v_mul_f32 v156, s70, v156\n"
"v_mfma_f32_16x16x32_bf16 v[52:55], a[232:235], a[40:43], v[52:55]\n"
"v_mul_f32 v157, s70, v157\n"
"v_mul_f32 v158, s70, v158\n"
"v_mfma_f32_16x16x32_bf16 v[52:55], a[236:239], a[56:59], v[52:55]\n"
"buffer_load_dwordx4 v244, s[36:39], s69 offen offset:3072  lds\n"
"v_mfma_f32_16x16x32_bf16 v[44:47], a[232:235], a[36:39], v[44:47]\n"
"ds_read_b64_tr_b16 a[252:253], v251 offset:53248\n"
"v_mul_f32 v159, s70, v159\n"
"v_mfma_f32_16x16x32_bf16 v[44:47], a[236:239], a[52:55], v[44:47]\n"
"s_add_u32 s78, s80, 262400\n"
"ds_read_b64_tr_b16 a[254:255], v251 offset:53504\n"
"v_add_u32 v242, s78, v255\n"
"v_xor_b32 v244, 32, v242\n"
"s_add_u32 m0, s85, 69632\n"
"v_mfma_f32_16x16x32_bf16 v[36:39], a[232:235], a[32:35], v[36:39]\n"
"buffer_load_dwordx4 v242, s[36:39], 0 offen offset:0  lds\n"
"v_mfma_f32_16x16x32_bf16 v[36:39], a[236:239], a[48:51], v[36:39]\n"
"s_waitcnt lgkmcnt(4)\n"
"v_cvt_pk_bf16_f32 v154, v156, v157\n"
"v_cvt_pk_bf16_f32 v155, v158, v159\n"
"v_mfma_f32_16x16x32_bf16 v[0:3], a[240:243], a[64:67], v[0:3]\n"
"ds_read_b64_tr_b16 a[232:233], v250 offset:49664\n"
"v_mul_f32 v160, s70, v160\n"
"v_mfma_f32_16x16x32_bf16 v[0:3], a[244:247], a[80:83], v[0:3]\n"
"ds_read_b64_tr_b16 a[234:235], v250 offset:49920\n"
"v_mul_f32 v161, s70, v161\n"
"v_mfma_f32_16x16x32_bf16 v[8:11], a[240:243], a[68:71], v[8:11]\n"
"buffer_load_dwordx4 v242, s[36:39], s67 offen offset:1024  lds\n"
"v_mfma_f32_16x16x32_bf16 v[8:11], a[244:247], a[84:87], v[8:11]\n"
"v_permlane16_swap_b32 v152, v154\n"
"v_mfma_f32_16x16x32_bf16 v[16:19], a[240:243], a[72:75], v[16:19]\n"
"buffer_load_dwordx4 v244, s[36:39], s68 offen offset:2048  lds\n"
"ds_read_b64_tr_b16 a[236:237], v250 offset:53760\n"
"v_mfma_f32_16x16x32_bf16 v[16:19], a[244:247], a[88:91], v[16:19]\n"
"ds_read_b64_tr_b16 a[238:239], v250 offset:54016\n"
"v_mul_f32 v162, s70, v162\n"
"v_mfma_f32_16x16x32_bf16 v[24:27], a[240:243], a[76:79], v[24:27]\n"
"v_permlane16_swap_b32 v153, v155\n"
"v_mfma_f32_16x16x32_bf16 v[24:27], a[244:247], a[92:95], v[24:27]\n"
"s_waitcnt lgkmcnt(4)\n"
"buffer_load_dwordx4 v244, s[36:39], s69 offen offset:3072  lds\n"
"v_mfma_f32_16x16x32_bf16 v[28:31], a[248:251], a[76:79], v[28:31]\n"
"ds_read_b64_tr_b16 a[240:241], v251 offset:49664\n"
"v_mul_f32 v163, s70, v163\n"
"v_mfma_f32_16x16x32_bf16 v[28:31], a[252:255], a[92:95], v[28:31]\n"
"ds_read_b64_tr_b16 a[242:243], v251 offset:49920\n"
"v_cvt_pk_bf16_f32 v160, v160, v161\n"
"v_mfma_f32_16x16x32_bf16 v[20:23], a[248:251], a[72:75], v[20:23]\n"
"v_cvt_pk_bf16_f32 v161, v162, v163\n"
"v_mul_f32 v164, s70, v164\n"
"v_mfma_f32_16x16x32_bf16 v[20:23], a[252:255], a[88:91], v[20:23]\n"
"v_mul_f32 v165, s70, v165\n"
"v_mul_f32 v166, s70, v166\n"
"v_mul_f32 v167, s70, v167\n"
"v_mfma_f32_16x16x32_bf16 v[12:15], a[248:251], a[68:71], v[12:15]\n"
"s_add_u32 s80, s80, 524288\n"
"ds_read_b64_tr_b16 a[244:245], v251 offset:53760\n"
"v_cvt_pk_bf16_f32 v162, v164, v165\n"
"v_mfma_f32_16x16x32_bf16 v[12:15], a[252:255], a[84:87], v[12:15]\n"
"s_sub_u32 s78, s80, 3670016\n"
"ds_read_b64_tr_b16 a[246:247], v251 offset:54016\n"
"v_cvt_pk_bf16_f32 v163, v166, v167\n"
"v_mfma_f32_16x16x32_bf16 v[4:7], a[248:251], a[64:67], v[4:7]\n"
"s_min_u32 s80, s80, s78\n"
"v_permlane16_swap_b32 v160, v162\n"
"v_mfma_f32_16x16x32_bf16 v[4:7], a[252:255], a[80:83], v[4:7]\n"
"s_waitcnt lgkmcnt(4)\n"
"v_permlane16_swap_b32 v161, v163\n"
"v_mfma_f32_16x16x32_bf16 v[32:35], a[232:235], a[64:67], v[32:35]\n"
"s_waitcnt vmcnt(17)\n"
"s_barrier\n"
"ds_read_b64_tr_b16 a[248:249], v252 offset:0\n"
"v_mfma_f32_16x16x32_bf16 v[32:35], a[236:239], a[80:83], v[32:35]\n"
"s_add_u32 s78, s80, 256\n"
"ds_read_b64_tr_b16 a[250:251], v252 offset:256\n"
"v_add_u32 v242, s78, v255\n"
"v_xor_b32 v244, 32, v242\n"
"s_add_u32 m0, s85, 81920\n"
"v_mfma_f32_16x16x32_bf16 v[40:43], a[232:235], a[68:71], v[40:43]\n"
"buffer_load_dwordx4 v242, s[36:39], 0 offen offset:0  lds\n"
"v_mfma_f32_16x16x32_bf16 v[40:43], a[236:239], a[84:87], v[40:43]\n"
"v_mul_f32 v168, s70, v168\n"
"v_mul_f32 v169, s70, v169\n"
"v_mfma_f32_16x16x32_bf16 v[48:51], a[232:235], a[72:75], v[48:51]\n"
"ds_read_b64_tr_b16 a[252:253], v252 offset:4096\n"
"v_mul_f32 v170, s70, v170\n"
"v_mfma_f32_16x16x32_bf16 v[48:51], a[236:239], a[88:91], v[48:51]\n"
"ds_read_b64_tr_b16 a[254:255], v252 offset:4352\n"
"v_mul_f32 v171, s70, v171\n"
"v_mfma_f32_16x16x32_bf16 v[56:59], a[232:235], a[76:79], v[56:59]\n"
"buffer_load_dwordx4 v242, s[36:39], s67 offen offset:1024  lds\n"
"v_mfma_f32_16x16x32_bf16 v[56:59], a[236:239], a[92:95], v[56:59]\n"
"s_waitcnt lgkmcnt(4)\n"
"v_cvt_pk_bf16_f32 v168, v168, v169\n"
"v_cvt_pk_bf16_f32 v169, v170, v171\n"
"v_mfma_f32_16x16x32_bf16 v[60:63], a[240:243], a[76:79], v[60:63]\n"
"buffer_load_dwordx4 v244, s[36:39], s68 offen offset:2048  lds\n"
"ds_read_b64_tr_b16 a[232:233], v253 offset:0\n"
"v_mfma_f32_16x16x32_bf16 v[60:63], a[244:247], a[92:95], v[60:63]\n"
"ds_read_b64_tr_b16 a[234:235], v253 offset:256\n"
"v_mul_f32 v172, s70, v172\n"
"v_mfma_f32_16x16x32_bf16 v[52:55], a[240:243], a[72:75], v[52:55]\n"
"v_mul_f32 v173, s70, v173\n"
"v_mul_f32 v174, s70, v174\n"
"v_mfma_f32_16x16x32_bf16 v[52:55], a[244:247], a[88:91], v[52:55]\n"
"buffer_load_dwordx4 v244, s[36:39], s69 offen offset:3072  lds\n"
"v_mfma_f32_16x16x32_bf16 v[44:47], a[240:243], a[68:71], v[44:47]\n"
"ds_read_b64_tr_b16 a[236:237], v253 offset:4096\n"
"v_mul_f32 v175, s70, v175\n"
"v_mfma_f32_16x16x32_bf16 v[44:47], a[244:247], a[84:87], v[44:47]\n"
"s_add_u32 s78, s80, 262400\n"
"ds_read_b64_tr_b16 a[238:239], v253 offset:4352\n"
"v_add_u32 v242, s78, v255\n"
"v_xor_b32 v244, 32, v242\n"
"s_add_u32 m0, s85, 86016\n"
"v_mfma_f32_16x16x32_bf16 v[36:39], a[240:243], a[64:67], v[36:39]\n"
"buffer_load_dwordx4 v242, s[36:39], 0 offen offset:0  lds\n"
"v_mfma_f32_16x16x32_bf16 v[36:39], a[244:247], a[80:83], v[36:39]\n"
"s_waitcnt lgkmcnt(4)\n"
"v_cvt_pk_bf16_f32 v170, v172, v173\n"
"v_cvt_pk_bf16_f32 v171, v174, v175\n"
"v_mul_f32 v176, s70, v176\n"
"v_mfma_f32_16x16x32_bf16 v[0:3], a[248:251], a[96:99], v[0:3]\n"
"ds_read_b64_tr_b16 a[240:241], v252 offset:512\n"
"v_mul_f32 v177, s70, v177\n"
"v_mfma_f32_16x16x32_bf16 v[0:3], a[252:255], a[112:115], v[0:3]\n"
"ds_read_b64_tr_b16 a[242:243], v252 offset:768\n"
"v_mul_f32 v178, s70, v178\n"
"v_mfma_f32_16x16x32_bf16 v[8:11], a[248:251], a[100:103], v[8:11]\n"
"buffer_load_dwordx4 v242, s[36:39], s67 offen offset:1024  lds\n"
"v_mfma_f32_16x16x32_bf16 v[8:11], a[252:255], a[116:119], v[8:11]\n"
"v_permlane16_swap_b32 v168, v170\n"
"v_mfma_f32_16x16x32_bf16 v[16:19], a[248:251], a[104:107], v[16:19]\n"
"buffer_load_dwordx4 v244, s[36:39], s68 offen offset:2048  lds\n"
"ds_read_b64_tr_b16 a[244:245], v252 offset:4608\n"
"v_mfma_f32_16x16x32_bf16 v[16:19], a[252:255], a[120:123], v[16:19]\n"
"ds_read_b64_tr_b16 a[246:247], v252 offset:4864\n"
"v_mul_f32 v179, s70, v179\n"
"v_mfma_f32_16x16x32_bf16 v[24:27], a[248:251], a[108:111], v[24:27]\n"
"v_permlane16_swap_b32 v169, v171\n"
"v_mfma_f32_16x16x32_bf16 v[24:27], a[252:255], a[124:127], v[24:27]\n"
"s_waitcnt lgkmcnt(4)\n"
"buffer_load_dwordx4 v244, s[36:39], s69 offen offset:3072  lds\n"
"v_mfma_f32_16x16x32_bf16 v[28:31], a[232:235], a[108:111], v[28:31]\n"
"ds_read_b64_tr_b16 a[248:249], v253 offset:512\n"
"v_cvt_pk_bf16_f32 v176, v176, v177\n"
"v_mfma_f32_16x16x32_bf16 v[28:31], a[236:239], a[124:127], v[28:31]\n"
"ds_read_b64_tr_b16 a[250:251], v253 offset:768\n"
"v_cvt_pk_bf16_f32 v177, v178, v179\n"
"v_mfma_f32_16x16x32_bf16 v[20:23], a[232:235], a[104:107], v[20:23]\n"
"v_mul_f32 v180, s70, v180\n"
"v_mul_f32 v181, s70, v181\n"
"v_mfma_f32_16x16x32_bf16 v[20:23], a[236:239], a[120:123], v[20:23]\n"
"v_mul_f32 v182, s70, v182\n"
"v_mul_f32 v183, s70, v183\n"
"v_mfma_f32_16x16x32_bf16 v[12:15], a[232:235], a[100:103], v[12:15]\n"
"s_add_u32 s80, s80, 524288\n"
"ds_read_b64_tr_b16 a[252:253], v253 offset:4608\n"
"v_cvt_pk_bf16_f32 v178, v180, v181\n"
"v_mfma_f32_16x16x32_bf16 v[12:15], a[236:239], a[116:119], v[12:15]\n"
"s_sub_u32 s78, s80, 3670016\n"
"ds_read_b64_tr_b16 a[254:255], v253 offset:4864\n"
"v_cvt_pk_bf16_f32 v179, v182, v183\n"
"v_mfma_f32_16x16x32_bf16 v[4:7], a[232:235], a[96:99], v[4:7]\n"
"s_min_u32 s80, s80, s78\n"
"v_permlane16_swap_b32 v176, v178\n"
"v_mfma_f32_16x16x32_bf16 v[4:7], a[236:239], a[112:115], v[4:7]\n"
"s_waitcnt lgkmcnt(4)\n"
"v_permlane16_swap_b32 v177, v179\n"
"v_mfma_f32_16x16x32_bf16 v[32:35], a[240:243], a[96:99], v[32:35]\n"
"s_waitcnt vmcnt(16)\n"
"s_barrier\n"
"ds_read_b64_tr_b16 a[232:233], v250 offset:0\n"
"v_mfma_f32_16x16x32_bf16 v[32:35], a[244:247], a[112:115], v[32:35]\n"
"s_add_u32 s78, s80, 384\n"
"ds_read_b64_tr_b16 a[234:235], v250 offset:256\n"
"v_add_u32 v242, s78, v255\n"
"v_xor_b32 v244, 32, v242\n"
"s_add_u32 m0, s85, 98304\n"
"v_mfma_f32_16x16x32_bf16 v[40:43], a[240:243], a[100:103], v[40:43]\n"
"buffer_load_dwordx4 v242, s[36:39], 0 offen offset:0  lds\n"
"v_mfma_f32_16x16x32_bf16 v[40:43], a[244:247], a[116:119], v[40:43]\n"
"v_mul_f32 v184, s70, v184\n"
"v_mul_f32 v185, s70, v185\n"
"v_mul_f32 v186, s70, v186\n"
"v_mfma_f32_16x16x32_bf16 v[48:51], a[240:243], a[104:107], v[48:51]\n"
"ds_read_b64_tr_b16 a[236:237], v250 offset:4096\n"
"v_mul_f32 v187, s70, v187\n"
"v_mfma_f32_16x16x32_bf16 v[48:51], a[244:247], a[120:123], v[48:51]\n"
"ds_read_b64_tr_b16 a[238:239], v250 offset:4352\n"
"v_cvt_pk_bf16_f32 v184, v184, v185\n"
"v_mfma_f32_16x16x32_bf16 v[56:59], a[240:243], a[108:111], v[56:59]\n"
"buffer_load_dwordx4 v242, s[36:39], s67 offen offset:1024  lds\n"
"v_mfma_f32_16x16x32_bf16 v[56:59], a[244:247], a[124:127], v[56:59]\n"
"s_waitcnt lgkmcnt(4)\n"
"v_cvt_pk_bf16_f32 v185, v186, v187\n"
"v_mul_f32 v188, s70, v188\n"
"v_mfma_f32_16x16x32_bf16 v[60:63], a[248:251], a[108:111], v[60:63]\n"
"buffer_load_dwordx4 v244, s[36:39], s68 offen offset:2048  lds\n"
"ds_read_b64_tr_b16 a[240:241], v251 offset:0\n"
"v_mfma_f32_16x16x32_bf16 v[60:63], a[252:255], a[124:127], v[60:63]\n"
"ds_read_b64_tr_b16 a[242:243], v251 offset:256\n"
"v_mul_f32 v189, s70, v189\n"
"v_mfma_f32_16x16x32_bf16 v[52:55], a[248:251], a[104:107], v[52:55]\n"
"v_mul_f32 v190, s70, v190\n"
"v_mul_f32 v191, s70, v191\n"
"v_mfma_f32_16x16x32_bf16 v[52:55], a[252:255], a[120:123], v[52:55]\n"
"buffer_load_dwordx4 v244, s[36:39], s69 offen offset:3072  lds\n"
"v_mfma_f32_16x16x32_bf16 v[44:47], a[248:251], a[100:103], v[44:47]\n"
"ds_read_b64_tr_b16 a[244:245], v251 offset:4096\n"
"v_cvt_pk_bf16_f32 v186, v188, v189\n"
"v_mfma_f32_16x16x32_bf16 v[44:47], a[252:255], a[116:119], v[44:47]\n"
"s_add_u32 s78, s80, 262528\n"
"ds_read_b64_tr_b16 a[246:247], v251 offset:4352\n"
"v_add_u32 v242, s78, v255\n"
"v_xor_b32 v244, 32, v242\n"
"s_add_u32 m0, s85, 102400\n"
"v_mfma_f32_16x16x32_bf16 v[36:39], a[248:251], a[96:99], v[36:39]\n"
"buffer_load_dwordx4 v242, s[36:39], 0 offen offset:0  lds\n"
"v_mfma_f32_16x16x32_bf16 v[36:39], a[252:255], a[112:115], v[36:39]\n"
"s_waitcnt lgkmcnt(4)\n"
"v_cvt_pk_bf16_f32 v187, v190, v191\n"
"v_mul_f32 v192, s70, v192\n"
"v_mfma_f32_16x16x32_bf16 v[0:3], a[232:235], a[128:131], v[0:3]\n"
"ds_read_b64_tr_b16 a[248:249], v250 offset:512\n"
"v_mul_f32 v193, s70, v193\n"
"v_mfma_f32_16x16x32_bf16 v[0:3], a[236:239], a[144:147], v[0:3]\n"
"ds_read_b64_tr_b16 a[250:251], v250 offset:768\n"
"v_mul_f32 v194, s70, v194\n"
"v_mfma_f32_16x16x32_bf16 v[8:11], a[232:235], a[132:135], v[8:11]\n"
"buffer_load_dwordx4 v242, s[36:39], s67 offen offset:1024  lds\n"
"v_mfma_f32_16x16x32_bf16 v[8:11], a[236:239], a[148:151], v[8:11]\n"
"v_permlane16_swap_b32 v184, v186\n"
"v_mfma_f32_16x16x32_bf16 v[16:19], a[232:235], a[136:139], v[16:19]\n"
"buffer_load_dwordx4 v244, s[36:39], s68 offen offset:2048  lds\n"
"ds_read_b64_tr_b16 a[252:253], v250 offset:4608\n"
"v_mfma_f32_16x16x32_bf16 v[16:19], a[236:239], a[152:155], v[16:19]\n"
"ds_read_b64_tr_b16 a[254:255], v250 offset:4864\n"
"v_mul_f32 v195, s70, v195\n"
"v_mfma_f32_16x16x32_bf16 v[24:27], a[232:235], a[140:143], v[24:27]\n"
"v_permlane16_swap_b32 v185, v187\n"
"v_mfma_f32_16x16x32_bf16 v[24:27], a[236:239], a[156:159], v[24:27]\n"
"s_waitcnt lgkmcnt(4)\n"
"buffer_load_dwordx4 v244, s[36:39], s69 offen offset:3072  lds\n"
"v_cvt_pk_bf16_f32 v192, v192, v193\n"
"v_mfma_f32_16x16x32_bf16 v[28:31], a[240:243], a[140:143], v[28:31]\n"
"ds_read_b64_tr_b16 a[232:233], v251 offset:512\n"
"v_cvt_pk_bf16_f32 v193, v194, v195\n"
"v_mfma_f32_16x16x32_bf16 v[28:31], a[244:247], a[156:159], v[28:31]\n"
"ds_read_b64_tr_b16 a[234:235], v251 offset:768\n"
"v_mul_f32 v196, s70, v196\n"
"v_mfma_f32_16x16x32_bf16 v[20:23], a[240:243], a[136:139], v[20:23]\n"
"v_mul_f32 v197, s70, v197\n"
"v_mul_f32 v198, s70, v198\n"
"v_mfma_f32_16x16x32_bf16 v[20:23], a[244:247], a[152:155], v[20:23]\n"
"v_mul_f32 v199, s70, v199\n"
"v_cvt_pk_bf16_f32 v194, v196, v197\n"
"v_mfma_f32_16x16x32_bf16 v[12:15], a[240:243], a[132:135], v[12:15]\n"
"s_add_u32 s80, s80, 524288\n"
"ds_read_b64_tr_b16 a[236:237], v251 offset:4608\n"
"v_cvt_pk_bf16_f32 v195, v198, v199\n"
"v_mfma_f32_16x16x32_bf16 v[12:15], a[244:247], a[148:151], v[12:15]\n"
"s_sub_u32 s78, s80, 3670016\n"
"ds_read_b64_tr_b16 a[238:239], v251 offset:4864\n"
"v_mul_f32 v200, s70, v200\n"
"v_mfma_f32_16x16x32_bf16 v[4:7], a[240:243], a[128:131], v[4:7]\n"
"s_min_u32 s80, s80, s78\n"
"v_permlane16_swap_b32 v192, v194\n"
"v_mfma_f32_16x16x32_bf16 v[4:7], a[244:247], a[144:147], v[4:7]\n"
"s_waitcnt lgkmcnt(4)\n"
"v_permlane16_swap_b32 v193, v195\n"
"v_mfma_f32_16x16x32_bf16 v[32:35], a[248:251], a[128:131], v[32:35]\n"
"s_waitcnt vmcnt(16)\n"
"s_barrier\n"
"ds_read_b64_tr_b16 a[240:241], v250 offset:16384\n"
"v_mfma_f32_16x16x32_bf16 v[32:35], a[252:255], a[144:147], v[32:35]\n"
"s_add_u32 s78, s80, 384\n"
"ds_read_b64_tr_b16 a[242:243], v250 offset:16640\n"
"v_add_u32 v242, s78, v255\n"
"v_xor_b32 v244, 32, v242\n"
"s_add_u32 m0, s85, 114688\n"
"v_mfma_f32_16x16x32_bf16 v[40:43], a[248:251], a[132:135], v[40:43]\n"
"buffer_load_dwordx4 v242, s[36:39], 0 offen offset:0  lds\n"
"v_mfma_f32_16x16x32_bf16 v[40:43], a[252:255], a[148:151], v[40:43]\n"
"v_mul_f32 v201, s70, v201\n"
"v_mul_f32 v202, s70, v202\n"
"v_mfma_f32_16x16x32_bf16 v[48:51], a[248:251], a[136:139], v[48:51]\n"
"ds_read_b64_tr_b16 a[244:245], v250 offset:20480\n"
"v_mul_f32 v203, s70, v203\n"
"v_mfma_f32_16x16x32_bf16 v[48:51], a[252:255], a[152:155], v[48:51]\n"
"ds_read_b64_tr_b16 a[246:247], v250 offset:20736\n"
"v_cvt_pk_bf16_f32 v200, v200, v201\n"
"v_mfma_f32_16x16x32_bf16 v[56:59], a[248:251], a[140:143], v[56:59]\n"
"buffer_load_dwordx4 v242, s[36:39], s67 offen offset:1024  lds\n"
"v_mfma_f32_16x16x32_bf16 v[56:59], a[252:255], a[156:159], v[56:59]\n"
"s_waitcnt lgkmcnt(4)\n"
"v_cvt_pk_bf16_f32 v201, v202, v203\n"
"v_mul_f32 v204, s70, v204\n"
"v_mfma_f32_16x16x32_bf16 v[60:63], a[232:235], a[140:143], v[60:63]\n"
"buffer_load_dwordx4 v244, s[36:39], s68 offen offset:2048  lds\n"
"ds_read_b64_tr_b16 a[248:249], v251 offset:16384\n"
"v_mfma_f32_16x16x32_bf16 v[60:63], a[236:239], a[156:159], v[60:63]\n"
"ds_read_b64_tr_b16 a[250:251], v251 offset:16640\n"
"v_mul_f32 v205, s70, v205\n"
"v_mfma_f32_16x16x32_bf16 v[52:55], a[232:235], a[136:139], v[52:55]\n"
"v_mul_f32 v206, s70, v206\n"
"v_mul_f32 v207, s70, v207\n"
"v_mfma_f32_16x16x32_bf16 v[52:55], a[236:239], a[152:155], v[52:55]\n"
"buffer_load_dwordx4 v244, s[36:39], s69 offen offset:3072  lds\n"
"v_cvt_pk_bf16_f32 v202, v204, v205\n"
"v_mfma_f32_16x16x32_bf16 v[44:47], a[232:235], a[132:135], v[44:47]\n"
"ds_read_b64_tr_b16 a[252:253], v251 offset:20480\n"
"v_cvt_pk_bf16_f32 v203, v206, v207\n"
"v_mfma_f32_16x16x32_bf16 v[44:47], a[236:239], a[148:151], v[44:47]\n"
"s_add_u32 s78, s80, 262528\n"
"ds_read_b64_tr_b16 a[254:255], v251 offset:20736\n"
"v_add_u32 v242, s78, v255\n"
"v_xor_b32 v244, 32, v242\n"
"s_add_u32 m0, s85, 118784\n"
"v_mfma_f32_16x16x32_bf16 v[36:39], a[232:235], a[128:131], v[36:39]\n"
"buffer_load_dwordx4 v242, s[36:39], 0 offen offset:0  lds\n"
"v_mfma_f32_16x16x32_bf16 v[36:39], a[236:239], a[144:147], v[36:39]\n"
"s_waitcnt lgkmcnt(4)\n"
"v_permlane16_swap_b32 v200, v202\n"
"v_mfma_f32_16x16x32_bf16 v[0:3], a[240:243], a[160:163], v[0:3]\n"
"ds_read_b64_tr_b16 a[232:233], v250 offset:16896\n"
"v_mul_f32 v208, s70, v208\n"
"v_mfma_f32_16x16x32_bf16 v[0:3], a[244:247], a[176:179], v[0:3]\n"
"buffer_load_dwordx4 v242, s[36:39], s67 offen offset:1024  lds\n"
"ds_read_b64_tr_b16 a[234:235], v250 offset:17152\n"
"v_mfma_f32_16x16x32_bf16 v[8:11], a[240:243], a[164:167], v[8:11]\n"
"v_readfirstlane_b32 s72, v64\n"
"v_readfirstlane_b32 s73, v65\n"
"v_mfma_f32_16x16x32_bf16 v[8:11], a[244:247], a[180:183], v[8:11]\n"
"v_permlane16_swap_b32 v201, v203\n"
"v_mfma_f32_16x16x32_bf16 v[16:19], a[240:243], a[168:171], v[16:19]\n"
"buffer_load_dwordx4 v244, s[36:39], s68 offen offset:2048  lds\n"
"ds_read_b64_tr_b16 a[236:237], v250 offset:20992\n"
"v_mfma_f32_16x16x32_bf16 v[16:19], a[244:247], a[184:187], v[16:19]\n"
"ds_read_b64_tr_b16 a[238:239], v250 offset:21248\n"
"v_mul_f32 v209, s70, v209\n"
"v_mfma_f32_16x16x32_bf16 v[24:27], a[240:243], a[172:175], v[24:27]\n"
"s_or_b32 s72, s72, 1\n"
"s_add_u32 s72, s72, 2\n"
"s_addc_u32 s73, s73, 0\n"
"v_mfma_f32_16x16x32_bf16 v[24:27], a[244:247], a[188:191], v[24:27]\n"
"s_waitcnt lgkmcnt(4)\n"
"buffer_load_dwordx4 v244, s[36:39], s69 offen offset:3072  lds\n"
"v_mfma_f32_16x16x32_bf16 v[28:31], a[248:251], a[172:175], v[28:31]\n"
"ds_read_b64_tr_b16 a[240:241], v251 offset:16896\n"
"v_mul_f32 v210, s70, v210\n"
"v_mfma_f32_16x16x32_bf16 v[28:31], a[252:255], a[188:191], v[28:31]\n"
"ds_read_b64_tr_b16 a[242:243], v251 offset:17152\n"
"v_mul_f32 v211, s70, v211\n"
"v_mfma_f32_16x16x32_bf16 v[20:23], a[248:251], a[168:171], v[20:23]\n"
"v_cvt_pk_bf16_f32 v208, v208, v209\n"
"v_cvt_pk_bf16_f32 v209, v210, v211\n"
"v_mfma_f32_16x16x32_bf16 v[20:23], a[252:255], a[184:187], v[20:23]\n"
"v_mul_f32 v212, s70, v212\n"
"v_mul_f32 v213, s70, v213\n"
"v_mfma_f32_16x16x32_bf16 v[12:15], a[248:251], a[164:167], v[12:15]\n"
"s_add_u32 s80, s80, 524288\n"
"ds_read_b64_tr_b16 a[244:245], v251 offset:20992\n"
"v_mul_f32 v214, s70, v214\n"
"v_mfma_f32_16x16x32_bf16 v[12:15], a[252:255], a[180:183], v[12:15]\n"
"s_sub_u32 s78, s80, 3670016\n"
"ds_read_b64_tr_b16 a[246:247], v251 offset:21248\n"
"v_mul_f32 v215, s70, v215\n"
"v_mfma_f32_16x16x32_bf16 v[4:7], a[248:251], a[160:163], v[4:7]\n"
"s_min_u32 s80, s80, s78\n"
"v_cvt_pk_bf16_f32 v210, v212, v213\n"
"v_cvt_pk_bf16_f32 v211, v214, v215\n"
"v_mfma_f32_16x16x32_bf16 v[4:7], a[252:255], a[176:179], v[4:7]\n"
"s_waitcnt lgkmcnt(4)\n"
"v_mul_f32 v216, s70, v216\n"
"v_mul_f32 v217, s70, v217\n"
"v_mul_f32 v218, s70, v218\n"
"v_mfma_f32_16x16x32_bf16 v[32:35], a[232:235], a[160:163], v[32:35]\n"
"s_waitcnt vmcnt(16)\n"
"s_barrier\n"
"ds_read_b64_tr_b16 a[248:249], v250 offset:32768\n"
"v_mfma_f32_16x16x32_bf16 v[32:35], a[236:239], a[176:179], v[32:35]\n"
"s_add_u32 s78, s80, 384\n"
"ds_read_b64_tr_b16 a[250:251], v250 offset:33024\n"
"v_add_u32 v242, s78, v255\n"
"v_xor_b32 v244, 32, v242\n"
"s_add_u32 m0, s85, 49152\n"
"v_mfma_f32_16x16x32_bf16 v[40:43], a[232:235], a[164:167], v[40:43]\n"
"buffer_load_dwordx4 v242, s[36:39], 0 offen offset:0  lds\n"
"v_mfma_f32_16x16x32_bf16 v[40:43], a[236:239], a[180:183], v[40:43]\n"
"v_permlane16_swap_b32 v208, v210\n"
"v_mfma_f32_16x16x32_bf16 v[48:51], a[232:235], a[168:171], v[48:51]\n"
"ds_read_b64_tr_b16 a[252:253], v250 offset:36864\n"
"v_mul_f32 v219, s70, v219\n"
"v_mfma_f32_16x16x32_bf16 v[48:51], a[236:239], a[184:187], v[48:51]\n"
"ds_read_b64_tr_b16 a[254:255], v250 offset:37120\n"
"v_cvt_pk_bf16_f32 v216, v216, v217\n"
"v_mfma_f32_16x16x32_bf16 v[56:59], a[232:235], a[172:175], v[56:59]\n"
"buffer_load_dwordx4 v242, s[36:39], s67 offen offset:1024  lds\n"
"v_mfma_f32_16x16x32_bf16 v[56:59], a[236:239], a[188:191], v[56:59]\n"
"s_waitcnt lgkmcnt(4)\n"
"v_permlane16_swap_b32 v209, v211\n"
"v_mfma_f32_16x16x32_bf16 v[60:63], a[240:243], a[172:175], v[60:63]\n"
"buffer_load_dwordx4 v244, s[36:39], s68 offen offset:2048  lds\n"
"ds_read_b64_tr_b16 a[232:233], v251 offset:32768\n"
"v_mfma_f32_16x16x32_bf16 v[60:63], a[244:247], a[188:191], v[60:63]\n"
"ds_read_b64_tr_b16 a[234:235], v251 offset:33024\n"
"v_cvt_pk_bf16_f32 v217, v218, v219\n"
"v_mfma_f32_16x16x32_bf16 v[52:55], a[240:243], a[168:171], v[52:55]\n"
"v_mul_f32 v220, s70, v220\n"
"v_mul_f32 v221, s70, v221\n"
"v_mfma_f32_16x16x32_bf16 v[52:55], a[244:247], a[184:187], v[52:55]\n"
"buffer_load_dwordx4 v244, s[36:39], s69 offen offset:3072  lds\n"
"v_mfma_f32_16x16x32_bf16 v[44:47], a[240:243], a[164:167], v[44:47]\n"
"ds_read_b64_tr_b16 a[236:237], v251 offset:36864\n"
"v_mul_f32 v222, s70, v222\n"
"v_mfma_f32_16x16x32_bf16 v[44:47], a[244:247], a[180:183], v[44:47]\n"
"s_add_u32 s78, s80, 262528\n"
"ds_read_b64_tr_b16 a[238:239], v251 offset:37120\n"
"v_add_u32 v242, s78, v255\n"
"v_xor_b32 v244, 32, v242\n"
"s_add_u32 m0, s85, 53248\n"
"v_mfma_f32_16x16x32_bf16 v[36:39], a[240:243], a[160:163], v[36:39]\n"
"buffer_load_dwordx4 v242, s[36:39], 0 offen offset:0  lds\n"
"v_mfma_f32_16x16x32_bf16 v[36:39], a[244:247], a[176:179], v[36:39]\n"
"s_waitcnt lgkmcnt(4)\n"
"v_mul_f32 v223, s70, v223\n"
"v_cvt_pk_bf16_f32 v218, v220, v221\n"
"v_mfma_f32_16x16x32_bf16 v[0:3], a[248:251], a[192:195], v[0:3]\n"
"ds_read_b64_tr_b16 a[240:241], v250 offset:33280\n"
"v_cvt_pk_bf16_f32 v219, v222, v223\n"
"v_mfma_f32_16x16x32_bf16 v[0:3], a[252:255], a[208:211], v[0:3]\n"
"ds_read_b64_tr_b16 a[242:243], v250 offset:33536\n"
"v_mul_f32 v224, s70, v224\n"
"v_mfma_f32_16x16x32_bf16 v[8:11], a[248:251], a[196:199], v[8:11]\n"
"buffer_load_dwordx4 v242, s[36:39], s67 offen offset:1024  lds\n"
"v_mfma_f32_16x16x32_bf16 v[8:11], a[252:255], a[212:215], v[8:11]\n"
"v_permlane16_swap_b32 v216, v218\n"
"v_mul_f32 v225, s70, v225\n"
"v_mfma_f32_16x16x32_bf16 v[16:19], a[248:251], a[200:203], v[16:19]\n"
"buffer_load_dwordx4 v244, s[36:39], s68 offen offset:2048  lds\n"
"ds_read_b64_tr_b16 a[244:245], v250 offset:37376\n"
"v_mfma_f32_16x16x32_bf16 v[16:19], a[252:255], a[216:219], v[16:19]\n"
"ds_read_b64_tr_b16 a[246:247], v250 offset:37632\n"
"v_mul_f32 v226, s70, v226\n"
"v_mfma_f32_16x16x32_bf16 v[24:27], a[248:251], a[204:207], v[24:27]\n"
"v_permlane16_swap_b32 v217, v219\n"
"v_mfma_f32_16x16x32_bf16 v[24:27], a[252:255], a[220:223], v[24:27]\n"
"s_waitcnt lgkmcnt(4)\n"
"buffer_load_dwordx4 v244, s[36:39], s69 offen offset:3072  lds\n"
"v_mfma_f32_16x16x32_bf16 v[28:31], a[232:235], a[204:207], v[28:31]\n"
"ds_read_b64_tr_b16 a[248:249], v251 offset:33280\n"
"v_mul_f32 v227, s70, v227\n"
"v_mfma_f32_16x16x32_bf16 v[28:31], a[236:239], a[220:223], v[28:31]\n"
"ds_read_b64_tr_b16 a[250:251], v251 offset:33536\n"
"v_cvt_pk_bf16_f32 v224, v224, v225\n"
"v_mfma_f32_16x16x32_bf16 v[20:23], a[232:235], a[200:203], v[20:23]\n"
"v_cvt_pk_bf16_f32 v225, v226, v227\n"
"v_mul_f32 v228, s70, v228\n"
"v_mfma_f32_16x16x32_bf16 v[20:23], a[236:239], a[216:219], v[20:23]\n"
"v_mul_f32 v229, s70, v229\n"
"v_mul_f32 v230, s70, v230\n"
"v_mfma_f32_16x16x32_bf16 v[12:15], a[232:235], a[196:199], v[12:15]\n"
"s_add_u32 s80, s80, 524288\n"
"ds_read_b64_tr_b16 a[252:253], v251 offset:37376\n"
"v_mul_f32 v231, s70, v231\n"
"v_mfma_f32_16x16x32_bf16 v[12:15], a[236:239], a[212:215], v[12:15]\n"
"s_sub_u32 s78, s80, 3670016\n"
"ds_read_b64_tr_b16 a[254:255], v251 offset:37632\n"
"v_cvt_pk_bf16_f32 v226, v228, v229\n"
"v_mfma_f32_16x16x32_bf16 v[4:7], a[232:235], a[192:195], v[4:7]\n"
"s_min_u32 s80, s80, s78\n"
"v_cvt_pk_bf16_f32 v227, v230, v231\n"
"v_mfma_f32_16x16x32_bf16 v[4:7], a[236:239], a[208:211], v[4:7]\n"
"s_waitcnt lgkmcnt(4)\n"
"v_permlane16_swap_b32 v224, v226\n"
"v_mfma_f32_16x16x32_bf16 v[32:35], a[240:243], a[192:195], v[32:35]\n"
"s_waitcnt vmcnt(16)\n"
"s_barrier\n"
"ds_read_b64_tr_b16 a[232:233], v250 offset:49152\n"
"v_mfma_f32_16x16x32_bf16 v[32:35], a[244:247], a[208:211], v[32:35]\n"
"s_add_u32 s78, s80, 384\n"
"ds_read_b64_tr_b16 a[234:235], v250 offset:49408\n"
"v_add_u32 v242, s78, v255\n"
"v_xor_b32 v244, 32, v242\n"
"s_add_u32 m0, s85, 65536\n"
"v_mfma_f32_16x16x32_bf16 v[40:43], a[240:243], a[196:199], v[40:43]\n"
"buffer_load_dwordx4 v242, s[36:39], 0 offen offset:0  lds\n"
"v_mfma_f32_16x16x32_bf16 v[40:43], a[244:247], a[212:215], v[40:43]\n"
"v_permlane16_swap_b32 v225, v227\n"
"v_mul_f32 v8, s70, v8\n"
"v_mfma_f32_16x16x32_bf16 v[48:51], a[240:243], a[200:203], v[48:51]\n"
"ds_read_b64_tr_b16 a[236:237], v250 offset:53248\n"
"v_mul_f32 v0, s70, v0\n"
"v_mfma_f32_16x16x32_bf16 v[48:51], a[244:247], a[216:219], v[48:51]\n"
"ds_read_b64_tr_b16 a[238:239], v250 offset:53504\n"
"v_mul_f32 v1, s70, v1\n"
"v_mfma_f32_16x16x32_bf16 v[56:59], a[240:243], a[204:207], v[56:59]\n"
"buffer_load_dwordx4 v242, s[36:39], s67 offen offset:1024  lds\n"
"v_mfma_f32_16x16x32_bf16 v[56:59], a[244:247], a[220:223], v[56:59]\n"
"s_waitcnt lgkmcnt(4)\n"
"v_mul_f32 v2, s70, v2\n"
"v_mul_f32 v3, s70, v3\n"
"v_cvt_pk_bf16_f32 v0, v0, v1\n"
"v_mfma_f32_16x16x32_bf16 v[60:63], a[248:251], a[204:207], v[60:63]\n"
"buffer_load_dwordx4 v244, s[36:39], s68 offen offset:2048  lds\n"
"ds_read_b64_tr_b16 a[240:241], v251 offset:49152\n"
"v_mfma_f32_16x16x32_bf16 v[60:63], a[252:255], a[220:223], v[60:63]\n"
"ds_read_b64_tr_b16 a[242:243], v251 offset:49408\n"
"v_cvt_pk_bf16_f32 v1, v2, v3\n"
"v_mfma_f32_16x16x32_bf16 v[52:55], a[248:251], a[200:203], v[52:55]\n"
"v_mul_f32 v4, s70, v4\n"
"v_mul_f32 v5, s70, v5\n"
"v_mfma_f32_16x16x32_bf16 v[52:55], a[252:255], a[216:219], v[52:55]\n"
"buffer_load_dwordx4 v244, s[36:39], s69 offen offset:3072  lds\n"
"v_mul_f32 v6, s70, v6\n"
"v_mfma_f32_16x16x32_bf16 v[44:47], a[248:251], a[196:199], v[44:47]\n"
"ds_read_b64_tr_b16 a[244:245], v251 offset:53248\n"
"v_mul_f32 v7, s70, v7\n"
"v_mfma_f32_16x16x32_bf16 v[44:47], a[252:255], a[212:215], v[44:47]\n"
"s_add_u32 s78, s80, 262528\n"
"ds_read_b64_tr_b16 a[246:247], v251 offset:53504\n"
"v_add_u32 v242, s78, v255\n"
"v_xor_b32 v244, 32, v242\n"
"s_add_u32 m0, s85, 69632\n"
"v_mfma_f32_16x16x32_bf16 v[36:39], a[248:251], a[192:195], v[36:39]\n"
"buffer_load_dwordx4 v242, s[36:39], 0 offen offset:0  lds\n"
"v_mfma_f32_16x16x32_bf16 v[36:39], a[252:255], a[208:211], v[36:39]\n"
"s_waitcnt lgkmcnt(4)\n"
"v_cvt_pk_bf16_f32 v2, v4, v5\n"
"v_cvt_pk_bf16_f32 v3, v6, v7\n"
"v_mul_f32 v9, s70, v9\n"
"v_mfma_f32_16x16x32_bf16 v[64:67], a[232:235], a[0:3], 0\n"
"ds_read_b64_tr_b16 a[248:249], v250 offset:49664\n"
"v_mul_f32 v10, s70, v10\n"
"v_mfma_f32_16x16x32_bf16 v[64:67], a[236:239], a[16:19], v[64:67]\n"
"ds_read_b64_tr_b16 a[250:251], v250 offset:49920\n"
"v_mul_f32 v11, s70, v11\n"
"v_mfma_f32_16x16x32_bf16 v[72:75], a[232:235], a[4:7], 0\n"
"buffer_load_dwordx4 v242, s[36:39], s67 offen offset:1024  lds\n"
"v_mfma_f32_16x16x32_bf16 v[72:75], a[236:239], a[20:23], v[72:75]\n"
"v_permlane16_swap_b32 v0, v2\n"
"v_cvt_pk_bf16_f32 v8, v8, v9\n"
"v_mfma_f32_16x16x32_bf16 v[80:83], a[232:235], a[8:11], 0\n"
"buffer_load_dwordx4 v244, s[36:39], s68 offen offset:2048  lds\n"
"ds_read_b64_tr_b16 a[252:253], v250 offset:53760\n"
"v_mfma_f32_16x16x32_bf16 v[80:83], a[236:239], a[24:27], v[80:83]\n"
"ds_read_b64_tr_b16 a[254:255], v250 offset:54016\n"
"v_cvt_pk_bf16_f32 v9, v10, v11\n"
"v_mfma_f32_16x16x32_bf16 v[88:91], a[232:235], a[12:15], 0\n"
"v_permlane16_swap_b32 v1, v3\n"
"v_mfma_f32_16x16x32_bf16 v[88:91], a[236:239], a[28:31], v[88:91]\n"
"s_waitcnt lgkmcnt(4)\n"
"buffer_load_dwordx4 v244, s[36:39], s69 offen offset:3072  lds\n"
"v_mul_f32 v12, s70, v12\n"
"v_mfma_f32_16x16x32_bf16 v[92:95], a[240:243], a[12:15], 0\n"
"ds_read_b64_tr_b16 a[232:233], v251 offset:49664\n"
"v_mul_f32 v13, s70, v13\n"
"v_mfma_f32_16x16x32_bf16 v[92:95], a[244:247], a[28:31], v[92:95]\n"
"ds_read_b64_tr_b16 a[234:235], v251 offset:49920\n"
"v_mul_f32 v14, s70, v14\n"
"v_mfma_f32_16x16x32_bf16 v[84:87], a[240:243], a[8:11], 0\n"
"v_mul_f32 v15, s70, v15\n"
"v_cvt_pk_bf16_f32 v10, v12, v13\n"
"v_mfma_f32_16x16x32_bf16 v[84:87], a[244:247], a[24:27], v[84:87]\n"
"v_cvt_pk_bf16_f32 v11, v14, v15\n"
"v_mul_f32 v16, s70, v16\n"
"v_mul_f32 v17, s70, v17\n"
"v_mfma_f32_16x16x32_bf16 v[76:79], a[240:243], a[4:7], 0\n"
"s_add_u32 s80, s80, 524288\n"
"ds_read_b64_tr_b16 a[236:237], v251 offset:53760\n"
"v_mul_f32 v18, s70, v18\n"
"v_mfma_f32_16x16x32_bf16 v[76:79], a[244:247], a[20:23], v[76:79]\n"
"s_sub_u32 s78, s80, 3670016\n"
"ds_read_b64_tr_b16 a[238:239], v251 offset:54016\n"
"v_mul_f32 v19, s70, v19\n"
"v_mfma_f32_16x16x32_bf16 v[68:71], a[240:243], a[0:3], 0\n"
"s_min_u32 s80, s80, s78\n"
"v_permlane16_swap_b32 v8, v10\n"
"v_mfma_f32_16x16x32_bf16 v[68:71], a[244:247], a[16:19], v[68:71]\n"
"s_waitcnt lgkmcnt(4)\n"
"v_permlane16_swap_b32 v9, v11\n"
"v_cvt_pk_bf16_f32 v16, v16, v17\n"
"v_mfma_f32_16x16x32_bf16 v[96:99], a[248:251], a[0:3], 0\n"
"s_waitcnt vmcnt(16)\n"
"s_barrier\n"
"ds_read_b64_tr_b16 a[240:241], v252 offset:0\n"
"v_mfma_f32_16x16x32_bf16 v[96:99], a[252:255], a[16:19], v[96:99]\n"
"s_add_u32 s78, s80, 384\n"
"ds_read_b64_tr_b16 a[242:243], v252 offset:256\n"
"v_add_u32 v242, s78, v255\n"
"v_xor_b32 v244, 32, v242\n"
"s_add_u32 m0, s85, 81920\n"
"v_mfma_f32_16x16x32_bf16 v[108:111], a[248:251], a[4:7], 0\n"
"buffer_load_dwordx4 v242, s[36:39], 0 offen offset:0  lds\n"
"v_mfma_f32_16x16x32_bf16 v[108:111], a[252:255], a[20:23], v[108:111]\n"
"v_cvt_pk_bf16_f32 v17, v18, v19\n"
"v_mul_f32 v20, s70, v20\n"
"v_mul_f32 v21, s70, v21\n"
"v_mfma_f32_16x16x32_bf16 v[124:127], a[248:251], a[8:11], 0\n"
"ds_read_b64_tr_b16 a[244:245], v252 offset:4096\n"
"v_mul_f32 v22, s70, v22\n"
"v_mfma_f32_16x16x32_bf16 v[124:127], a[252:255], a[24:27], v[124:127]\n"
"ds_read_b64_tr_b16 a[246:247], v252 offset:4352\n"
"v_mul_f32 v23, s70, v23\n"
"v_mfma_f32_16x16x32_bf16 v[140:143], a[248:251], a[12:15], 0\n"
"buffer_load_dwordx4 v242, s[36:39], s67 offen offset:1024  lds\n"
"v_mfma_f32_16x16x32_bf16 v[140:143], a[252:255], a[28:31], v[140:143]\n"
"s_waitcnt lgkmcnt(4)\n"
"v_cvt_pk_bf16_f32 v18, v20, v21\n"
"v_cvt_pk_bf16_f32 v19, v22, v23\n"
"v_mul_f32 v24, s70, v24\n"
"v_mfma_f32_16x16x32_bf16 v[148:151], a[232:235], a[12:15], 0\n"
"buffer_load_dwordx4 v244, s[36:39], s68 offen offset:2048  lds\n"
"ds_read_b64_tr_b16 a[248:249], v253 offset:0\n"
"v_mfma_f32_16x16x32_bf16 v[148:151], a[236:239], a[28:31], v[148:151]\n"
"ds_read_b64_tr_b16 a[250:251], v253 offset:256\n"
"v_mul_f32 v25, s70, v25\n"
"v_mfma_f32_16x16x32_bf16 v[132:135], a[232:235], a[8:11], 0\n"
"v_permlane16_swap_b32 v16, v18\n"
"v_mfma_f32_16x16x32_bf16 v[132:135], a[236:239], a[24:27], v[132:135]\n"
"buffer_load_dwordx4 v244, s[36:39], s69 offen offset:3072  lds\n"
"v_mul_f32 v26, s70, v26\n"
"v_mfma_f32_16x16x32_bf16 v[116:119], a[232:235], a[4:7], 0\n"
"ds_read_b64_tr_b16 a[252:253], v253 offset:4096\n"
"v_mul_f32 v27, s70, v27\n"
"v_mfma_f32_16x16x32_bf16 v[116:119], a[236:239], a[20:23], v[116:119]\n"
"s_add_u32 s78, s80, 262528\n"
"ds_read_b64_tr_b16 a[254:255], v253 offset:4352\n"
"v_add_u32 v242, s78, v255\n"
"v_xor_b32 v244, 32, v242\n"
"s_add_u32 m0, s85, 86016\n"
"v_mfma_f32_16x16x32_bf16 v[100:103], a[232:235], a[0:3], 0\n"
"buffer_load_dwordx4 v242, s[36:39], 0 offen offset:0  lds\n"
"v_mfma_f32_16x16x32_bf16 v[100:103], a[236:239], a[16:19], v[100:103]\n"
"s_waitcnt lgkmcnt(4)\n"
"v_permlane16_swap_b32 v17, v19\n"
"v_cvt_pk_bf16_f32 v24, v24, v25\n"
"v_mfma_f32_16x16x32_bf16 v[64:67], a[240:243], a[32:35], v[64:67]\n"
"ds_read_b64_tr_b16 a[232:233], v252 offset:512\n"
"v_cvt_pk_bf16_f32 v25, v26, v27\n"
"v_mfma_f32_16x16x32_bf16 v[64:67], a[244:247], a[48:51], v[64:67]\n"
"ds_read_b64_tr_b16 a[234:235], v252 offset:768\n"
"v_mul_f32 v28, s70, v28\n"
"v_mfma_f32_16x16x32_bf16 v[72:75], a[240:243], a[36:39], v[72:75]\n"
"buffer_load_dwordx4 v242, s[36:39], s67 offen offset:1024  lds\n"
"v_mfma_f32_16x16x32_bf16 v[72:75], a[244:247], a[52:55], v[72:75]\n"
"v_mul_f32 v29, s70, v29\n"
"v_mul_f32 v30, s70, v30\n"
"v_mul_f32 v31, s70, v31\n"
"v_mfma_f32_16x16x32_bf16 v[80:83], a[240:243], a[40:43], v[80:83]\n"
"buffer_load_dwordx4 v244, s[36:39], s68 offen offset:2048  lds\n"
"ds_read_b64_tr_b16 a[236:237], v252 offset:4608\n"
"v_mfma_f32_16x16x32_bf16 v[80:83], a[244:247], a[56:59], v[80:83]\n"
"ds_read_b64_tr_b16 a[238:239], v252 offset:4864\n"
"v_cvt_pk_bf16_f32 v26, v28, v29\n"
"v_mfma_f32_16x16x32_bf16 v[88:91], a[240:243], a[44:47], v[88:91]\n"
"v_cvt_pk_bf16_f32 v27, v30, v31\n"
"v_mul_f32 v32, s70, v32\n"
"v_mfma_f32_16x16x32_bf16 v[88:91], a[244:247], a[60:63], v[88:91]\n"
"s_waitcnt lgkmcnt(4)\n"
"buffer_load_dwordx4 v244, s[36:39], s69 offen offset:3072  lds\n"
"v_mul_f32 v33, s70, v33\n"
"v_mfma_f32_16x16x32_bf16 v[92:95], a[248:251], a[44:47], v[92:95]\n"
"ds_read_b64_tr_b16 a[240:241], v253 offset:512\n"
"v_mul_f32 v34, s70, v34\n"
"v_mfma_f32_16x16x32_bf16 v[92:95], a[252:255], a[60:63], v[92:95]\n"
"ds_read_b64_tr_b16 a[242:243], v253 offset:768\n"
"v_mul_f32 v35, s70, v35\n"
"v_mfma_f32_16x16x32_bf16 v[84:87], a[248:251], a[40:43], v[84:87]\n"
"v_permlane16_swap_b32 v24, v26\n"
"v_mfma_f32_16x16x32_bf16 v[84:87], a[252:255], a[56:59], v[84:87]\n"
"v_permlane16_swap_b32 v25, v27\n"
"v_cvt_pk_bf16_f32 v32, v32, v33\n"
"v_mfma_f32_16x16x32_bf16 v[76:79], a[248:251], a[36:39], v[76:79]\n"
"s_add_u32 s80, s80, 524288\n"
"ds_read_b64_tr_b16 a[244:245], v253 offset:4608\n"
"v_cvt_pk_bf16_f32 v33, v34, v35\n"
"v_mfma_f32_16x16x32_bf16 v[76:79], a[252:255], a[52:55], v[76:79]\n"
"s_sub_u32 s78, s80, 3670016\n"
"ds_read_b64_tr_b16 a[246:247], v253 offset:4864\n"
"v_mul_f32 v36, s70, v36\n"
"v_mfma_f32_16x16x32_bf16 v[68:71], a[248:251], a[32:35], v[68:71]\n"
"s_min_u32 s80, s80, s78\n"
"v_mul_f32 v37, s70, v37\n"
"v_mul_f32 v38, s70, v38\n"
"v_mfma_f32_16x16x32_bf16 v[68:71], a[252:255], a[48:51], v[68:71]\n"
"s_waitcnt lgkmcnt(4)\n"
"v_mul_f32 v39, s70, v39\n"
"v_cvt_pk_bf16_f32 v34, v36, v37\n"
"v_cvt_pk_bf16_f32 v35, v38, v39\n"
"v_mfma_f32_16x16x32_bf16 v[96:99], a[232:235], a[32:35], v[96:99]\n"
"s_waitcnt vmcnt(16)\n"
"s_barrier\n"
"ds_read_b64_tr_b16 a[248:249], v250 offset:0\n"
"v_mfma_f32_16x16x32_bf16 v[96:99], a[236:239], a[48:51], v[96:99]\n"
"s_add_u32 s78, s80, 384\n"
"ds_read_b64_tr_b16 a[250:251], v250 offset:256\n"
"v_add_u32 v242, s78, v255\n"
"v_xor_b32 v244, 32, v242\n"
"s_add_u32 m0, s85, 98304\n"
"v_mfma_f32_16x16x32_bf16 v[108:111], a[232:235], a[36:39], v[108:111]\n"
"buffer_load_dwordx4 v242, s[36:39], 0 offen offset:0  lds\n"
"v_mfma_f32_16x16x32_bf16 v[108:111], a[236:239], a[52:55], v[108:111]\n"
"v_permlane16_swap_b32 v32, v34\n"
"v_mul_f32 v40, s70, v40\n"
"v_mfma_f32_16x16x32_bf16 v[124:127], a[232:235], a[40:43], v[124:127]\n"
"ds_read_b64_tr_b16 a[252:253], v250 offset:4096\n"
"v_mul_f32 v41, s70, v41\n"
"v_mfma_f32_16x16x32_bf16 v[124:127], a[236:239], a[56:59], v[124:127]\n"
"ds_read_b64_tr_b16 a[254:255], v250 offset:4352\n"
"v_mul_f32 v42, s70, v42\n"
"v_mfma_f32_16x16x32_bf16 v[140:143], a[232:235], a[44:47], v[140:143]\n"
"buffer_load_dwordx4 v242, s[36:39], s67 offen offset:1024  lds\n"
"v_mfma_f32_16x16x32_bf16 v[140:143], a[236:239], a[60:63], v[140:143]\n"
"s_waitcnt lgkmcnt(4)\n"
"v_permlane16_swap_b32 v33, v35\n"
"v_mul_f32 v43, s70, v43\n"
"v_mfma_f32_16x16x32_bf16 v[148:151], a[240:243], a[44:47], v[148:151]\n"
"buffer_load_dwordx4 v244, s[36:39], s68 offen offset:2048  lds\n"
"ds_read_b64_tr_b16 a[232:233], v251 offset:0\n"
"v_mfma_f32_16x16x32_bf16 v[148:151], a[244:247], a[60:63], v[148:151]\n"
"ds_read_b64_tr_b16 a[234:235], v251 offset:256\n"
"v_cvt_pk_bf16_f32 v40, v40, v41\n"
"v_mfma_f32_16x16x32_bf16 v[132:135], a[240:243], a[40:43], v[132:135]\n"
"v_cvt_pk_bf16_f32 v41, v42, v43\n"
"v_mul_f32 v44, s70, v44\n"
"v_mfma_f32_16x16x32_bf16 v[132:135], a[244:247], a[56:59], v[132:135]\n"
"buffer_load_dwordx4 v244, s[36:39], s69 offen offset:3072  lds\n"
"v_mul_f32 v45, s70, v45\n"
"v_mfma_f32_16x16x32_bf16 v[116:119], a[240:243], a[36:39], v[116:119]\n"
"ds_read_b64_tr_b16 a[236:237], v251 offset:4096\n"
"v_mul_f32 v46, s70, v46\n"
"v_mfma_f32_16x16x32_bf16 v[116:119], a[244:247], a[52:55], v[116:119]\n"
"s_add_u32 s78, s80, 262528\n"
"ds_read_b64_tr_b16 a[238:239], v251 offset:4352\n"
"v_add_u32 v242, s78, v255\n"
"v_xor_b32 v244, 32, v242\n"
"s_add_u32 m0, s85, 102400\n"
"v_mfma_f32_16x16x32_bf16 v[100:103], a[240:243], a[32:35], v[100:103]\n"
"buffer_load_dwordx4 v242, s[36:39], 0 offen offset:0  lds\n"
"v_mfma_f32_16x16x32_bf16 v[100:103], a[244:247], a[48:51], v[100:103]\n"
"s_waitcnt lgkmcnt(4)\n"
"v_mul_f32 v47, s70, v47\n"
"v_cvt_pk_bf16_f32 v42, v44, v45\n"
"v_cvt_pk_bf16_f32 v43, v46, v47\n"
"v_mfma_f32_16x16x32_bf16 v[64:67], a[248:251], a[64:67], v[64:67]\n"
"ds_read_b64_tr_b16 a[240:241], v250 offset:512\n"
"v_mul_f32 v48, s70, v48\n"
"v_mfma_f32_16x16x32_bf16 v[64:67], a[252:255], a[80:83], v[64:67]\n"
"ds_read_b64_tr_b16 a[242:243], v250 offset:768\n"
"v_mul_f32 v49, s70, v49\n"
"v_mfma_f32_16x16x32_bf16 v[72:75], a[248:251], a[68:71], v[72:75]\n"
"buffer_load_dwordx4 v242, s[36:39], s67 offen offset:1024  lds\n"
"v_mfma_f32_16x16x32_bf16 v[72:75], a[252:255], a[84:87], v[72:75]\n"
"v_permlane16_swap_b32 v40, v42\n"
"v_mul_f32 v50, s70, v50\n"
"v_mfma_f32_16x16x32_bf16 v[80:83], a[248:251], a[72:75], v[80:83]\n"
"buffer_load_dwordx4 v244, s[36:39], s68 offen offset:2048  lds\n"
"ds_read_b64_tr_b16 a[244:245], v250 offset:4608\n"
"v_mfma_f32_16x16x32_bf16 v[80:83], a[252:255], a[88:91], v[80:83]\n"
"ds_read_b64_tr_b16 a[246:247], v250 offset:4864\n"
"v_mul_f32 v51, s70, v51\n"
"v_mfma_f32_16x16x32_bf16 v[88:91], a[248:251], a[76:79], v[88:91]\n"
"v_permlane16_swap_b32 v41, v43\n"
"v_mfma_f32_16x16x32_bf16 v[88:91], a[252:255], a[92:95], v[88:91]\n"
"s_waitcnt lgkmcnt(4)\n"
"buffer_load_dwordx4 v244, s[36:39], s69 offen offset:3072  lds\n"
"v_cvt_pk_bf16_f32 v48, v48, v49\n"
"v_mfma_f32_16x16x32_bf16 v[92:95], a[232:235], a[76:79], v[92:95]\n"
"ds_read_b64_tr_b16 a[248:249], v251 offset:512\n"
"v_cvt_pk_bf16_f32 v49, v50, v51\n"
"v_mfma_f32_16x16x32_bf16 v[92:95], a[236:239], a[92:95], v[92:95]\n"
"ds_read_b64_tr_b16 a[250:251], v251 offset:768\n"
"v_mul_f32 v52, s70, v52\n"
"v_mfma_f32_16x16x32_bf16 v[84:87], a[232:235], a[72:75], v[84:87]\n"
"v_mul_f32 v53, s70, v53\n"
"v_mul_f32 v54, s70, v54\n"
"v_mfma_f32_16x16x32_bf16 v[84:87], a[236:239], a[88:91], v[84:87]\n"
"v_mul_f32 v55, s70, v55\n"
"v_cvt_pk_bf16_f32 v50, v52, v53\n"
"v_cvt_pk_bf16_f32 v51, v54, v55\n"
"v_mfma_f32_16x16x32_bf16 v[76:79], a[232:235], a[68:71], v[76:79]\n"
"s_add_u32 s80, s80, 524288\n"
"ds_read_b64_tr_b16 a[252:253], v251 offset:4608\n"
"v_mul_f32 v56, s70, v56\n"
"v_mfma_f32_16x16x32_bf16 v[76:79], a[236:239], a[84:87], v[76:79]\n"
"s_sub_u32 s78, s80, 3670016\n"
"ds_read_b64_tr_b16 a[254:255], v251 offset:4864\n"
"v_mul_f32 v57, s70, v57\n"
"v_mfma_f32_16x16x32_bf16 v[68:71], a[232:235], a[64:67], v[68:71]\n"
"s_min_u32 s80, s80, s78\n"
"v_permlane16_swap_b32 v48, v50\n"
"v_mfma_f32_16x16x32_bf16 v[68:71], a[236:239], a[80:83], v[68:71]\n"
"s_waitcnt lgkmcnt(4)\n"
"v_permlane16_swap_b32 v49, v51\n"
"v_mul_f32 v58, s70, v58\n"
"v_mfma_f32_16x16x32_bf16 v[96:99], a[240:243], a[64:67], v[96:99]\n"
"s_waitcnt vmcnt(16)\n"
"s_barrier\n"
"ds_read_b64_tr_b16 a[232:233], v250 offset:16384\n"
"v_mfma_f32_16x16x32_bf16 v[96:99], a[244:247], a[80:83], v[96:99]\n"
"s_add_u32 s78, s80, 384\n"
"ds_read_b64_tr_b16 a[234:235], v250 offset:16640\n"
"v_add_u32 v242, s78, v255\n"
"v_xor_b32 v244, 32, v242\n"
"s_add_u32 m0, s85, 114688\n"
"v_mfma_f32_16x16x32_bf16 v[108:111], a[240:243], a[68:71], v[108:111]\n"
"buffer_load_dwordx4 v242, s[36:39], 0 offen offset:0  lds\n"
"v_mfma_f32_16x16x32_bf16 v[108:111], a[244:247], a[84:87], v[108:111]\n"
"v_mul_f32 v59, s70, v59\n"
"v_cvt_pk_bf16_f32 v56, v56, v57\n"
"v_cvt_pk_bf16_f32 v57, v58, v59\n"
"v_mfma_f32_16x16x32_bf16 v[124:127], a[240:243], a[72:75], v[124:127]\n"
"ds_read_b64_tr_b16 a[236:237], v250 offset:20480\n"
"v_mul_f32 v60, s70, v60\n"
"v_mfma_f32_16x16x32_bf16 v[124:127], a[244:247], a[88:91], v[124:127]\n"
"ds_read_b64_tr_b16 a[238:239], v250 offset:20736\n"
"v_mul_f32 v61, s70, v61\n"
"v_mfma_f32_16x16x32_bf16 v[140:143], a[240:243], a[76:79], v[140:143]\n"
"buffer_load_dwordx4 v242, s[36:39], s67 offen offset:1024  lds\n"
"v_mfma_f32_16x16x32_bf16 v[140:143], a[244:247], a[92:95], v[140:143]\n"
"s_waitcnt lgkmcnt(4)\n"
"v_mul_f32 v62, s70, v62\n"
"v_mul_f32 v63, s70, v63\n"
"v_cvt_pk_bf16_f32 v58, v60, v61\n"
"v_mfma_f32_16x16x32_bf16 v[148:151], a[248:251], a[76:79], v[148:151]\n"
"buffer_load_dwordx4 v244, s[36:39], s68 offen offset:2048  lds\n"
"ds_read_b64_tr_b16 a[240:241], v251 offset:16384\n"
"v_mfma_f32_16x16x32_bf16 v[148:151], a[252:255], a[92:95], v[148:151]\n"
"ds_read_b64_tr_b16 a[242:243], v251 offset:16640\n"
"v_cvt_pk_bf16_f32 v59, v62, v63\n"
"v_mfma_f32_16x16x32_bf16 v[132:135], a[248:251], a[72:75], v[132:135]\n"
"v_permlane16_swap_b32 v56, v58\n"
"v_mfma_f32_16x16x32_bf16 v[132:135], a[252:255], a[88:91], v[132:135]\n"
"buffer_load_dwordx4 v244, s[36:39], s69 offen offset:3072  lds\n"
"v_accvgpr_write_b32 a0, v104\n"
"v_mfma_f32_16x16x32_bf16 v[116:119], a[248:251], a[68:71], v[116:119]\n"
"ds_read_b64_tr_b16 a[244:245], v251 offset:20480\n"
"v_accvgpr_write_b32 a1, v105\n"
"v_mfma_f32_16x16x32_bf16 v[116:119], a[252:255], a[84:87], v[116:119]\n"
"s_add_u32 s78, s80, 262528\n"
"ds_read_b64_tr_b16 a[246:247], v251 offset:20736\n"
"v_add_u32 v242, s78, v255\n"
"v_xor_b32 v244, 32, v242\n"
"s_add_u32 m0, s85, 118784\n"
"v_mfma_f32_16x16x32_bf16 v[100:103], a[248:251], a[64:67], v[100:103]\n"
"buffer_load_dwordx4 v242, s[36:39], 0 offen offset:0  lds\n"
"v_mfma_f32_16x16x32_bf16 v[100:103], a[252:255], a[80:83], v[100:103]\n"
"s_waitcnt lgkmcnt(4)\n"
"v_permlane16_swap_b32 v57, v59\n"
"v_accvgpr_write_b32 a2, v106\n"
"v_mfma_f32_16x16x32_bf16 v[64:67], a[232:235], a[96:99], v[64:67]\n"
"ds_read_b64_tr_b16 a[248:249], v250 offset:16896\n"
"v_accvgpr_write_b32 a3, v107\n"
"v_mfma_f32_16x16x32_bf16 v[64:67], a[236:239], a[112:115], v[64:67]\n"
"ds_read_b64_tr_b16 a[250:251], v250 offset:17152\n"
"v_accvgpr_write_b32 a4, v112\n"
"v_mfma_f32_16x16x32_bf16 v[72:75], a[232:235], a[100:103], v[72:75]\n"
"buffer_load_dwordx4 v242, s[36:39], s67 offen offset:1024  lds\n"
"v_mfma_f32_16x16x32_bf16 v[72:75], a[236:239], a[116:119], v[72:75]\n"
"v_accvgpr_write_b32 a5, v113\n"
"v_accvgpr_write_b32 a6, v114\n"
"v_accvgpr_write_b32 a7, v115\n"
"v_mfma_f32_16x16x32_bf16 v[80:83], a[232:235], a[104:107], v[80:83]\n"
"buffer_load_dwordx4 v244, s[36:39], s68 offen offset:2048  lds\n"
"ds_read_b64_tr_b16 a[252:253], v250 offset:20992\n"
"v_mfma_f32_16x16x32_bf16 v[80:83], a[236:239], a[120:123], v[80:83]\n"
"ds_read_b64_tr_b16 a[254:255], v250 offset:21248\n"
"v_accvgpr_write_b32 a8, v120\n"
"v_mfma_f32_16x16x32_bf16 v[88:91], a[232:235], a[108:111], v[88:91]\n"
"v_accvgpr_write_b32 a9, v121\n"
"v_accvgpr_write_b32 a10, v122\n"
"v_mfma_f32_16x16x32_bf16 v[88:91], a[236:239], a[124:127], v[88:91]\n"
"s_waitcnt lgkmcnt(4)\n"
"buffer_load_dwordx4 v244, s[36:39], s69 offen offset:3072  lds\n"
"v_accvgpr_write_b32 a11, v123\n"
"v_mfma_f32_16x16x32_bf16 v[68:71], a[240:243], a[96:99], v[68:71]\n"
"ds_read_b64_tr_b16 a[232:233], v251 offset:16896\n"
"v_accvgpr_write_b32 a12, v128\n"
"v_mfma_f32_16x16x32_bf16 v[68:71], a[244:247], a[112:115], v[68:71]\n"
"ds_read_b64_tr_b16 a[234:235], v251 offset:17152\n"
"v_accvgpr_write_b32 a13, v129\n"
"v_mfma_f32_16x16x32_bf16 v[76:79], a[240:243], a[100:103], v[76:79]\n"
"v_accvgpr_write_b32 a14, v130\n"
"v_accvgpr_write_b32 a15, v131\n"
"v_mfma_f32_16x16x32_bf16 v[76:79], a[244:247], a[116:119], v[76:79]\n"
"v_accvgpr_write_b32 a16, v136\n"
"v_accvgpr_write_b32 a17, v137\n"
"v_accvgpr_write_b32 a18, v138\n"
"v_mfma_f32_16x16x32_bf16 v[84:87], a[240:243], a[104:107], v[84:87]\n"
"ds_read_b64_tr_b16 a[236:237], v251 offset:20992\n"
"v_accvgpr_write_b32 a19, v139\n"
"v_mfma_f32_16x16x32_bf16 v[84:87], a[244:247], a[120:123], v[84:87]\n"
"ds_read_b64_tr_b16 a[238:239], v251 offset:21248\n"
"v_accvgpr_write_b32 a20, v144\n"
"v_mfma_f32_16x16x32_bf16 v[92:95], a[240:243], a[108:111], v[92:95]\n"
"v_accvgpr_write_b32 a21, v145\n"
"v_accvgpr_write_b32 a22, v146\n"
"v_mfma_f32_16x16x32_bf16 v[92:95], a[244:247], a[124:127], v[92:95]\n"
"s_waitcnt lgkmcnt(4)\n"
"v_accvgpr_write_b32 a23, v147\n"
"v_accvgpr_write_b32 a24, v152\n"
"v_accvgpr_write_b32 a25, v153\n"
"v_mfma_f32_16x16x32_bf16 v[96:99], a[248:251], a[96:99], v[96:99]\n"
"s_waitcnt vmcnt(16)\n"
"s_barrier\n"
"ds_read_b64_tr_b16 a[240:241], v250 offset:32768\n"
"v_mfma_f32_16x16x32_bf16 v[96:99], a[252:255], a[112:115], v[96:99]\n"
"ds_read_b64_tr_b16 a[242:243], v250 offset:33024\n"
"v_add_u32 v242, 49152, v245\n"
"s_add_u32 m0, s85, 49152\n"
"v_mfma_f32_16x16x32_bf16 v[108:111], a[248:251], a[100:103], v[108:111]\n"
"buffer_load_dwordx4 v242, s[40:43], 0 offen offset:0 sc0 sc1 nt lds\n"
"v_mfma_f32_16x16x32_bf16 v[108:111], a[252:255], a[116:119], v[108:111]\n"
"v_accvgpr_write_b32 a26, v154\n"
"v_accvgpr_write_b32 a27, v155\n"
"v_accvgpr_write_b32 a28, v160\n"
"v_mfma_f32_16x16x32_bf16 v[124:127], a[248:251], a[104:107], v[124:127]\n"
"ds_read_b64_tr_b16 a[244:245], v250 offset:36864\n"
"v_accvgpr_write_b32 a29, v161\n"
"v_mfma_f32_16x16x32_bf16 v[124:127], a[252:255], a[120:123], v[124:127]\n"
"ds_read_b64_tr_b16 a[246:247], v250 offset:37120\n"
"v_accvgpr_write_b32 a30, v162\n"
"v_mfma_f32_16x16x32_bf16 v[140:143], a[248:251], a[108:111], v[140:143]\n"
"buffer_load_dwordx4 v242, s[40:43], s61 offen offset:1024 sc0 sc1 nt lds\n"
"v_mfma_f32_16x16x32_bf16 v[140:143], a[252:255], a[124:127], v[140:143]\n"
"s_waitcnt lgkmcnt(4)\n"
"v_accvgpr_write_b32 a31, v163\n"
"v_accvgpr_write_b32 a32, v168\n"
"v_accvgpr_write_b32 a33, v169\n"
"v_mfma_f32_16x16x32_bf16 v[100:103], a[232:235], a[96:99], v[100:103]\n"
"buffer_load_dwordx4 v242, s[40:43], s62 offen offset:2048 sc0 sc1 nt lds\n"
"ds_read_b64_tr_b16 a[248:249], v251 offset:32768\n"
"v_mfma_f32_16x16x32_bf16 v[100:103], a[236:239], a[112:115], v[100:103]\n"
"ds_read_b64_tr_b16 a[250:251], v251 offset:33024\n"
"v_accvgpr_write_b32 a34, v170\n"
"v_mfma_f32_16x16x32_bf16 v[116:119], a[232:235], a[100:103], v[116:119]\n"
"v_accvgpr_write_b32 a35, v171\n"
"v_accvgpr_write_b32 a36, v176\n"
"v_mfma_f32_16x16x32_bf16 v[116:119], a[236:239], a[116:119], v[116:119]\n"
"buffer_load_dwordx4 v242, s[40:43], s63 offen offset:3072 sc0 sc1 nt lds\n"
"v_accvgpr_write_b32 a37, v177\n"
"v_mfma_f32_16x16x32_bf16 v[132:135], a[232:235], a[104:107], v[132:135]\n"
"ds_read_b64_tr_b16 a[252:253], v251 offset:36864\n"
"v_accvgpr_write_b32 a38, v178\n"
"v_mfma_f32_16x16x32_bf16 v[132:135], a[236:239], a[120:123], v[132:135]\n"
"ds_read_b64_tr_b16 a[254:255], v251 offset:37120\n"
"v_add_u32 v242, 49280, v245\n"
"s_add_u32 m0, s85, 53248\n"
"v_mfma_f32_16x16x32_bf16 v[148:151], a[232:235], a[108:111], v[148:151]\n"
"buffer_load_dwordx4 v242, s[40:43], 0 offen offset:0 sc0 sc1 nt lds\n"
"v_mfma_f32_16x16x32_bf16 v[148:151], a[236:239], a[124:127], v[148:151]\n"
"s_waitcnt lgkmcnt(4)\n"
"v_accvgpr_write_b32 a39, v179\n"
"v_accvgpr_write_b32 a40, v184\n"
"v_accvgpr_write_b32 a41, v185\n"
"v_mfma_f32_16x16x32_bf16 v[64:67], a[240:243], a[128:131], v[64:67]\n"
"s_waitcnt vmcnt(13)\n"
"s_barrier\n"
"ds_read_b64_tr_b16 a[232:233], v250 offset:49152\n"
"v_mfma_f32_16x16x32_bf16 v[64:67], a[244:247], a[144:147], v[64:67]\n"
"ds_read_b64_tr_b16 a[234:235], v250 offset:49408\n"
"v_accvgpr_write_b32 a42, v186\n"
"v_mfma_f32_16x16x32_bf16 v[72:75], a[240:243], a[132:135], v[72:75]\n"
"buffer_load_dwordx4 v242, s[40:43], s61 offen offset:1024 sc0 sc1 nt lds\n"
"v_mfma_f32_16x16x32_bf16 v[72:75], a[244:247], a[148:151], v[72:75]\n"
"v_accvgpr_write_b32 a43, v187\n"
"v_accvgpr_write_b32 a44, v192\n"
"v_accvgpr_write_b32 a45, v193\n"
"v_mfma_f32_16x16x32_bf16 v[80:83], a[240:243], a[136:139], v[80:83]\n"
"buffer_load_dwordx4 v242, s[40:43], s62 offen offset:2048 sc0 sc1 nt lds\n"
"ds_read_b64_tr_b16 a[236:237], v250 offset:53248\n"
"v_mfma_f32_16x16x32_bf16 v[80:83], a[244:247], a[152:155], v[80:83]\n"
"ds_read_b64_tr_b16 a[238:239], v250 offset:53504\n"
"v_accvgpr_write_b32 a46, v194\n"
"v_mfma_f32_16x16x32_bf16 v[88:91], a[240:243], a[140:143], v[88:91]\n"
"v_accvgpr_write_b32 a47, v195\n"
"v_accvgpr_write_b32 a48, v200\n"
"v_mfma_f32_16x16x32_bf16 v[88:91], a[244:247], a[156:159], v[88:91]\n"
"s_waitcnt lgkmcnt(4)\n"
"buffer_load_dwordx4 v242, s[40:43], s63 offen offset:3072 sc0 sc1 nt lds\n"
"v_accvgpr_write_b32 a49, v201\n"
"v_mfma_f32_16x16x32_bf16 v[68:71], a[248:251], a[128:131], v[68:71]\n"
"ds_read_b64_tr_b16 a[240:241], v251 offset:49152\n"
"v_accvgpr_write_b32 a50, v202\n"
"v_mfma_f32_16x16x32_bf16 v[68:71], a[252:255], a[144:147], v[68:71]\n"
"ds_read_b64_tr_b16 a[242:243], v251 offset:49408\n"
"v_add_u32 v242, 32768, v245\n"
"s_add_u32 m0, s85, 65536\n"
"v_mfma_f32_16x16x32_bf16 v[76:79], a[248:251], a[132:135], v[76:79]\n"
"buffer_load_dwordx4 v242, s[44:47], 0 offen offset:0 sc0 sc1 nt lds\n"
"v_mfma_f32_16x16x32_bf16 v[76:79], a[252:255], a[148:151], v[76:79]\n"
"v_accvgpr_write_b32 a51, v203\n"
"v_accvgpr_write_b32 a52, v208\n"
"v_accvgpr_write_b32 a53, v209\n"
"v_mfma_f32_16x16x32_bf16 v[84:87], a[248:251], a[136:139], v[84:87]\n"
"ds_read_b64_tr_b16 a[244:245], v251 offset:53248\n"
"v_accvgpr_write_b32 a54, v210\n"
"v_mfma_f32_16x16x32_bf16 v[84:87], a[252:255], a[152:155], v[84:87]\n"
"ds_read_b64_tr_b16 a[246:247], v251 offset:53504\n"
"v_accvgpr_write_b32 a55, v211\n"
"v_mfma_f32_16x16x32_bf16 v[92:95], a[248:251], a[140:143], v[92:95]\n"
"buffer_load_dwordx4 v242, s[44:47], s61 offen offset:1024 sc0 sc1 nt lds\n"
"v_mfma_f32_16x16x32_bf16 v[92:95], a[252:255], a[156:159], v[92:95]\n"
"s_waitcnt lgkmcnt(4)\n"
"v_accvgpr_write_b32 a56, v216\n"
"v_accvgpr_write_b32 a57, v217\n"
"v_accvgpr_write_b32 a58, v218\n"
"v_mfma_f32_16x16x32_bf16 v[64:67], a[232:235], a[160:163], v[64:67]\n"
"s_waitcnt vmcnt(10)\n"
"s_barrier\n"
"ds_read_b64_tr_b16 a[248:249], v252 offset:0\n"
"v_mfma_f32_16x16x32_bf16 v[64:67], a[236:239], a[176:179], v[64:67]\n"
"buffer_load_dwordx4 v242, s[44:47], s62 offen offset:2048 sc0 sc1 nt lds\n"
"ds_read_b64_tr_b16 a[250:251], v252 offset:256\n"
"v_mfma_f32_16x16x32_bf16 v[72:75], a[232:235], a[164:167], v[72:75]\n"
"v_accvgpr_write_b32 a59, v219\n"
"v_accvgpr_write_b32 a60, v224\n"
"v_mfma_f32_16x16x32_bf16 v[72:75], a[236:239], a[180:183], v[72:75]\n"
"buffer_load_dwordx4 v242, s[44:47], s63 offen offset:3072 sc0 sc1 nt lds\n"
"v_accvgpr_write_b32 a61, v225\n"
"v_mfma_f32_16x16x32_bf16 v[80:83], a[232:235], a[168:171], v[80:83]\n"
"ds_read_b64_tr_b16 a[252:253], v252 offset:4096\n"
"v_accvgpr_write_b32 a62, v226\n"
"v_mfma_f32_16x16x32_bf16 v[80:83], a[236:239], a[184:187], v[80:83]\n"
"ds_read_b64_tr_b16 a[254:255], v252 offset:4352\n"
"v_add_u32 v242, 32896, v245\n"
"s_add_u32 m0, s85, 69632\n"
"v_mfma_f32_16x16x32_bf16 v[88:91], a[232:235], a[172:175], v[88:91]\n"
"buffer_load_dwordx4 v242, s[44:47], 0 offen offset:0 sc0 sc1 nt lds\n"
"v_mfma_f32_16x16x32_bf16 v[88:91], a[236:239], a[188:191], v[88:91]\n"
"s_waitcnt lgkmcnt(4)\n"
"v_accvgpr_write_b32 a63, v227\n"
"v_accvgpr_write_b32 a64, v0\n"
"v_accvgpr_write_b32 a65, v1\n"
"v_mfma_f32_16x16x32_bf16 v[68:71], a[240:243], a[160:163], v[68:71]\n"
"ds_read_b64_tr_b16 a[232:233], v253 offset:0\n"
"v_accvgpr_write_b32 a66, v2\n"
"v_mfma_f32_16x16x32_bf16 v[68:71], a[244:247], a[176:179], v[68:71]\n"
"ds_read_b64_tr_b16 a[234:235], v253 offset:256\n"
"v_accvgpr_write_b32 a67, v3\n"
"v_mfma_f32_16x16x32_bf16 v[76:79], a[240:243], a[164:167], v[76:79]\n"
"buffer_load_dwordx4 v242, s[44:47], s61 offen offset:1024 sc0 sc1 nt lds\n"
"v_mfma_f32_16x16x32_bf16 v[76:79], a[244:247], a[180:183], v[76:79]\n"
"v_accvgpr_write_b32 a68, v8\n"
"v_accvgpr_write_b32 a69, v9\n"
"v_accvgpr_write_b32 a70, v10\n"
"v_mfma_f32_16x16x32_bf16 v[84:87], a[240:243], a[168:171], v[84:87]\n"
"buffer_load_dwordx4 v242, s[44:47], s62 offen offset:2048 sc0 sc1 nt lds\n"
"ds_read_b64_tr_b16 a[236:237], v253 offset:4096\n"
"v_mfma_f32_16x16x32_bf16 v[84:87], a[244:247], a[184:187], v[84:87]\n"
"ds_read_b64_tr_b16 a[238:239], v253 offset:4352\n"
"v_accvgpr_write_b32 a71, v11\n"
"v_mfma_f32_16x16x32_bf16 v[92:95], a[240:243], a[172:175], v[92:95]\n"
"v_accvgpr_write_b32 a72, v16\n"
"v_accvgpr_write_b32 a73, v17\n"
"v_mfma_f32_16x16x32_bf16 v[92:95], a[244:247], a[188:191], v[92:95]\n"
"s_waitcnt lgkmcnt(4)\n"
"buffer_load_dwordx4 v242, s[44:47], s63 offen offset:3072 sc0 sc1 nt lds\n"
"v_accvgpr_write_b32 a74, v18\n"
"v_mfma_f32_16x16x32_bf16 v[64:67], a[248:251], a[192:195], v[64:67]\n"
"ds_read_b64_tr_b16 a[240:241], v250 offset:33280\n"
"v_accvgpr_write_b32 a75, v19\n"
"v_mfma_f32_16x16x32_bf16 v[64:67], a[252:255], a[208:211], v[64:67]\n"
"ds_read_b64_tr_b16 a[242:243], v250 offset:33536\n"
"v_accvgpr_write_b32 a76, v24\n"
"v_mfma_f32_16x16x32_bf16 v[72:75], a[248:251], a[196:199], v[72:75]\n"
"v_accvgpr_write_b32 a77, v25\n"
"v_accvgpr_write_b32 a78, v26\n"
"v_mfma_f32_16x16x32_bf16 v[72:75], a[252:255], a[212:215], v[72:75]\n"
"v_accvgpr_write_b32 a79, v27\n"
"v_accvgpr_write_b32 a80, v32\n"
"v_accvgpr_write_b32 a81, v33\n"
"v_mfma_f32_16x16x32_bf16 v[80:83], a[248:251], a[200:203], v[80:83]\n"
"ds_read_b64_tr_b16 a[244:245], v250 offset:37376\n"
"v_accvgpr_write_b32 a82, v34\n"
"v_mfma_f32_16x16x32_bf16 v[80:83], a[252:255], a[216:219], v[80:83]\n"
"ds_read_b64_tr_b16 a[246:247], v250 offset:37632\n"
"v_accvgpr_write_b32 a83, v35\n"
"v_mfma_f32_16x16x32_bf16 v[88:91], a[248:251], a[204:207], v[88:91]\n"
"v_accvgpr_write_b32 a84, v40\n"
"v_accvgpr_write_b32 a85, v41\n"
"v_mfma_f32_16x16x32_bf16 v[88:91], a[252:255], a[220:223], v[88:91]\n"
"s_waitcnt lgkmcnt(4)\n"
"v_accvgpr_write_b32 a86, v42\n"
"v_accvgpr_write_b32 a87, v43\n"
"v_accvgpr_write_b32 a88, v48\n"
"v_mfma_f32_16x16x32_bf16 v[68:71], a[232:235], a[192:195], v[68:71]\n"
"ds_read_b64_tr_b16 a[248:249], v251 offset:33280\n"
"v_accvgpr_write_b32 a89, v49\n"
"v_mfma_f32_16x16x32_bf16 v[68:71], a[236:239], a[208:211], v[68:71]\n"
"ds_read_b64_tr_b16 a[250:251], v251 offset:33536\n"
"v_accvgpr_write_b32 a90, v50\n"
"v_mfma_f32_16x16x32_bf16 v[76:79], a[232:235], a[196:199], v[76:79]\n"
"v_accvgpr_write_b32 a91, v51\n"
"v_accvgpr_write_b32 a92, v56\n"
"v_mfma_f32_16x16x32_bf16 v[76:79], a[236:239], a[212:215], v[76:79]\n"
"v_accvgpr_write_b32 a93, v57\n"
"v_accvgpr_write_b32 a94, v58\n"
"v_accvgpr_write_b32 a95, v59\n"
"v_mfma_f32_16x16x32_bf16 v[84:87], a[232:235], a[200:203], v[84:87]\n"
"ds_read_b64_tr_b16 a[252:253], v251 offset:37376\n"
"v_mfma_f32_16x16x32_bf16 v[84:87], a[236:239], a[216:219], v[84:87]\n"
"ds_read_b64_tr_b16 a[254:255], v251 offset:37632\n"
"v_mfma_f32_16x16x32_bf16 v[92:95], a[232:235], a[204:207], v[92:95]\n"
"v_mul_f32 v64, s70, v64\n"
"v_mul_f32 v65, s70, v65\n"
"v_mfma_f32_16x16x32_bf16 v[92:95], a[236:239], a[220:223], v[92:95]\n"
"s_waitcnt lgkmcnt(4)\n"
"v_mul_f32 v66, s70, v66\n"
"v_mul_f32 v67, s70, v67\n"
"v_mfma_f32_16x16x32_bf16 v[96:99], a[240:243], a[128:131], v[96:99]\n"
"ds_read_b64_tr_b16 a[232:233], v250 offset:49664\n"
"v_cvt_pk_bf16_f32 v64, v64, v65\n"
"v_mfma_f32_16x16x32_bf16 v[96:99], a[244:247], a[144:147], v[96:99]\n"
"ds_read_b64_tr_b16 a[234:235], v250 offset:49920\n"
"v_cvt_pk_bf16_f32 v65, v66, v67\n"
"v_mfma_f32_16x16x32_bf16 v[108:111], a[240:243], a[132:135], v[108:111]\n"
"v_mul_f32 v68, s70, v68\n"
"v_mul_f32 v69, s70, v69\n"
"v_mfma_f32_16x16x32_bf16 v[108:111], a[244:247], a[148:151], v[108:111]\n"
"v_mul_f32 v70, s70, v70\n"
"v_mul_f32 v71, s70, v71\n"
"v_mfma_f32_16x16x32_bf16 v[124:127], a[240:243], a[136:139], v[124:127]\n"
"ds_read_b64_tr_b16 a[236:237], v250 offset:53760\n"
"v_cvt_pk_bf16_f32 v66, v68, v69\n"
"v_mfma_f32_16x16x32_bf16 v[124:127], a[244:247], a[152:155], v[124:127]\n"
"ds_read_b64_tr_b16 a[238:239], v250 offset:54016\n"
"v_cvt_pk_bf16_f32 v67, v70, v71\n"
"v_mul_f32 v72, s70, v72\n"
"v_mfma_f32_16x16x32_bf16 v[140:143], a[240:243], a[140:143], v[140:143]\n"
"v_permlane16_swap_b32 v64, v66\n"
"v_mfma_f32_16x16x32_bf16 v[140:143], a[244:247], a[156:159], v[140:143]\n"
"s_waitcnt lgkmcnt(4)\n"
"v_permlane16_swap_b32 v65, v67\n"
"v_mfma_f32_16x16x32_bf16 v[100:103], a[248:251], a[128:131], v[100:103]\n"
"ds_read_b64_tr_b16 a[240:241], v251 offset:49664\n"
"v_accvgpr_write_b32 a96, v64\n"
"v_mfma_f32_16x16x32_bf16 v[100:103], a[252:255], a[144:147], v[100:103]\n"
"ds_read_b64_tr_b16 a[242:243], v251 offset:49920\n"
"v_accvgpr_write_b32 a97, v65\n"
"v_mfma_f32_16x16x32_bf16 v[116:119], a[248:251], a[132:135], v[116:119]\n"
"v_accvgpr_write_b32 a98, v66\n"
"v_accvgpr_write_b32 a99, v67\n"
"v_mfma_f32_16x16x32_bf16 v[116:119], a[252:255], a[148:151], v[116:119]\n"
"v_mul_f32 v73, s70, v73\n"
"v_mul_f32 v74, s70, v74\n"
"v_mfma_f32_16x16x32_bf16 v[132:135], a[248:251], a[136:139], v[132:135]\n"
"ds_read_b64_tr_b16 a[244:245], v251 offset:53760\n"
"v_mul_f32 v75, s70, v75\n"
"v_mfma_f32_16x16x32_bf16 v[132:135], a[252:255], a[152:155], v[132:135]\n"
"ds_read_b64_tr_b16 a[246:247], v251 offset:54016\n"
"v_cvt_pk_bf16_f32 v72, v72, v73\n"
"v_mfma_f32_16x16x32_bf16 v[148:151], a[248:251], a[140:143], v[148:151]\n"
"v_cvt_pk_bf16_f32 v73, v74, v75\n"
"v_mul_f32 v76, s70, v76\n"
"v_mfma_f32_16x16x32_bf16 v[148:151], a[252:255], a[156:159], v[148:151]\n"
"s_waitcnt lgkmcnt(4)\n"
"v_mul_f32 v77, s70, v77\n"
"v_mul_f32 v78, s70, v78\n"
"v_mul_f32 v79, s70, v79\n"
"v_mfma_f32_16x16x32_bf16 v[96:99], a[232:235], a[160:163], v[96:99]\n"
"ds_read_b64_tr_b16 a[248:249], v252 offset:512\n"
"v_cvt_pk_bf16_f32 v74, v76, v77\n"
"v_mfma_f32_16x16x32_bf16 v[96:99], a[236:239], a[176:179], v[96:99]\n"
"ds_read_b64_tr_b16 a[250:251], v252 offset:768\n"
"v_cvt_pk_bf16_f32 v75, v78, v79\n"
"v_mfma_f32_16x16x32_bf16 v[108:111], a[232:235], a[164:167], v[108:111]\n"
"v_permlane16_swap_b32 v72, v74\n"
"v_mfma_f32_16x16x32_bf16 v[108:111], a[236:239], a[180:183], v[108:111]\n"
"v_permlane16_swap_b32 v73, v75\n"
"v_mfma_f32_16x16x32_bf16 v[124:127], a[232:235], a[168:171], v[124:127]\n"
"ds_read_b64_tr_b16 a[252:253], v252 offset:4608\n"
"v_accvgpr_write_b32 a100, v72\n"
"v_mfma_f32_16x16x32_bf16 v[124:127], a[236:239], a[184:187], v[124:127]\n"
"ds_read_b64_tr_b16 a[254:255], v252 offset:4864\n"
"v_accvgpr_write_b32 a101, v73\n"
"v_mfma_f32_16x16x32_bf16 v[140:143], a[232:235], a[172:175], v[140:143]\n"
"v_accvgpr_write_b32 a102, v74\n"
"v_accvgpr_write_b32 a103, v75\n"
"v_mfma_f32_16x16x32_bf16 v[140:143], a[236:239], a[188:191], v[140:143]\n"
"s_waitcnt lgkmcnt(4)\n"
"v_mul_f32 v80, s70, v80\n"
"v_mul_f32 v81, s70, v81\n"
"v_mfma_f32_16x16x32_bf16 v[100:103], a[240:243], a[160:163], v[100:103]\n"
"ds_read_b64_tr_b16 a[232:233], v253 offset:512\n"
"v_mul_f32 v82, s70, v82\n"
"v_mfma_f32_16x16x32_bf16 v[100:103], a[244:247], a[176:179], v[100:103]\n"
"ds_read_b64_tr_b16 a[234:235], v253 offset:768\n"
"v_mul_f32 v83, s70, v83\n"
"v_cvt_pk_bf16_f32 v80, v80, v81\n"
"v_mfma_f32_16x16x32_bf16 v[116:119], a[240:243], a[164:167], v[116:119]\n"
"v_cvt_pk_bf16_f32 v81, v82, v83\n"
"v_mul_f32 v84, s70, v84\n"
"v_mfma_f32_16x16x32_bf16 v[116:119], a[244:247], a[180:183], v[116:119]\n"
"v_mul_f32 v85, s70, v85\n"
"v_mul_f32 v86, s70, v86\n"
"v_mfma_f32_16x16x32_bf16 v[132:135], a[240:243], a[168:171], v[132:135]\n"
"ds_read_b64_tr_b16 a[236:237], v253 offset:4608\n"
"v_mul_f32 v87, s70, v87\n"
"v_mfma_f32_16x16x32_bf16 v[132:135], a[244:247], a[184:187], v[132:135]\n"
"ds_read_b64_tr_b16 a[238:239], v253 offset:4864\n"
"v_cvt_pk_bf16_f32 v82, v84, v85\n"
"v_mfma_f32_16x16x32_bf16 v[148:151], a[240:243], a[172:175], v[148:151]\n"
"v_cvt_pk_bf16_f32 v83, v86, v87\n"
"v_mul_f32 v88, s70, v88\n"
"v_mfma_f32_16x16x32_bf16 v[148:151], a[244:247], a[188:191], v[148:151]\n"
"s_waitcnt lgkmcnt(4)\n"
"v_permlane16_swap_b32 v80, v82\n"
"v_mfma_f32_16x16x32_bf16 v[96:99], a[248:251], a[192:195], v[96:99]\n"
"s_barrier\n"
"ds_read_b128 a[240:243], v234 offset:0\n"
"v_accvgpr_write_b32 a104, v80\n"
"v_mfma_f32_16x16x32_bf16 v[96:99], a[252:255], a[208:211], v[96:99]\n"
"v_permlane16_swap_b32 v81, v83\n"
"v_mfma_f32_16x16x32_bf16 v[108:111], a[248:251], a[196:199], v[108:111]\n"
"v_accvgpr_write_b32 a105, v81\n"
"v_accvgpr_write_b32 a106, v82\n"
"v_mfma_f32_16x16x32_bf16 v[108:111], a[252:255], a[212:215], v[108:111]\n"
"v_accvgpr_write_b32 a107, v83\n"
"v_mul_f32 v89, s70, v89\n"
"v_mul_f32 v90, s70, v90\n"
"v_mfma_f32_16x16x32_bf16 v[124:127], a[248:251], a[200:203], v[124:127]\n"
"ds_read_b128 a[244:247], v234 offset:512\n"
"v_mul_f32 v91, s70, v91\n"
"v_mfma_f32_16x16x32_bf16 v[124:127], a[252:255], a[216:219], v[124:127]\n"
"v_cvt_pk_bf16_f32 v88, v88, v89\n"
"v_cvt_pk_bf16_f32 v89, v90, v91\n"
"v_mfma_f32_16x16x32_bf16 v[140:143], a[248:251], a[204:207], v[140:143]\n"
"v_mul_f32 v92, s70, v92\n"
"v_mul_f32 v93, s70, v93\n"
"v_mfma_f32_16x16x32_bf16 v[140:143], a[252:255], a[220:223], v[140:143]\n"
"s_waitcnt lgkmcnt(2)\n"
"v_mul_f32 v94, s70, v94\n"
"v_mul_f32 v95, s70, v95\n"
"v_mfma_f32_16x16x32_bf16 v[100:103], a[232:235], a[192:195], v[100:103]\n"
"ds_read_b128 a[248:251], v234 offset:2048\n"
"v_cvt_pk_bf16_f32 v90, v92, v93\n"
"v_mfma_f32_16x16x32_bf16 v[100:103], a[236:239], a[208:211], v[100:103]\n"
"v_cvt_pk_bf16_f32 v91, v94, v95\n"
"v_mfma_f32_16x16x32_bf16 v[116:119], a[232:235], a[196:199], v[116:119]\n"
"v_permlane16_swap_b32 v88, v90\n"
"v_mfma_f32_16x16x32_bf16 v[116:119], a[236:239], a[212:215], v[116:119]\n"
"v_permlane16_swap_b32 v89, v91\n"
"v_mfma_f32_16x16x32_bf16 v[132:135], a[232:235], a[200:203], v[132:135]\n"
"ds_read_b128 a[252:255], v234 offset:2560\n"
"v_accvgpr_write_b32 a108, v88\n"
"v_mfma_f32_16x16x32_bf16 v[132:135], a[236:239], a[216:219], v[132:135]\n"
"v_and_b32 v255, 0xfff, v246\n"
"v_lshlrev_b32 v255, 3, v255\n"
"v_accvgpr_write_b32 a109, v89\n"
"v_mfma_f32_16x16x32_bf16 v[148:151], a[232:235], a[204:207], v[148:151]\n"
"v_accvgpr_write_b32 a110, v90\n"
"v_accvgpr_write_b32 a111, v91\n"
"v_mfma_f32_16x16x32_bf16 v[148:151], a[236:239], a[220:223], v[148:151]\n"
"s_waitcnt lgkmcnt(2)\n"
"s_branch L_JOIN\n"
"L_SLOW:\n"
"s_waitcnt vmcnt(0) lgkmcnt(0)\n"
"s_nop 15\n"
"v_add_u32 v184, s71, v233\n"
"v_add_u32 v185, s71, v237\n"
"v_add_u32 v186, s71, v238\n"
"s_add_u32 m0, s71, 114688\n"
"s_lshl_b32 s81, s60, 7\n"
"v_mov_b32 v152, 0xff800000\n"
"v_mov_b32 v156, 0xff800000\n"
"v_mov_b32 v160, 0xff800000\n"
"v_mov_b32 v164, 0xff800000\n"
"s_mov_b32 s82, 0\n"
"L_SP:\n"
"s_mov_b32 s78, 0\n"
"L_ST:\n"
"s_sub_u32 s83, s78, s81\n"
"v_add_u32 v187, s83, v241\n"
"buffer_load_dwordx4 v187, s[40:43], 0 offen offset:0  lds\n"
"buffer_load_dwordx4 v187, s[40:43], s61 offen offset:1024  lds\n"
"buffer_load_dwordx4 v187, s[40:43], s62 offen offset:2048  lds\n"
"buffer_load_dwordx4 v187, s[40:43], s63 offen offset:3072  lds\n"
"v_add_u32 v187, 128, v187\n"
"s_waitcnt vmcnt(0)\n"
"ds_read_b128 v[168:171], v184 offset:49152\n"
"ds_read_b128 v[172:175], v184 offset:51200\n"
"ds_read_b128 v[176:179], v184 offset:49664\n"
"ds_read_b128 v[180:183], v184 offset:51712\n"
"s_waitcnt lgkmcnt(0)\n"
"v_mfma_f32_16x16x32_bf16 v[200:203], v[168:171], a[0:3], 0\n"
"v_mfma_f32_16x16x32_bf16 v[208:211], v[168:171], a[4:7], 0\n"
"v_mfma_f32_16x16x32_bf16 v[216:219], v[168:171], a[8:11], 0\n"
"v_mfma_f32_16x16x32_bf16 v[224:227], v[168:171], a[12:15], 0\n"
"v_mfma_f32_16x16x32_bf16 v[204:207], v[172:175], a[0:3], 0\n"
"v_mfma_f32_16x16x32_bf16 v[212:215], v[172:175], a[4:7], 0\n"
"v_mfma_f32_16x16x32_bf16 v[220:223], v[172:175], a[8:11], 0\n"
"v_mfma_f32_16x16x32_bf16 v[228:231], v[172:175], a[12:15], 0\n"
"v_mfma_f32_16x16x32_bf16 v[200:203], v[176:179], a[16:19], v[200:203]\n"
"v_mfma_f32_16x16x32_bf16 v[208:211], v[176:179], a[20:23], v[208:211]\n"
"v_mfma_f32_16x16x32_bf16 v[216:219], v[176:179], a[24:27], v[216:219]\n"
"v_mfma_f32_16x16x32_bf16 v[224:227], v[176:179], a[28:31], v[224:227]\n"
"v_mfma_f32_16x16x32_bf16 v[204:207], v[180:183], a[16:19], v[204:207]\n"
"v_mfma_f32_16x16x32_bf16 v[212:215], v[180:183], a[20:23], v[212:215]\n"
"v_mfma_f32_16x16x32_bf16 v[220:223], v[180:183], a[24:27], v[220:223]\n"
"v_mfma_f32_16x16x32_bf16 v[228:231], v[180:183], a[28:31], v[228:231]\n"
"buffer_load_dwordx4 v187, s[40:43], 0 offen offset:0  lds\n"
"buffer_load_dwordx4 v187, s[40:43], s61 offen offset:1024  lds\n"
"buffer_load_dwordx4 v187, s[40:43], s62 offen offset:2048  lds\n"
"buffer_load_dwordx4 v187, s[40:43], s63 offen offset:3072  lds\n"
"v_add_u32 v187, 128, v187\n"
"s_waitcnt vmcnt(0)\n"
"ds_read_b128 v[168:171], v184 offset:49152\n"
"ds_read_b128 v[172:175], v184 offset:51200\n"
"ds_read_b128 v[176:179], v184 offset:49664\n"
"ds_read_b128 v[180:183], v184 offset:51712\n"
"s_waitcnt lgkmcnt(0)\n"
"v_mfma_f32_16x16x32_bf16 v[200:203], v[168:171], a[32:35], v[200:203]\n"
"v_mfma_f32_16x16x32_bf16 v[208:211], v[168:171], a[36:39], v[208:211]\n"
"v_mfma_f32_16x16x32_bf16 v[216:219], v[168:171], a[40:43], v[216:219]\n"
"v_mfma_f32_16x16x32_bf16 v[224:227], v[168:171], a[44:47], v[224:227]\n"
"v_mfma_f32_16x16x32_bf16 v[204:207], v[172:175], a[32:35], v[204:207]\n"
"v_mfma_f32_16x16x32_bf16 v[212:215], v[172:175], a[36:39], v[212:215]\n"
"v_mfma_f32_16x16x32_bf16 v[220:223], v[172:175], a[40:43], v[220:223]\n"
"v_mfma_f32_16x16x32_bf16 v[228:231], v[172:175], a[44:47], v[228:231]\n"
"v_mfma_f32_16x16x32_bf16 v[200:203], v[176:179], a[48:51], v[200:203]\n"
"v_mfma_f32_16x16x32_bf16 v[208:211], v[176:179], a[52:55], v[208:211]\n"
"v_mfma_f32_16x16x32_bf16 v[216:219], v[176:179], a[56:59], v[216:219]\n"
"v_mfma_f32_16x16x32_bf16 v[224:227], v[176:179], a[60:63], v[224:227]\n"
"v_mfma_f32_16x16x32_bf16 v[204:207], v[180:183], a[48:51], v[204:207]\n"
"v_mfma_f32_16x16x32_bf16 v[212:215], v[180:183], a[52:55], v[212:215]\n"
"v_mfma_f32_16x16x32_bf16 v[220:223], v[180:183], a[56:59], v[220:223]\n"
"v_mfma_f32_16x16x32_bf16 v[228:231], v[180:183], a[60:63], v[228:231]\n"
"buffer_load_dwordx4 v187, s[40:43], 0 offen offset:0  lds\n"
"buffer_load_dwordx4 v187, s[40:43], s61 offen offset:1024  lds\n"
"buffer_load_dwordx4 v187, s[40:43], s62 offen offset:2048  lds\n"
"buffer_load_dwordx4 v187, s[40:43], s63 offen offset:3072  lds\n"
"v_add_u32 v187, 128, v187\n"
"s_waitcnt vmcnt(0)\n"
"ds_read_b128 v[168:171], v184 offset:49152\n"
"ds_read_b128 v[172:175], v184 offset:51200\n"
"ds_read_b128 v[176:179], v184 offset:49664\n"
"ds_read_b128 v[180:183], v184 offset:51712\n"
"s_waitcnt lgkmcnt(0)\n"
"v_mfma_f32_16x16x32_bf16 v[200:203], v[168:171], a[64:67], v[200:203]\n"
"v_mfma_f32_16x16x32_bf16 v[208:211], v[168:171], a[68:71], v[208:211]\n"
"v_mfma_f32_16x16x32_bf16 v[216:219], v[168:171], a[72:75], v[216:219]\n"
"v_mfma_f32_16x16x32_bf16 v[224:227], v[168:171], a[76:79], v[224:227]\n"
"v_mfma_f32_16x16x32_bf16 v[204:207], v[172:175], a[64:67], v[204:207]\n"
"v_mfma_f32_16x16x32_bf16 v[212:215], v[172:175], a[68:71], v[212:215]\n"
"v_mfma_f32_16x16x32_bf16 v[220:223], v[172:175], a[72:75], v[220:223]\n"
"v_mfma_f32_16x16x32_bf16 v[228:231], v[172:175], a[76:79], v[228:231]\n"
"v_mfma_f32_16x16x32_bf16 v[200:203], v[176:179], a[80:83], v[200:203]\n"
"v_mfma_f32_16x16x32_bf16 v[208:211], v[176:179], a[84:87], v[208:211]\n"
"v_mfma_f32_16x16x32_bf16 v[216:219], v[176:179], a[88:91], v[216:219]\n"
"v_mfma_f32_16x16x32_bf16 v[224:227], v[176:179], a[92:95], v[224:227]\n"
"v_mfma_f32_16x16x32_bf16 v[204:207], v[180:183], a[80:83], v[204:207]\n"
"v_mfma_f32_16x16x32_bf16 v[212:215], v[180:183], a[84:87], v[212:215]\n"
"v_mfma_f32_16x16x32_bf16 v[220:223], v[180:183], a[88:91], v[220:223]\n"
"v_mfma_f32_16x16x32_bf16 v[228:231], v[180:183], a[92:95], v[228:231]\n"
"buffer_load_dwordx4 v187, s[40:43], 0 offen offset:0  lds\n"
"buffer_load_dwordx4 v187, s[40:43], s61 offen offset:1024  lds\n"
"buffer_load_dwordx4 v187, s[40:43], s62 offen offset:2048  lds\n"
"buffer_load_dwordx4 v187, s[40:43], s63 offen offset:3072  lds\n"
"v_add_u32 v187, 128, v187\n"
"s_waitcnt vmcnt(0)\n"
"ds_read_b128 v[168:171], v184 offset:49152\n"
"ds_read_b128 v[172:175], v184 offset:51200\n"
"ds_read_b128 v[176:179], v184 offset:49664\n"
"ds_read_b128 v[180:183], v184 offset:51712\n"
"s_waitcnt lgkmcnt(0)\n"
"v_mfma_f32_16x16x32_bf16 v[200:203], v[168:171], a[96:99], v[200:203]\n"
"v_mfma_f32_16x16x32_bf16 v[208:211], v[168:171], a[100:103], v[208:211]\n"
"v_mfma_f32_16x16x32_bf16 v[216:219], v[168:171], a[104:107], v[216:219]\n"
"v_mfma_f32_16x16x32_bf16 v[224:227], v[168:171], a[108:111], v[224:227]\n"
"v_mfma_f32_16x16x32_bf16 v[204:207], v[172:175], a[96:99], v[204:207]\n"
"v_mfma_f32_16x16x32_bf16 v[212:215], v[172:175], a[100:103], v[212:215]\n"
"v_mfma_f32_16x16x32_bf16 v[220:223], v[172:175], a[104:107], v[220:223]\n"
"v_mfma_f32_16x16x32_bf16 v[228:231], v[172:175], a[108:111], v[228:231]\n"
"v_mfma_f32_16x16x32_bf16 v[200:203], v[176:179], a[112:115], v[200:203]\n"
"v_mfma_f32_16x16x32_bf16 v[208:211], v[176:179], a[116:119], v[208:211]\n"
"v_mfma_f32_16x16x32_bf16 v[216:219], v[176:179], a[120:123], v[216:219]\n"
"v_mfma_f32_16x16x32_bf16 v[224:227], v[176:179], a[124:127], v[224:227]\n"
"v_mfma_f32_16x16x32_bf16 v[204:207], v[180:183], a[112:115], v[204:207]\n"
"v_mfma_f32_16x16x32_bf16 v[212:215], v[180:183], a[116:119], v[212:215]\n"
"v_mfma_f32_16x16x32_bf16 v[220:223], v[180:183], a[120:123], v[220:223]\n"
"v_mfma_f32_16x16x32_bf16 v[228:231], v[180:183], a[124:127], v[228:231]\n"
"s_nop 15\n"
"s_cmp_lg_u32 s82, 0\n"
"s_cbranch_scc1 L_S2\n"
"v_max3_f32 v152, v152, v200, v201\n"
"v_max3_f32 v152, v152, v202, v203\n"
"v_max3_f32 v152, v152, v204, v205\n"
"v_max3_f32 v152, v152, v206, v207\n"
"v_max3_f32 v156, v156, v208, v209\n"
"v_max3_f32 v156, v156, v210, v211\n"
"v_max3_f32 v156, v156, v212, v213\n"
"v_max3_f32 v156, v156, v214, v215\n"
"v_max3_f32 v160, v160, v216, v217\n"
"v_max3_f32 v160, v160, v218, v219\n"
"v_max3_f32 v160, v160, v220, v221\n"
"v_max3_f32 v160, v160, v222, v223\n"
"v_max3_f32 v164, v164, v224, v225\n"
"v_max3_f32 v164, v164, v226, v227\n"
"v_max3_f32 v164, v164, v228, v229\n"
"v_max3_f32 v164, v164, v230, v231\n"
"s_add_u32 s78, s78, 16384\n"
"s_cmp_lt_u32 s78, 262144\n"
"s_cbranch_scc1 L_ST\n"
"s_nop 1\n"
"v_permlane32_swap_b32 v152, v156\n"
"v_permlane32_swap_b32 v160, v164\n"
"v_max_f32 v152, v152, v156\n"
"v_max_f32 v160, v160, v164\n"
"s_nop 1\n"
"v_permlane16_swap_b32 v152, v160\n"
"v_min_f32 v152, -v152, -v160\n"
"v_mov_b32 v160, v152\n"
"s_nop 1\n"
"v_permlane16_swap_b32 v152, v160\n"
"v_mov_b32 v156, v152\n"
"v_mov_b32 v164, v160\n"
"s_nop 0\n"
"v_permlane32_swap_b32 v152, v156\n"
"v_permlane32_swap_b32 v160, v164\n"
"v_mov_b32 v244, 0\n"
"v_mov_b32 v245, 0\n"
"v_mov_b32 v246, 0\n"
"v_mov_b32 v247, 0\n"
"v_mov_b32 v0, 0\n"
"v_mov_b32 v1, 0\n"
"v_mov_b32 v2, 0\n"
"v_mov_b32 v3, 0\n"
"v_mov_b32 v4, 0\n"
"v_mov_b32 v5, 0\n"
"v_mov_b32 v6, 0\n"
"v_mov_b32 v7, 0\n"
"v_mov_b32 v8, 0\n"
"v_mov_b32 v9, 0\n"
"v_mov_b32 v10, 0\n"
"v_mov_b32 v11, 0\n"
"v_mov_b32 v12, 0\n"
"v_mov_b32 v13, 0\n"
"v_mov_b32 v14, 0\n"
"v_mov_b32 v15, 0\n"
"v_mov_b32 v16, 0\n"
"v_mov_b32 v17, 0\n"
"v_mov_b32 v18, 0\n"
"v_mov_b32 v19, 0\n"
"v_mov_b32 v20, 0\n"
"v_mov_b32 v21, 0\n"
"v_mov_b32 v22, 0\n"
"v_mov_b32 v23, 0\n"
"v_mov_b32 v24, 0\n"
"v_mov_b32 v25, 0\n"
"v_mov_b32 v26, 0\n"
"v_mov_b32 v27, 0\n"
"v_mov_b32 v28, 0\n"
"v_mov_b32 v29, 0\n"
"v_mov_b32 v30, 0\n"
"v_mov_b32 v31, 0\n"
"v_mov_b32 v32, 0\n"
"v_mov_b32 v33, 0\n"
"v_mov_b32 v34, 0\n"
"v_mov_b32 v35, 0\n"
"v_mov_b32 v36, 0\n"
"v_mov_b32 v37, 0\n"
"v_mov_b32 v38, 0\n"
"v_mov_b32 v39, 0\n"
"v_mov_b32 v40, 0\n"
"v_mov_b32 v41, 0\n"
"v_mov_b32 v42, 0\n"
"v_mov_b32 v43, 0\n"
"v_mov_b32 v44, 0\n"
"v_mov_b32 v45, 0\n"
"v_mov_b32 v46, 0\n"
"v_mov_b32 v47, 0\n"
"v_mov_b32 v48, 0\n"
"v_mov_b32 v49, 0\n"
"v_mov_b32 v50, 0\n"
"v_mov_b32 v51, 0\n"
"v_mov_b32 v52, 0\n"
"v_mov_b32 v53, 0\n"
"v_mov_b32 v54, 0\n"
"v_mov_b32 v55, 0\n"
"v_mov_b32 v56, 0\n"
"v_mov_b32 v57, 0\n"
"v_mov_b32 v58, 0\n"
"v_mov_b32 v59, 0\n"
"v_mov_b32 v60, 0\n"
"v_mov_b32 v61, 0\n"
"v_mov_b32 v62, 0\n"
"v_mov_b32 v63, 0\n"
"v_mov_b32 v64, 0\n"
"v_mov_b32 v65, 0\n"
"v_mov_b32 v66, 0\n"
"v_mov_b32 v67, 0\n"
"v_mov_b32 v68, 0\n"
"v_mov_b32 v69, 0\n"
"v_mov_b32 v70, 0\n"
"v_mov_b32 v71, 0\n"
"v_mov_b32 v72, 0\n"
"v_mov_b32 v73, 0\n"
"v_mov_b32 v74, 0\n"
"v_mov_b32 v75, 0\n"
"v_mov_b32 v76, 0\n"
"v_mov_b32 v77, 0\n"
"v_mov_b32 v78, 0\n"
"v_mov_b32 v79, 0\n"
"v_mov_b32 v80, 0\n"
"v_mov_b32 v81, 0\n"
"v_mov_b32 v82, 0\n"
"v_mov_b32 v83, 0\n"
"v_mov_b32 v84, 0\n"
"v_mov_b32 v85, 0\n"
"v_mov_b32 v86, 0\n"
"v_mov_b32 v87, 0\n"
"v_mov_b32 v88, 0\n"
"v_mov_b32 v89, 0\n"
"v_mov_b32 v90, 0\n"
"v_mov_b32 v91, 0\n"
"v_mov_b32 v92, 0\n"
"v_mov_b32 v93, 0\n"
"v_mov_b32 v94, 0\n"
"v_mov_b32 v95, 0\n"
"v_mov_b32 v96, 0\n"
"v_mov_b32 v97, 0\n"
"v_mov_b32 v98, 0\n"
"v_mov_b32 v99, 0\n"
"v_mov_b32 v100, 0\n"
"v_mov_b32 v101, 0\n"
"v_mov_b32 v102, 0\n"
"v_mov_b32 v103, 0\n"
"v_mov_b32 v104, 0\n"
"v_mov_b32 v105, 0\n"
"v_mov_b32 v106, 0\n"
"v_mov_b32 v107, 0\n"
"v_mov_b32 v108, 0\n"
"v_mov_b32 v109, 0\n"
"v_mov_b32 v110, 0\n"
"v_mov_b32 v111, 0\n"
"v_mov_b32 v112, 0\n"
"v_mov_b32 v113, 0\n"
"v_mov_b32 v114, 0\n"
"v_mov_b32 v115, 0\n"
"v_mov_b32 v116, 0\n"
"v_mov_b32 v117, 0\n"
"v_mov_b32 v118, 0\n"
"v_mov_b32 v119, 0\n"
"v_mov_b32 v120, 0\n"
"v_mov_b32 v121, 0\n"
"v_mov_b32 v122, 0\n"
"v_mov_b32 v123, 0\n"
"v_mov_b32 v124, 0\n"
"v_mov_b32 v125, 0\n"
"v_mov_b32 v126, 0\n"
"v_mov_b32 v127, 0\n"
"v_mov_b32 v128, 0\n"
"v_mov_b32 v129, 0\n"
"v_mov_b32 v130, 0\n"
"v_mov_b32 v131, 0\n"
"v_mov_b32 v132, 0\n"
"v_mov_b32 v133, 0\n"
"v_mov_b32 v134, 0\n"
"v_mov_b32 v135, 0\n"
"v_mov_b32 v136, 0\n"
"v_mov_b32 v137, 0\n"
"v_mov_b32 v138, 0\n"
"v_mov_b32 v139, 0\n"
"v_mov_b32 v140, 0\n"
"v_mov_b32 v141, 0\n"
"v_mov_b32 v142, 0\n"
"v_mov_b32 v143, 0\n"
"v_mov_b32 v144, 0\n"
"v_mov_b32 v145, 0\n"
"v_mov_b32 v146, 0\n"
"v_mov_b32 v147, 0\n"
"v_mov_b32 v148, 0\n"
"v_mov_b32 v149, 0\n"
"v_mov_b32 v150, 0\n"
"v_mov_b32 v151, 0\n"
"v_accvgpr_write_b32 a128, 0\n"
"v_accvgpr_write_b32 a129, 0\n"
"v_accvgpr_write_b32 a130, 0\n"
"v_accvgpr_write_b32 a131, 0\n"
"v_accvgpr_write_b32 a132, 0\n"
"v_accvgpr_write_b32 a133, 0\n"
"v_accvgpr_write_b32 a134, 0\n"
"v_accvgpr_write_b32 a135, 0\n"
"v_accvgpr_write_b32 a136, 0\n"
"v_accvgpr_write_b32 a137, 0\n"
"v_accvgpr_write_b32 a138, 0\n"
"v_accvgpr_write_b32 a139, 0\n"
"v_accvgpr_write_b32 a140, 0\n"
"v_accvgpr_write_b32 a141, 0\n"
"v_accvgpr_write_b32 a142, 0\n"
"v_accvgpr_write_b32 a143, 0\n"
"v_accvgpr_write_b32 a144, 0\n"
"v_accvgpr_write_b32 a145, 0\n"
"v_accvgpr_write_b32 a146, 0\n"
"v_accvgpr_write_b32 a147, 0\n"
"v_accvgpr_write_b32 a148, 0\n"
"v_accvgpr_write_b32 a149, 0\n"
"v_accvgpr_write_b32 a150, 0\n"
"v_accvgpr_write_b32 a151, 0\n"
"v_accvgpr_write_b32 a152, 0\n"
"v_accvgpr_write_b32 a153, 0\n"
"v_accvgpr_write_b32 a154, 0\n"
"v_accvgpr_write_b32 a155, 0\n"
"v_accvgpr_write_b32 a156, 0\n"
"v_accvgpr_write_b32 a157, 0\n"
"v_accvgpr_write_b32 a158, 0\n"
"v_accvgpr_write_b32 a159, 0\n"
"v_accvgpr_write_b32 a160, 0\n"
"v_accvgpr_write_b32 a161, 0\n"
"v_accvgpr_write_b32 a162, 0\n"
"v_accvgpr_write_b32 a163, 0\n"
"v_accvgpr_write_b32 a164, 0\n"
"v_accvgpr_write_b32 a165, 0\n"
"v_accvgpr_write_b32 a166, 0\n"
"v_accvgpr_write_b32 a167, 0\n"
"v_accvgpr_write_b32 a168, 0\n"
"v_accvgpr_write_b32 a169, 0\n"
"v_accvgpr_write_b32 a170, 0\n"
"v_accvgpr_write_b32 a171, 0\n"
"v_accvgpr_write_b32 a172, 0\n"
"v_accvgpr_write_b32 a173, 0\n"
"v_accvgpr_write_b32 a174, 0\n"
"v_accvgpr_write_b32 a175, 0\n"
"v_accvgpr_write_b32 a176, 0\n"
"v_accvgpr_write_b32 a177, 0\n"
"v_accvgpr_write_b32 a178, 0\n"
"v_accvgpr_write_b32 a179, 0\n"
"v_accvgpr_write_b32 a180, 0\n"
"v_accvgpr_write_b32 a181, 0\n"
"v_accvgpr_write_b32 a182, 0\n"
"v_accvgpr_write_b32 a183, 0\n"
"v_accvgpr_write_b32 a184, 0\n"
"v_accvgpr_write_b32 a185, 0\n"
"v_accvgpr_write_b32 a186, 0\n"
"v_accvgpr_write_b32 a187, 0\n"
"v_accvgpr_write_b32 a188, 0\n"
"v_accvgpr_write_b32 a189, 0\n"
"v_accvgpr_write_b32 a190, 0\n"
"v_accvgpr_write_b32 a191, 0\n"
"v_accvgpr_write_b32 a192, 0\n"
"v_accvgpr_write_b32 a193, 0\n"
"v_accvgpr_write_b32 a194, 0\n"
"v_accvgpr_write_b32 a195, 0\n"
"v_accvgpr_write_b32 a196, 0\n"
"v_accvgpr_write_b32 a197, 0\n"
"v_accvgpr_write_b32 a198, 0\n"
"v_accvgpr_write_b32 a199, 0\n"
"v_accvgpr_write_b32 a200, 0\n"
"v_accvgpr_write_b32 a201, 0\n"
"v_accvgpr_write_b32 a202, 0\n"
"v_accvgpr_write_b32 a203, 0\n"
"v_accvgpr_write_b32 a204, 0\n"
"v_accvgpr_write_b32 a205, 0\n"
"v_accvgpr_write_b32 a206, 0\n"
"v_accvgpr_write_b32 a207, 0\n"
"v_accvgpr_write_b32 a208, 0\n"
"v_accvgpr_write_b32 a209, 0\n"
"v_accvgpr_write_b32 a210, 0\n"
"v_accvgpr_write_b32 a211, 0\n"
"v_accvgpr_write_b32 a212, 0\n"
"v_accvgpr_write_b32 a213, 0\n"
"v_accvgpr_write_b32 a214, 0\n"
"v_accvgpr_write_b32 a215, 0\n"
"v_accvgpr_write_b32 a216, 0\n"
"v_accvgpr_write_b32 a217, 0\n"
"v_accvgpr_write_b32 a218, 0\n"
"v_accvgpr_write_b32 a219, 0\n"
"v_accvgpr_write_b32 a220, 0\n"
"v_accvgpr_write_b32 a221, 0\n"
"v_accvgpr_write_b32 a222, 0\n"
"v_accvgpr_write_b32 a223, 0\n"
"v_accvgpr_write_b32 a224, 0\n"
"v_accvgpr_write_b32 a225, 0\n"
"v_accvgpr_write_b32 a226, 0\n"
"v_accvgpr_write_b32 a227, 0\n"
"v_accvgpr_write_b32 a228, 0\n"
"v_accvgpr_write_b32 a229, 0\n"
"v_accvgpr_write_b32 a230, 0\n"
"v_accvgpr_write_b32 a231, 0\n"
"s_mov_b32 s82, 1\n"
"s_branch L_SP\n"
"L_S2:\n"
"v_add_f32 v200, v200, v152\n"
"v_add_f32 v201, v201, v152\n"
"v_add_f32 v202, v202, v152\n"
"v_add_f32 v203, v203, v152\n"
"v_add_f32 v204, v204, v152\n"
"v_add_f32 v205, v205, v152\n"
"v_add_f32 v206, v206, v152\n"
"v_add_f32 v207, v207, v152\n"
"v_exp_f32 v200, v200\n"
"v_exp_f32 v201, v201\n"
"v_exp_f32 v202, v202\n"
"v_exp_f32 v203, v203\n"
"v_exp_f32 v204, v204\n"
"v_exp_f32 v205, v205\n"
"v_exp_f32 v206, v206\n"
"v_exp_f32 v207, v207\n"
"s_nop 0\n"
"v_add_f32 v244, v244, v200\n"
"v_add_f32 v244, v244, v201\n"
"v_add_f32 v244, v244, v202\n"
"v_add_f32 v244, v244, v203\n"
"v_add_f32 v244, v244, v204\n"
"v_add_f32 v244, v244, v205\n"
"v_add_f32 v244, v244, v206\n"
"v_add_f32 v244, v244, v207\n"
"v_cvt_pk_bf16_f32 v200, v200, v201\n"
"v_cvt_pk_bf16_f32 v201, v202, v203\n"
"v_cvt_pk_bf16_f32 v202, v204, v205\n"
"v_cvt_pk_bf16_f32 v203, v206, v207\n"
"v_add_f32 v208, v208, v156\n"
"v_add_f32 v209, v209, v156\n"
"v_add_f32 v210, v210, v156\n"
"v_add_f32 v211, v211, v156\n"
"v_add_f32 v212, v212, v156\n"
"v_add_f32 v213, v213, v156\n"
"v_add_f32 v214, v214, v156\n"
"v_add_f32 v215, v215, v156\n"
"v_exp_f32 v208, v208\n"
"v_exp_f32 v209, v209\n"
"v_exp_f32 v210, v210\n"
"v_exp_f32 v211, v211\n"
"v_exp_f32 v212, v212\n"
"v_exp_f32 v213, v213\n"
"v_exp_f32 v214, v214\n"
"v_exp_f32 v215, v215\n"
"s_nop 0\n"
"v_add_f32 v245, v245, v208\n"
"v_add_f32 v245, v245, v209\n"
"v_add_f32 v245, v245, v210\n"
"v_add_f32 v245, v245, v211\n"
"v_add_f32 v245, v245, v212\n"
"v_add_f32 v245, v245, v213\n"
"v_add_f32 v245, v245, v214\n"
"v_add_f32 v245, v245, v215\n"
"v_cvt_pk_bf16_f32 v208, v208, v209\n"
"v_cvt_pk_bf16_f32 v209, v210, v211\n"
"v_cvt_pk_bf16_f32 v210, v212, v213\n"
"v_cvt_pk_bf16_f32 v211, v214, v215\n"
"v_add_f32 v216, v216, v160\n"
"v_add_f32 v217, v217, v160\n"
"v_add_f32 v218, v218, v160\n"
"v_add_f32 v219, v219, v160\n"
"v_add_f32 v220, v220, v160\n"
"v_add_f32 v221, v221, v160\n"
"v_add_f32 v222, v222, v160\n"
"v_add_f32 v223, v223, v160\n"
"v_exp_f32 v216, v216\n"
"v_exp_f32 v217, v217\n"
"v_exp_f32 v218, v218\n"
"v_exp_f32 v219, v219\n"
"v_exp_f32 v220, v220\n"
"v_exp_f32 v221, v221\n"
"v_exp_f32 v222, v222\n"
"v_exp_f32 v223, v223\n"
"s_nop 0\n"
"v_add_f32 v246, v246, v216\n"
"v_add_f32 v246, v246, v217\n"
"v_add_f32 v246, v246, v218\n"
"v_add_f32 v246, v246, v219\n"
"v_add_f32 v246, v246, v220\n"
"v_add_f32 v246, v246, v221\n"
"v_add_f32 v246, v246, v222\n"
"v_add_f32 v246, v246, v223\n"
"v_cvt_pk_bf16_f32 v216, v216, v217\n"
"v_cvt_pk_bf16_f32 v217, v218, v219\n"
"v_cvt_pk_bf16_f32 v218, v220, v221\n"
"v_cvt_pk_bf16_f32 v219, v222, v223\n"
"v_add_f32 v224, v224, v164\n"
"v_add_f32 v225, v225, v164\n"
"v_add_f32 v226, v226, v164\n"
"v_add_f32 v227, v227, v164\n"
"v_add_f32 v228, v228, v164\n"
"v_add_f32 v229, v229, v164\n"
"v_add_f32 v230, v230, v164\n"
"v_add_f32 v231, v231, v164\n"
"v_exp_f32 v224, v224\n"
"v_exp_f32 v225, v225\n"
"v_exp_f32 v226, v226\n"
"v_exp_f32 v227, v227\n"
"v_exp_f32 v228, v228\n"
"v_exp_f32 v229, v229\n"
"v_exp_f32 v230, v230\n"
"v_exp_f32 v231, v231\n"
"s_nop 0\n"
"v_add_f32 v247, v247, v224\n"
"v_add_f32 v247, v247, v225\n"
"v_add_f32 v247, v247, v226\n"
"v_add_f32 v247, v247, v227\n"
"v_add_f32 v247, v247, v228\n"
"v_add_f32 v247, v247, v229\n"
"v_add_f32 v247, v247, v230\n"
"v_add_f32 v247, v247, v231\n"
"v_cvt_pk_bf16_f32 v224, v224, v225\n"
"v_cvt_pk_bf16_f32 v225, v226, v227\n"
"v_cvt_pk_bf16_f32 v226, v228, v229\n"
"v_cvt_pk_bf16_f32 v227, v230, v231\n"
"s_cmp_eq_u32 s78, 245760\n"
"s_nop 3\n"
"s_cbranch_scc1 L_BACK\n"
"v_add_u32 v187, s83, v241\n"
"buffer_load_dwordx4 v187, s[44:47], 0 offen offset:0  lds\n"
"buffer_load_dwordx4 v187, s[44:47], s61 offen offset:1024  lds\n"
"buffer_load_dwordx4 v187, s[44:47], s62 offen offset:2048  lds\n"
"buffer_load_dwordx4 v187, s[44:47], s63 offen offset:3072  lds\n"
"v_add_u32 v187, 128, v187\n"
"s_waitcnt vmcnt(0)\n"
"ds_read_b64_tr_b16 v[168:169], v185 offset:49152\n"
"ds_read_b64_tr_b16 v[170:171], v185 offset:51200\n"
"ds_read_b64_tr_b16 v[172:173], v186 offset:49152\n"
"ds_read_b64_tr_b16 v[174:175], v186 offset:51200\n"
"ds_read_b64_tr_b16 v[176:177], v185 offset:49664\n"
"ds_read_b64_tr_b16 v[178:179], v185 offset:51712\n"
"ds_read_b64_tr_b16 v[180:181], v186 offset:49664\n"
"ds_read_b64_tr_b16 v[182:183], v186 offset:51712\n"
"s_waitcnt lgkmcnt(0)\n"
"v_mfma_f32_16x16x32_bf16 v[0:3], v[168:171], v[200:203], v[0:3]\n"
"v_mfma_f32_16x16x32_bf16 v[8:11], v[168:171], v[208:211], v[8:11]\n"
"v_mfma_f32_16x16x32_bf16 v[16:19], v[168:171], v[216:219], v[16:19]\n"
"v_mfma_f32_16x16x32_bf16 v[24:27], v[168:171], v[224:227], v[24:27]\n"
"v_mfma_f32_16x16x32_bf16 v[4:7], v[172:175], v[200:203], v[4:7]\n"
"v_mfma_f32_16x16x32_bf16 v[12:15], v[172:175], v[208:211], v[12:15]\n"
"v_mfma_f32_16x16x32_bf16 v[20:23], v[172:175], v[216:219], v[20:23]\n"
"v_mfma_f32_16x16x32_bf16 v[28:31], v[172:175], v[224:227], v[28:31]\n"
"v_mfma_f32_16x16x32_bf16 v[32:35], v[176:179], v[200:203], v[32:35]\n"
"v_mfma_f32_16x16x32_bf16 v[40:43], v[176:179], v[208:211], v[40:43]\n"
"v_mfma_f32_16x16x32_bf16 v[48:51], v[176:179], v[216:219], v[48:51]\n"
"v_mfma_f32_16x16x32_bf16 v[56:59], v[176:179], v[224:227], v[56:59]\n"
"v_mfma_f32_16x16x32_bf16 v[36:39], v[180:183], v[200:203], v[36:39]\n"
"v_mfma_f32_16x16x32_bf16 v[44:47], v[180:183], v[208:211], v[44:47]\n"
"v_mfma_f32_16x16x32_bf16 v[52:55], v[180:183], v[216:219], v[52:55]\n"
"v_mfma_f32_16x16x32_bf16 v[60:63], v[180:183], v[224:227], v[60:63]\n"
"buffer_load_dwordx4 v187, s[44:47], 0 offen offset:0  lds\n"
"buffer_load_dwordx4 v187, s[44:47], s61 offen offset:1024  lds\n"
"buffer_load_dwordx4 v187, s[44:47], s62 offen offset:2048  lds\n"
"buffer_load_dwordx4 v187, s[44:47], s63 offen offset:3072  lds\n"
"v_add_u32 v187, 128, v187\n"
"s_waitcnt vmcnt(0)\n"
"ds_read_b64_tr_b16 v[168:169], v185 offset:49152\n"
"ds_read_b64_tr_b16 v[170:171], v185 offset:51200\n"
"ds_read_b64_tr_b16 v[172:173], v186 offset:49152\n"
"ds_read_b64_tr_b16 v[174:175], v186 offset:51200\n"
"ds_read_b64_tr_b16 v[176:177], v185 offset:49664\n"
"ds_read_b64_tr_b16 v[178:179], v185 offset:51712\n"
"ds_read_b64_tr_b16 v[180:181], v186 offset:49664\n"
"ds_read_b64_tr_b16 v[182:183], v186 offset:51712\n"
"s_waitcnt lgkmcnt(0)\n"
"v_mfma_f32_16x16x32_bf16 v[64:67], v[168:171], v[200:203], v[64:67]\n"
"v_mfma_f32_16x16x32_bf16 v[72:75], v[168:171], v[208:211], v[72:75]\n"
"v_mfma_f32_16x16x32_bf16 v[80:83], v[168:171], v[216:219], v[80:83]\n"
"v_mfma_f32_16x16x32_bf16 v[88:91], v[168:171], v[224:227], v[88:91]\n"
"v_mfma_f32_16x16x32_bf16 v[68:71], v[172:175], v[200:203], v[68:71]\n"
"v_mfma_f32_16x16x32_bf16 v[76:79], v[172:175], v[208:211], v[76:79]\n"
"v_mfma_f32_16x16x32_bf16 v[84:87], v[172:175], v[216:219], v[84:87]\n"
"v_mfma_f32_16x16x32_bf16 v[92:95], v[172:175], v[224:227], v[92:95]\n"
"v_mfma_f32_16x16x32_bf16 v[96:99], v[176:179], v[200:203], v[96:99]\n"
"v_mfma_f32_16x16x32_bf16 v[104:107], v[176:179], v[208:211], v[104:107]\n"
"v_mfma_f32_16x16x32_bf16 v[112:115], v[176:179], v[216:219], v[112:115]\n"
"v_mfma_f32_16x16x32_bf16 v[120:123], v[176:179], v[224:227], v[120:123]\n"
"v_mfma_f32_16x16x32_bf16 v[100:103], v[180:183], v[200:203], v[100:103]\n"
"v_mfma_f32_16x16x32_bf16 v[108:111], v[180:183], v[208:211], v[108:111]\n"
"v_mfma_f32_16x16x32_bf16 v[116:119], v[180:183], v[216:219], v[116:119]\n"
"v_mfma_f32_16x16x32_bf16 v[124:127], v[180:183], v[224:227], v[124:127]\n"
"buffer_load_dwordx4 v187, s[44:47], 0 offen offset:0  lds\n"
"buffer_load_dwordx4 v187, s[44:47], s61 offen offset:1024  lds\n"
"buffer_load_dwordx4 v187, s[44:47], s62 offen offset:2048  lds\n"
"buffer_load_dwordx4 v187, s[44:47], s63 offen offset:3072  lds\n"
"v_add_u32 v187, 128, v187\n"
"s_waitcnt vmcnt(0)\n"
"ds_read_b64_tr_b16 v[168:169], v185 offset:49152\n"
"ds_read_b64_tr_b16 v[170:171], v185 offset:51200\n"
"ds_read_b64_tr_b16 v[172:173], v186 offset:49152\n"
"ds_read_b64_tr_b16 v[174:175], v186 offset:51200\n"
"ds_read_b64_tr_b16 v[176:177], v185 offset:49664\n"
"ds_read_b64_tr_b16 v[178:179], v185 offset:51712\n"
"ds_read_b64_tr_b16 v[180:181], v186 offset:49664\n"
"ds_read_b64_tr_b16 v[182:183], v186 offset:51712\n"
"s_waitcnt lgkmcnt(0)\n"
"v_mfma_f32_16x16x32_bf16 v[128:131], v[168:171], v[200:203], v[128:131]\n"
"v_mfma_f32_16x16x32_bf16 v[136:139], v[168:171], v[208:211], v[136:139]\n"
"v_mfma_f32_16x16x32_bf16 v[144:147], v[168:171], v[216:219], v[144:147]\n"
"v_mfma_f32_16x16x32_bf16 a[128:131], v[168:171], v[224:227], a[128:131]\n"
"v_mfma_f32_16x16x32_bf16 v[132:135], v[172:175], v[200:203], v[132:135]\n"
"v_mfma_f32_16x16x32_bf16 v[140:143], v[172:175], v[208:211], v[140:143]\n"
"v_mfma_f32_16x16x32_bf16 v[148:151], v[172:175], v[216:219], v[148:151]\n"
"v_mfma_f32_16x16x32_bf16 a[132:135], v[172:175], v[224:227], a[132:135]\n"
"v_mfma_f32_16x16x32_bf16 a[136:139], v[176:179], v[200:203], a[136:139]\n"
"v_mfma_f32_16x16x32_bf16 a[144:147], v[176:179], v[208:211], a[144:147]\n"
"v_mfma_f32_16x16x32_bf16 a[152:155], v[176:179], v[216:219], a[152:155]\n"
"v_mfma_f32_16x16x32_bf16 a[160:163], v[176:179], v[224:227], a[160:163]\n"
"v_mfma_f32_16x16x32_bf16 a[140:143], v[180:183], v[200:203], a[140:143]\n"
"v_mfma_f32_16x16x32_bf16 a[148:151], v[180:183], v[208:211], a[148:151]\n"
"v_mfma_f32_16x16x32_bf16 a[156:159], v[180:183], v[216:219], a[156:159]\n"
"v_mfma_f32_16x16x32_bf16 a[164:167], v[180:183], v[224:227], a[164:167]\n"
"buffer_load_dwordx4 v187, s[44:47], 0 offen offset:0  lds\n"
"buffer_load_dwordx4 v187, s[44:47], s61 offen offset:1024  lds\n"
"buffer_load_dwordx4 v187, s[44:47], s62 offen offset:2048  lds\n"
"buffer_load_dwordx4 v187, s[44:47], s63 offen offset:3072  lds\n"
"v_add_u32 v187, 128, v187\n"
"s_waitcnt vmcnt(0)\n"
"ds_read_b64_tr_b16 v[168:169], v185 offset:49152\n"
"ds_read_b64_tr_b16 v[170:171], v185 offset:51200\n"
"ds_read_b64_tr_b16 v[172:173], v186 offset:49152\n"
"ds_read_b64_tr_b16 v[174:175], v186 offset:51200\n"
"ds_read_b64_tr_b16 v[176:177], v185 offset:49664\n"
"ds_read_b64_tr_b16 v[178:179], v185 offset:51712\n"
"ds_read_b64_tr_b16 v[180:181], v186 offset:49664\n"
"ds_read_b64_tr_b16 v[182:183], v186 offset:51712\n"
"s_waitcnt lgkmcnt(0)\n"
"v_mfma_f32_16x16x32_bf16 a[168:171], v[168:171], v[200:203], a[168:171]\n"
"v_mfma_f32_16x16x32_bf16 a[176:179], v[168:171], v[208:211], a[176:179]\n"
"v_mfma_f32_16x16x32_bf16 a[184:187], v[168:171], v[216:219], a[184:187]\n"
"v_mfma_f32_16x16x32_bf16 a[192:195], v[168:171], v[224:227], a[192:195]\n"
"v_mfma_f32_16x16x32_bf16 a[172:175], v[172:175], v[200:203], a[172:175]\n"
"v_mfma_f32_16x16x32_bf16 a[180:183], v[172:175], v[208:211], a[180:183]\n"
"v_mfma_f32_16x16x32_bf16 a[188:191], v[172:175], v[216:219], a[188:191]\n"
"v_mfma_f32_16x16x32_bf16 a[196:199], v[172:175], v[224:227], a[196:199]\n"
"v_mfma_f32_16x16x32_bf16 a[200:203], v[176:179], v[200:203], a[200:203]\n"
"v_mfma_f32_16x16x32_bf16 a[208:211], v[176:179], v[208:211], a[208:211]\n"
"v_mfma_f32_16x16x32_bf16 a[216:219], v[176:179], v[216:219], a[216:219]\n"
"v_mfma_f32_16x16x32_bf16 a[224:227], v[176:179], v[224:227], a[224:227]\n"
"v_mfma_f32_16x16x32_bf16 a[204:207], v[180:183], v[200:203], a[204:207]\n"
"v_mfma_f32_16x16x32_bf16 a[212:215], v[180:183], v[208:211], a[212:215]\n"
"v_mfma_f32_16x16x32_bf16 a[220:223], v[180:183], v[216:219], a[220:223]\n"
"v_mfma_f32_16x16x32_bf16 a[228:231], v[180:183], v[224:227], a[228:231]\n"
"s_add_u32 s78, s78, 16384\n"
"s_branch L_ST\n"
"L_ROLEB:\n"
"s_mov_b32 exec_lo, 0x33333333\n"
"s_mov_b32 exec_hi, 0x33333333\n"
"buffer_load_dword v248, v247, s[36:39], 0 offen\n"
"s_mov_b64 exec, -1\n"
"s_mov_b32 s83, 0\n"
"buffer_load_dword v248, v246, s[56:59], s83 offen\n"
"s_mov_b64 s[40:41], %[k]\n"
"s_and_b32 s41, s41, 0xffff\n"
"s_mov_b32 s42, 262144\n"
"s_mov_b32 s43, 0x00020000\n"
"s_mov_b64 s[44:45], %[v]\n"
"s_and_b32 s45, s45, 0xffff\n"
"s_mov_b32 s46, 262144\n"
"s_mov_b32 s47, 0x00020000\n"
"s_mov_b64 s[48:49], %[wo]\n"
"s_and_b32 s49, s49, 0xffff\n"
"s_mov_b32 s50, 458752\n"
"s_mov_b32 s51, 0x00020000\n"
"s_mov_b64 s[52:53], %[ws]\n"
"s_and_b32 s53, s53, 0xffff\n"
"s_mov_b32 s54, 1855488\n"
"s_mov_b32 s55, 0x00020000\n"
"s_mov_b32 s83, 1835008\n"
"buffer_load_dwordx2 v[64:65], off, s[52:55], s83 sc1\n"
"s_mov_b32 s61, 3072\n"
"s_mov_b32 s62, 6144\n"
"s_mov_b32 s63, 9216\n"
"s_lshr_b32 s81, s85, 5\n"
"s_lshl_b32 s82, s60, 7\n"
"v_lshl_add_u32 v241, v3, 9, v6\n"
"v_add_u32 v245, s81, v241\n"
"v_add_u32 v241, s82, v241\n"
"v_bfe_u32 v8, v2, 3, 1\n"
"v_and_b32 v9, 7, v2\n"
"v_lshlrev_b32 v9, 6, v9\n"
"v_lshl_add_u32 v8, v8, 10, v9\n"
"v_and_b32 v9, 2, v11\n"
"v_add_u32 v9, v9, v4\n"
"v_bfe_i32 v10, v2, 2, 1\n"
"v_and_b32 v10, 3, v10\n"
"v_xor_b32 v10, v10, v9\n"
"v_lshl_add_u32 v232, v10, 4, v8\n"
"v_add_u32 v233, 65536, v232\n"
"v_add_u32 v234, 131072, v232\n"
"s_lshr_b32 s81, s60, 1\n"
"s_lshl_b32 s82, s81, 13\n"
"v_add_u32 v254, s82, v232\n"
"v_lshlrev_b32 v8, 3, v5\n"
"v_bfe_u32 v10, v2, 2, 2\n"
"v_lshl_add_u32 v10, v10, 6, v8\n"
"v_lshl_add_u32 v10, v9, 10, v10\n"
"v_and_b32 v9, 32, v11\n"
"s_add_u32 s82, s85, 49152\n"
"v_add3_u32 v250, v10, v9, s82\n"
"v_xor_b32 v251, 32, v250\n"
"v_add_u32 v252, 65536, v250\n"
"v_add_u32 v253, 65536, v251\n"
"v_and_b32 v9, 48, v11\n"
"v_xor_b32 v8, v8, v9\n"
"v_lshl_add_u32 v8, v3, 6, v8\n"
"v_lshl_add_u32 v235, v4, 10, v8\n"
"v_xor_b32 v236, 32, v235\n"
"v_add_u32 v237, 65536, v235\n"
"v_add_u32 v238, 65536, v236\n"
"v_add_u32 v239, 131072, v235\n"
"v_add_u32 v240, 131072, v236\n"
"v_and_b32 v249, 127, %[tid]\n"
"v_lshlrev_b32 v249, 7, v249\n"
"s_mul_i32 s87, %[b], 28672\n"
"s_lshl_b32 s81, s81, 14\n"
"s_add_u32 s87, s87, s81\n"
"s_mov_b32 s70, 0x3db8aa3b\n"
"s_add_u32 s79, s79, 128\n"
"s_sub_u32 s78, s79, 896\n"
"s_min_u32 s79, s79, s78\n"
"v_add_u32 v242, s79, v243\n"
"s_add_u32 m0, s71, 16384\n"
"s_nop 0\n"
"buffer_load_dwordx4 v242, s[32:35], 0 offen offset:0  lds\n"
"buffer_load_dwordx4 v242, s[32:35], s64 offen offset:1024  lds\n"
"buffer_load_dwordx4 v242, s[32:35], s65 offen offset:2048  lds\n"
"buffer_load_dwordx4 v242, s[32:35], s66 offen offset:3072  lds\n"
"s_waitcnt vmcnt(7)\n"
"s_barrier\n"
"ds_read_b128 a[0:3], v254 offset:0\n"
"ds_read_b128 a[4:7], v254 offset:2048\n"
"ds_read_b128 a[8:11], v254 offset:4096\n"
"ds_read_b128 a[12:15], v254 offset:6144\n"
"ds_read_b64_tr_b16 a[232:233], v250 offset:32768\n"
"ds_read_b64_tr_b16 a[234:235], v250 offset:33024\n"
"ds_read_b128 a[16:19], v254 offset:512\n"
"ds_read_b128 a[20:23], v254 offset:2560\n"
"ds_read_b128 a[24:27], v254 offset:4608\n"
"ds_read_b128 a[28:31], v254 offset:6656\n"
"ds_read_b64_tr_b16 a[236:237], v250 offset:36864\n"
"ds_read_b64_tr_b16 a[238:239], v250 offset:37120\n"
"ds_read_b64_tr_b16 a[240:241], v251 offset:32768\n"
"ds_read_b64_tr_b16 a[242:243], v251 offset:33024\n"
"s_waitcnt lgkmcnt(10)\n"
"ds_read_b64_tr_b16 a[244:245], v251 offset:36864\n"
"ds_read_b64_tr_b16 a[246:247], v251 offset:37120\n"
"s_waitcnt lgkmcnt(4)\n"
"v_mfma_f32_16x16x32_bf16 v[104:107], a[232:235], a[0:3], 0\n"
"ds_read_b64_tr_b16 a[248:249], v250 offset:33280\n"
"v_mfma_f32_16x16x32_bf16 v[104:107], a[236:239], a[16:19], v[104:107]\n"
"ds_read_b64_tr_b16 a[250:251], v250 offset:33536\n"
"v_mfma_f32_16x16x32_bf16 v[112:115], a[232:235], a[4:7], 0\n"
"v_mfma_f32_16x16x32_bf16 v[112:115], a[236:239], a[20:23], v[112:115]\n"
"v_mfma_f32_16x16x32_bf16 v[120:123], a[232:235], a[8:11], 0\n"
"ds_read_b64_tr_b16 a[252:253], v250 offset:37376\n"
"v_mfma_f32_16x16x32_bf16 v[120:123], a[236:239], a[24:27], v[120:123]\n"
"ds_read_b64_tr_b16 a[254:255], v250 offset:37632\n"
"v_mfma_f32_16x16x32_bf16 v[128:131], a[232:235], a[12:15], 0\n"
"v_mfma_f32_16x16x32_bf16 v[128:131], a[236:239], a[28:31], v[128:131]\n"
"s_waitcnt lgkmcnt(4)\n"
"v_mfma_f32_16x16x32_bf16 v[132:135], a[240:243], a[12:15], 0\n"
"ds_read_b64_tr_b16 a[232:233], v251 offset:33280\n"
"v_mfma_f32_16x16x32_bf16 v[132:135], a[244:247], a[28:31], v[132:135]\n"
"s_add_u32 s79, s79, 128\n"
"ds_read_b64_tr_b16 a[234:235], v251 offset:33536\n"
"v_mfma_f32_16x16x32_bf16 v[124:127], a[240:243], a[8:11], 0\n"
"s_sub_u32 s78, s79, 896\n"
"v_mfma_f32_16x16x32_bf16 v[124:127], a[244:247], a[24:27], v[124:127]\n"
"s_min_u32 s79, s79, s78\n"
"v_add_u32 v242, s79, v243\n"
"s_add_u32 m0, s71, 32768\n"
"v_mfma_f32_16x16x32_bf16 v[116:119], a[240:243], a[4:7], 0\n"
"buffer_load_dwordx4 v242, s[32:35], 0 offen offset:0  lds\n"
"ds_read_b64_tr_b16 a[236:237], v251 offset:37376\n"
"v_mfma_f32_16x16x32_bf16 v[116:119], a[244:247], a[20:23], v[116:119]\n"
"ds_read_b64_tr_b16 a[238:239], v251 offset:37632\n"
"v_mfma_f32_16x16x32_bf16 v[108:111], a[240:243], a[0:3], 0\n"
"v_mfma_f32_16x16x32_bf16 v[108:111], a[244:247], a[16:19], v[108:111]\n"
"s_waitcnt lgkmcnt(4)\n"
"buffer_load_dwordx4 v242, s[32:35], s64 offen offset:1024  lds\n"
"v_mfma_f32_16x16x32_bf16 v[136:139], a[248:251], a[0:3], 0\n"
"s_barrier\n"
"ds_read_b64_tr_b16 a[240:241], v250 offset:49152\n"
"v_mfma_f32_16x16x32_bf16 v[136:139], a[252:255], a[16:19], v[136:139]\n"
"ds_read_b64_tr_b16 a[242:243], v250 offset:49408\n"
"v_mfma_f32_16x16x32_bf16 v[144:147], a[248:251], a[4:7], 0\n"
"buffer_load_dwordx4 v242, s[32:35], s65 offen offset:2048  lds\n"
"v_mfma_f32_16x16x32_bf16 v[144:147], a[252:255], a[20:23], v[144:147]\n"
"v_mfma_f32_16x16x32_bf16 v[152:155], a[248:251], a[8:11], 0\n"
"ds_read_b64_tr_b16 a[244:245], v250 offset:53248\n"
"v_mfma_f32_16x16x32_bf16 v[152:155], a[252:255], a[24:27], v[152:155]\n"
"ds_read_b64_tr_b16 a[246:247], v250 offset:53504\n"
"v_mfma_f32_16x16x32_bf16 v[160:163], a[248:251], a[12:15], 0\n"
"buffer_load_dwordx4 v242, s[32:35], s66 offen offset:3072  lds\n"
"v_mfma_f32_16x16x32_bf16 v[160:163], a[252:255], a[28:31], v[160:163]\n"
"s_waitcnt lgkmcnt(4)\n"
"v_mfma_f32_16x16x32_bf16 v[164:167], a[232:235], a[12:15], 0\n"
"ds_read_b64_tr_b16 a[248:249], v251 offset:49152\n"
"v_mfma_f32_16x16x32_bf16 v[164:167], a[236:239], a[28:31], v[164:167]\n"
"ds_read_b64_tr_b16 a[250:251], v251 offset:49408\n"
"v_mfma_f32_16x16x32_bf16 v[156:159], a[232:235], a[8:11], 0\n"
"v_mfma_f32_16x16x32_bf16 v[156:159], a[236:239], a[24:27], v[156:159]\n"
"v_mfma_f32_16x16x32_bf16 v[148:151], a[232:235], a[4:7], 0\n"
"ds_read_b64_tr_b16 a[252:253], v251 offset:53248\n"
"v_mfma_f32_16x16x32_bf16 v[148:151], a[236:239], a[20:23], v[148:151]\n"
"ds_read_b64_tr_b16 a[254:255], v251 offset:53504\n"
"v_mfma_f32_16x16x32_bf16 v[140:143], a[232:235], a[0:3], 0\n"
"v_mfma_f32_16x16x32_bf16 v[140:143], a[236:239], a[16:19], v[140:143]\n"
"s_waitcnt lgkmcnt(4)\n"
"v_mfma_f32_16x16x32_bf16 v[168:171], a[240:243], a[0:3], 0\n"
"ds_read_b64_tr_b16 a[232:233], v250 offset:49664\n"
"v_mfma_f32_16x16x32_bf16 v[168:171], a[244:247], a[16:19], v[168:171]\n"
"ds_read_b64_tr_b16 a[234:235], v250 offset:49920\n"
"v_mfma_f32_16x16x32_bf16 v[176:179], a[240:243], a[4:7], 0\n"
"v_mfma_f32_16x16x32_bf16 v[176:179], a[244:247], a[20:23], v[176:179]\n"
"v_mfma_f32_16x16x32_bf16 v[184:187], a[240:243], a[8:11], 0\n"
"ds_read_b64_tr_b16 a[236:237], v250 offset:53760\n"
"v_mfma_f32_16x16x32_bf16 v[184:187], a[244:247], a[24:27], v[184:187]\n"
"ds_read_b64_tr_b16 a[238:239], v250 offset:54016\n"
"v_mfma_f32_16x16x32_bf16 v[192:195], a[240:243], a[12:15], 0\n"
"v_mfma_f32_16x16x32_bf16 v[192:195], a[244:247], a[28:31], v[192:195]\n"
"s_waitcnt lgkmcnt(4)\n"
"v_mfma_f32_16x16x32_bf16 v[196:199], a[248:251], a[12:15], 0\n"
"ds_read_b64_tr_b16 a[240:241], v251 offset:49664\n"
"v_mfma_f32_16x16x32_bf16 v[196:199], a[252:255], a[28:31], v[196:199]\n"
"ds_read_b64_tr_b16 a[242:243], v251 offset:49920\n"
"v_mfma_f32_16x16x32_bf16 v[188:191], a[248:251], a[8:11], 0\n"
"v_mfma_f32_16x16x32_bf16 v[188:191], a[252:255], a[24:27], v[188:191]\n"
"v_mfma_f32_16x16x32_bf16 v[180:183], a[248:251], a[4:7], 0\n"
"ds_read_b64_tr_b16 a[244:245], v251 offset:53760\n"
"v_mfma_f32_16x16x32_bf16 v[180:183], a[252:255], a[20:23], v[180:183]\n"
"ds_read_b64_tr_b16 a[246:247], v251 offset:54016\n"
"v_mfma_f32_16x16x32_bf16 v[172:175], a[248:251], a[0:3], 0\n"
"v_mfma_f32_16x16x32_bf16 v[172:175], a[252:255], a[16:19], v[172:175]\n"
"s_waitcnt lgkmcnt(4)\n"
"v_mfma_f32_16x16x32_bf16 v[200:203], a[232:235], a[0:3], 0\n"
"s_waitcnt vmcnt(4)\n"
"s_barrier\n"
"ds_read_b64_tr_b16 a[248:249], v252 offset:0\n"
"v_mfma_f32_16x16x32_bf16 v[200:203], a[236:239], a[16:19], v[200:203]\n"
"ds_read_b64_tr_b16 a[250:251], v252 offset:256\n"
"v_mfma_f32_16x16x32_bf16 v[208:211], a[232:235], a[4:7], 0\n"
"ds_read_b128 a[32:35], v254 offset:16384\n"
"ds_read_b128 a[36:39], v254 offset:18432\n"
"s_mov_b32 exec_lo, 0xcccccccc\n"
"s_mov_b32 exec_hi, 0xcccccccc\n"
"buffer_load_dword v248, v247, s[36:39], 0 offen\n"
"s_mov_b64 exec, -1\n"
"v_mfma_f32_16x16x32_bf16 v[208:211], a[236:239], a[20:23], v[208:211]\n"
"ds_read_b128 a[40:43], v254 offset:20480\n"
"ds_read_b128 a[44:47], v254 offset:22528\n"
"v_mfma_f32_16x16x32_bf16 v[216:219], a[232:235], a[8:11], 0\n"
"ds_read_b64_tr_b16 a[252:253], v252 offset:4096\n"
"v_mfma_f32_16x16x32_bf16 v[216:219], a[236:239], a[24:27], v[216:219]\n"
"ds_read_b64_tr_b16 a[254:255], v252 offset:4352\n"
"v_mfma_f32_16x16x32_bf16 v[224:227], a[232:235], a[12:15], 0\n"
"ds_read_b128 a[48:51], v254 offset:16896\n"
"ds_read_b128 a[52:55], v254 offset:18944\n"
"v_mfma_f32_16x16x32_bf16 v[224:227], a[236:239], a[28:31], v[224:227]\n"
"s_waitcnt lgkmcnt(10)\n"
"ds_read_b128 a[56:59], v254 offset:20992\n"
"ds_read_b128 a[60:63], v254 offset:23040\n"
"v_mfma_f32_16x16x32_bf16 v[228:231], a[240:243], a[12:15], 0\n"
"ds_read_b64_tr_b16 a[232:233], v253 offset:0\n"
"v_mfma_f32_16x16x32_bf16 v[228:231], a[244:247], a[28:31], v[228:231]\n"
"ds_read_b64_tr_b16 a[234:235], v253 offset:256\n"
"v_mfma_f32_16x16x32_bf16 v[220:223], a[240:243], a[8:11], 0\n"
"v_mfma_f32_16x16x32_bf16 v[220:223], a[244:247], a[24:27], v[220:223]\n"
"s_waitcnt lgkmcnt(12)\n"
"v_mfma_f32_16x16x32_bf16 v[212:215], a[240:243], a[4:7], 0\n"
"ds_read_b64_tr_b16 a[236:237], v253 offset:4096\n"
"v_mfma_f32_16x16x32_bf16 v[212:215], a[244:247], a[20:23], v[212:215]\n"
"ds_read_b64_tr_b16 a[238:239], v253 offset:4352\n"
"v_mfma_f32_16x16x32_bf16 v[204:207], a[240:243], a[0:3], 0\n"
"v_mfma_f32_16x16x32_bf16 v[204:207], a[244:247], a[16:19], v[204:207]\n"
"s_waitcnt lgkmcnt(4)\n"
"v_mfma_f32_16x16x32_bf16 v[104:107], a[248:251], a[32:35], v[104:107]\n"
"ds_read_b64_tr_b16 a[240:241], v252 offset:512\n"
"v_mfma_f32_16x16x32_bf16 v[104:107], a[252:255], a[48:51], v[104:107]\n"
"ds_read_b64_tr_b16 a[242:243], v252 offset:768\n"
"v_mfma_f32_16x16x32_bf16 v[112:115], a[248:251], a[36:39], v[112:115]\n"
"v_mfma_f32_16x16x32_bf16 v[112:115], a[252:255], a[52:55], v[112:115]\n"
"v_mfma_f32_16x16x32_bf16 v[120:123], a[248:251], a[40:43], v[120:123]\n"
"ds_read_b64_tr_b16 a[244:245], v252 offset:4608\n"
"v_mfma_f32_16x16x32_bf16 v[120:123], a[252:255], a[56:59], v[120:123]\n"
"ds_read_b64_tr_b16 a[246:247], v252 offset:4864\n"
"v_mfma_f32_16x16x32_bf16 v[128:131], a[248:251], a[44:47], v[128:131]\n"
"v_mfma_f32_16x16x32_bf16 v[128:131], a[252:255], a[60:63], v[128:131]\n"
"s_waitcnt lgkmcnt(4)\n"
"v_mfma_f32_16x16x32_bf16 v[132:135], a[232:235], a[44:47], v[132:135]\n"
"ds_read_b64_tr_b16 a[248:249], v253 offset:512\n"
"v_mfma_f32_16x16x32_bf16 v[132:135], a[236:239], a[60:63], v[132:135]\n"
"s_add_u32 s79, s79, 128\n"
"ds_read_b64_tr_b16 a[250:251], v253 offset:768\n"
"v_mfma_f32_16x16x32_bf16 v[124:127], a[232:235], a[40:43], v[124:127]\n"
"s_sub_u32 s78, s79, 896\n"
"v_mfma_f32_16x16x32_bf16 v[124:127], a[236:239], a[56:59], v[124:127]\n"
"s_min_u32 s79, s79, s78\n"
"v_add_u32 v242, s79, v243\n"
"s_add_u32 m0, s71, 0\n"
"v_mfma_f32_16x16x32_bf16 v[116:119], a[232:235], a[36:39], v[116:119]\n"
"buffer_load_dwordx4 v242, s[32:35], 0 offen offset:0  lds\n"
"ds_read_b64_tr_b16 a[252:253], v253 offset:4608\n"
"v_mfma_f32_16x16x32_bf16 v[116:119], a[236:239], a[52:55], v[116:119]\n"
"ds_read_b64_tr_b16 a[254:255], v253 offset:4864\n"
"v_mfma_f32_16x16x32_bf16 v[108:111], a[232:235], a[32:35], v[108:111]\n"
"v_mfma_f32_16x16x32_bf16 v[108:111], a[236:239], a[48:51], v[108:111]\n"
"s_waitcnt lgkmcnt(4)\n"
"buffer_load_dwordx4 v242, s[32:35], s64 offen offset:1024  lds\n"
"v_mfma_f32_16x16x32_bf16 v[136:139], a[240:243], a[32:35], v[136:139]\n"
"s_barrier\n"
"ds_read_b64_tr_b16 a[232:233], v250 offset:0\n"
"v_mfma_f32_16x16x32_bf16 v[136:139], a[244:247], a[48:51], v[136:139]\n"
"ds_read_b64_tr_b16 a[234:235], v250 offset:256\n"
"v_mfma_f32_16x16x32_bf16 v[144:147], a[240:243], a[36:39], v[144:147]\n"
"buffer_load_dwordx4 v242, s[32:35], s65 offen offset:2048  lds\n"
"v_mfma_f32_16x16x32_bf16 v[144:147], a[244:247], a[52:55], v[144:147]\n"
"v_mfma_f32_16x16x32_bf16 v[152:155], a[240:243], a[40:43], v[152:155]\n"
"ds_read_b64_tr_b16 a[236:237], v250 offset:4096\n"
"v_mfma_f32_16x16x32_bf16 v[152:155], a[244:247], a[56:59], v[152:155]\n"
"ds_read_b64_tr_b16 a[238:239], v250 offset:4352\n"
"v_mfma_f32_16x16x32_bf16 v[160:163], a[240:243], a[44:47], v[160:163]\n"
"buffer_load_dwordx4 v242, s[32:35], s66 offen offset:3072  lds\n"
"v_mfma_f32_16x16x32_bf16 v[160:163], a[244:247], a[60:63], v[160:163]\n"
"s_waitcnt lgkmcnt(4)\n"
"v_mfma_f32_16x16x32_bf16 v[164:167], a[248:251], a[44:47], v[164:167]\n"
"ds_read_b64_tr_b16 a[240:241], v251 offset:0\n"
"v_mfma_f32_16x16x32_bf16 v[164:167], a[252:255], a[60:63], v[164:167]\n"
"ds_read_b64_tr_b16 a[242:243], v251 offset:256\n"
"v_mfma_f32_16x16x32_bf16 v[156:159], a[248:251], a[40:43], v[156:159]\n"
"v_mfma_f32_16x16x32_bf16 v[156:159], a[252:255], a[56:59], v[156:159]\n"
"v_mfma_f32_16x16x32_bf16 v[148:151], a[248:251], a[36:39], v[148:151]\n"
"ds_read_b64_tr_b16 a[244:245], v251 offset:4096\n"
"v_mfma_f32_16x16x32_bf16 v[148:151], a[252:255], a[52:55], v[148:151]\n"
"ds_read_b64_tr_b16 a[246:247], v251 offset:4352\n"
"v_mfma_f32_16x16x32_bf16 v[140:143], a[248:251], a[32:35], v[140:143]\n"
"v_mfma_f32_16x16x32_bf16 v[140:143], a[252:255], a[48:51], v[140:143]\n"
"s_waitcnt lgkmcnt(4)\n"
"v_mfma_f32_16x16x32_bf16 v[168:171], a[232:235], a[32:35], v[168:171]\n"
"ds_read_b64_tr_b16 a[248:249], v250 offset:512\n"
"v_mfma_f32_16x16x32_bf16 v[168:171], a[236:239], a[48:51], v[168:171]\n"
"ds_read_b64_tr_b16 a[250:251], v250 offset:768\n"
"v_mfma_f32_16x16x32_bf16 v[176:179], a[232:235], a[36:39], v[176:179]\n"
"v_mfma_f32_16x16x32_bf16 v[176:179], a[236:239], a[52:55], v[176:179]\n"
"v_mfma_f32_16x16x32_bf16 v[184:187], a[232:235], a[40:43], v[184:187]\n"
"ds_read_b64_tr_b16 a[252:253], v250 offset:4608\n"
"v_mfma_f32_16x16x32_bf16 v[184:187], a[236:239], a[56:59], v[184:187]\n"
"ds_read_b64_tr_b16 a[254:255], v250 offset:4864\n"
"v_mfma_f32_16x16x32_bf16 v[192:195], a[232:235], a[44:47], v[192:195]\n"
"v_mfma_f32_16x16x32_bf16 v[192:195], a[236:239], a[60:63], v[192:195]\n"
"s_waitcnt lgkmcnt(4)\n"
"v_mfma_f32_16x16x32_bf16 v[196:199], a[240:243], a[44:47], v[196:199]\n"
"ds_read_b64_tr_b16 a[232:233], v251 offset:512\n"
"v_mfma_f32_16x16x32_bf16 v[196:199], a[244:247], a[60:63], v[196:199]\n"
"ds_read_b64_tr_b16 a[234:235], v251 offset:768\n"
"v_mfma_f32_16x16x32_bf16 v[188:191], a[240:243], a[40:43], v[188:191]\n"
"v_mfma_f32_16x16x32_bf16 v[188:191], a[244:247], a[56:59], v[188:191]\n"
"v_mfma_f32_16x16x32_bf16 v[180:183], a[240:243], a[36:39], v[180:183]\n"
"ds_read_b64_tr_b16 a[236:237], v251 offset:4608\n"
"v_mfma_f32_16x16x32_bf16 v[180:183], a[244:247], a[52:55], v[180:183]\n"
"ds_read_b64_tr_b16 a[238:239], v251 offset:4864\n"
"v_mfma_f32_16x16x32_bf16 v[172:175], a[240:243], a[32:35], v[172:175]\n"
"v_mfma_f32_16x16x32_bf16 v[172:175], a[244:247], a[48:51], v[172:175]\n"
"s_waitcnt lgkmcnt(4)\n"
"v_mfma_f32_16x16x32_bf16 v[200:203], a[248:251], a[32:35], v[200:203]\n"
"s_waitcnt vmcnt(5)\n"
"s_barrier\n"
"ds_read_b64_tr_b16 a[240:241], v250 offset:16384\n"
"v_mfma_f32_16x16x32_bf16 v[200:203], a[252:255], a[48:51], v[200:203]\n"
"ds_read_b64_tr_b16 a[242:243], v250 offset:16640\n"
"v_mfma_f32_16x16x32_bf16 v[208:211], a[248:251], a[36:39], v[208:211]\n"
"ds_read_b128 a[64:67], v254 offset:32768\n"
"ds_read_b128 a[68:71], v254 offset:34816\n"
"v_mfma_f32_16x16x32_bf16 v[208:211], a[252:255], a[52:55], v[208:211]\n"
"ds_read_b128 a[72:75], v254 offset:36864\n"
"ds_read_b128 a[76:79], v254 offset:38912\n"
"v_mfma_f32_16x16x32_bf16 v[216:219], a[248:251], a[40:43], v[216:219]\n"
"ds_read_b64_tr_b16 a[244:245], v250 offset:20480\n"
"v_mfma_f32_16x16x32_bf16 v[216:219], a[252:255], a[56:59], v[216:219]\n"
"ds_read_b64_tr_b16 a[246:247], v250 offset:20736\n"
"v_mfma_f32_16x16x32_bf16 v[224:227], a[248:251], a[44:47], v[224:227]\n"
"ds_read_b128 a[80:83], v254 offset:33280\n"
"ds_read_b128 a[84:87], v254 offset:35328\n"
"v_mfma_f32_16x16x32_bf16 v[224:227], a[252:255], a[60:63], v[224:227]\n"
"s_waitcnt lgkmcnt(10)\n"
"ds_read_b128 a[88:91], v254 offset:37376\n"
"ds_read_b128 a[92:95], v254 offset:39424\n"
"v_mfma_f32_16x16x32_bf16 v[228:231], a[232:235], a[44:47], v[228:231]\n"
"ds_read_b64_tr_b16 a[248:249], v251 offset:16384\n"
"v_mfma_f32_16x16x32_bf16 v[228:231], a[236:239], a[60:63], v[228:231]\n"
"ds_read_b64_tr_b16 a[250:251], v251 offset:16640\n"
"v_mfma_f32_16x16x32_bf16 v[220:223], a[232:235], a[40:43], v[220:223]\n"
"v_mfma_f32_16x16x32_bf16 v[220:223], a[236:239], a[56:59], v[220:223]\n"
"s_waitcnt lgkmcnt(12)\n"
"v_mfma_f32_16x16x32_bf16 v[212:215], a[232:235], a[36:39], v[212:215]\n"
"ds_read_b64_tr_b16 a[252:253], v251 offset:20480\n"
"v_mfma_f32_16x16x32_bf16 v[212:215], a[236:239], a[52:55], v[212:215]\n"
"ds_read_b64_tr_b16 a[254:255], v251 offset:20736\n"
"v_mfma_f32_16x16x32_bf16 v[204:207], a[232:235], a[32:35], v[204:207]\n"
"v_mfma_f32_16x16x32_bf16 v[204:207], a[236:239], a[48:51], v[204:207]\n"
"s_waitcnt lgkmcnt(4)\n"
"v_mfma_f32_16x16x32_bf16 v[104:107], a[240:243], a[64:67], v[104:107]\n"
"ds_read_b64_tr_b16 a[232:233], v250 offset:16896\n"
"v_mfma_f32_16x16x32_bf16 v[104:107], a[244:247], a[80:83], v[104:107]\n"
"ds_read_b64_tr_b16 a[234:235], v250 offset:17152\n"
"v_mfma_f32_16x16x32_bf16 v[112:115], a[240:243], a[68:71], v[112:115]\n"
"v_mfma_f32_16x16x32_bf16 v[112:115], a[244:247], a[84:87], v[112:115]\n"
"v_mfma_f32_16x16x32_bf16 v[120:123], a[240:243], a[72:75], v[120:123]\n"
"ds_read_b64_tr_b16 a[236:237], v250 offset:20992\n"
"v_mfma_f32_16x16x32_bf16 v[120:123], a[244:247], a[88:91], v[120:123]\n"
"ds_read_b64_tr_b16 a[238:239], v250 offset:21248\n"
"v_mfma_f32_16x16x32_bf16 v[128:131], a[240:243], a[76:79], v[128:131]\n"
"v_mfma_f32_16x16x32_bf16 v[128:131], a[244:247], a[92:95], v[128:131]\n"
"s_waitcnt lgkmcnt(4)\n"
"v_mfma_f32_16x16x32_bf16 v[132:135], a[248:251], a[76:79], v[132:135]\n"
"ds_read_b64_tr_b16 a[240:241], v251 offset:16896\n"
"v_mfma_f32_16x16x32_bf16 v[132:135], a[252:255], a[92:95], v[132:135]\n"
"s_add_u32 s79, s79, 128\n"
"ds_read_b64_tr_b16 a[242:243], v251 offset:17152\n"
"v_mfma_f32_16x16x32_bf16 v[124:127], a[248:251], a[72:75], v[124:127]\n"
"s_sub_u32 s78, s79, 896\n"
"v_mfma_f32_16x16x32_bf16 v[124:127], a[252:255], a[88:91], v[124:127]\n"
"s_min_u32 s79, s79, s78\n"
"v_add_u32 v242, s79, v243\n"
"s_add_u32 m0, s71, 16384\n"
"v_mfma_f32_16x16x32_bf16 v[116:119], a[248:251], a[68:71], v[116:119]\n"
"buffer_load_dwordx4 v242, s[32:35], 0 offen offset:0  lds\n"
"ds_read_b64_tr_b16 a[244:245], v251 offset:20992\n"
"v_mfma_f32_16x16x32_bf16 v[116:119], a[252:255], a[84:87], v[116:119]\n"
"ds_read_b64_tr_b16 a[246:247], v251 offset:21248\n"
"v_mfma_f32_16x16x32_bf16 v[108:111], a[248:251], a[64:67], v[108:111]\n"
"v_mfma_f32_16x16x32_bf16 v[108:111], a[252:255], a[80:83], v[108:111]\n"
"s_waitcnt lgkmcnt(4)\n"
"buffer_load_dwordx4 v242, s[32:35], s64 offen offset:1024  lds\n"
"v_mfma_f32_16x16x32_bf16 v[136:139], a[232:235], a[64:67], v[136:139]\n"
"s_barrier\n"
"ds_read_b64_tr_b16 a[248:249], v250 offset:32768\n"
"v_mfma_f32_16x16x32_bf16 v[136:139], a[236:239], a[80:83], v[136:139]\n"
"ds_read_b64_tr_b16 a[250:251], v250 offset:33024\n"
"v_mfma_f32_16x16x32_bf16 v[144:147], a[232:235], a[68:71], v[144:147]\n"
"buffer_load_dwordx4 v242, s[32:35], s65 offen offset:2048  lds\n"
"v_mfma_f32_16x16x32_bf16 v[144:147], a[236:239], a[84:87], v[144:147]\n"
"v_mfma_f32_16x16x32_bf16 v[152:155], a[232:235], a[72:75], v[152:155]\n"
"ds_read_b64_tr_b16 a[252:253], v250 offset:36864\n"
"v_mfma_f32_16x16x32_bf16 v[152:155], a[236:239], a[88:91], v[152:155]\n"
"ds_read_b64_tr_b16 a[254:255], v250 offset:37120\n"
"v_mfma_f32_16x16x32_bf16 v[160:163], a[232:235], a[76:79], v[160:163]\n"
"buffer_load_dwordx4 v242, s[32:35], s66 offen offset:3072  lds\n"
"v_mfma_f32_16x16x32_bf16 v[160:163], a[236:239], a[92:95], v[160:163]\n"
"s_waitcnt lgkmcnt(4)\n"
"v_mfma_f32_16x16x32_bf16 v[164:167], a[240:243], a[76:79], v[164:167]\n"
"ds_read_b64_tr_b16 a[232:233], v251 offset:32768\n"
"v_mfma_f32_16x16x32_bf16 v[164:167], a[244:247], a[92:95], v[164:167]\n"
"ds_read_b64_tr_b16 a[234:235], v251 offset:33024\n"
"v_mfma_f32_16x16x32_bf16 v[156:159], a[240:243], a[72:75], v[156:159]\n"
"v_mfma_f32_16x16x32_bf16 v[156:159], a[244:247], a[88:91], v[156:159]\n"
"v_mfma_f32_16x16x32_bf16 v[148:151], a[240:243], a[68:71], v[148:151]\n"
"ds_read_b64_tr_b16 a[236:237], v251 offset:36864\n"
"v_mfma_f32_16x16x32_bf16 v[148:151], a[244:247], a[84:87], v[148:151]\n"
"ds_read_b64_tr_b16 a[238:239], v251 offset:37120\n"
"v_mfma_f32_16x16x32_bf16 v[140:143], a[240:243], a[64:67], v[140:143]\n"
"v_mfma_f32_16x16x32_bf16 v[140:143], a[244:247], a[80:83], v[140:143]\n"
"s_waitcnt lgkmcnt(4)\n"
"v_mfma_f32_16x16x32_bf16 v[168:171], a[248:251], a[64:67], v[168:171]\n"
"ds_read_b64_tr_b16 a[240:241], v250 offset:33280\n"
"v_mfma_f32_16x16x32_bf16 v[168:171], a[252:255], a[80:83], v[168:171]\n"
"ds_read_b64_tr_b16 a[242:243], v250 offset:33536\n"
"v_mfma_f32_16x16x32_bf16 v[176:179], a[248:251], a[68:71], v[176:179]\n"
"v_mfma_f32_16x16x32_bf16 v[176:179], a[252:255], a[84:87], v[176:179]\n"
"v_mfma_f32_16x16x32_bf16 v[184:187], a[248:251], a[72:75], v[184:187]\n"
"ds_read_b64_tr_b16 a[244:245], v250 offset:37376\n"
"v_mfma_f32_16x16x32_bf16 v[184:187], a[252:255], a[88:91], v[184:187]\n"
"ds_read_b64_tr_b16 a[246:247], v250 offset:37632\n"
"v_mfma_f32_16x16x32_bf16 v[192:195], a[248:251], a[76:79], v[192:195]\n"
"v_mfma_f32_16x16x32_bf16 v[192:195], a[252:255], a[92:95], v[192:195]\n"
"s_waitcnt lgkmcnt(4)\n"
"v_mfma_f32_16x16x32_bf16 v[196:199], a[232:235], a[76:79], v[196:199]\n"
"ds_read_b64_tr_b16 a[248:249], v251 offset:33280\n"
"v_mfma_f32_16x16x32_bf16 v[196:199], a[236:239], a[92:95], v[196:199]\n"
"ds_read_b64_tr_b16 a[250:251], v251 offset:33536\n"
"v_mfma_f32_16x16x32_bf16 v[188:191], a[232:235], a[72:75], v[188:191]\n"
"v_mfma_f32_16x16x32_bf16 v[188:191], a[236:239], a[88:91], v[188:191]\n"
"v_mfma_f32_16x16x32_bf16 v[180:183], a[232:235], a[68:71], v[180:183]\n"
"ds_read_b64_tr_b16 a[252:253], v251 offset:37376\n"
"v_mfma_f32_16x16x32_bf16 v[180:183], a[236:239], a[84:87], v[180:183]\n"
"ds_read_b64_tr_b16 a[254:255], v251 offset:37632\n"
"v_mfma_f32_16x16x32_bf16 v[172:175], a[232:235], a[64:67], v[172:175]\n"
"v_mfma_f32_16x16x32_bf16 v[172:175], a[236:239], a[80:83], v[172:175]\n"
"s_waitcnt lgkmcnt(4)\n"
"v_mfma_f32_16x16x32_bf16 v[200:203], a[240:243], a[64:67], v[200:203]\n"
"s_waitcnt vmcnt(4)\n"
"s_barrier\n"
"ds_read_b64_tr_b16 a[232:233], v250 offset:49152\n"
"v_mfma_f32_16x16x32_bf16 v[200:203], a[244:247], a[80:83], v[200:203]\n"
"ds_read_b64_tr_b16 a[234:235], v250 offset:49408\n"
"v_mfma_f32_16x16x32_bf16 v[208:211], a[240:243], a[68:71], v[208:211]\n"
"ds_read_b128 a[96:99], v254 offset:0\n"
"ds_read_b128 a[100:103], v254 offset:2048\n"
"v_mfma_f32_16x16x32_bf16 v[208:211], a[244:247], a[84:87], v[208:211]\n"
"ds_read_b128 a[104:107], v254 offset:4096\n"
"ds_read_b128 a[108:111], v254 offset:6144\n"
"v_mfma_f32_16x16x32_bf16 v[216:219], a[240:243], a[72:75], v[216:219]\n"
"ds_read_b64_tr_b16 a[236:237], v250 offset:53248\n"
"v_mfma_f32_16x16x32_bf16 v[216:219], a[244:247], a[88:91], v[216:219]\n"
"ds_read_b64_tr_b16 a[238:239], v250 offset:53504\n"
"v_mfma_f32_16x16x32_bf16 v[224:227], a[240:243], a[76:79], v[224:227]\n"
"ds_read_b128 a[112:115], v254 offset:512\n"
"ds_read_b128 a[116:119], v254 offset:2560\n"
"v_mfma_f32_16x16x32_bf16 v[224:227], a[244:247], a[92:95], v[224:227]\n"
"s_waitcnt lgkmcnt(10)\n"
"ds_read_b128 a[120:123], v254 offset:4608\n"
"ds_read_b128 a[124:127], v254 offset:6656\n"
"v_mfma_f32_16x16x32_bf16 v[228:231], a[248:251], a[76:79], v[228:231]\n"
"ds_read_b64_tr_b16 a[240:241], v251 offset:49152\n"
"v_mfma_f32_16x16x32_bf16 v[228:231], a[252:255], a[92:95], v[228:231]\n"
"ds_read_b64_tr_b16 a[242:243], v251 offset:49408\n"
"v_mfma_f32_16x16x32_bf16 v[220:223], a[248:251], a[72:75], v[220:223]\n"
"v_mfma_f32_16x16x32_bf16 v[220:223], a[252:255], a[88:91], v[220:223]\n"
"s_waitcnt lgkmcnt(12)\n"
"v_mfma_f32_16x16x32_bf16 v[212:215], a[248:251], a[68:71], v[212:215]\n"
"ds_read_b64_tr_b16 a[244:245], v251 offset:53248\n"
"v_mfma_f32_16x16x32_bf16 v[212:215], a[252:255], a[84:87], v[212:215]\n"
"ds_read_b64_tr_b16 a[246:247], v251 offset:53504\n"
"v_mfma_f32_16x16x32_bf16 v[204:207], a[248:251], a[64:67], v[204:207]\n"
"v_mfma_f32_16x16x32_bf16 v[204:207], a[252:255], a[80:83], v[204:207]\n"
"s_waitcnt lgkmcnt(4)\n"
"v_mfma_f32_16x16x32_bf16 v[104:107], a[232:235], a[96:99], v[104:107]\n"
"ds_read_b64_tr_b16 a[248:249], v250 offset:49664\n"
"v_mfma_f32_16x16x32_bf16 v[104:107], a[236:239], a[112:115], v[104:107]\n"
"ds_read_b64_tr_b16 a[250:251], v250 offset:49920\n"
"v_mfma_f32_16x16x32_bf16 v[112:115], a[232:235], a[100:103], v[112:115]\n"
"v_mfma_f32_16x16x32_bf16 v[112:115], a[236:239], a[116:119], v[112:115]\n"
"v_mfma_f32_16x16x32_bf16 v[120:123], a[232:235], a[104:107], v[120:123]\n"
"ds_read_b64_tr_b16 a[252:253], v250 offset:53760\n"
"v_mfma_f32_16x16x32_bf16 v[120:123], a[236:239], a[120:123], v[120:123]\n"
"ds_read_b64_tr_b16 a[254:255], v250 offset:54016\n"
"v_mfma_f32_16x16x32_bf16 v[128:131], a[232:235], a[108:111], v[128:131]\n"
"v_mfma_f32_16x16x32_bf16 v[128:131], a[236:239], a[124:127], v[128:131]\n"
"s_waitcnt lgkmcnt(4)\n"
"v_mfma_f32_16x16x32_bf16 v[132:135], a[240:243], a[108:111], v[132:135]\n"
"ds_read_b64_tr_b16 a[232:233], v251 offset:49664\n"
"v_mfma_f32_16x16x32_bf16 v[132:135], a[244:247], a[124:127], v[132:135]\n"
"s_add_u32 s79, s79, 128\n"
"ds_read_b64_tr_b16 a[234:235], v251 offset:49920\n"
"v_mfma_f32_16x16x32_bf16 v[124:127], a[240:243], a[104:107], v[124:127]\n"
"s_sub_u32 s78, s79, 896\n"
"v_mfma_f32_16x16x32_bf16 v[124:127], a[244:247], a[120:123], v[124:127]\n"
"s_min_u32 s79, s79, s78\n"
"v_add_u32 v242, s79, v243\n"
"s_add_u32 m0, s71, 32768\n"
"v_mfma_f32_16x16x32_bf16 v[116:119], a[240:243], a[100:103], v[116:119]\n"
"buffer_load_dwordx4 v242, s[32:35], 0 offen offset:0  lds\n"
"ds_read_b64_tr_b16 a[236:237], v251 offset:53760\n"
"v_mfma_f32_16x16x32_bf16 v[116:119], a[244:247], a[116:119], v[116:119]\n"
"ds_read_b64_tr_b16 a[238:239], v251 offset:54016\n"
"v_mfma_f32_16x16x32_bf16 v[108:111], a[240:243], a[96:99], v[108:111]\n"
"v_mfma_f32_16x16x32_bf16 v[108:111], a[244:247], a[112:115], v[108:111]\n"
"s_waitcnt lgkmcnt(4)\n"
"buffer_load_dwordx4 v242, s[32:35], s64 offen offset:1024  lds\n"
"v_mfma_f32_16x16x32_bf16 v[136:139], a[248:251], a[96:99], v[136:139]\n"
"s_barrier\n"
"ds_read_b64_tr_b16 a[240:241], v252 offset:0\n"
"v_mfma_f32_16x16x32_bf16 v[136:139], a[252:255], a[112:115], v[136:139]\n"
"ds_read_b64_tr_b16 a[242:243], v252 offset:256\n"
"v_mfma_f32_16x16x32_bf16 v[144:147], a[248:251], a[100:103], v[144:147]\n"
"buffer_load_dwordx4 v242, s[32:35], s65 offen offset:2048  lds\n"
"v_mfma_f32_16x16x32_bf16 v[144:147], a[252:255], a[116:119], v[144:147]\n"
"v_mfma_f32_16x16x32_bf16 v[152:155], a[248:251], a[104:107], v[152:155]\n"
"ds_read_b64_tr_b16 a[244:245], v252 offset:4096\n"
"v_mfma_f32_16x16x32_bf16 v[152:155], a[252:255], a[120:123], v[152:155]\n"
"ds_read_b64_tr_b16 a[246:247], v252 offset:4352\n"
"v_mfma_f32_16x16x32_bf16 v[160:163], a[248:251], a[108:111], v[160:163]\n"
"buffer_load_dwordx4 v242, s[32:35], s66 offen offset:3072  lds\n"
"v_mfma_f32_16x16x32_bf16 v[160:163], a[252:255], a[124:127], v[160:163]\n"
"s_waitcnt lgkmcnt(4)\n"
"v_mfma_f32_16x16x32_bf16 v[164:167], a[232:235], a[108:111], v[164:167]\n"
"ds_read_b64_tr_b16 a[248:249], v253 offset:0\n"
"v_mfma_f32_16x16x32_bf16 v[164:167], a[236:239], a[124:127], v[164:167]\n"
"ds_read_b64_tr_b16 a[250:251], v253 offset:256\n"
"v_mfma_f32_16x16x32_bf16 v[156:159], a[232:235], a[104:107], v[156:159]\n"
"v_mfma_f32_16x16x32_bf16 v[156:159], a[236:239], a[120:123], v[156:159]\n"
"v_mfma_f32_16x16x32_bf16 v[148:151], a[232:235], a[100:103], v[148:151]\n"
"ds_read_b64_tr_b16 a[252:253], v253 offset:4096\n"
"v_mfma_f32_16x16x32_bf16 v[148:151], a[236:239], a[116:119], v[148:151]\n"
"ds_read_b64_tr_b16 a[254:255], v253 offset:4352\n"
"v_mfma_f32_16x16x32_bf16 v[140:143], a[232:235], a[96:99], v[140:143]\n"
"v_mfma_f32_16x16x32_bf16 v[140:143], a[236:239], a[112:115], v[140:143]\n"
"s_waitcnt lgkmcnt(4)\n"
"v_mfma_f32_16x16x32_bf16 v[168:171], a[240:243], a[96:99], v[168:171]\n"
"ds_read_b64_tr_b16 a[232:233], v252 offset:512\n"
"v_mfma_f32_16x16x32_bf16 v[168:171], a[244:247], a[112:115], v[168:171]\n"
"ds_read_b64_tr_b16 a[234:235], v252 offset:768\n"
"v_mfma_f32_16x16x32_bf16 v[176:179], a[240:243], a[100:103], v[176:179]\n"
"v_mfma_f32_16x16x32_bf16 v[176:179], a[244:247], a[116:119], v[176:179]\n"
"v_mfma_f32_16x16x32_bf16 v[184:187], a[240:243], a[104:107], v[184:187]\n"
"ds_read_b64_tr_b16 a[236:237], v252 offset:4608\n"
"v_mfma_f32_16x16x32_bf16 v[184:187], a[244:247], a[120:123], v[184:187]\n"
"ds_read_b64_tr_b16 a[238:239], v252 offset:4864\n"
"v_mfma_f32_16x16x32_bf16 v[192:195], a[240:243], a[108:111], v[192:195]\n"
"v_mfma_f32_16x16x32_bf16 v[192:195], a[244:247], a[124:127], v[192:195]\n"
"s_waitcnt lgkmcnt(4)\n"
"v_mfma_f32_16x16x32_bf16 v[196:199], a[248:251], a[108:111], v[196:199]\n"
"ds_read_b64_tr_b16 a[240:241], v253 offset:512\n"
"v_mfma_f32_16x16x32_bf16 v[196:199], a[252:255], a[124:127], v[196:199]\n"
"ds_read_b64_tr_b16 a[242:243], v253 offset:768\n"
"v_mfma_f32_16x16x32_bf16 v[188:191], a[248:251], a[104:107], v[188:191]\n"
"v_mfma_f32_16x16x32_bf16 v[188:191], a[252:255], a[120:123], v[188:191]\n"
"v_mfma_f32_16x16x32_bf16 v[180:183], a[248:251], a[100:103], v[180:183]\n"
"ds_read_b64_tr_b16 a[244:245], v253 offset:4608\n"
"v_mfma_f32_16x16x32_bf16 v[180:183], a[252:255], a[116:119], v[180:183]\n"
"ds_read_b64_tr_b16 a[246:247], v253 offset:4864\n"
"v_mfma_f32_16x16x32_bf16 v[172:175], a[248:251], a[96:99], v[172:175]\n"
"v_mfma_f32_16x16x32_bf16 v[172:175], a[252:255], a[112:115], v[172:175]\n"
"s_waitcnt lgkmcnt(4)\n"
"v_mfma_f32_16x16x32_bf16 v[200:203], a[232:235], a[96:99], v[200:203]\n"
"s_waitcnt vmcnt(4)\n"
"s_barrier\n"
"ds_read_b64_tr_b16 a[248:249], v250 offset:0\n"
"v_mfma_f32_16x16x32_bf16 v[200:203], a[236:239], a[112:115], v[200:203]\n"
"ds_read_b64_tr_b16 a[250:251], v250 offset:256\n"
"v_mfma_f32_16x16x32_bf16 v[208:211], a[232:235], a[100:103], v[208:211]\n"
"ds_read_b128 a[128:131], v254 offset:16384\n"
"ds_read_b128 a[132:135], v254 offset:18432\n"
"v_mfma_f32_16x16x32_bf16 v[208:211], a[236:239], a[116:119], v[208:211]\n"
"ds_read_b128 a[136:139], v254 offset:20480\n"
"ds_read_b128 a[140:143], v254 offset:22528\n"
"v_mfma_f32_16x16x32_bf16 v[216:219], a[232:235], a[104:107], v[216:219]\n"
"ds_read_b64_tr_b16 a[252:253], v250 offset:4096\n"
"v_mfma_f32_16x16x32_bf16 v[216:219], a[236:239], a[120:123], v[216:219]\n"
"ds_read_b64_tr_b16 a[254:255], v250 offset:4352\n"
"v_mfma_f32_16x16x32_bf16 v[224:227], a[232:235], a[108:111], v[224:227]\n"
"ds_read_b128 a[144:147], v254 offset:16896\n"
"ds_read_b128 a[148:151], v254 offset:18944\n"
"v_mfma_f32_16x16x32_bf16 v[224:227], a[236:239], a[124:127], v[224:227]\n"
"s_waitcnt lgkmcnt(10)\n"
"ds_read_b128 a[152:155], v254 offset:20992\n"
"ds_read_b128 a[156:159], v254 offset:23040\n"
"v_mfma_f32_16x16x32_bf16 v[228:231], a[240:243], a[108:111], v[228:231]\n"
"ds_read_b64_tr_b16 a[232:233], v251 offset:0\n"
"v_mfma_f32_16x16x32_bf16 v[228:231], a[244:247], a[124:127], v[228:231]\n"
"ds_read_b64_tr_b16 a[234:235], v251 offset:256\n"
"v_mfma_f32_16x16x32_bf16 v[220:223], a[240:243], a[104:107], v[220:223]\n"
"v_mfma_f32_16x16x32_bf16 v[220:223], a[244:247], a[120:123], v[220:223]\n"
"s_waitcnt lgkmcnt(12)\n"
"v_mfma_f32_16x16x32_bf16 v[212:215], a[240:243], a[100:103], v[212:215]\n"
"ds_read_b64_tr_b16 a[236:237], v251 offset:4096\n"
"v_mfma_f32_16x16x32_bf16 v[212:215], a[244:247], a[116:119], v[212:215]\n"
"ds_read_b64_tr_b16 a[238:239], v251 offset:4352\n"
"v_mfma_f32_16x16x32_bf16 v[204:207], a[240:243], a[96:99], v[204:207]\n"
"v_mfma_f32_16x16x32_bf16 v[204:207], a[244:247], a[112:115], v[204:207]\n"
"s_waitcnt lgkmcnt(4)\n"
"v_mfma_f32_16x16x32_bf16 v[104:107], a[248:251], a[128:131], v[104:107]\n"
"ds_read_b64_tr_b16 a[240:241], v250 offset:512\n"
"v_mfma_f32_16x16x32_bf16 v[104:107], a[252:255], a[144:147], v[104:107]\n"
"ds_read_b64_tr_b16 a[242:243], v250 offset:768\n"
"v_mfma_f32_16x16x32_bf16 v[112:115], a[248:251], a[132:135], v[112:115]\n"
"v_mfma_f32_16x16x32_bf16 v[112:115], a[252:255], a[148:151], v[112:115]\n"
"v_mfma_f32_16x16x32_bf16 v[120:123], a[248:251], a[136:139], v[120:123]\n"
"ds_read_b64_tr_b16 a[244:245], v250 offset:4608\n"
"v_mfma_f32_16x16x32_bf16 v[120:123], a[252:255], a[152:155], v[120:123]\n"
"ds_read_b64_tr_b16 a[246:247], v250 offset:4864\n"
"v_mfma_f32_16x16x32_bf16 v[128:131], a[248:251], a[140:143], v[128:131]\n"
"v_mfma_f32_16x16x32_bf16 v[128:131], a[252:255], a[156:159], v[128:131]\n"
"s_waitcnt lgkmcnt(4)\n"
"v_mfma_f32_16x16x32_bf16 v[132:135], a[232:235], a[140:143], v[132:135]\n"
"ds_read_b64_tr_b16 a[248:249], v251 offset:512\n"
"v_mfma_f32_16x16x32_bf16 v[132:135], a[236:239], a[156:159], v[132:135]\n"
"s_add_u32 s79, s79, 128\n"
"ds_read_b64_tr_b16 a[250:251], v251 offset:768\n"
"v_mfma_f32_16x16x32_bf16 v[124:127], a[232:235], a[136:139], v[124:127]\n"
"s_sub_u32 s78, s79, 896\n"
"v_mfma_f32_16x16x32_bf16 v[124:127], a[236:239], a[152:155], v[124:127]\n"
"s_min_u32 s79, s79, s78\n"
"v_add_u32 v242, s79, v243\n"
"s_add_u32 m0, s71, 0\n"
"v_mfma_f32_16x16x32_bf16 v[116:119], a[232:235], a[132:135], v[116:119]\n"
"buffer_load_dwordx4 v242, s[32:35], 0 offen offset:0  lds\n"
"ds_read_b64_tr_b16 a[252:253], v251 offset:4608\n"
"v_mfma_f32_16x16x32_bf16 v[116:119], a[236:239], a[148:151], v[116:119]\n"
"ds_read_b64_tr_b16 a[254:255], v251 offset:4864\n"
"v_mfma_f32_16x16x32_bf16 v[108:111], a[232:235], a[128:131], v[108:111]\n"
"v_mfma_f32_16x16x32_bf16 v[108:111], a[236:239], a[144:147], v[108:111]\n"
"s_waitcnt lgkmcnt(4)\n"
"buffer_load_dwordx4 v242, s[32:35], s64 offen offset:1024  lds\n"
"v_mfma_f32_16x16x32_bf16 v[136:139], a[240:243], a[128:131], v[136:139]\n"
"s_barrier\n"
"ds_read_b64_tr_b16 a[232:233], v250 offset:16384\n"
"v_mfma_f32_16x16x32_bf16 v[136:139], a[244:247], a[144:147], v[136:139]\n"
"ds_read_b64_tr_b16 a[234:235], v250 offset:16640\n"
"v_mfma_f32_16x16x32_bf16 v[144:147], a[240:243], a[132:135], v[144:147]\n"
"buffer_load_dwordx4 v242, s[32:35], s65 offen offset:2048  lds\n"
"v_mfma_f32_16x16x32_bf16 v[144:147], a[244:247], a[148:151], v[144:147]\n"
"v_mfma_f32_16x16x32_bf16 v[152:155], a[240:243], a[136:139], v[152:155]\n"
"ds_read_b64_tr_b16 a[236:237], v250 offset:20480\n"
"v_mfma_f32_16x16x32_bf16 v[152:155], a[244:247], a[152:155], v[152:155]\n"
"ds_read_b64_tr_b16 a[238:239], v250 offset:20736\n"
"v_mfma_f32_16x16x32_bf16 v[160:163], a[240:243], a[140:143], v[160:163]\n"
"buffer_load_dwordx4 v242, s[32:35], s66 offen offset:3072  lds\n"
"v_mfma_f32_16x16x32_bf16 v[160:163], a[244:247], a[156:159], v[160:163]\n"
"s_waitcnt lgkmcnt(4)\n"
"v_mfma_f32_16x16x32_bf16 v[164:167], a[248:251], a[140:143], v[164:167]\n"
"ds_read_b64_tr_b16 a[240:241], v251 offset:16384\n"
"v_mfma_f32_16x16x32_bf16 v[164:167], a[252:255], a[156:159], v[164:167]\n"
"ds_read_b64_tr_b16 a[242:243], v251 offset:16640\n"
"v_mfma_f32_16x16x32_bf16 v[156:159], a[248:251], a[136:139], v[156:159]\n"
"v_mfma_f32_16x16x32_bf16 v[156:159], a[252:255], a[152:155], v[156:159]\n"
"v_mfma_f32_16x16x32_bf16 v[148:151], a[248:251], a[132:135], v[148:151]\n"
"ds_read_b64_tr_b16 a[244:245], v251 offset:20480\n"
"v_mfma_f32_16x16x32_bf16 v[148:151], a[252:255], a[148:151], v[148:151]\n"
"ds_read_b64_tr_b16 a[246:247], v251 offset:20736\n"
"v_mfma_f32_16x16x32_bf16 v[140:143], a[248:251], a[128:131], v[140:143]\n"
"v_mfma_f32_16x16x32_bf16 v[140:143], a[252:255], a[144:147], v[140:143]\n"
"s_waitcnt lgkmcnt(4)\n"
"v_mfma_f32_16x16x32_bf16 v[168:171], a[232:235], a[128:131], v[168:171]\n"
"ds_read_b64_tr_b16 a[248:249], v250 offset:16896\n"
"v_mfma_f32_16x16x32_bf16 v[168:171], a[236:239], a[144:147], v[168:171]\n"
"ds_read_b64_tr_b16 a[250:251], v250 offset:17152\n"
"v_mfma_f32_16x16x32_bf16 v[176:179], a[232:235], a[132:135], v[176:179]\n"
"v_mfma_f32_16x16x32_bf16 v[176:179], a[236:239], a[148:151], v[176:179]\n"
"v_mfma_f32_16x16x32_bf16 v[184:187], a[232:235], a[136:139], v[184:187]\n"
"ds_read_b64_tr_b16 a[252:253], v250 offset:20992\n"
"v_mfma_f32_16x16x32_bf16 v[184:187], a[236:239], a[152:155], v[184:187]\n"
"ds_read_b64_tr_b16 a[254:255], v250 offset:21248\n"
"v_mfma_f32_16x16x32_bf16 v[192:195], a[232:235], a[140:143], v[192:195]\n"
"v_mfma_f32_16x16x32_bf16 v[192:195], a[236:239], a[156:159], v[192:195]\n"
"s_waitcnt lgkmcnt(4)\n"
"v_mfma_f32_16x16x32_bf16 v[196:199], a[240:243], a[140:143], v[196:199]\n"
"ds_read_b64_tr_b16 a[232:233], v251 offset:16896\n"
"v_mfma_f32_16x16x32_bf16 v[196:199], a[244:247], a[156:159], v[196:199]\n"
"ds_read_b64_tr_b16 a[234:235], v251 offset:17152\n"
"v_mfma_f32_16x16x32_bf16 v[188:191], a[240:243], a[136:139], v[188:191]\n"
"v_mfma_f32_16x16x32_bf16 v[188:191], a[244:247], a[152:155], v[188:191]\n"
"v_add_u32 v242, 0, v245\n"
"s_add_u32 m0, s85, 131072\n"
"v_mfma_f32_16x16x32_bf16 v[180:183], a[240:243], a[132:135], v[180:183]\n"
"buffer_load_dwordx4 v242, s[40:43], 0 offen offset:0 sc0 sc1 nt lds\n"
"ds_read_b64_tr_b16 a[236:237], v251 offset:20992\n"
"v_mfma_f32_16x16x32_bf16 v[180:183], a[244:247], a[148:151], v[180:183]\n"
"ds_read_b64_tr_b16 a[238:239], v251 offset:21248\n"
"v_mfma_f32_16x16x32_bf16 v[172:175], a[240:243], a[128:131], v[172:175]\n"
"v_mfma_f32_16x16x32_bf16 v[172:175], a[244:247], a[144:147], v[172:175]\n"
"s_waitcnt lgkmcnt(4)\n"
"v_mfma_f32_16x16x32_bf16 v[200:203], a[248:251], a[128:131], v[200:203]\n"
"s_waitcnt vmcnt(5)\n"
"s_barrier\n"
"ds_read_b64_tr_b16 a[240:241], v250 offset:32768\n"
"v_mfma_f32_16x16x32_bf16 v[200:203], a[252:255], a[144:147], v[200:203]\n"
"buffer_load_dwordx4 v242, s[40:43], s61 offen offset:1024 sc0 sc1 nt lds\n"
"ds_read_b64_tr_b16 a[242:243], v250 offset:33024\n"
"v_mfma_f32_16x16x32_bf16 v[208:211], a[248:251], a[132:135], v[208:211]\n"
"ds_read_b128 a[160:163], v254 offset:32768\n"
"ds_read_b128 a[164:167], v254 offset:34816\n"
"v_mfma_f32_16x16x32_bf16 v[208:211], a[252:255], a[148:151], v[208:211]\n"
"ds_read_b128 a[168:171], v254 offset:36864\n"
"ds_read_b128 a[172:175], v254 offset:38912\n"
"v_mfma_f32_16x16x32_bf16 v[216:219], a[248:251], a[136:139], v[216:219]\n"
"buffer_load_dwordx4 v242, s[40:43], s62 offen offset:2048 sc0 sc1 nt lds\n"
"ds_read_b64_tr_b16 a[244:245], v250 offset:36864\n"
"v_mfma_f32_16x16x32_bf16 v[216:219], a[252:255], a[152:155], v[216:219]\n"
"ds_read_b64_tr_b16 a[246:247], v250 offset:37120\n"
"v_mfma_f32_16x16x32_bf16 v[224:227], a[248:251], a[140:143], v[224:227]\n"
"ds_read_b128 a[176:179], v254 offset:33280\n"
"ds_read_b128 a[180:183], v254 offset:35328\n"
"v_mfma_f32_16x16x32_bf16 v[224:227], a[252:255], a[156:159], v[224:227]\n"
"s_waitcnt lgkmcnt(10)\n"
"ds_read_b128 a[184:187], v254 offset:37376\n"
"ds_read_b128 a[188:191], v254 offset:39424\n"
"v_mfma_f32_16x16x32_bf16 v[228:231], a[232:235], a[140:143], v[228:231]\n"
"buffer_load_dwordx4 v242, s[40:43], s63 offen offset:3072 sc0 sc1 nt lds\n"
"ds_read_b64_tr_b16 a[248:249], v251 offset:32768\n"
"v_mfma_f32_16x16x32_bf16 v[228:231], a[236:239], a[156:159], v[228:231]\n"
"ds_read_b64_tr_b16 a[250:251], v251 offset:33024\n"
"v_mfma_f32_16x16x32_bf16 v[220:223], a[232:235], a[136:139], v[220:223]\n"
"v_mfma_f32_16x16x32_bf16 v[220:223], a[236:239], a[152:155], v[220:223]\n"
"s_waitcnt lgkmcnt(12)\n"
"v_mfma_f32_16x16x32_bf16 v[212:215], a[232:235], a[132:135], v[212:215]\n"
"ds_read_b64_tr_b16 a[252:253], v251 offset:36864\n"
"v_mfma_f32_16x16x32_bf16 v[212:215], a[236:239], a[148:151], v[212:215]\n"
"ds_read_b64_tr_b16 a[254:255], v251 offset:37120\n"
"v_mfma_f32_16x16x32_bf16 v[204:207], a[232:235], a[128:131], v[204:207]\n"
"v_mfma_f32_16x16x32_bf16 v[204:207], a[236:239], a[144:147], v[204:207]\n"
"s_waitcnt lgkmcnt(4)\n"
"v_mfma_f32_16x16x32_bf16 v[104:107], a[240:243], a[160:163], v[104:107]\n"
"ds_read_b64_tr_b16 a[232:233], v250 offset:33280\n"
"v_mfma_f32_16x16x32_bf16 v[104:107], a[244:247], a[176:179], v[104:107]\n"
"ds_read_b64_tr_b16 a[234:235], v250 offset:33536\n"
"v_mfma_f32_16x16x32_bf16 v[112:115], a[240:243], a[164:167], v[112:115]\n"
"v_mfma_f32_16x16x32_bf16 v[112:115], a[244:247], a[180:183], v[112:115]\n"
"v_mfma_f32_16x16x32_bf16 v[120:123], a[240:243], a[168:171], v[120:123]\n"
"ds_read_b64_tr_b16 a[236:237], v250 offset:37376\n"
"v_mfma_f32_16x16x32_bf16 v[120:123], a[244:247], a[184:187], v[120:123]\n"
"ds_read_b64_tr_b16 a[238:239], v250 offset:37632\n"
"v_mfma_f32_16x16x32_bf16 v[128:131], a[240:243], a[172:175], v[128:131]\n"
"v_mfma_f32_16x16x32_bf16 v[128:131], a[244:247], a[188:191], v[128:131]\n"
"s_waitcnt lgkmcnt(4)\n"
"v_mfma_f32_16x16x32_bf16 v[132:135], a[248:251], a[172:175], v[132:135]\n"
"ds_read_b64_tr_b16 a[240:241], v251 offset:33280\n"
"v_mfma_f32_16x16x32_bf16 v[132:135], a[252:255], a[188:191], v[132:135]\n"
"ds_read_b64_tr_b16 a[242:243], v251 offset:33536\n"
"v_mfma_f32_16x16x32_bf16 v[124:127], a[248:251], a[168:171], v[124:127]\n"
"v_mfma_f32_16x16x32_bf16 v[124:127], a[252:255], a[184:187], v[124:127]\n"
"v_add_u32 v242, 128, v245\n"
"s_add_u32 m0, s85, 135168\n"
"v_mfma_f32_16x16x32_bf16 v[116:119], a[248:251], a[164:167], v[116:119]\n"
"buffer_load_dwordx4 v242, s[40:43], 0 offen offset:0 sc0 sc1 nt lds\n"
"ds_read_b64_tr_b16 a[244:245], v251 offset:37376\n"
"v_mfma_f32_16x16x32_bf16 v[116:119], a[252:255], a[180:183], v[116:119]\n"
"ds_read_b64_tr_b16 a[246:247], v251 offset:37632\n"
"v_mfma_f32_16x16x32_bf16 v[108:111], a[248:251], a[160:163], v[108:111]\n"
"v_mfma_f32_16x16x32_bf16 v[108:111], a[252:255], a[176:179], v[108:111]\n"
"s_waitcnt lgkmcnt(4)\n"
"v_mfma_f32_16x16x32_bf16 v[136:139], a[232:235], a[160:163], v[136:139]\n"
"s_barrier\n"
"buffer_load_dwordx4 v242, s[40:43], s61 offen offset:1024 sc0 sc1 nt lds\n"
"ds_read_b64_tr_b16 a[248:249], v250 offset:49152\n"
"v_mfma_f32_16x16x32_bf16 v[136:139], a[236:239], a[176:179], v[136:139]\n"
"ds_read_b64_tr_b16 a[250:251], v250 offset:49408\n"
"v_mfma_f32_16x16x32_bf16 v[144:147], a[232:235], a[164:167], v[144:147]\n"
"v_mfma_f32_16x16x32_bf16 v[144:147], a[236:239], a[180:183], v[144:147]\n"
"v_mfma_f32_16x16x32_bf16 v[152:155], a[232:235], a[168:171], v[152:155]\n"
"buffer_load_dwordx4 v242, s[40:43], s62 offen offset:2048 sc0 sc1 nt lds\n"
"ds_read_b64_tr_b16 a[252:253], v250 offset:53248\n"
"v_mfma_f32_16x16x32_bf16 v[152:155], a[236:239], a[184:187], v[152:155]\n"
"ds_read_b64_tr_b16 a[254:255], v250 offset:53504\n"
"v_mfma_f32_16x16x32_bf16 v[160:163], a[232:235], a[172:175], v[160:163]\n"
"v_mfma_f32_16x16x32_bf16 v[160:163], a[236:239], a[188:191], v[160:163]\n"
"s_waitcnt lgkmcnt(4)\n"
"v_mfma_f32_16x16x32_bf16 v[164:167], a[240:243], a[172:175], v[164:167]\n"
"buffer_load_dwordx4 v242, s[40:43], s63 offen offset:3072 sc0 sc1 nt lds\n"
"ds_read_b64_tr_b16 a[232:233], v251 offset:49152\n"
"v_mfma_f32_16x16x32_bf16 v[164:167], a[244:247], a[188:191], v[164:167]\n"
"ds_read_b64_tr_b16 a[234:235], v251 offset:49408\n"
"v_mfma_f32_16x16x32_bf16 v[156:159], a[240:243], a[168:171], v[156:159]\n"
"v_mfma_f32_16x16x32_bf16 v[156:159], a[244:247], a[184:187], v[156:159]\n"
"v_mfma_f32_16x16x32_bf16 v[148:151], a[240:243], a[164:167], v[148:151]\n"
"ds_read_b64_tr_b16 a[236:237], v251 offset:53248\n"
"v_mfma_f32_16x16x32_bf16 v[148:151], a[244:247], a[180:183], v[148:151]\n"
"ds_read_b64_tr_b16 a[238:239], v251 offset:53504\n"
"v_mfma_f32_16x16x32_bf16 v[140:143], a[240:243], a[160:163], v[140:143]\n"
"v_mfma_f32_16x16x32_bf16 v[140:143], a[244:247], a[176:179], v[140:143]\n"
"s_waitcnt lgkmcnt(4)\n"
"v_mfma_f32_16x16x32_bf16 v[168:171], a[248:251], a[160:163], v[168:171]\n"
"ds_read_b64_tr_b16 a[240:241], v250 offset:49664\n"
"v_mfma_f32_16x16x32_bf16 v[168:171], a[252:255], a[176:179], v[168:171]\n"
"ds_read_b64_tr_b16 a[242:243], v250 offset:49920\n"
"v_mfma_f32_16x16x32_bf16 v[176:179], a[248:251], a[164:167], v[176:179]\n"
"v_mfma_f32_16x16x32_bf16 v[176:179], a[252:255], a[180:183], v[176:179]\n"
"v_mfma_f32_16x16x32_bf16 v[184:187], a[248:251], a[168:171], v[184:187]\n"
"ds_read_b64_tr_b16 a[244:245], v250 offset:53760\n"
"v_mfma_f32_16x16x32_bf16 v[184:187], a[252:255], a[184:187], v[184:187]\n"
"ds_read_b64_tr_b16 a[246:247], v250 offset:54016\n"
"v_mfma_f32_16x16x32_bf16 v[192:195], a[248:251], a[172:175], v[192:195]\n"
"v_mfma_f32_16x16x32_bf16 v[192:195], a[252:255], a[188:191], v[192:195]\n"
"s_waitcnt lgkmcnt(4)\n"
"v_mfma_f32_16x16x32_bf16 v[196:199], a[232:235], a[172:175], v[196:199]\n"
"ds_read_b64_tr_b16 a[248:249], v251 offset:49664\n"
"v_mfma_f32_16x16x32_bf16 v[196:199], a[236:239], a[188:191], v[196:199]\n"
"ds_read_b64_tr_b16 a[250:251], v251 offset:49920\n"
"v_mfma_f32_16x16x32_bf16 v[188:191], a[232:235], a[168:171], v[188:191]\n"
"v_mfma_f32_16x16x32_bf16 v[188:191], a[236:239], a[184:187], v[188:191]\n"
"v_mfma_f32_16x16x32_bf16 v[180:183], a[232:235], a[164:167], v[180:183]\n"
"ds_read_b64_tr_b16 a[252:253], v251 offset:53760\n"
"v_mfma_f32_16x16x32_bf16 v[180:183], a[236:239], a[180:183], v[180:183]\n"
"ds_read_b64_tr_b16 a[254:255], v251 offset:54016\n"
"v_mfma_f32_16x16x32_bf16 v[172:175], a[232:235], a[160:163], v[172:175]\n"
"v_mfma_f32_16x16x32_bf16 v[172:175], a[236:239], a[176:179], v[172:175]\n"
"s_waitcnt lgkmcnt(4)\n"
"v_mfma_f32_16x16x32_bf16 v[200:203], a[240:243], a[160:163], v[200:203]\n"
"s_waitcnt vmcnt(8)\n"
"s_barrier\n"
"ds_read_b64_tr_b16 a[232:233], v252 offset:0\n"
"v_mfma_f32_16x16x32_bf16 v[200:203], a[244:247], a[176:179], v[200:203]\n"
"ds_read_b64_tr_b16 a[234:235], v252 offset:256\n"
"v_mfma_f32_16x16x32_bf16 v[208:211], a[240:243], a[164:167], v[208:211]\n"
"ds_read_b128 a[192:195], v254 offset:0\n"
"ds_read_b128 a[196:199], v254 offset:2048\n"
"v_mfma_f32_16x16x32_bf16 v[208:211], a[244:247], a[180:183], v[208:211]\n"
"ds_read_b128 a[200:203], v254 offset:4096\n"
"ds_read_b128 a[204:207], v254 offset:6144\n"
"v_mfma_f32_16x16x32_bf16 v[216:219], a[240:243], a[168:171], v[216:219]\n"
"ds_read_b64_tr_b16 a[236:237], v252 offset:4096\n"
"v_mfma_f32_16x16x32_bf16 v[216:219], a[244:247], a[184:187], v[216:219]\n"
"ds_read_b64_tr_b16 a[238:239], v252 offset:4352\n"
"v_mfma_f32_16x16x32_bf16 v[224:227], a[240:243], a[172:175], v[224:227]\n"
"ds_read_b128 a[208:211], v254 offset:512\n"
"ds_read_b128 a[212:215], v254 offset:2560\n"
"v_mfma_f32_16x16x32_bf16 v[224:227], a[244:247], a[188:191], v[224:227]\n"
"s_waitcnt lgkmcnt(10)\n"
"ds_read_b128 a[216:219], v254 offset:4608\n"
"ds_read_b128 a[220:223], v254 offset:6656\n"
"v_mfma_f32_16x16x32_bf16 v[228:231], a[248:251], a[172:175], v[228:231]\n"
"ds_read_b64_tr_b16 a[240:241], v253 offset:0\n"
"v_mfma_f32_16x16x32_bf16 v[228:231], a[252:255], a[188:191], v[228:231]\n"
"ds_read_b64_tr_b16 a[242:243], v253 offset:256\n"
"v_mfma_f32_16x16x32_bf16 v[220:223], a[248:251], a[168:171], v[220:223]\n"
"v_mfma_f32_16x16x32_bf16 v[220:223], a[252:255], a[184:187], v[220:223]\n"
"s_waitcnt lgkmcnt(12)\n"
"v_add_u32 v242, 16384, v245\n"
"s_add_u32 m0, s85, 147456\n"
"v_mfma_f32_16x16x32_bf16 v[212:215], a[248:251], a[164:167], v[212:215]\n"
"buffer_load_dwordx4 v242, s[40:43], 0 offen offset:0 sc0 sc1 nt lds\n"
"ds_read_b64_tr_b16 a[244:245], v253 offset:4096\n"
"v_mfma_f32_16x16x32_bf16 v[212:215], a[252:255], a[180:183], v[212:215]\n"
"ds_read_b64_tr_b16 a[246:247], v253 offset:4352\n"
"v_mfma_f32_16x16x32_bf16 v[204:207], a[248:251], a[160:163], v[204:207]\n"
"v_mfma_f32_16x16x32_bf16 v[204:207], a[252:255], a[176:179], v[204:207]\n"
"s_waitcnt lgkmcnt(4)\n"
"v_mfma_f32_16x16x32_bf16 v[104:107], a[232:235], a[192:195], v[104:107]\n"
"buffer_load_dwordx4 v242, s[40:43], s61 offen offset:1024 sc0 sc1 nt lds\n"
"ds_read_b64_tr_b16 a[248:249], v252 offset:512\n"
"v_mfma_f32_16x16x32_bf16 v[104:107], a[236:239], a[208:211], v[104:107]\n"
"ds_read_b64_tr_b16 a[250:251], v252 offset:768\n"
"v_mfma_f32_16x16x32_bf16 v[112:115], a[232:235], a[196:199], v[112:115]\n"
"v_mfma_f32_16x16x32_bf16 v[112:115], a[236:239], a[212:215], v[112:115]\n"
"v_mfma_f32_16x16x32_bf16 v[120:123], a[232:235], a[200:203], v[120:123]\n"
"buffer_load_dwordx4 v242, s[40:43], s62 offen offset:2048 sc0 sc1 nt lds\n"
"ds_read_b64_tr_b16 a[252:253], v252 offset:4608\n"
"v_mfma_f32_16x16x32_bf16 v[120:123], a[236:239], a[216:219], v[120:123]\n"
"ds_read_b64_tr_b16 a[254:255], v252 offset:4864\n"
"v_mfma_f32_16x16x32_bf16 v[128:131], a[232:235], a[204:207], v[128:131]\n"
"v_mfma_f32_16x16x32_bf16 v[128:131], a[236:239], a[220:223], v[128:131]\n"
"s_waitcnt lgkmcnt(4)\n"
"v_mfma_f32_16x16x32_bf16 v[132:135], a[240:243], a[204:207], v[132:135]\n"
"buffer_load_dwordx4 v242, s[40:43], s63 offen offset:3072 sc0 sc1 nt lds\n"
"ds_read_b64_tr_b16 a[232:233], v253 offset:512\n"
"v_mfma_f32_16x16x32_bf16 v[132:135], a[244:247], a[220:223], v[132:135]\n"
"ds_read_b64_tr_b16 a[234:235], v253 offset:768\n"
"v_mfma_f32_16x16x32_bf16 v[124:127], a[240:243], a[200:203], v[124:127]\n"
"v_mfma_f32_16x16x32_bf16 v[124:127], a[244:247], a[216:219], v[124:127]\n"
"v_mfma_f32_16x16x32_bf16 v[116:119], a[240:243], a[196:199], v[116:119]\n"
"ds_read_b64_tr_b16 a[236:237], v253 offset:4608\n"
"v_mfma_f32_16x16x32_bf16 v[116:119], a[244:247], a[212:215], v[116:119]\n"
"ds_read_b64_tr_b16 a[238:239], v253 offset:4864\n"
"v_mfma_f32_16x16x32_bf16 v[108:111], a[240:243], a[192:195], v[108:111]\n"
"v_mfma_f32_16x16x32_bf16 v[108:111], a[244:247], a[208:211], v[108:111]\n"
"s_waitcnt lgkmcnt(4)\n"
"v_mfma_f32_16x16x32_bf16 v[136:139], a[248:251], a[192:195], v[136:139]\n"
"s_barrier\n"
"ds_read_b64_tr_b16 a[240:241], v250 offset:0\n"
"v_mfma_f32_16x16x32_bf16 v[136:139], a[252:255], a[208:211], v[136:139]\n"
"ds_read_b64_tr_b16 a[242:243], v250 offset:256\n"
"v_mfma_f32_16x16x32_bf16 v[144:147], a[248:251], a[196:199], v[144:147]\n"
"v_mul_f32 v112, s70, v112\n"
"v_mul_f32 v113, s70, v113\n"
"v_mfma_f32_16x16x32_bf16 v[144:147], a[252:255], a[212:215], v[144:147]\n"
"v_mul_f32 v114, s70, v114\n"
"v_mul_f32 v115, s70, v115\n"
"v_mfma_f32_16x16x32_bf16 v[152:155], a[248:251], a[200:203], v[152:155]\n"
"ds_read_b64_tr_b16 a[244:245], v250 offset:4096\n"
"v_mul_f32 v104, s70, v104\n"
"v_mfma_f32_16x16x32_bf16 v[152:155], a[252:255], a[216:219], v[152:155]\n"
"ds_read_b64_tr_b16 a[246:247], v250 offset:4352\n"
"v_mul_f32 v105, s70, v105\n"
"v_mfma_f32_16x16x32_bf16 v[160:163], a[248:251], a[204:207], v[160:163]\n"
"v_mul_f32 v106, s70, v106\n"
"v_mul_f32 v107, s70, v107\n"
"v_mfma_f32_16x16x32_bf16 v[160:163], a[252:255], a[220:223], v[160:163]\n"
"s_waitcnt lgkmcnt(4)\n"
"v_cvt_pk_bf16_f32 v104, v104, v105\n"
"v_cvt_pk_bf16_f32 v105, v106, v107\n"
"v_mfma_f32_16x16x32_bf16 v[164:167], a[232:235], a[204:207], v[164:167]\n"
"ds_read_b64_tr_b16 a[248:249], v251 offset:0\n"
"v_mul_f32 v108, s70, v108\n"
"v_mfma_f32_16x16x32_bf16 v[164:167], a[236:239], a[220:223], v[164:167]\n"
"ds_read_b64_tr_b16 a[250:251], v251 offset:256\n"
"v_mul_f32 v109, s70, v109\n"
"v_mfma_f32_16x16x32_bf16 v[156:159], a[232:235], a[200:203], v[156:159]\n"
"v_mul_f32 v110, s70, v110\n"
"v_mul_f32 v111, s70, v111\n"
"v_mfma_f32_16x16x32_bf16 v[156:159], a[236:239], a[216:219], v[156:159]\n"
"v_add_u32 v242, 16512, v245\n"
"s_add_u32 m0, s85, 151552\n"
"v_mfma_f32_16x16x32_bf16 v[148:151], a[232:235], a[196:199], v[148:151]\n"
"buffer_load_dwordx4 v242, s[40:43], 0 offen offset:0 sc0 sc1 nt lds\n"
"ds_read_b64_tr_b16 a[252:253], v251 offset:4096\n"
"v_mfma_f32_16x16x32_bf16 v[148:151], a[236:239], a[212:215], v[148:151]\n"
"ds_read_b64_tr_b16 a[254:255], v251 offset:4352\n"
"v_cvt_pk_bf16_f32 v106, v108, v109\n"
"v_mfma_f32_16x16x32_bf16 v[140:143], a[232:235], a[192:195], v[140:143]\n"
"v_cvt_pk_bf16_f32 v107, v110, v111\n"
"v_cvt_pk_bf16_f32 v112, v112, v113\n"
"v_mfma_f32_16x16x32_bf16 v[140:143], a[236:239], a[208:211], v[140:143]\n"
"s_waitcnt lgkmcnt(4)\n"
"v_permlane16_swap_b32 v104, v106\n"
"v_mfma_f32_16x16x32_bf16 v[168:171], a[240:243], a[192:195], v[168:171]\n"
"buffer_load_dwordx4 v242, s[40:43], s61 offen offset:1024 sc0 sc1 nt lds\n"
"ds_read_b64_tr_b16 a[232:233], v250 offset:512\n"
"v_mfma_f32_16x16x32_bf16 v[168:171], a[244:247], a[208:211], v[168:171]\n"
"ds_read_b64_tr_b16 a[234:235], v250 offset:768\n"
"v_cvt_pk_bf16_f32 v113, v114, v115\n"
"v_mfma_f32_16x16x32_bf16 v[176:179], a[240:243], a[196:199], v[176:179]\n"
"v_permlane16_swap_b32 v105, v107\n"
"v_mfma_f32_16x16x32_bf16 v[176:179], a[244:247], a[212:215], v[176:179]\n"
"v_mul_f32 v116, s70, v116\n"
"v_mul_f32 v117, s70, v117\n"
"v_mfma_f32_16x16x32_bf16 v[184:187], a[240:243], a[200:203], v[184:187]\n"
"buffer_load_dwordx4 v242, s[40:43], s62 offen offset:2048 sc0 sc1 nt lds\n"
"ds_read_b64_tr_b16 a[236:237], v250 offset:4608\n"
"v_mfma_f32_16x16x32_bf16 v[184:187], a[244:247], a[216:219], v[184:187]\n"
"ds_read_b64_tr_b16 a[238:239], v250 offset:4864\n"
"v_mul_f32 v118, s70, v118\n"
"v_mfma_f32_16x16x32_bf16 v[192:195], a[240:243], a[204:207], v[192:195]\n"
"v_mul_f32 v119, s70, v119\n"
"v_cvt_pk_bf16_f32 v114, v116, v117\n"
"v_mfma_f32_16x16x32_bf16 v[192:195], a[244:247], a[220:223], v[192:195]\n"
"s_waitcnt lgkmcnt(4)\n"
"v_cvt_pk_bf16_f32 v115, v118, v119\n"
"v_mul_f32 v120, s70, v120\n"
"v_mfma_f32_16x16x32_bf16 v[196:199], a[248:251], a[204:207], v[196:199]\n"
"buffer_load_dwordx4 v242, s[40:43], s63 offen offset:3072 sc0 sc1 nt lds\n"
"ds_read_b64_tr_b16 a[240:241], v251 offset:512\n"
"v_mfma_f32_16x16x32_bf16 v[196:199], a[252:255], a[220:223], v[196:199]\n"
"ds_read_b64_tr_b16 a[242:243], v251 offset:768\n"
"v_mul_f32 v121, s70, v121\n"
"v_mfma_f32_16x16x32_bf16 v[188:191], a[248:251], a[200:203], v[188:191]\n"
"v_permlane16_swap_b32 v112, v114\n"
"v_mfma_f32_16x16x32_bf16 v[188:191], a[252:255], a[216:219], v[188:191]\n"
"v_permlane16_swap_b32 v113, v115\n"
"v_mfma_f32_16x16x32_bf16 v[180:183], a[248:251], a[196:199], v[180:183]\n"
"ds_read_b64_tr_b16 a[244:245], v251 offset:4608\n"
"v_mul_f32 v122, s70, v122\n"
"v_mfma_f32_16x16x32_bf16 v[180:183], a[252:255], a[212:215], v[180:183]\n"
"ds_read_b64_tr_b16 a[246:247], v251 offset:4864\n"
"v_mul_f32 v123, s70, v123\n"
"v_mfma_f32_16x16x32_bf16 v[172:175], a[248:251], a[192:195], v[172:175]\n"
"v_cvt_pk_bf16_f32 v120, v120, v121\n"
"v_cvt_pk_bf16_f32 v121, v122, v123\n"
"v_mfma_f32_16x16x32_bf16 v[172:175], a[252:255], a[208:211], v[172:175]\n"
"s_waitcnt lgkmcnt(4)\n"
"v_mul_f32 v124, s70, v124\n"
"v_mul_f32 v125, s70, v125\n"
"v_mfma_f32_16x16x32_bf16 v[200:203], a[232:235], a[192:195], v[200:203]\n"
"s_barrier\n"
"ds_read_b64_tr_b16 a[248:249], v250 offset:16384\n"
"v_mul_f32 v126, s70, v126\n"
"v_mfma_f32_16x16x32_bf16 v[200:203], a[236:239], a[208:211], v[200:203]\n"
"ds_read_b64_tr_b16 a[250:251], v250 offset:16640\n"
"v_mul_f32 v127, s70, v127\n"
"v_mfma_f32_16x16x32_bf16 v[208:211], a[232:235], a[196:199], v[208:211]\n"
"v_cvt_pk_bf16_f32 v122, v124, v125\n"
"v_cvt_pk_bf16_f32 v123, v126, v127\n"
"v_mfma_f32_16x16x32_bf16 v[208:211], a[236:239], a[212:215], v[208:211]\n"
"v_mul_f32 v128, s70, v128\n"
"v_mul_f32 v129, s70, v129\n"
"v_mfma_f32_16x16x32_bf16 v[216:219], a[232:235], a[200:203], v[216:219]\n"
"ds_read_b64_tr_b16 a[252:253], v250 offset:20480\n"
"v_mul_f32 v130, s70, v130\n"
"v_mfma_f32_16x16x32_bf16 v[216:219], a[236:239], a[216:219], v[216:219]\n"
"ds_read_b64_tr_b16 a[254:255], v250 offset:20736\n"
"v_mul_f32 v131, s70, v131\n"
"v_mfma_f32_16x16x32_bf16 v[224:227], a[232:235], a[204:207], v[224:227]\n"
"v_permlane16_swap_b32 v120, v122\n"
"v_mfma_f32_16x16x32_bf16 v[224:227], a[236:239], a[220:223], v[224:227]\n"
"s_waitcnt lgkmcnt(4)\n"
"v_permlane16_swap_b32 v121, v123\n"
"v_mfma_f32_16x16x32_bf16 v[228:231], a[240:243], a[204:207], v[228:231]\n"
"ds_read_b64_tr_b16 a[232:233], v251 offset:16384\n"
"v_cvt_pk_bf16_f32 v128, v128, v129\n"
"v_mfma_f32_16x16x32_bf16 v[228:231], a[244:247], a[220:223], v[228:231]\n"
"ds_read_b64_tr_b16 a[234:235], v251 offset:16640\n"
"v_cvt_pk_bf16_f32 v129, v130, v131\n"
"v_mfma_f32_16x16x32_bf16 v[220:223], a[240:243], a[200:203], v[220:223]\n"
"v_mul_f32 v132, s70, v132\n"
"v_mul_f32 v133, s70, v133\n"
"v_mfma_f32_16x16x32_bf16 v[220:223], a[244:247], a[216:219], v[220:223]\n"
"v_mul_f32 v134, s70, v134\n"
"v_mul_f32 v135, s70, v135\n"
"v_mfma_f32_16x16x32_bf16 v[212:215], a[240:243], a[196:199], v[212:215]\n"
"ds_read_b64_tr_b16 a[236:237], v251 offset:20480\n"
"v_cvt_pk_bf16_f32 v130, v132, v133\n"
"v_mfma_f32_16x16x32_bf16 v[212:215], a[244:247], a[212:215], v[212:215]\n"
"ds_read_b64_tr_b16 a[238:239], v251 offset:20736\n"
"v_cvt_pk_bf16_f32 v131, v134, v135\n"
"v_mfma_f32_16x16x32_bf16 v[204:207], a[240:243], a[192:195], v[204:207]\n"
"v_permlane16_swap_b32 v128, v130\n"
"v_mfma_f32_16x16x32_bf16 v[204:207], a[244:247], a[208:211], v[204:207]\n"
"s_waitcnt lgkmcnt(4)\n"
"v_permlane16_swap_b32 v129, v131\n"
"v_mfma_f32_16x16x32_bf16 v[0:3], a[248:251], a[0:3], 0\n"
"ds_read_b64_tr_b16 a[240:241], v250 offset:16896\n"
"s_lshl2_add_u32 s84, s76, s60\n"
"s_lshl_b32 s84, s84, 7\n"
"v_mov_b32 v254, s84\n"
"v_mfma_f32_16x16x32_bf16 v[0:3], a[252:255], a[16:19], v[0:3]\n"
"ds_read_b64_tr_b16 a[242:243], v250 offset:17152\n"
"v_mul_f32 v136, s70, v136\n"
"v_mfma_f32_16x16x32_bf16 v[8:11], a[248:251], a[4:7], 0\n"
"v_mul_f32 v137, s70, v137\n"
"v_mul_f32 v138, s70, v138\n"
"v_mfma_f32_16x16x32_bf16 v[8:11], a[252:255], a[20:23], v[8:11]\n"
"v_mul_f32 v139, s70, v139\n"
"v_cvt_pk_bf16_f32 v136, v136, v137\n"
"v_mfma_f32_16x16x32_bf16 v[16:19], a[248:251], a[8:11], 0\n"
"ds_read_b64_tr_b16 a[244:245], v250 offset:20992\n"
"v_cvt_pk_bf16_f32 v137, v138, v139\n"
"v_mfma_f32_16x16x32_bf16 v[16:19], a[252:255], a[24:27], v[16:19]\n"
"ds_read_b64_tr_b16 a[246:247], v250 offset:21248\n"
"v_mul_f32 v140, s70, v140\n"
"v_mfma_f32_16x16x32_bf16 v[24:27], a[248:251], a[12:15], 0\n"
"v_mul_f32 v141, s70, v141\n"
"v_mul_f32 v142, s70, v142\n"
"v_mfma_f32_16x16x32_bf16 v[24:27], a[252:255], a[28:31], v[24:27]\n"
"s_waitcnt lgkmcnt(4)\n"
"v_mul_f32 v143, s70, v143\n"
"v_cvt_pk_bf16_f32 v138, v140, v141\n"
"v_mfma_f32_16x16x32_bf16 v[28:31], a[232:235], a[12:15], 0\n"
"ds_read_b64_tr_b16 a[248:249], v251 offset:16896\n"
"v_cvt_pk_bf16_f32 v139, v142, v143\n"
"v_mfma_f32_16x16x32_bf16 v[28:31], a[236:239], a[28:31], v[28:31]\n"
"ds_read_b64_tr_b16 a[250:251], v251 offset:17152\n"
"v_mul_f32 v144, s70, v144\n"
"v_mfma_f32_16x16x32_bf16 v[20:23], a[232:235], a[8:11], 0\n"
"L_CW2:\n"
"s_mov_b32 s75, L_CW2-L_PC-1024\n"
"buffer_load_dword v248, v254, s[56:59], s75 offen\n"
"v_mfma_f32_16x16x32_bf16 v[20:23], a[236:239], a[24:27], v[20:23]\n"
"v_add_u32 v242, 0, v245\n"
"s_add_u32 m0, s85, 0\n"
"v_mfma_f32_16x16x32_bf16 v[12:15], a[232:235], a[4:7], 0\n"
"buffer_load_dwordx4 v242, s[44:47], 0 offen offset:0 sc0 sc1 nt lds\n"
"ds_read_b64_tr_b16 a[252:253], v251 offset:20992\n"
"v_mfma_f32_16x16x32_bf16 v[12:15], a[236:239], a[20:23], v[12:15]\n"
"ds_read_b64_tr_b16 a[254:255], v251 offset:21248\n"
"v_mul_f32 v145, s70, v145\n"
"v_mfma_f32_16x16x32_bf16 v[4:7], a[232:235], a[0:3], 0\n"
"v_permlane16_swap_b32 v136, v138\n"
"v_mfma_f32_16x16x32_bf16 v[4:7], a[236:239], a[16:19], v[4:7]\n"
"s_waitcnt lgkmcnt(4)\n"
"v_permlane16_swap_b32 v137, v139\n"
"v_mfma_f32_16x16x32_bf16 v[32:35], a[240:243], a[0:3], 0\n"
"s_barrier\n"
"buffer_load_dwordx4 v242, s[44:47], s61 offen offset:1024 sc0 sc1 nt lds\n"
"ds_read_b64_tr_b16 a[232:233], v250 offset:32768\n"
"v_mfma_f32_16x16x32_bf16 v[32:35], a[244:247], a[16:19], v[32:35]\n"
"ds_read_b64_tr_b16 a[234:235], v250 offset:33024\n"
"v_mul_f32 v146, s70, v146\n"
"v_mfma_f32_16x16x32_bf16 v[40:43], a[240:243], a[4:7], 0\n"
"v_mul_f32 v147, s70, v147\n"
"v_cvt_pk_bf16_f32 v144, v144, v145\n"
"v_mfma_f32_16x16x32_bf16 v[40:43], a[244:247], a[20:23], v[40:43]\n"
"v_cvt_pk_bf16_f32 v145, v146, v147\n"
"v_mul_f32 v148, s70, v148\n"
"v_mfma_f32_16x16x32_bf16 v[48:51], a[240:243], a[8:11], 0\n"
"buffer_load_dwordx4 v242, s[44:47], s62 offen offset:2048 sc0 sc1 nt lds\n"
"ds_read_b64_tr_b16 a[236:237], v250 offset:36864\n"
"v_mfma_f32_16x16x32_bf16 v[48:51], a[244:247], a[24:27], v[48:51]\n"
"ds_read_b64_tr_b16 a[238:239], v250 offset:37120\n"
"v_mul_f32 v149, s70, v149\n"
"v_mfma_f32_16x16x32_bf16 v[56:59], a[240:243], a[12:15], 0\n"
"v_mul_f32 v150, s70, v150\n"
"v_mul_f32 v151, s70, v151\n"
"v_mfma_f32_16x16x32_bf16 v[56:59], a[244:247], a[28:31], v[56:59]\n"
"s_waitcnt lgkmcnt(4)\n"
"v_cvt_pk_bf16_f32 v146, v148, v149\n"
"v_cvt_pk_bf16_f32 v147, v150, v151\n"
"v_mfma_f32_16x16x32_bf16 v[60:63], a[248:251], a[12:15], 0\n"
"buffer_load_dwordx4 v242, s[44:47], s63 offen offset:3072 sc0 sc1 nt lds\n"
"ds_read_b64_tr_b16 a[240:241], v251 offset:32768\n"
"v_mfma_f32_16x16x32_bf16 v[60:63], a[252:255], a[28:31], v[60:63]\n"
"ds_read_b64_tr_b16 a[242:243], v251 offset:33024\n"
"v_mul_f32 v152, s70, v152\n"
"v_mfma_f32_16x16x32_bf16 v[52:55], a[248:251], a[8:11], 0\n"
"v_permlane16_swap_b32 v144, v146\n"
"v_mfma_f32_16x16x32_bf16 v[52:55], a[252:255], a[24:27], v[52:55]\n"
"v_permlane16_swap_b32 v145, v147\n"
"v_mfma_f32_16x16x32_bf16 v[44:47], a[248:251], a[4:7], 0\n"
"ds_read_b64_tr_b16 a[244:245], v251 offset:36864\n"
"v_mul_f32 v153, s70, v153\n"
"v_mfma_f32_16x16x32_bf16 v[44:47], a[252:255], a[20:23], v[44:47]\n"
"ds_read_b64_tr_b16 a[246:247], v251 offset:37120\n"
"v_mul_f32 v154, s70, v154\n"
"v_mfma_f32_16x16x32_bf16 v[36:39], a[248:251], a[0:3], 0\n"
"v_mul_f32 v155, s70, v155\n"
"v_cvt_pk_bf16_f32 v152, v152, v153\n"
"v_mfma_f32_16x16x32_bf16 v[36:39], a[252:255], a[16:19], v[36:39]\n"
"s_waitcnt lgkmcnt(4)\n"
"v_cvt_pk_bf16_f32 v153, v154, v155\n"
"v_mul_f32 v156, s70, v156\n"
"v_mfma_f32_16x16x32_bf16 v[0:3], a[232:235], a[32:35], v[0:3]\n"
"ds_read_b64_tr_b16 a[248:249], v250 offset:33280\n"
"v_mul_f32 v157, s70, v157\n"
"v_mfma_f32_16x16x32_bf16 v[0:3], a[236:239], a[48:51], v[0:3]\n"
"ds_read_b64_tr_b16 a[250:251], v250 offset:33536\n"
"v_mul_f32 v158, s70, v158\n"
"v_mfma_f32_16x16x32_bf16 v[8:11], a[232:235], a[36:39], v[8:11]\n"
"v_mul_f32 v159, s70, v159\n"
"v_mfma_f32_16x16x32_bf16 v[8:11], a[236:239], a[52:55], v[8:11]\n"
"v_cvt_pk_bf16_f32 v154, v156, v157\n"
"v_cvt_pk_bf16_f32 v155, v158, v159\n"
"v_mfma_f32_16x16x32_bf16 v[16:19], a[232:235], a[40:43], v[16:19]\n"
"ds_read_b64_tr_b16 a[252:253], v250 offset:37376\n"
"v_mul_f32 v160, s70, v160\n"
"v_mfma_f32_16x16x32_bf16 v[16:19], a[236:239], a[56:59], v[16:19]\n"
"ds_read_b64_tr_b16 a[254:255], v250 offset:37632\n"
"v_mfma_f32_16x16x32_bf16 v[24:27], a[232:235], a[44:47], v[24:27]\n"
"v_permlane16_swap_b32 v152, v154\n"
"v_mfma_f32_16x16x32_bf16 v[24:27], a[236:239], a[60:63], v[24:27]\n"
"s_waitcnt lgkmcnt(4)\n"
"v_permlane16_swap_b32 v153, v155\n"
"v_mfma_f32_16x16x32_bf16 v[28:31], a[240:243], a[44:47], v[28:31]\n"
"ds_read_b64_tr_b16 a[232:233], v251 offset:33280\n"
"v_mfma_f32_16x16x32_bf16 v[28:31], a[244:247], a[60:63], v[28:31]\n"
"ds_read_b64_tr_b16 a[234:235], v251 offset:33536\n"
"v_mul_f32 v161, s70, v161\n"
"v_mfma_f32_16x16x32_bf16 v[20:23], a[240:243], a[40:43], v[20:23]\n"
"v_mul_f32 v162, s70, v162\n"
"v_mul_f32 v163, s70, v163\n"
"v_mfma_f32_16x16x32_bf16 v[20:23], a[244:247], a[56:59], v[20:23]\n"
"v_add_u32 v242, 128, v245\n"
"s_add_u32 m0, s85, 4096\n"
"v_mfma_f32_16x16x32_bf16 v[12:15], a[240:243], a[36:39], v[12:15]\n"
"buffer_load_dwordx4 v242, s[44:47], 0 offen offset:0 sc0 sc1 nt lds\n"
"ds_read_b64_tr_b16 a[236:237], v251 offset:37376\n"
"v_mfma_f32_16x16x32_bf16 v[12:15], a[244:247], a[52:55], v[12:15]\n"
"ds_read_b64_tr_b16 a[238:239], v251 offset:37632\n"
"v_cvt_pk_bf16_f32 v160, v160, v161\n"
"v_mfma_f32_16x16x32_bf16 v[4:7], a[240:243], a[32:35], v[4:7]\n"
"v_cvt_pk_bf16_f32 v161, v162, v163\n"
"v_mfma_f32_16x16x32_bf16 v[4:7], a[244:247], a[48:51], v[4:7]\n"
"s_waitcnt lgkmcnt(4)\n"
"v_mul_f32 v164, s70, v164\n"
"v_mul_f32 v165, s70, v165\n"
"v_mfma_f32_16x16x32_bf16 v[32:35], a[248:251], a[32:35], v[32:35]\n"
"s_barrier\n"
"buffer_load_dwordx4 v242, s[44:47], s61 offen offset:1024 sc0 sc1 nt lds\n"
"ds_read_b64_tr_b16 a[240:241], v250 offset:49152\n"
"v_mfma_f32_16x16x32_bf16 v[32:35], a[252:255], a[48:51], v[32:35]\n"
"ds_read_b64_tr_b16 a[242:243], v250 offset:49408\n"
"v_mul_f32 v166, s70, v166\n"
"v_mfma_f32_16x16x32_bf16 v[40:43], a[248:251], a[36:39], v[40:43]\n"
"v_mul_f32 v167, s70, v167\n"
"v_mfma_f32_16x16x32_bf16 v[40:43], a[252:255], a[52:55], v[40:43]\n"
"v_cvt_pk_bf16_f32 v162, v164, v165\n"
"v_cvt_pk_bf16_f32 v163, v166, v167\n"
"v_mfma_f32_16x16x32_bf16 v[48:51], a[248:251], a[40:43], v[48:51]\n"
"buffer_load_dwordx4 v242, s[44:47], s62 offen offset:2048 sc0 sc1 nt lds\n"
"ds_read_b64_tr_b16 a[244:245], v250 offset:53248\n"
"v_mfma_f32_16x16x32_bf16 v[48:51], a[252:255], a[56:59], v[48:51]\n"
"ds_read_b64_tr_b16 a[246:247], v250 offset:53504\n"
"v_mul_f32 v168, s70, v168\n"
"v_mfma_f32_16x16x32_bf16 v[56:59], a[248:251], a[44:47], v[56:59]\n"
"v_permlane16_swap_b32 v160, v162\n"
"v_mfma_f32_16x16x32_bf16 v[56:59], a[252:255], a[60:63], v[56:59]\n"
"s_waitcnt lgkmcnt(4)\n"
"v_permlane16_swap_b32 v161, v163\n"
"v_mfma_f32_16x16x32_bf16 v[60:63], a[232:235], a[44:47], v[60:63]\n"
"buffer_load_dwordx4 v242, s[44:47], s63 offen offset:3072 sc0 sc1 nt lds\n"
"ds_read_b64_tr_b16 a[248:249], v251 offset:49152\n"
"v_mfma_f32_16x16x32_bf16 v[60:63], a[236:239], a[60:63], v[60:63]\n"
"ds_read_b64_tr_b16 a[250:251], v251 offset:49408\n"
"v_mfma_f32_16x16x32_bf16 v[52:55], a[232:235], a[40:43], v[52:55]\n"
"v_mul_f32 v169, s70, v169\n"
"v_mfma_f32_16x16x32_bf16 v[52:55], a[236:239], a[56:59], v[52:55]\n"
"v_mul_f32 v170, s70, v170\n"
"v_mul_f32 v171, s70, v171\n"
"v_mfma_f32_16x16x32_bf16 v[44:47], a[232:235], a[36:39], v[44:47]\n"
"ds_read_b64_tr_b16 a[252:253], v251 offset:53248\n"
"v_cvt_pk_bf16_f32 v168, v168, v169\n"
"v_mfma_f32_16x16x32_bf16 v[44:47], a[236:239], a[52:55], v[44:47]\n"
"ds_read_b64_tr_b16 a[254:255], v251 offset:53504\n"
"v_mfma_f32_16x16x32_bf16 v[36:39], a[232:235], a[32:35], v[36:39]\n"
"v_cvt_pk_bf16_f32 v169, v170, v171\n"
"v_mul_f32 v172, s70, v172\n"
"v_mfma_f32_16x16x32_bf16 v[36:39], a[236:239], a[48:51], v[36:39]\n"
"s_waitcnt lgkmcnt(4)\n"
"v_mul_f32 v173, s70, v173\n"
"v_mul_f32 v174, s70, v174\n"
"v_mfma_f32_16x16x32_bf16 v[0:3], a[240:243], a[64:67], v[0:3]\n"
"ds_read_b64_tr_b16 a[232:233], v250 offset:49664\n"
"v_mfma_f32_16x16x32_bf16 v[0:3], a[244:247], a[80:83], v[0:3]\n"
"ds_read_b64_tr_b16 a[234:235], v250 offset:49920\n"
"v_mul_f32 v175, s70, v175\n"
"v_mfma_f32_16x16x32_bf16 v[8:11], a[240:243], a[68:71], v[8:11]\n"
"v_cvt_pk_bf16_f32 v170, v172, v173\n"
"v_cvt_pk_bf16_f32 v171, v174, v175\n"
"v_mfma_f32_16x16x32_bf16 v[8:11], a[244:247], a[84:87], v[8:11]\n"
"v_mul_f32 v176, s70, v176\n"
"v_mfma_f32_16x16x32_bf16 v[16:19], a[240:243], a[72:75], v[16:19]\n"
"ds_read_b64_tr_b16 a[236:237], v250 offset:53760\n"
"v_mul_f32 v177, s70, v177\n"
"v_mfma_f32_16x16x32_bf16 v[16:19], a[244:247], a[88:91], v[16:19]\n"
"ds_read_b64_tr_b16 a[238:239], v250 offset:54016\n"
"v_mul_f32 v178, s70, v178\n"
"v_mfma_f32_16x16x32_bf16 v[24:27], a[240:243], a[76:79], v[24:27]\n"
"v_permlane16_swap_b32 v168, v170\n"
"v_mfma_f32_16x16x32_bf16 v[24:27], a[244:247], a[92:95], v[24:27]\n"
"s_waitcnt lgkmcnt(4)\n"
"v_permlane16_swap_b32 v169, v171\n"
"v_mfma_f32_16x16x32_bf16 v[28:31], a[248:251], a[76:79], v[28:31]\n"
"ds_read_b64_tr_b16 a[240:241], v251 offset:49664\n"
"v_mfma_f32_16x16x32_bf16 v[28:31], a[252:255], a[92:95], v[28:31]\n"
"ds_read_b64_tr_b16 a[242:243], v251 offset:49920\n"
"v_mul_f32 v179, s70, v179\n"
"v_mfma_f32_16x16x32_bf16 v[20:23], a[248:251], a[72:75], v[20:23]\n"
"v_cvt_pk_bf16_f32 v176, v176, v177\n"
"v_mfma_f32_16x16x32_bf16 v[20:23], a[252:255], a[88:91], v[20:23]\n"
"v_cvt_pk_bf16_f32 v177, v178, v179\n"
"v_mul_f32 v180, s70, v180\n"
"v_mfma_f32_16x16x32_bf16 v[12:15], a[248:251], a[68:71], v[12:15]\n"
"ds_read_b64_tr_b16 a[244:245], v251 offset:53760\n"
"v_mul_f32 v181, s70, v181\n"
"v_mfma_f32_16x16x32_bf16 v[12:15], a[252:255], a[84:87], v[12:15]\n"
"ds_read_b64_tr_b16 a[246:247], v251 offset:54016\n"
"v_mfma_f32_16x16x32_bf16 v[4:7], a[248:251], a[64:67], v[4:7]\n"
"v_mul_f32 v182, s70, v182\n"
"v_mul_f32 v183, s70, v183\n"
"v_mfma_f32_16x16x32_bf16 v[4:7], a[252:255], a[80:83], v[4:7]\n"
"s_waitcnt lgkmcnt(4)\n"
"v_cvt_pk_bf16_f32 v178, v180, v181\n"
"v_cvt_pk_bf16_f32 v179, v182, v183\n"
"v_mfma_f32_16x16x32_bf16 v[32:35], a[232:235], a[64:67], v[32:35]\n"
"s_barrier\n"
"ds_read_b64_tr_b16 a[248:249], v252 offset:0\n"
"v_mfma_f32_16x16x32_bf16 v[32:35], a[236:239], a[80:83], v[32:35]\n"
"ds_read_b64_tr_b16 a[250:251], v252 offset:256\n"
"v_mul_f32 v184, s70, v184\n"
"v_mfma_f32_16x16x32_bf16 v[40:43], a[232:235], a[68:71], v[40:43]\n"
"v_permlane16_swap_b32 v176, v178\n"
"v_mfma_f32_16x16x32_bf16 v[40:43], a[236:239], a[84:87], v[40:43]\n"
"v_permlane16_swap_b32 v177, v179\n"
"v_mfma_f32_16x16x32_bf16 v[48:51], a[232:235], a[72:75], v[48:51]\n"
"ds_read_b64_tr_b16 a[252:253], v252 offset:4096\n"
"v_mfma_f32_16x16x32_bf16 v[48:51], a[236:239], a[88:91], v[48:51]\n"
"ds_read_b64_tr_b16 a[254:255], v252 offset:4352\n"
"v_mul_f32 v185, s70, v185\n"
"v_mfma_f32_16x16x32_bf16 v[56:59], a[232:235], a[76:79], v[56:59]\n"
"v_mul_f32 v186, s70, v186\n"
"v_mul_f32 v187, s70, v187\n"
"v_mfma_f32_16x16x32_bf16 v[56:59], a[236:239], a[92:95], v[56:59]\n"
"s_waitcnt lgkmcnt(4)\n"
"v_cvt_pk_bf16_f32 v184, v184, v185\n"
"v_mfma_f32_16x16x32_bf16 v[60:63], a[240:243], a[76:79], v[60:63]\n"
"ds_read_b64_tr_b16 a[232:233], v253 offset:0\n"
"v_cvt_pk_bf16_f32 v185, v186, v187\n"
"v_mfma_f32_16x16x32_bf16 v[60:63], a[244:247], a[92:95], v[60:63]\n"
"ds_read_b64_tr_b16 a[234:235], v253 offset:256\n"
"v_mul_f32 v188, s70, v188\n"
"v_mfma_f32_16x16x32_bf16 v[52:55], a[240:243], a[72:75], v[52:55]\n"
"v_mul_f32 v189, s70, v189\n"
"v_mfma_f32_16x16x32_bf16 v[52:55], a[244:247], a[88:91], v[52:55]\n"
"v_add_u32 v242, 32768, v245\n"
"s_add_u32 m0, s85, 16384\n"
"v_mfma_f32_16x16x32_bf16 v[44:47], a[240:243], a[68:71], v[44:47]\n"
"buffer_load_dwordx4 v242, s[40:43], 0 offen offset:0 sc0 sc1 nt lds\n"
"ds_read_b64_tr_b16 a[236:237], v253 offset:4096\n"
"v_mfma_f32_16x16x32_bf16 v[44:47], a[244:247], a[84:87], v[44:47]\n"
"ds_read_b64_tr_b16 a[238:239], v253 offset:4352\n"
"v_mul_f32 v190, s70, v190\n"
"v_mfma_f32_16x16x32_bf16 v[36:39], a[240:243], a[64:67], v[36:39]\n"
"v_mul_f32 v191, s70, v191\n"
"v_cvt_pk_bf16_f32 v186, v188, v189\n"
"v_mfma_f32_16x16x32_bf16 v[36:39], a[244:247], a[80:83], v[36:39]\n"
"s_waitcnt lgkmcnt(4)\n"
"v_cvt_pk_bf16_f32 v187, v190, v191\n"
"v_mfma_f32_16x16x32_bf16 v[0:3], a[248:251], a[96:99], v[0:3]\n"
"buffer_load_dwordx4 v242, s[40:43], s61 offen offset:1024 sc0 sc1 nt lds\n"
"ds_read_b64_tr_b16 a[240:241], v252 offset:512\n"
"v_mfma_f32_16x16x32_bf16 v[0:3], a[252:255], a[112:115], v[0:3]\n"
"ds_read_b64_tr_b16 a[242:243], v252 offset:768\n"
"v_mul_f32 v192, s70, v192\n"
"v_mfma_f32_16x16x32_bf16 v[8:11], a[248:251], a[100:103], v[8:11]\n"
"v_permlane16_swap_b32 v184, v186\n"
"v_mfma_f32_16x16x32_bf16 v[8:11], a[252:255], a[116:119], v[8:11]\n"
"v_permlane16_swap_b32 v185, v187\n"
"v_mfma_f32_16x16x32_bf16 v[16:19], a[248:251], a[104:107], v[16:19]\n"
"buffer_load_dwordx4 v242, s[40:43], s62 offen offset:2048 sc0 sc1 nt lds\n"
"ds_read_b64_tr_b16 a[244:245], v252 offset:4608\n"
"v_mfma_f32_16x16x32_bf16 v[16:19], a[252:255], a[120:123], v[16:19]\n"
"ds_read_b64_tr_b16 a[246:247], v252 offset:4864\n"
"v_mfma_f32_16x16x32_bf16 v[24:27], a[248:251], a[108:111], v[24:27]\n"
"v_mul_f32 v193, s70, v193\n"
"v_mul_f32 v194, s70, v194\n"
"v_mfma_f32_16x16x32_bf16 v[24:27], a[252:255], a[124:127], v[24:27]\n"
"s_waitcnt lgkmcnt(4)\n"
"v_mul_f32 v195, s70, v195\n"
"v_mfma_f32_16x16x32_bf16 v[28:31], a[232:235], a[108:111], v[28:31]\n"
"buffer_load_dwordx4 v242, s[40:43], s63 offen offset:3072 sc0 sc1 nt lds\n"
"ds_read_b64_tr_b16 a[248:249], v253 offset:512\n"
"v_mfma_f32_16x16x32_bf16 v[28:31], a[236:239], a[124:127], v[28:31]\n"
"ds_read_b64_tr_b16 a[250:251], v253 offset:768\n"
"v_cvt_pk_bf16_f32 v192, v192, v193\n"
"v_mfma_f32_16x16x32_bf16 v[20:23], a[232:235], a[104:107], v[20:23]\n"
"v_cvt_pk_bf16_f32 v193, v194, v195\n"
"v_mul_f32 v196, s70, v196\n"
"v_mfma_f32_16x16x32_bf16 v[20:23], a[236:239], a[120:123], v[20:23]\n"
"v_mul_f32 v197, s70, v197\n"
"v_mfma_f32_16x16x32_bf16 v[12:15], a[232:235], a[100:103], v[12:15]\n"
"ds_read_b64_tr_b16 a[252:253], v253 offset:4608\n"
"v_mul_f32 v198, s70, v198\n"
"v_mfma_f32_16x16x32_bf16 v[12:15], a[236:239], a[116:119], v[12:15]\n"
"ds_read_b64_tr_b16 a[254:255], v253 offset:4864\n"
"v_mul_f32 v199, s70, v199\n"
"v_mfma_f32_16x16x32_bf16 v[4:7], a[232:235], a[96:99], v[4:7]\n"
"v_cvt_pk_bf16_f32 v194, v196, v197\n"
"v_cvt_pk_bf16_f32 v195, v198, v199\n"
"v_mfma_f32_16x16x32_bf16 v[4:7], a[236:239], a[112:115], v[4:7]\n"
"s_waitcnt lgkmcnt(4)\n"
"v_mul_f32 v200, s70, v200\n"
"v_mfma_f32_16x16x32_bf16 v[32:35], a[240:243], a[96:99], v[32:35]\n"
"s_barrier\n"
"ds_read_b64_tr_b16 a[232:233], v250 offset:0\n"
"v_mul_f32 v201, s70, v201\n"
"v_mfma_f32_16x16x32_bf16 v[32:35], a[244:247], a[112:115], v[32:35]\n"
"ds_read_b64_tr_b16 a[234:235], v250 offset:256\n"
"v_mul_f32 v202, s70, v202\n"
"v_mfma_f32_16x16x32_bf16 v[40:43], a[240:243], a[100:103], v[40:43]\n"
"v_permlane16_swap_b32 v192, v194\n"
"v_mfma_f32_16x16x32_bf16 v[40:43], a[244:247], a[116:119], v[40:43]\n"
"v_permlane16_swap_b32 v193, v195\n"
"v_mfma_f32_16x16x32_bf16 v[48:51], a[240:243], a[104:107], v[48:51]\n"
"ds_read_b64_tr_b16 a[236:237], v250 offset:4096\n"
"v_mfma_f32_16x16x32_bf16 v[48:51], a[244:247], a[120:123], v[48:51]\n"
"ds_read_b64_tr_b16 a[238:239], v250 offset:4352\n"
"v_mul_f32 v203, s70, v203\n"
"v_mfma_f32_16x16x32_bf16 v[56:59], a[240:243], a[108:111], v[56:59]\n"
"v_cvt_pk_bf16_f32 v200, v200, v201\n"
"v_mfma_f32_16x16x32_bf16 v[56:59], a[244:247], a[124:127], v[56:59]\n"
"s_waitcnt lgkmcnt(4)\n"
"v_cvt_pk_bf16_f32 v201, v202, v203\n"
"v_mul_f32 v204, s70, v204\n"
"v_mfma_f32_16x16x32_bf16 v[60:63], a[248:251], a[108:111], v[60:63]\n"
"ds_read_b64_tr_b16 a[240:241], v251 offset:0\n"
"v_mul_f32 v205, s70, v205\n"
"v_mfma_f32_16x16x32_bf16 v[60:63], a[252:255], a[124:127], v[60:63]\n"
"ds_read_b64_tr_b16 a[242:243], v251 offset:256\n"
"v_mfma_f32_16x16x32_bf16 v[52:55], a[248:251], a[104:107], v[52:55]\n"
"v_mul_f32 v206, s70, v206\n"
"v_mul_f32 v207, s70, v207\n"
"v_mfma_f32_16x16x32_bf16 v[52:55], a[252:255], a[120:123], v[52:55]\n"
"v_add_u32 v242, 32896, v245\n"
"s_add_u32 m0, s85, 20480\n"
"v_mfma_f32_16x16x32_bf16 v[44:47], a[248:251], a[100:103], v[44:47]\n"
"buffer_load_dwordx4 v242, s[40:43], 0 offen offset:0 sc0 sc1 nt lds\n"
"ds_read_b64_tr_b16 a[244:245], v251 offset:4096\n"
"v_mfma_f32_16x16x32_bf16 v[44:47], a[252:255], a[116:119], v[44:47]\n"
"ds_read_b64_tr_b16 a[246:247], v251 offset:4352\n"
"v_cvt_pk_bf16_f32 v202, v204, v205\n"
"v_mfma_f32_16x16x32_bf16 v[36:39], a[248:251], a[96:99], v[36:39]\n"
"v_cvt_pk_bf16_f32 v203, v206, v207\n"
"v_mfma_f32_16x16x32_bf16 v[36:39], a[252:255], a[112:115], v[36:39]\n"
"s_waitcnt lgkmcnt(4)\n"
"v_permlane16_swap_b32 v200, v202\n"
"v_mfma_f32_16x16x32_bf16 v[0:3], a[232:235], a[128:131], v[0:3]\n"
"buffer_load_dwordx4 v242, s[40:43], s61 offen offset:1024 sc0 sc1 nt lds\n"
"ds_read_b64_tr_b16 a[248:249], v250 offset:512\n"
"v_mfma_f32_16x16x32_bf16 v[0:3], a[236:239], a[144:147], v[0:3]\n"
"ds_read_b64_tr_b16 a[250:251], v250 offset:768\n"
"v_mul_f32 v208, s70, v208\n"
"v_mfma_f32_16x16x32_bf16 v[8:11], a[232:235], a[132:135], v[8:11]\n"
"v_permlane16_swap_b32 v201, v203\n"
"v_mfma_f32_16x16x32_bf16 v[8:11], a[236:239], a[148:151], v[8:11]\n"
"v_mul_f32 v209, s70, v209\n"
"v_mfma_f32_16x16x32_bf16 v[16:19], a[232:235], a[136:139], v[16:19]\n"
"buffer_load_dwordx4 v242, s[40:43], s62 offen offset:2048 sc0 sc1 nt lds\n"
"ds_read_b64_tr_b16 a[252:253], v250 offset:4608\n"
"v_mfma_f32_16x16x32_bf16 v[16:19], a[236:239], a[152:155], v[16:19]\n"
"ds_read_b64_tr_b16 a[254:255], v250 offset:4864\n"
"v_mul_f32 v210, s70, v210\n"
"v_mfma_f32_16x16x32_bf16 v[24:27], a[232:235], a[140:143], v[24:27]\n"
"v_mul_f32 v211, s70, v211\n"
"v_mfma_f32_16x16x32_bf16 v[24:27], a[236:239], a[156:159], v[24:27]\n"
"s_waitcnt lgkmcnt(4)\n"
"v_cvt_pk_bf16_f32 v208, v208, v209\n"
"v_cvt_pk_bf16_f32 v209, v210, v211\n"
"v_mfma_f32_16x16x32_bf16 v[28:31], a[240:243], a[140:143], v[28:31]\n"
"buffer_load_dwordx4 v242, s[40:43], s63 offen offset:3072 sc0 sc1 nt lds\n"
"ds_read_b64_tr_b16 a[232:233], v251 offset:512\n"
"v_mfma_f32_16x16x32_bf16 v[28:31], a[244:247], a[156:159], v[28:31]\n"
"ds_read_b64_tr_b16 a[234:235], v251 offset:768\n"
"v_mul_f32 v212, s70, v212\n"
"v_mfma_f32_16x16x32_bf16 v[20:23], a[240:243], a[136:139], v[20:23]\n"
"v_mul_f32 v213, s70, v213\n"
"v_mfma_f32_16x16x32_bf16 v[20:23], a[244:247], a[152:155], v[20:23]\n"
"v_mul_f32 v214, s70, v214\n"
"v_mul_f32 v215, s70, v215\n"
"v_mfma_f32_16x16x32_bf16 v[12:15], a[240:243], a[132:135], v[12:15]\n"
"ds_read_b64_tr_b16 a[236:237], v251 offset:4608\n"
"v_cvt_pk_bf16_f32 v210, v212, v213\n"
"v_mfma_f32_16x16x32_bf16 v[12:15], a[244:247], a[148:151], v[12:15]\n"
"ds_read_b64_tr_b16 a[238:239], v251 offset:4864\n"
"v_cvt_pk_bf16_f32 v211, v214, v215\n"
"v_mfma_f32_16x16x32_bf16 v[4:7], a[240:243], a[128:131], v[4:7]\n"
"v_permlane16_swap_b32 v208, v210\n"
"v_mfma_f32_16x16x32_bf16 v[4:7], a[244:247], a[144:147], v[4:7]\n"
"s_waitcnt lgkmcnt(4)\n"
"v_permlane16_swap_b32 v209, v211\n"
"v_mfma_f32_16x16x32_bf16 v[32:35], a[248:251], a[128:131], v[32:35]\n"
"s_barrier\n"
"ds_read_b64_tr_b16 a[240:241], v250 offset:16384\n"
"v_mfma_f32_16x16x32_bf16 v[32:35], a[252:255], a[144:147], v[32:35]\n"
"ds_read_b64_tr_b16 a[242:243], v250 offset:16640\n"
"v_mfma_f32_16x16x32_bf16 v[40:43], a[248:251], a[132:135], v[40:43]\n"
"v_mul_f32 v216, s70, v216\n"
"v_mul_f32 v217, s70, v217\n"
"v_mfma_f32_16x16x32_bf16 v[40:43], a[252:255], a[148:151], v[40:43]\n"
"v_mul_f32 v218, s70, v218\n"
"v_mfma_f32_16x16x32_bf16 v[48:51], a[248:251], a[136:139], v[48:51]\n"
"ds_read_b64_tr_b16 a[244:245], v250 offset:20480\n"
"v_mul_f32 v219, s70, v219\n"
"v_mfma_f32_16x16x32_bf16 v[48:51], a[252:255], a[152:155], v[48:51]\n"
"ds_read_b64_tr_b16 a[246:247], v250 offset:20736\n"
"v_cvt_pk_bf16_f32 v216, v216, v217\n"
"v_mfma_f32_16x16x32_bf16 v[56:59], a[248:251], a[140:143], v[56:59]\n"
"v_cvt_pk_bf16_f32 v217, v218, v219\n"
"v_mul_f32 v220, s70, v220\n"
"v_mfma_f32_16x16x32_bf16 v[56:59], a[252:255], a[156:159], v[56:59]\n"
"s_waitcnt lgkmcnt(4)\n"
"v_mul_f32 v221, s70, v221\n"
"v_mfma_f32_16x16x32_bf16 v[60:63], a[232:235], a[140:143], v[60:63]\n"
"ds_read_b64_tr_b16 a[248:249], v251 offset:16384\n"
"v_mul_f32 v222, s70, v222\n"
"v_mfma_f32_16x16x32_bf16 v[60:63], a[236:239], a[156:159], v[60:63]\n"
"ds_read_b64_tr_b16 a[250:251], v251 offset:16640\n"
"v_mul_f32 v223, s70, v223\n"
"v_mfma_f32_16x16x32_bf16 v[52:55], a[232:235], a[136:139], v[52:55]\n"
"v_cvt_pk_bf16_f32 v218, v220, v221\n"
"v_mfma_f32_16x16x32_bf16 v[52:55], a[236:239], a[152:155], v[52:55]\n"
"v_cvt_pk_bf16_f32 v219, v222, v223\n"
"v_mul_f32 v224, s70, v224\n"
"v_mfma_f32_16x16x32_bf16 v[44:47], a[232:235], a[132:135], v[44:47]\n"
"ds_read_b64_tr_b16 a[252:253], v251 offset:20480\n"
"v_mul_f32 v225, s70, v225\n"
"v_mfma_f32_16x16x32_bf16 v[44:47], a[236:239], a[148:151], v[44:47]\n"
"ds_read_b64_tr_b16 a[254:255], v251 offset:20736\n"
"v_mul_f32 v226, s70, v226\n"
"v_mfma_f32_16x16x32_bf16 v[36:39], a[232:235], a[128:131], v[36:39]\n"
"v_permlane16_swap_b32 v216, v218\n"
"v_mfma_f32_16x16x32_bf16 v[36:39], a[236:239], a[144:147], v[36:39]\n"
"s_waitcnt lgkmcnt(4)\n"
"v_permlane16_swap_b32 v217, v219\n"
"v_mfma_f32_16x16x32_bf16 v[0:3], a[240:243], a[160:163], v[0:3]\n"
"ds_read_b64_tr_b16 a[232:233], v250 offset:16896\n"
"v_mfma_f32_16x16x32_bf16 v[0:3], a[244:247], a[176:179], v[0:3]\n"
"ds_read_b64_tr_b16 a[234:235], v250 offset:17152\n"
"v_mfma_f32_16x16x32_bf16 v[8:11], a[240:243], a[164:167], v[8:11]\n"
"v_readfirstlane_b32 s72, v64\n"
"v_readfirstlane_b32 s73, v65\n"
"v_mfma_f32_16x16x32_bf16 v[8:11], a[244:247], a[180:183], v[8:11]\n"
"v_mul_f32 v227, s70, v227\n"
"v_cvt_pk_bf16_f32 v224, v224, v225\n"
"v_mfma_f32_16x16x32_bf16 v[16:19], a[240:243], a[168:171], v[16:19]\n"
"ds_read_b64_tr_b16 a[236:237], v250 offset:20992\n"
"v_cvt_pk_bf16_f32 v225, v226, v227\n"
"v_mfma_f32_16x16x32_bf16 v[16:19], a[244:247], a[184:187], v[16:19]\n"
"ds_read_b64_tr_b16 a[238:239], v250 offset:21248\n"
"v_mul_f32 v228, s70, v228\n"
"v_mfma_f32_16x16x32_bf16 v[24:27], a[240:243], a[172:175], v[24:27]\n"
"s_or_b32 s72, s72, 1\n"
"s_add_u32 s72, s72, 2\n"
"s_addc_u32 s73, s73, 0\n"
"v_mfma_f32_16x16x32_bf16 v[24:27], a[244:247], a[188:191], v[24:27]\n"
"s_waitcnt lgkmcnt(4)\n"
"v_mul_f32 v229, s70, v229\n"
"v_mfma_f32_16x16x32_bf16 v[28:31], a[248:251], a[172:175], v[28:31]\n"
"ds_read_b64_tr_b16 a[240:241], v251 offset:16896\n"
"v_mul_f32 v230, s70, v230\n"
"v_mfma_f32_16x16x32_bf16 v[28:31], a[252:255], a[188:191], v[28:31]\n"
"ds_read_b64_tr_b16 a[242:243], v251 offset:17152\n"
"v_mul_f32 v231, s70, v231\n"
"v_mfma_f32_16x16x32_bf16 v[20:23], a[248:251], a[168:171], v[20:23]\n"
"v_cvt_pk_bf16_f32 v226, v228, v229\n"
"v_mfma_f32_16x16x32_bf16 v[20:23], a[252:255], a[184:187], v[20:23]\n"
"v_add_u32 v242, 16384, v245\n"
"s_add_u32 m0, s85, 32768\n"
"v_mfma_f32_16x16x32_bf16 v[12:15], a[248:251], a[164:167], v[12:15]\n"
"buffer_load_dwordx4 v242, s[44:47], 0 offen offset:0 sc0 sc1 nt lds\n"
"ds_read_b64_tr_b16 a[244:245], v251 offset:20992\n"
"v_mfma_f32_16x16x32_bf16 v[12:15], a[252:255], a[180:183], v[12:15]\n"
"ds_read_b64_tr_b16 a[246:247], v251 offset:21248\n"
"v_cvt_pk_bf16_f32 v227, v230, v231\n"
"v_mfma_f32_16x16x32_bf16 v[4:7], a[248:251], a[160:163], v[4:7]\n"
"v_permlane16_swap_b32 v224, v226\n"
"v_mfma_f32_16x16x32_bf16 v[4:7], a[252:255], a[176:179], v[4:7]\n"
"s_waitcnt lgkmcnt(4)\n"
"v_permlane16_swap_b32 v225, v227\n"
"v_mfma_f32_16x16x32_bf16 v[32:35], a[232:235], a[160:163], v[32:35]\n"
"s_barrier\n"
"buffer_load_dwordx4 v242, s[44:47], s61 offen offset:1024 sc0 sc1 nt lds\n"
"ds_read_b64_tr_b16 a[248:249], v250 offset:32768\n"
"v_mfma_f32_16x16x32_bf16 v[32:35], a[236:239], a[176:179], v[32:35]\n"
"ds_read_b64_tr_b16 a[250:251], v250 offset:33024\n"
"v_mfma_f32_16x16x32_bf16 v[40:43], a[232:235], a[164:167], v[40:43]\n"
"v_mfma_f32_16x16x32_bf16 v[40:43], a[236:239], a[180:183], v[40:43]\n"
"v_mfma_f32_16x16x32_bf16 v[48:51], a[232:235], a[168:171], v[48:51]\n"
"buffer_load_dwordx4 v242, s[44:47], s62 offen offset:2048 sc0 sc1 nt lds\n"
"ds_read_b64_tr_b16 a[252:253], v250 offset:36864\n"
"v_mfma_f32_16x16x32_bf16 v[48:51], a[236:239], a[184:187], v[48:51]\n"
"ds_read_b64_tr_b16 a[254:255], v250 offset:37120\n"
"v_mfma_f32_16x16x32_bf16 v[56:59], a[232:235], a[172:175], v[56:59]\n"
"v_mfma_f32_16x16x32_bf16 v[56:59], a[236:239], a[188:191], v[56:59]\n"
"s_waitcnt lgkmcnt(4)\n"
"v_mfma_f32_16x16x32_bf16 v[60:63], a[240:243], a[172:175], v[60:63]\n"
"buffer_load_dwordx4 v242, s[44:47], s63 offen offset:3072 sc0 sc1 nt lds\n"
"ds_read_b64_tr_b16 a[232:233], v251 offset:32768\n"
"v_mfma_f32_16x16x32_bf16 v[60:63], a[244:247], a[188:191], v[60:63]\n"
"ds_read_b64_tr_b16 a[234:235], v251 offset:33024\n"
"v_mfma_f32_16x16x32_bf16 v[52:55], a[240:243], a[168:171], v[52:55]\n"
"v_mfma_f32_16x16x32_bf16 v[52:55], a[244:247], a[184:187], v[52:55]\n"
"v_mfma_f32_16x16x32_bf16 v[44:47], a[240:243], a[164:167], v[44:47]\n"
"ds_read_b64_tr_b16 a[236:237], v251 offset:36864\n"
"v_mfma_f32_16x16x32_bf16 v[44:47], a[244:247], a[180:183], v[44:47]\n"
"ds_read_b64_tr_b16 a[238:239], v251 offset:37120\n"
"v_mfma_f32_16x16x32_bf16 v[36:39], a[240:243], a[160:163], v[36:39]\n"
"v_mfma_f32_16x16x32_bf16 v[36:39], a[244:247], a[176:179], v[36:39]\n"
"s_waitcnt lgkmcnt(4)\n"
"v_mfma_f32_16x16x32_bf16 v[0:3], a[248:251], a[192:195], v[0:3]\n"
"ds_read_b64_tr_b16 a[240:241], v250 offset:33280\n"
"v_mfma_f32_16x16x32_bf16 v[0:3], a[252:255], a[208:211], v[0:3]\n"
"ds_read_b64_tr_b16 a[242:243], v250 offset:33536\n"
"v_mfma_f32_16x16x32_bf16 v[8:11], a[248:251], a[196:199], v[8:11]\n"
"v_mfma_f32_16x16x32_bf16 v[8:11], a[252:255], a[212:215], v[8:11]\n"
"v_mfma_f32_16x16x32_bf16 v[16:19], a[248:251], a[200:203], v[16:19]\n"
"ds_read_b64_tr_b16 a[244:245], v250 offset:37376\n"
"v_mfma_f32_16x16x32_bf16 v[16:19], a[252:255], a[216:219], v[16:19]\n"
"ds_read_b64_tr_b16 a[246:247], v250 offset:37632\n"
"v_mfma_f32_16x16x32_bf16 v[24:27], a[248:251], a[204:207], v[24:27]\n"
"v_mfma_f32_16x16x32_bf16 v[24:27], a[252:255], a[220:223], v[24:27]\n"
"s_waitcnt lgkmcnt(4)\n"
"v_mfma_f32_16x16x32_bf16 v[28:31], a[232:235], a[204:207], v[28:31]\n"
"ds_read_b64_tr_b16 a[248:249], v251 offset:33280\n"
"v_mfma_f32_16x16x32_bf16 v[28:31], a[236:239], a[220:223], v[28:31]\n"
"ds_read_b64_tr_b16 a[250:251], v251 offset:33536\n"
"v_mfma_f32_16x16x32_bf16 v[20:23], a[232:235], a[200:203], v[20:23]\n"
"v_mfma_f32_16x16x32_bf16 v[20:23], a[236:239], a[216:219], v[20:23]\n"
"v_add_u32 v242, 16512, v245\n"
"s_add_u32 m0, s85, 36864\n"
"v_mfma_f32_16x16x32_bf16 v[12:15], a[232:235], a[196:199], v[12:15]\n"
"buffer_load_dwordx4 v242, s[44:47], 0 offen offset:0 sc0 sc1 nt lds\n"
"ds_read_b64_tr_b16 a[252:253], v251 offset:37376\n"
"v_mfma_f32_16x16x32_bf16 v[12:15], a[236:239], a[212:215], v[12:15]\n"
"ds_read_b64_tr_b16 a[254:255], v251 offset:37632\n"
"v_mfma_f32_16x16x32_bf16 v[4:7], a[232:235], a[192:195], v[4:7]\n"
"v_mfma_f32_16x16x32_bf16 v[4:7], a[236:239], a[208:211], v[4:7]\n"
"s_waitcnt lgkmcnt(4)\n"
"v_mfma_f32_16x16x32_bf16 v[32:35], a[240:243], a[192:195], v[32:35]\n"
"s_barrier\n"
"buffer_load_dwordx4 v242, s[44:47], s61 offen offset:1024 sc0 sc1 nt lds\n"
"ds_read_b64_tr_b16 a[232:233], v250 offset:49152\n"
"v_mfma_f32_16x16x32_bf16 v[32:35], a[244:247], a[208:211], v[32:35]\n"
"ds_read_b64_tr_b16 a[234:235], v250 offset:49408\n"
"v_mfma_f32_16x16x32_bf16 v[40:43], a[240:243], a[196:199], v[40:43]\n"
"v_mul_f32 v8, s70, v8\n"
"v_mul_f32 v9, s70, v9\n"
"v_mfma_f32_16x16x32_bf16 v[40:43], a[244:247], a[212:215], v[40:43]\n"
"v_mul_f32 v10, s70, v10\n"
"v_mul_f32 v11, s70, v11\n"
"v_mfma_f32_16x16x32_bf16 v[48:51], a[240:243], a[200:203], v[48:51]\n"
"buffer_load_dwordx4 v242, s[44:47], s62 offen offset:2048 sc0 sc1 nt lds\n"
"ds_read_b64_tr_b16 a[236:237], v250 offset:53248\n"
"v_mfma_f32_16x16x32_bf16 v[48:51], a[244:247], a[216:219], v[48:51]\n"
"ds_read_b64_tr_b16 a[238:239], v250 offset:53504\n"
"v_mul_f32 v0, s70, v0\n"
"v_mfma_f32_16x16x32_bf16 v[56:59], a[240:243], a[204:207], v[56:59]\n"
"v_mul_f32 v1, s70, v1\n"
"v_mul_f32 v2, s70, v2\n"
"v_mfma_f32_16x16x32_bf16 v[56:59], a[244:247], a[220:223], v[56:59]\n"
"s_waitcnt lgkmcnt(4)\n"
"v_mul_f32 v3, s70, v3\n"
"v_cvt_pk_bf16_f32 v0, v0, v1\n"
"v_mfma_f32_16x16x32_bf16 v[60:63], a[248:251], a[204:207], v[60:63]\n"
"buffer_load_dwordx4 v242, s[44:47], s63 offen offset:3072 sc0 sc1 nt lds\n"
"ds_read_b64_tr_b16 a[240:241], v251 offset:49152\n"
"v_mfma_f32_16x16x32_bf16 v[60:63], a[252:255], a[220:223], v[60:63]\n"
"ds_read_b64_tr_b16 a[242:243], v251 offset:49408\n"
"v_cvt_pk_bf16_f32 v1, v2, v3\n"
"v_mfma_f32_16x16x32_bf16 v[52:55], a[248:251], a[200:203], v[52:55]\n"
"v_mul_f32 v4, s70, v4\n"
"v_mul_f32 v5, s70, v5\n"
"v_mfma_f32_16x16x32_bf16 v[52:55], a[252:255], a[216:219], v[52:55]\n"
"v_mul_f32 v6, s70, v6\n"
"v_mul_f32 v7, s70, v7\n"
"v_mfma_f32_16x16x32_bf16 v[44:47], a[248:251], a[196:199], v[44:47]\n"
"ds_read_b64_tr_b16 a[244:245], v251 offset:53248\n"
"v_cvt_pk_bf16_f32 v2, v4, v5\n"
"v_mfma_f32_16x16x32_bf16 v[44:47], a[252:255], a[212:215], v[44:47]\n"
"ds_read_b64_tr_b16 a[246:247], v251 offset:53504\n"
"v_cvt_pk_bf16_f32 v3, v6, v7\n"
"v_mfma_f32_16x16x32_bf16 v[36:39], a[248:251], a[192:195], v[36:39]\n"
"v_permlane16_swap_b32 v0, v2\n"
"v_mfma_f32_16x16x32_bf16 v[36:39], a[252:255], a[208:211], v[36:39]\n"
"s_waitcnt lgkmcnt(4)\n"
"v_permlane16_swap_b32 v1, v3\n"
"v_mfma_f32_16x16x32_bf16 v[64:67], a[232:235], a[0:3], 0\n"
"ds_read_b64_tr_b16 a[248:249], v250 offset:49664\n"
"v_cvt_pk_bf16_f32 v8, v8, v9\n"
"v_mfma_f32_16x16x32_bf16 v[64:67], a[236:239], a[16:19], v[64:67]\n"
"ds_read_b64_tr_b16 a[250:251], v250 offset:49920\n"
"v_cvt_pk_bf16_f32 v9, v10, v11\n"
"v_mfma_f32_16x16x32_bf16 v[72:75], a[232:235], a[4:7], 0\n"
"v_mul_f32 v12, s70, v12\n"
"v_mul_f32 v13, s70, v13\n"
"v_mfma_f32_16x16x32_bf16 v[72:75], a[236:239], a[20:23], v[72:75]\n"
"v_mul_f32 v14, s70, v14\n"
"v_mul_f32 v15, s70, v15\n"
"v_mfma_f32_16x16x32_bf16 v[80:83], a[232:235], a[8:11], 0\n"
"ds_read_b64_tr_b16 a[252:253], v250 offset:53760\n"
"v_cvt_pk_bf16_f32 v10, v12, v13\n"
"v_mfma_f32_16x16x32_bf16 v[80:83], a[236:239], a[24:27], v[80:83]\n"
"ds_read_b64_tr_b16 a[254:255], v250 offset:54016\n"
"v_cvt_pk_bf16_f32 v11, v14, v15\n"
"v_mfma_f32_16x16x32_bf16 v[88:91], a[232:235], a[12:15], 0\n"
"v_permlane16_swap_b32 v8, v10\n"
"v_mfma_f32_16x16x32_bf16 v[88:91], a[236:239], a[28:31], v[88:91]\n"
"s_waitcnt lgkmcnt(4)\n"
"v_permlane16_swap_b32 v9, v11\n"
"v_mfma_f32_16x16x32_bf16 v[92:95], a[240:243], a[12:15], 0\n"
"ds_read_b64_tr_b16 a[232:233], v251 offset:49664\n"
"v_mul_f32 v16, s70, v16\n"
"v_mfma_f32_16x16x32_bf16 v[92:95], a[244:247], a[28:31], v[92:95]\n"
"ds_read_b64_tr_b16 a[234:235], v251 offset:49920\n"
"v_mul_f32 v17, s70, v17\n"
"v_mfma_f32_16x16x32_bf16 v[84:87], a[240:243], a[8:11], 0\n"
"v_mul_f32 v18, s70, v18\n"
"v_mul_f32 v19, s70, v19\n"
"v_mfma_f32_16x16x32_bf16 v[84:87], a[244:247], a[24:27], v[84:87]\n"
"v_cvt_pk_bf16_f32 v16, v16, v17\n"
"v_cvt_pk_bf16_f32 v17, v18, v19\n"
"v_mfma_f32_16x16x32_bf16 v[76:79], a[240:243], a[4:7], 0\n"
"ds_read_b64_tr_b16 a[236:237], v251 offset:53760\n"
"v_mul_f32 v20, s70, v20\n"
"v_mfma_f32_16x16x32_bf16 v[76:79], a[244:247], a[20:23], v[76:79]\n"
"ds_read_b64_tr_b16 a[238:239], v251 offset:54016\n"
"v_mul_f32 v21, s70, v21\n"
"v_mfma_f32_16x16x32_bf16 v[68:71], a[240:243], a[0:3], 0\n"
"v_mul_f32 v22, s70, v22\n"
"v_mul_f32 v23, s70, v23\n"
"v_mfma_f32_16x16x32_bf16 v[68:71], a[244:247], a[16:19], v[68:71]\n"
"s_waitcnt lgkmcnt(4)\n"
"v_cvt_pk_bf16_f32 v18, v20, v21\n"
"v_cvt_pk_bf16_f32 v19, v22, v23\n"
"v_mfma_f32_16x16x32_bf16 v[96:99], a[248:251], a[0:3], 0\n"
"s_barrier\n"
"ds_read_b64_tr_b16 a[240:241], v252 offset:0\n"
"v_mul_f32 v24, s70, v24\n"
"v_mfma_f32_16x16x32_bf16 v[96:99], a[252:255], a[16:19], v[96:99]\n"
"ds_read_b64_tr_b16 a[242:243], v252 offset:256\n"
"v_mul_f32 v25, s70, v25\n"
"v_mfma_f32_16x16x32_bf16 v[108:111], a[248:251], a[4:7], 0\n"
"v_permlane16_swap_b32 v16, v18\n"
"v_mfma_f32_16x16x32_bf16 v[108:111], a[252:255], a[20:23], v[108:111]\n"
"v_permlane16_swap_b32 v17, v19\n"
"v_mfma_f32_16x16x32_bf16 v[124:127], a[248:251], a[8:11], 0\n"
"ds_read_b64_tr_b16 a[244:245], v252 offset:4096\n"
"v_mul_f32 v26, s70, v26\n"
"v_mfma_f32_16x16x32_bf16 v[124:127], a[252:255], a[24:27], v[124:127]\n"
"ds_read_b64_tr_b16 a[246:247], v252 offset:4352\n"
"v_mul_f32 v27, s70, v27\n"
"v_mfma_f32_16x16x32_bf16 v[140:143], a[248:251], a[12:15], 0\n"
"v_cvt_pk_bf16_f32 v24, v24, v25\n"
"v_cvt_pk_bf16_f32 v25, v26, v27\n"
"v_mfma_f32_16x16x32_bf16 v[140:143], a[252:255], a[28:31], v[140:143]\n"
"s_waitcnt lgkmcnt(4)\n"
"v_mul_f32 v28, s70, v28\n"
"v_mul_f32 v29, s70, v29\n"
"v_mfma_f32_16x16x32_bf16 v[148:151], a[232:235], a[12:15], 0\n"
"ds_read_b64_tr_b16 a[248:249], v253 offset:0\n"
"v_mul_f32 v30, s70, v30\n"
"v_mfma_f32_16x16x32_bf16 v[148:151], a[236:239], a[28:31], v[148:151]\n"
"ds_read_b64_tr_b16 a[250:251], v253 offset:256\n"
"v_mul_f32 v31, s70, v31\n"
"v_mfma_f32_16x16x32_bf16 v[132:135], a[232:235], a[8:11], 0\n"
"v_cvt_pk_bf16_f32 v26, v28, v29\n"
"v_cvt_pk_bf16_f32 v27, v30, v31\n"
"v_mfma_f32_16x16x32_bf16 v[132:135], a[236:239], a[24:27], v[132:135]\n"
"v_mul_f32 v32, s70, v32\n"
"v_mul_f32 v33, s70, v33\n"
"v_mfma_f32_16x16x32_bf16 v[116:119], a[232:235], a[4:7], 0\n"
"ds_read_b64_tr_b16 a[252:253], v253 offset:4096\n"
"v_mul_f32 v34, s70, v34\n"
"v_mfma_f32_16x16x32_bf16 v[116:119], a[236:239], a[20:23], v[116:119]\n"
"ds_read_b64_tr_b16 a[254:255], v253 offset:4352\n"
"v_mul_f32 v35, s70, v35\n"
"v_mfma_f32_16x16x32_bf16 v[100:103], a[232:235], a[0:3], 0\n"
"v_permlane16_swap_b32 v24, v26\n"
"v_mfma_f32_16x16x32_bf16 v[100:103], a[236:239], a[16:19], v[100:103]\n"
"s_waitcnt lgkmcnt(4)\n"
"v_permlane16_swap_b32 v25, v27\n"
"v_mfma_f32_16x16x32_bf16 v[64:67], a[240:243], a[32:35], v[64:67]\n"
"ds_read_b64_tr_b16 a[232:233], v252 offset:512\n"
"v_cvt_pk_bf16_f32 v32, v32, v33\n"
"v_mfma_f32_16x16x32_bf16 v[64:67], a[244:247], a[48:51], v[64:67]\n"
"ds_read_b64_tr_b16 a[234:235], v252 offset:768\n"
"v_cvt_pk_bf16_f32 v33, v34, v35\n"
"v_mfma_f32_16x16x32_bf16 v[72:75], a[240:243], a[36:39], v[72:75]\n"
"v_mul_f32 v36, s70, v36\n"
"v_mul_f32 v37, s70, v37\n"
"v_mfma_f32_16x16x32_bf16 v[72:75], a[244:247], a[52:55], v[72:75]\n"
"v_mul_f32 v38, s70, v38\n"
"v_mul_f32 v39, s70, v39\n"
"v_mfma_f32_16x16x32_bf16 v[80:83], a[240:243], a[40:43], v[80:83]\n"
"ds_read_b64_tr_b16 a[236:237], v252 offset:4608\n"
"v_cvt_pk_bf16_f32 v34, v36, v37\n"
"v_mfma_f32_16x16x32_bf16 v[80:83], a[244:247], a[56:59], v[80:83]\n"
"ds_read_b64_tr_b16 a[238:239], v252 offset:4864\n"
"v_cvt_pk_bf16_f32 v35, v38, v39\n"
"v_mfma_f32_16x16x32_bf16 v[88:91], a[240:243], a[44:47], v[88:91]\n"
"v_permlane16_swap_b32 v32, v34\n"
"v_mfma_f32_16x16x32_bf16 v[88:91], a[244:247], a[60:63], v[88:91]\n"
"s_waitcnt lgkmcnt(4)\n"
"v_permlane16_swap_b32 v33, v35\n"
"v_mfma_f32_16x16x32_bf16 v[92:95], a[248:251], a[44:47], v[92:95]\n"
"ds_read_b64_tr_b16 a[240:241], v253 offset:512\n"
"v_mul_f32 v40, s70, v40\n"
"v_mfma_f32_16x16x32_bf16 v[92:95], a[252:255], a[60:63], v[92:95]\n"
"ds_read_b64_tr_b16 a[242:243], v253 offset:768\n"
"v_mul_f32 v41, s70, v41\n"
"v_mfma_f32_16x16x32_bf16 v[84:87], a[248:251], a[40:43], v[84:87]\n"
"v_mul_f32 v42, s70, v42\n"
"v_mul_f32 v43, s70, v43\n"
"v_mfma_f32_16x16x32_bf16 v[84:87], a[252:255], a[56:59], v[84:87]\n"
"v_cvt_pk_bf16_f32 v40, v40, v41\n"
"v_cvt_pk_bf16_f32 v41, v42, v43\n"
"v_mfma_f32_16x16x32_bf16 v[76:79], a[248:251], a[36:39], v[76:79]\n"
"ds_read_b64_tr_b16 a[244:245], v253 offset:4608\n"
"v_mul_f32 v44, s70, v44\n"
"v_mfma_f32_16x16x32_bf16 v[76:79], a[252:255], a[52:55], v[76:79]\n"
"ds_read_b64_tr_b16 a[246:247], v253 offset:4864\n"
"v_mul_f32 v45, s70, v45\n"
"v_mfma_f32_16x16x32_bf16 v[68:71], a[248:251], a[32:35], v[68:71]\n"
"v_mul_f32 v46, s70, v46\n"
"v_mul_f32 v47, s70, v47\n"
"v_mfma_f32_16x16x32_bf16 v[68:71], a[252:255], a[48:51], v[68:71]\n"
"s_waitcnt lgkmcnt(4)\n"
"v_cvt_pk_bf16_f32 v42, v44, v45\n"
"v_cvt_pk_bf16_f32 v43, v46, v47\n"
"v_mfma_f32_16x16x32_bf16 v[96:99], a[232:235], a[32:35], v[96:99]\n"
"s_barrier\n"
"ds_read_b64_tr_b16 a[248:249], v250 offset:0\n"
"v_mul_f32 v48, s70, v48\n"
"v_mfma_f32_16x16x32_bf16 v[96:99], a[236:239], a[48:51], v[96:99]\n"
"ds_read_b64_tr_b16 a[250:251], v250 offset:256\n"
"v_mul_f32 v49, s70, v49\n"
"v_mfma_f32_16x16x32_bf16 v[108:111], a[232:235], a[36:39], v[108:111]\n"
"v_permlane16_swap_b32 v40, v42\n"
"v_mfma_f32_16x16x32_bf16 v[108:111], a[236:239], a[52:55], v[108:111]\n"
"v_permlane16_swap_b32 v41, v43\n"
"v_mfma_f32_16x16x32_bf16 v[124:127], a[232:235], a[40:43], v[124:127]\n"
"ds_read_b64_tr_b16 a[252:253], v250 offset:4096\n"
"v_mul_f32 v50, s70, v50\n"
"v_mfma_f32_16x16x32_bf16 v[124:127], a[236:239], a[56:59], v[124:127]\n"
"ds_read_b64_tr_b16 a[254:255], v250 offset:4352\n"
"v_mul_f32 v51, s70, v51\n"
"v_mfma_f32_16x16x32_bf16 v[140:143], a[232:235], a[44:47], v[140:143]\n"
"v_cvt_pk_bf16_f32 v48, v48, v49\n"
"v_cvt_pk_bf16_f32 v49, v50, v51\n"
"v_mfma_f32_16x16x32_bf16 v[140:143], a[236:239], a[60:63], v[140:143]\n"
"s_waitcnt lgkmcnt(4)\n"
"v_mul_f32 v52, s70, v52\n"
"v_mul_f32 v53, s70, v53\n"
"v_mfma_f32_16x16x32_bf16 v[148:151], a[240:243], a[44:47], v[148:151]\n"
"ds_read_b64_tr_b16 a[232:233], v251 offset:0\n"
"v_mul_f32 v54, s70, v54\n"
"v_mfma_f32_16x16x32_bf16 v[148:151], a[244:247], a[60:63], v[148:151]\n"
"ds_read_b64_tr_b16 a[234:235], v251 offset:256\n"
"v_mul_f32 v55, s70, v55\n"
"v_mfma_f32_16x16x32_bf16 v[132:135], a[240:243], a[40:43], v[132:135]\n"
"v_cvt_pk_bf16_f32 v50, v52, v53\n"
"v_cvt_pk_bf16_f32 v51, v54, v55\n"
"v_mfma_f32_16x16x32_bf16 v[132:135], a[244:247], a[56:59], v[132:135]\n"
"v_mul_f32 v56, s70, v56\n"
"v_mul_f32 v57, s70, v57\n"
"v_mfma_f32_16x16x32_bf16 v[116:119], a[240:243], a[36:39], v[116:119]\n"
"ds_read_b64_tr_b16 a[236:237], v251 offset:4096\n"
"v_mul_f32 v58, s70, v58\n"
"v_mfma_f32_16x16x32_bf16 v[116:119], a[244:247], a[52:55], v[116:119]\n"
"ds_read_b64_tr_b16 a[238:239], v251 offset:4352\n"
"v_mul_f32 v59, s70, v59\n"
"v_mfma_f32_16x16x32_bf16 v[100:103], a[240:243], a[32:35], v[100:103]\n"
"v_permlane16_swap_b32 v48, v50\n"
"v_mfma_f32_16x16x32_bf16 v[100:103], a[244:247], a[48:51], v[100:103]\n"
"s_waitcnt lgkmcnt(4)\n"
"v_permlane16_swap_b32 v49, v51\n"
"v_mfma_f32_16x16x32_bf16 v[64:67], a[248:251], a[64:67], v[64:67]\n"
"ds_read_b64_tr_b16 a[240:241], v250 offset:512\n"
"v_cvt_pk_bf16_f32 v56, v56, v57\n"
"v_mfma_f32_16x16x32_bf16 v[64:67], a[252:255], a[80:83], v[64:67]\n"
"ds_read_b64_tr_b16 a[242:243], v250 offset:768\n"
"v_cvt_pk_bf16_f32 v57, v58, v59\n"
"v_mfma_f32_16x16x32_bf16 v[72:75], a[248:251], a[68:71], v[72:75]\n"
"v_mul_f32 v60, s70, v60\n"
"v_mul_f32 v61, s70, v61\n"
"v_mfma_f32_16x16x32_bf16 v[72:75], a[252:255], a[84:87], v[72:75]\n"
"v_mul_f32 v62, s70, v62\n"
"v_mul_f32 v63, s70, v63\n"
"v_mfma_f32_16x16x32_bf16 v[80:83], a[248:251], a[72:75], v[80:83]\n"
"ds_read_b64_tr_b16 a[244:245], v250 offset:4608\n"
"v_cvt_pk_bf16_f32 v58, v60, v61\n"
"v_mfma_f32_16x16x32_bf16 v[80:83], a[252:255], a[88:91], v[80:83]\n"
"ds_read_b64_tr_b16 a[246:247], v250 offset:4864\n"
"v_cvt_pk_bf16_f32 v59, v62, v63\n"
"v_mfma_f32_16x16x32_bf16 v[88:91], a[248:251], a[76:79], v[88:91]\n"
"v_permlane16_swap_b32 v56, v58\n"
"v_mfma_f32_16x16x32_bf16 v[88:91], a[252:255], a[92:95], v[88:91]\n"
"s_waitcnt lgkmcnt(4)\n"
"v_permlane16_swap_b32 v57, v59\n"
"v_mfma_f32_16x16x32_bf16 v[92:95], a[232:235], a[76:79], v[92:95]\n"
"ds_read_b64_tr_b16 a[248:249], v251 offset:512\n"
"v_accvgpr_write_b32 a0, v104\n"
"v_mfma_f32_16x16x32_bf16 v[92:95], a[236:239], a[92:95], v[92:95]\n"
"ds_read_b64_tr_b16 a[250:251], v251 offset:768\n"
"v_accvgpr_write_b32 a1, v105\n"
"v_mfma_f32_16x16x32_bf16 v[84:87], a[232:235], a[72:75], v[84:87]\n"
"v_accvgpr_write_b32 a2, v106\n"
"v_accvgpr_write_b32 a3, v107\n"
"v_mfma_f32_16x16x32_bf16 v[84:87], a[236:239], a[88:91], v[84:87]\n"
"v_accvgpr_write_b32 a4, v112\n"
"v_accvgpr_write_b32 a5, v113\n"
"v_mfma_f32_16x16x32_bf16 v[76:79], a[232:235], a[68:71], v[76:79]\n"
"ds_read_b64_tr_b16 a[252:253], v251 offset:4608\n"
"v_accvgpr_write_b32 a6, v114\n"
"v_mfma_f32_16x16x32_bf16 v[76:79], a[236:239], a[84:87], v[76:79]\n"
"ds_read_b64_tr_b16 a[254:255], v251 offset:4864\n"
"v_accvgpr_write_b32 a7, v115\n"
"v_mfma_f32_16x16x32_bf16 v[68:71], a[232:235], a[64:67], v[68:71]\n"
"v_accvgpr_write_b32 a8, v120\n"
"v_accvgpr_write_b32 a9, v121\n"
"v_mfma_f32_16x16x32_bf16 v[68:71], a[236:239], a[80:83], v[68:71]\n"
"s_waitcnt lgkmcnt(4)\n"
"v_accvgpr_write_b32 a10, v122\n"
"v_accvgpr_write_b32 a11, v123\n"
"v_mfma_f32_16x16x32_bf16 v[96:99], a[240:243], a[64:67], v[96:99]\n"
"s_barrier\n"
"ds_read_b64_tr_b16 a[232:233], v250 offset:16384\n"
"v_accvgpr_write_b32 a12, v128\n"
"v_mfma_f32_16x16x32_bf16 v[96:99], a[244:247], a[80:83], v[96:99]\n"
"ds_read_b64_tr_b16 a[234:235], v250 offset:16640\n"
"v_mfma_f32_16x16x32_bf16 v[108:111], a[240:243], a[68:71], v[108:111]\n"
"v_accvgpr_write_b32 a13, v129\n"
"v_accvgpr_write_b32 a14, v130\n"
"v_mfma_f32_16x16x32_bf16 v[108:111], a[244:247], a[84:87], v[108:111]\n"
"v_accvgpr_write_b32 a15, v131\n"
"v_accvgpr_write_b32 a16, v136\n"
"v_mfma_f32_16x16x32_bf16 v[124:127], a[240:243], a[72:75], v[124:127]\n"
"ds_read_b64_tr_b16 a[236:237], v250 offset:20480\n"
"v_accvgpr_write_b32 a17, v137\n"
"v_mfma_f32_16x16x32_bf16 v[124:127], a[244:247], a[88:91], v[124:127]\n"
"ds_read_b64_tr_b16 a[238:239], v250 offset:20736\n"
"v_accvgpr_write_b32 a18, v138\n"
"v_mfma_f32_16x16x32_bf16 v[140:143], a[240:243], a[76:79], v[140:143]\n"
"v_accvgpr_write_b32 a19, v139\n"
"v_mfma_f32_16x16x32_bf16 v[140:143], a[244:247], a[92:95], v[140:143]\n"
"s_waitcnt lgkmcnt(4)\n"
"v_accvgpr_write_b32 a20, v144\n"
"v_accvgpr_write_b32 a21, v145\n"
"v_mfma_f32_16x16x32_bf16 v[148:151], a[248:251], a[76:79], v[148:151]\n"
"ds_read_b64_tr_b16 a[240:241], v251 offset:16384\n"
"v_accvgpr_write_b32 a22, v146\n"
"v_mfma_f32_16x16x32_bf16 v[148:151], a[252:255], a[92:95], v[148:151]\n"
"ds_read_b64_tr_b16 a[242:243], v251 offset:16640\n"
"v_accvgpr_write_b32 a23, v147\n"
"v_mfma_f32_16x16x32_bf16 v[132:135], a[248:251], a[72:75], v[132:135]\n"
"v_accvgpr_write_b32 a24, v152\n"
"v_mfma_f32_16x16x32_bf16 v[132:135], a[252:255], a[88:91], v[132:135]\n"
"v_accvgpr_write_b32 a25, v153\n"
"v_accvgpr_write_b32 a26, v154\n"
"v_mfma_f32_16x16x32_bf16 v[116:119], a[248:251], a[68:71], v[116:119]\n"
"ds_read_b64_tr_b16 a[244:245], v251 offset:20480\n"
"v_accvgpr_write_b32 a27, v155\n"
"v_mfma_f32_16x16x32_bf16 v[116:119], a[252:255], a[84:87], v[116:119]\n"
"ds_read_b64_tr_b16 a[246:247], v251 offset:20736\n"
"v_accvgpr_write_b32 a28, v160\n"
"v_mfma_f32_16x16x32_bf16 v[100:103], a[248:251], a[64:67], v[100:103]\n"
"v_accvgpr_write_b32 a29, v161\n"
"v_mfma_f32_16x16x32_bf16 v[100:103], a[252:255], a[80:83], v[100:103]\n"
"s_waitcnt lgkmcnt(4)\n"
"v_accvgpr_write_b32 a30, v162\n"
"v_accvgpr_write_b32 a31, v163\n"
"v_mfma_f32_16x16x32_bf16 v[64:67], a[232:235], a[96:99], v[64:67]\n"
"ds_read_b64_tr_b16 a[248:249], v250 offset:16896\n"
"v_accvgpr_write_b32 a32, v168\n"
"v_mfma_f32_16x16x32_bf16 v[64:67], a[236:239], a[112:115], v[64:67]\n"
"ds_read_b64_tr_b16 a[250:251], v250 offset:17152\n"
"v_accvgpr_write_b32 a33, v169\n"
"v_mfma_f32_16x16x32_bf16 v[72:75], a[232:235], a[100:103], v[72:75]\n"
"v_accvgpr_write_b32 a34, v170\n"
"v_mfma_f32_16x16x32_bf16 v[72:75], a[236:239], a[116:119], v[72:75]\n"
"v_accvgpr_write_b32 a35, v171\n"
"v_accvgpr_write_b32 a36, v176\n"
"v_mfma_f32_16x16x32_bf16 v[80:83], a[232:235], a[104:107], v[80:83]\n"
"ds_read_b64_tr_b16 a[252:253], v250 offset:20992\n"
"v_accvgpr_write_b32 a37, v177\n"
"v_mfma_f32_16x16x32_bf16 v[80:83], a[236:239], a[120:123], v[80:83]\n"
"ds_read_b64_tr_b16 a[254:255], v250 offset:21248\n"
"v_accvgpr_write_b32 a38, v178\n"
"v_mfma_f32_16x16x32_bf16 v[88:91], a[232:235], a[108:111], v[88:91]\n"
"v_accvgpr_write_b32 a39, v179\n"
"v_accvgpr_write_b32 a40, v184\n"
"v_mfma_f32_16x16x32_bf16 v[88:91], a[236:239], a[124:127], v[88:91]\n"
"s_waitcnt lgkmcnt(4)\n"
"v_accvgpr_write_b32 a41, v185\n"
"v_mfma_f32_16x16x32_bf16 v[68:71], a[240:243], a[96:99], v[68:71]\n"
"ds_read_b64_tr_b16 a[232:233], v251 offset:16896\n"
"v_accvgpr_write_b32 a42, v186\n"
"v_mfma_f32_16x16x32_bf16 v[68:71], a[244:247], a[112:115], v[68:71]\n"
"ds_read_b64_tr_b16 a[234:235], v251 offset:17152\n"
"v_accvgpr_write_b32 a43, v187\n"
"v_mfma_f32_16x16x32_bf16 v[76:79], a[240:243], a[100:103], v[76:79]\n"
"v_accvgpr_write_b32 a44, v192\n"
"v_accvgpr_write_b32 a45, v193\n"
"v_mfma_f32_16x16x32_bf16 v[76:79], a[244:247], a[116:119], v[76:79]\n"
"v_accvgpr_write_b32 a46, v194\n"
"v_mfma_f32_16x16x32_bf16 v[84:87], a[240:243], a[104:107], v[84:87]\n"
"ds_read_b64_tr_b16 a[236:237], v251 offset:20992\n"
"v_accvgpr_write_b32 a47, v195\n"
"v_mfma_f32_16x16x32_bf16 v[84:87], a[244:247], a[120:123], v[84:87]\n"
"ds_read_b64_tr_b16 a[238:239], v251 offset:21248\n"
"v_accvgpr_write_b32 a48, v200\n"
"v_mfma_f32_16x16x32_bf16 v[92:95], a[240:243], a[108:111], v[92:95]\n"
"v_accvgpr_write_b32 a49, v201\n"
"v_accvgpr_write_b32 a50, v202\n"
"v_mfma_f32_16x16x32_bf16 v[92:95], a[244:247], a[124:127], v[92:95]\n"
"s_waitcnt lgkmcnt(4)\n"
"v_accvgpr_write_b32 a51, v203\n"
"v_mfma_f32_16x16x32_bf16 v[96:99], a[248:251], a[96:99], v[96:99]\n"
"s_barrier\n"
"ds_read_b64_tr_b16 a[240:241], v250 offset:32768\n"
"v_accvgpr_write_b32 a52, v208\n"
"v_mfma_f32_16x16x32_bf16 v[96:99], a[252:255], a[112:115], v[96:99]\n"
"ds_read_b64_tr_b16 a[242:243], v250 offset:33024\n"
"v_accvgpr_write_b32 a53, v209\n"
"v_mfma_f32_16x16x32_bf16 v[108:111], a[248:251], a[100:103], v[108:111]\n"
"v_accvgpr_write_b32 a54, v210\n"
"v_accvgpr_write_b32 a55, v211\n"
"v_mfma_f32_16x16x32_bf16 v[108:111], a[252:255], a[116:119], v[108:111]\n"
"v_accvgpr_write_b32 a56, v216\n"
"v_accvgpr_write_b32 a57, v217\n"
"v_mfma_f32_16x16x32_bf16 v[124:127], a[248:251], a[104:107], v[124:127]\n"
"ds_read_b64_tr_b16 a[244:245], v250 offset:36864\n"
"v_mfma_f32_16x16x32_bf16 v[124:127], a[252:255], a[120:123], v[124:127]\n"
"ds_read_b64_tr_b16 a[246:247], v250 offset:37120\n"
"v_accvgpr_write_b32 a58, v218\n"
"v_mfma_f32_16x16x32_bf16 v[140:143], a[248:251], a[108:111], v[140:143]\n"
"v_accvgpr_write_b32 a59, v219\n"
"v_accvgpr_write_b32 a60, v224\n"
"v_mfma_f32_16x16x32_bf16 v[140:143], a[252:255], a[124:127], v[140:143]\n"
"s_waitcnt lgkmcnt(4)\n"
"v_accvgpr_write_b32 a61, v225\n"
"v_accvgpr_write_b32 a62, v226\n"
"v_mfma_f32_16x16x32_bf16 v[100:103], a[232:235], a[96:99], v[100:103]\n"
"ds_read_b64_tr_b16 a[248:249], v251 offset:32768\n"
"v_mfma_f32_16x16x32_bf16 v[100:103], a[236:239], a[112:115], v[100:103]\n"
"ds_read_b64_tr_b16 a[250:251], v251 offset:33024\n"
"v_accvgpr_write_b32 a63, v227\n"
"v_mfma_f32_16x16x32_bf16 v[116:119], a[232:235], a[100:103], v[116:119]\n"
"v_accvgpr_write_b32 a64, v0\n"
"v_accvgpr_write_b32 a65, v1\n"
"v_mfma_f32_16x16x32_bf16 v[116:119], a[236:239], a[116:119], v[116:119]\n"
"v_accvgpr_write_b32 a66, v2\n"
"v_accvgpr_write_b32 a67, v3\n"
"v_mfma_f32_16x16x32_bf16 v[132:135], a[232:235], a[104:107], v[132:135]\n"
"ds_read_b64_tr_b16 a[252:253], v251 offset:36864\n"
"v_accvgpr_write_b32 a68, v8\n"
"v_mfma_f32_16x16x32_bf16 v[132:135], a[236:239], a[120:123], v[132:135]\n"
"ds_read_b64_tr_b16 a[254:255], v251 offset:37120\n"
"v_mfma_f32_16x16x32_bf16 v[148:151], a[232:235], a[108:111], v[148:151]\n"
"v_accvgpr_write_b32 a69, v9\n"
"v_accvgpr_write_b32 a70, v10\n"
"v_mfma_f32_16x16x32_bf16 v[148:151], a[236:239], a[124:127], v[148:151]\n"
"s_waitcnt lgkmcnt(4)\n"
"v_accvgpr_write_b32 a71, v11\n"
"v_accvgpr_write_b32 a72, v16\n"
"v_mfma_f32_16x16x32_bf16 v[64:67], a[240:243], a[128:131], v[64:67]\n"
"s_barrier\n"
"ds_read_b64_tr_b16 a[232:233], v250 offset:49152\n"
"v_accvgpr_write_b32 a73, v17\n"
"v_mfma_f32_16x16x32_bf16 v[64:67], a[244:247], a[144:147], v[64:67]\n"
"ds_read_b64_tr_b16 a[234:235], v250 offset:49408\n"
"v_mfma_f32_16x16x32_bf16 v[72:75], a[240:243], a[132:135], v[72:75]\n"
"v_accvgpr_write_b32 a74, v18\n"
"v_accvgpr_write_b32 a75, v19\n"
"v_mfma_f32_16x16x32_bf16 v[72:75], a[244:247], a[148:151], v[72:75]\n"
"v_accvgpr_write_b32 a76, v24\n"
"v_accvgpr_write_b32 a77, v25\n"
"v_mfma_f32_16x16x32_bf16 v[80:83], a[240:243], a[136:139], v[80:83]\n"
"ds_read_b64_tr_b16 a[236:237], v250 offset:53248\n"
"v_accvgpr_write_b32 a78, v26\n"
"v_mfma_f32_16x16x32_bf16 v[80:83], a[244:247], a[152:155], v[80:83]\n"
"ds_read_b64_tr_b16 a[238:239], v250 offset:53504\n"
"v_accvgpr_write_b32 a79, v27\n"
"v_mfma_f32_16x16x32_bf16 v[88:91], a[240:243], a[140:143], v[88:91]\n"
"v_accvgpr_write_b32 a80, v32\n"
"v_mfma_f32_16x16x32_bf16 v[88:91], a[244:247], a[156:159], v[88:91]\n"
"s_waitcnt lgkmcnt(4)\n"
"v_accvgpr_write_b32 a81, v33\n"
"v_accvgpr_write_b32 a82, v34\n"
"v_mfma_f32_16x16x32_bf16 v[68:71], a[248:251], a[128:131], v[68:71]\n"
"ds_read_b64_tr_b16 a[240:241], v251 offset:49152\n"
"v_accvgpr_write_b32 a83, v35\n"
"v_mfma_f32_16x16x32_bf16 v[68:71], a[252:255], a[144:147], v[68:71]\n"
"ds_read_b64_tr_b16 a[242:243], v251 offset:49408\n"
"v_accvgpr_write_b32 a84, v40\n"
"v_mfma_f32_16x16x32_bf16 v[76:79], a[248:251], a[132:135], v[76:79]\n"
"v_accvgpr_write_b32 a85, v41\n"
"v_mfma_f32_16x16x32_bf16 v[76:79], a[252:255], a[148:151], v[76:79]\n"
"v_accvgpr_write_b32 a86, v42\n"
"v_accvgpr_write_b32 a87, v43\n"
"v_mfma_f32_16x16x32_bf16 v[84:87], a[248:251], a[136:139], v[84:87]\n"
"ds_read_b64_tr_b16 a[244:245], v251 offset:53248\n"
"v_accvgpr_write_b32 a88, v48\n"
"v_mfma_f32_16x16x32_bf16 v[84:87], a[252:255], a[152:155], v[84:87]\n"
"ds_read_b64_tr_b16 a[246:247], v251 offset:53504\n"
"v_accvgpr_write_b32 a89, v49\n"
"v_mfma_f32_16x16x32_bf16 v[92:95], a[248:251], a[140:143], v[92:95]\n"
"v_accvgpr_write_b32 a90, v50\n"
"v_mfma_f32_16x16x32_bf16 v[92:95], a[252:255], a[156:159], v[92:95]\n"
"s_waitcnt lgkmcnt(4)\n"
"v_accvgpr_write_b32 a91, v51\n"
"v_accvgpr_write_b32 a92, v56\n"
"v_mfma_f32_16x16x32_bf16 v[64:67], a[232:235], a[160:163], v[64:67]\n"
"s_barrier\n"
"ds_read_b64_tr_b16 a[248:249], v252 offset:0\n"
"v_accvgpr_write_b32 a93, v57\n"
"v_mfma_f32_16x16x32_bf16 v[64:67], a[236:239], a[176:179], v[64:67]\n"
"ds_read_b64_tr_b16 a[250:251], v252 offset:256\n"
"v_accvgpr_write_b32 a94, v58\n"
"v_mfma_f32_16x16x32_bf16 v[72:75], a[232:235], a[164:167], v[72:75]\n"
"v_accvgpr_write_b32 a95, v59\n"
"v_mfma_f32_16x16x32_bf16 v[72:75], a[236:239], a[180:183], v[72:75]\n"
"v_mfma_f32_16x16x32_bf16 v[80:83], a[232:235], a[168:171], v[80:83]\n"
"ds_read_b64_tr_b16 a[252:253], v252 offset:4096\n"
"v_mfma_f32_16x16x32_bf16 v[80:83], a[236:239], a[184:187], v[80:83]\n"
"ds_read_b64_tr_b16 a[254:255], v252 offset:4352\n"
"v_mfma_f32_16x16x32_bf16 v[88:91], a[232:235], a[172:175], v[88:91]\n"
"v_mfma_f32_16x16x32_bf16 v[88:91], a[236:239], a[188:191], v[88:91]\n"
"s_waitcnt lgkmcnt(4)\n"
"v_mfma_f32_16x16x32_bf16 v[68:71], a[240:243], a[160:163], v[68:71]\n"
"ds_read_b64_tr_b16 a[232:233], v253 offset:0\n"
"v_mfma_f32_16x16x32_bf16 v[68:71], a[244:247], a[176:179], v[68:71]\n"
"ds_read_b64_tr_b16 a[234:235], v253 offset:256\n"
"v_mfma_f32_16x16x32_bf16 v[76:79], a[240:243], a[164:167], v[76:79]\n"
"v_mfma_f32_16x16x32_bf16 v[76:79], a[244:247], a[180:183], v[76:79]\n"
"v_mfma_f32_16x16x32_bf16 v[84:87], a[240:243], a[168:171], v[84:87]\n"
"ds_read_b64_tr_b16 a[236:237], v253 offset:4096\n"
"v_mfma_f32_16x16x32_bf16 v[84:87], a[244:247], a[184:187], v[84:87]\n"
"ds_read_b64_tr_b16 a[238:239], v253 offset:4352\n"
"v_mfma_f32_16x16x32_bf16 v[92:95], a[240:243], a[172:175], v[92:95]\n"
"v_mfma_f32_16x16x32_bf16 v[92:95], a[244:247], a[188:191], v[92:95]\n"
"s_waitcnt lgkmcnt(4)\n"
"v_mfma_f32_16x16x32_bf16 v[64:67], a[248:251], a[192:195], v[64:67]\n"
"ds_read_b64_tr_b16 a[240:241], v250 offset:33280\n"
"v_mfma_f32_16x16x32_bf16 v[64:67], a[252:255], a[208:211], v[64:67]\n"
"ds_read_b64_tr_b16 a[242:243], v250 offset:33536\n"
"v_mfma_f32_16x16x32_bf16 v[72:75], a[248:251], a[196:199], v[72:75]\n"
"v_mfma_f32_16x16x32_bf16 v[72:75], a[252:255], a[212:215], v[72:75]\n"
"v_mfma_f32_16x16x32_bf16 v[80:83], a[248:251], a[200:203], v[80:83]\n"
"ds_read_b64_tr_b16 a[244:245], v250 offset:37376\n"
"v_mfma_f32_16x16x32_bf16 v[80:83], a[252:255], a[216:219], v[80:83]\n"
"ds_read_b64_tr_b16 a[246:247], v250 offset:37632\n"
"v_mfma_f32_16x16x32_bf16 v[88:91], a[248:251], a[204:207], v[88:91]\n"
"v_mfma_f32_16x16x32_bf16 v[88:91], a[252:255], a[220:223], v[88:91]\n"
"s_waitcnt lgkmcnt(4)\n"
"v_mfma_f32_16x16x32_bf16 v[68:71], a[232:235], a[192:195], v[68:71]\n"
"ds_read_b64_tr_b16 a[248:249], v251 offset:33280\n"
"v_mfma_f32_16x16x32_bf16 v[68:71], a[236:239], a[208:211], v[68:71]\n"
"ds_read_b64_tr_b16 a[250:251], v251 offset:33536\n"
"v_mfma_f32_16x16x32_bf16 v[76:79], a[232:235], a[196:199], v[76:79]\n"
"v_mfma_f32_16x16x32_bf16 v[76:79], a[236:239], a[212:215], v[76:79]\n"
"v_mfma_f32_16x16x32_bf16 v[84:87], a[232:235], a[200:203], v[84:87]\n"
"ds_read_b64_tr_b16 a[252:253], v251 offset:37376\n"
"v_mfma_f32_16x16x32_bf16 v[84:87], a[236:239], a[216:219], v[84:87]\n"
"ds_read_b64_tr_b16 a[254:255], v251 offset:37632\n"
"v_mfma_f32_16x16x32_bf16 v[92:95], a[232:235], a[204:207], v[92:95]\n"
"v_mul_f32 v64, s70, v64\n"
"v_mul_f32 v65, s70, v65\n"
"v_mfma_f32_16x16x32_bf16 v[92:95], a[236:239], a[220:223], v[92:95]\n"
"s_waitcnt lgkmcnt(4)\n"
"v_mul_f32 v66, s70, v66\n"
"v_mul_f32 v67, s70, v67\n"
"v_mfma_f32_16x16x32_bf16 v[96:99], a[240:243], a[128:131], v[96:99]\n"
"ds_read_b64_tr_b16 a[232:233], v250 offset:49664\n"
"v_cvt_pk_bf16_f32 v64, v64, v65\n"
"v_mfma_f32_16x16x32_bf16 v[96:99], a[244:247], a[144:147], v[96:99]\n"
"ds_read_b64_tr_b16 a[234:235], v250 offset:49920\n"
"v_cvt_pk_bf16_f32 v65, v66, v67\n"
"v_mfma_f32_16x16x32_bf16 v[108:111], a[240:243], a[132:135], v[108:111]\n"
"v_mul_f32 v68, s70, v68\n"
"v_mul_f32 v69, s70, v69\n"
"v_mfma_f32_16x16x32_bf16 v[108:111], a[244:247], a[148:151], v[108:111]\n"
"v_mul_f32 v70, s70, v70\n"
"v_mul_f32 v71, s70, v71\n"
"v_mfma_f32_16x16x32_bf16 v[124:127], a[240:243], a[136:139], v[124:127]\n"
"ds_read_b64_tr_b16 a[236:237], v250 offset:53760\n"
"v_cvt_pk_bf16_f32 v66, v68, v69\n"
"v_mfma_f32_16x16x32_bf16 v[124:127], a[244:247], a[152:155], v[124:127]\n"
"ds_read_b64_tr_b16 a[238:239], v250 offset:54016\n"
"v_cvt_pk_bf16_f32 v67, v70, v71\n"
"v_mul_f32 v72, s70, v72\n"
"v_mfma_f32_16x16x32_bf16 v[140:143], a[240:243], a[140:143], v[140:143]\n"
"v_permlane16_swap_b32 v64, v66\n"
"v_mfma_f32_16x16x32_bf16 v[140:143], a[244:247], a[156:159], v[140:143]\n"
"s_waitcnt lgkmcnt(4)\n"
"v_permlane16_swap_b32 v65, v67\n"
"v_mfma_f32_16x16x32_bf16 v[100:103], a[248:251], a[128:131], v[100:103]\n"
"ds_read_b64_tr_b16 a[240:241], v251 offset:49664\n"
"v_accvgpr_write_b32 a96, v64\n"
"v_mfma_f32_16x16x32_bf16 v[100:103], a[252:255], a[144:147], v[100:103]\n"
"ds_read_b64_tr_b16 a[242:243], v251 offset:49920\n"
"v_accvgpr_write_b32 a97, v65\n"
"v_mfma_f32_16x16x32_bf16 v[116:119], a[248:251], a[132:135], v[116:119]\n"
"v_accvgpr_write_b32 a98, v66\n"
"v_accvgpr_write_b32 a99, v67\n"
"v_mfma_f32_16x16x32_bf16 v[116:119], a[252:255], a[148:151], v[116:119]\n"
"v_mul_f32 v73, s70, v73\n"
"v_mul_f32 v74, s70, v74\n"
"v_mfma_f32_16x16x32_bf16 v[132:135], a[248:251], a[136:139], v[132:135]\n"
"ds_read_b64_tr_b16 a[244:245], v251 offset:53760\n"
"v_mul_f32 v75, s70, v75\n"
"v_mfma_f32_16x16x32_bf16 v[132:135], a[252:255], a[152:155], v[132:135]\n"
"ds_read_b64_tr_b16 a[246:247], v251 offset:54016\n"
"v_cvt_pk_bf16_f32 v72, v72, v73\n"
"v_mfma_f32_16x16x32_bf16 v[148:151], a[248:251], a[140:143], v[148:151]\n"
"v_cvt_pk_bf16_f32 v73, v74, v75\n"
"v_mul_f32 v76, s70, v76\n"
"v_mfma_f32_16x16x32_bf16 v[148:151], a[252:255], a[156:159], v[148:151]\n"
"s_waitcnt lgkmcnt(4)\n"
"v_mul_f32 v77, s70, v77\n"
"v_mul_f32 v78, s70, v78\n"
"v_mul_f32 v79, s70, v79\n"
"v_mfma_f32_16x16x32_bf16 v[96:99], a[232:235], a[160:163], v[96:99]\n"
"ds_read_b64_tr_b16 a[248:249], v252 offset:512\n"
"v_cvt_pk_bf16_f32 v74, v76, v77\n"
"v_mfma_f32_16x16x32_bf16 v[96:99], a[236:239], a[176:179], v[96:99]\n"
"ds_read_b64_tr_b16 a[250:251], v252 offset:768\n"
"v_cvt_pk_bf16_f32 v75, v78, v79\n"
"v_mfma_f32_16x16x32_bf16 v[108:111], a[232:235], a[164:167], v[108:111]\n"
"v_permlane16_swap_b32 v72, v74\n"
"v_mfma_f32_16x16x32_bf16 v[108:111], a[236:239], a[180:183], v[108:111]\n"
"v_permlane16_swap_b32 v73, v75\n"
"v_mfma_f32_16x16x32_bf16 v[124:127], a[232:235], a[168:171], v[124:127]\n"
"ds_read_b64_tr_b16 a[252:253], v252 offset:4608\n"
"v_accvgpr_write_b32 a100, v72\n"
"v_mfma_f32_16x16x32_bf16 v[124:127], a[236:239], a[184:187], v[124:127]\n"
"ds_read_b64_tr_b16 a[254:255], v252 offset:4864\n"
"v_accvgpr_write_b32 a101, v73\n"
"v_mfma_f32_16x16x32_bf16 v[140:143], a[232:235], a[172:175], v[140:143]\n"
"v_accvgpr_write_b32 a102, v74\n"
"v_accvgpr_write_b32 a103, v75\n"
"v_mfma_f32_16x16x32_bf16 v[140:143], a[236:239], a[188:191], v[140:143]\n"
"s_waitcnt lgkmcnt(4)\n"
"v_mul_f32 v80, s70, v80\n"
"v_mul_f32 v81, s70, v81\n"
"v_mfma_f32_16x16x32_bf16 v[100:103], a[240:243], a[160:163], v[100:103]\n"
"ds_read_b64_tr_b16 a[232:233], v253 offset:512\n"
"v_mul_f32 v82, s70, v82\n"
"v_mfma_f32_16x16x32_bf16 v[100:103], a[244:247], a[176:179], v[100:103]\n"
"ds_read_b64_tr_b16 a[234:235], v253 offset:768\n"
"v_mul_f32 v83, s70, v83\n"
"v_cvt_pk_bf16_f32 v80, v80, v81\n"
"v_mfma_f32_16x16x32_bf16 v[116:119], a[240:243], a[164:167], v[116:119]\n"
"v_cvt_pk_bf16_f32 v81, v82, v83\n"
"v_mul_f32 v84, s70, v84\n"
"v_mfma_f32_16x16x32_bf16 v[116:119], a[244:247], a[180:183], v[116:119]\n"
"v_mul_f32 v85, s70, v85\n"
"v_mul_f32 v86, s70, v86\n"
"v_mfma_f32_16x16x32_bf16 v[132:135], a[240:243], a[168:171], v[132:135]\n"
"ds_read_b64_tr_b16 a[236:237], v253 offset:4608\n"
"v_mul_f32 v87, s70, v87\n"
"v_mfma_f32_16x16x32_bf16 v[132:135], a[244:247], a[184:187], v[132:135]\n"
"ds_read_b64_tr_b16 a[238:239], v253 offset:4864\n"
"v_cvt_pk_bf16_f32 v82, v84, v85\n"
"v_mfma_f32_16x16x32_bf16 v[148:151], a[240:243], a[172:175], v[148:151]\n"
"v_cvt_pk_bf16_f32 v83, v86, v87\n"
"v_mul_f32 v88, s70, v88\n"
"v_mfma_f32_16x16x32_bf16 v[148:151], a[244:247], a[188:191], v[148:151]\n"
"s_waitcnt lgkmcnt(4)\n"
"v_permlane16_swap_b32 v80, v82\n"
"v_mfma_f32_16x16x32_bf16 v[96:99], a[248:251], a[192:195], v[96:99]\n"
"s_waitcnt vmcnt(0)\n"
"s_barrier\n"
"ds_read_b128 a[240:243], v234 offset:0\n"
"v_mfma_f32_16x16x32_bf16 v[96:99], a[252:255], a[208:211], v[96:99]\n"
"v_permlane16_swap_b32 v81, v83\n"
"v_mfma_f32_16x16x32_bf16 v[108:111], a[248:251], a[196:199], v[108:111]\n"
"v_accvgpr_write_b32 a104, v80\n"
"v_accvgpr_write_b32 a105, v81\n"
"v_mfma_f32_16x16x32_bf16 v[108:111], a[252:255], a[212:215], v[108:111]\n"
"v_accvgpr_write_b32 a106, v82\n"
"v_accvgpr_write_b32 a107, v83\n"
"v_mul_f32 v89, s70, v89\n"
"v_mfma_f32_16x16x32_bf16 v[124:127], a[248:251], a[200:203], v[124:127]\n"
"ds_read_b128 a[244:247], v234 offset:512\n"
"v_mul_f32 v90, s70, v90\n"
"v_mfma_f32_16x16x32_bf16 v[124:127], a[252:255], a[216:219], v[124:127]\n"
"v_mul_f32 v91, s70, v91\n"
"v_cvt_pk_bf16_f32 v88, v88, v89\n"
"v_mfma_f32_16x16x32_bf16 v[140:143], a[248:251], a[204:207], v[140:143]\n"
"v_cvt_pk_bf16_f32 v89, v90, v91\n"
"v_mul_f32 v92, s70, v92\n"
"v_mfma_f32_16x16x32_bf16 v[140:143], a[252:255], a[220:223], v[140:143]\n"
"s_waitcnt lgkmcnt(2)\n"
"v_mul_f32 v93, s70, v93\n"
"v_mul_f32 v94, s70, v94\n"
"v_mfma_f32_16x16x32_bf16 v[100:103], a[232:235], a[192:195], v[100:103]\n"
"ds_read_b128 a[248:251], v234 offset:2048\n"
"v_mul_f32 v95, s70, v95\n"
"v_mfma_f32_16x16x32_bf16 v[100:103], a[236:239], a[208:211], v[100:103]\n"
"v_cvt_pk_bf16_f32 v90, v92, v93\n"
"v_cvt_pk_bf16_f32 v91, v94, v95\n"
"v_mfma_f32_16x16x32_bf16 v[116:119], a[232:235], a[196:199], v[116:119]\n"
"v_mfma_f32_16x16x32_bf16 v[116:119], a[236:239], a[212:215], v[116:119]\n"
"v_permlane16_swap_b32 v88, v90\n"
"v_mfma_f32_16x16x32_bf16 v[132:135], a[232:235], a[200:203], v[132:135]\n"
"ds_read_b128 a[252:255], v234 offset:2560\n"
"v_accvgpr_write_b32 a108, v88\n"
"v_mfma_f32_16x16x32_bf16 v[132:135], a[236:239], a[216:219], v[132:135]\n"
"v_and_b32 v255, 0xfff, v246\n"
"v_lshlrev_b32 v255, 3, v255\n"
"v_accvgpr_write_b32 a110, v90\n"
"v_mfma_f32_16x16x32_bf16 v[148:151], a[232:235], a[204:207], v[148:151]\n"
"v_permlane16_swap_b32 v89, v91\n"
"v_mfma_f32_16x16x32_bf16 v[148:151], a[236:239], a[220:223], v[148:151]\n"
"s_waitcnt lgkmcnt(2)\n"
"v_accvgpr_write_b32 a109, v89\n"
"v_accvgpr_write_b32 a111, v91\n"
"L_JOIN:\n"
"v_mfma_f32_16x16x32_bf16 v[168:171], a[240:243], a[0:3], 0\n"
"ds_read_b128 a[232:235], v234 offset:4096\n"
"v_mfma_f32_16x16x32_bf16 v[168:171], a[244:247], a[16:19], v[168:171]\n"
"v_mul_f32 v96, s70, v96\n"
"v_mul_f32 v97, s70, v97\n"
"v_mfma_f32_16x16x32_bf16 v[176:179], a[240:243], a[4:7], 0\n"
"v_mul_f32 v98, s70, v98\n"
"v_mul_f32 v99, s70, v99\n"
"v_mfma_f32_16x16x32_bf16 v[176:179], a[244:247], a[20:23], v[176:179]\n"
"v_cvt_pk_bf16_f32 v96, v96, v97\n"
"v_cvt_pk_bf16_f32 v97, v98, v99\n"
"v_mul_f32 v100, s70, v100\n"
"v_mfma_f32_16x16x32_bf16 v[184:187], a[240:243], a[8:11], 0\n"
"ds_read_b128 a[236:239], v234 offset:4608\n"
"v_mul_f32 v101, s70, v101\n"
"v_mfma_f32_16x16x32_bf16 v[184:187], a[244:247], a[24:27], v[184:187]\n"
"v_mul_f32 v102, s70, v102\n"
"v_mul_f32 v103, s70, v103\n"
"v_mfma_f32_16x16x32_bf16 v[192:195], a[240:243], a[12:15], 0\n"
"v_cvt_pk_bf16_f32 v98, v100, v101\n"
"v_cvt_pk_bf16_f32 v99, v102, v103\n"
"v_mfma_f32_16x16x32_bf16 v[192:195], a[244:247], a[28:31], v[192:195]\n"
"s_waitcnt lgkmcnt(2)\n"
"v_mul_f32 v108, s70, v108\n"
"v_mul_f32 v109, s70, v109\n"
"v_mul_f32 v110, s70, v110\n"
"v_mfma_f32_16x16x32_bf16 v[172:175], a[248:251], a[0:3], 0\n"
"ds_read_b128 a[240:243], v234 offset:6144\n"
"v_mul_f32 v111, s70, v111\n"
"v_mfma_f32_16x16x32_bf16 v[172:175], a[252:255], a[16:19], v[172:175]\n"
"v_permlane16_swap_b32 v96, v98\n"
"v_mfma_f32_16x16x32_bf16 v[180:183], a[248:251], a[4:7], 0\n"
"L_CW3:\n"
"s_mov_b32 s75, L_CW3-L_PC+3072\n"
"buffer_load_dword v248, v254, s[56:59], s75 offen\n"
"v_mfma_f32_16x16x32_bf16 v[180:183], a[252:255], a[20:23], v[180:183]\n"
"v_permlane16_swap_b32 v97, v99\n"
"v_accvgpr_write_b32 a112, v96\n"
"v_mfma_f32_16x16x32_bf16 v[188:191], a[248:251], a[8:11], 0\n"
"ds_read_b128 a[244:247], v234 offset:6656\n"
"v_accvgpr_write_b32 a113, v97\n"
"v_mfma_f32_16x16x32_bf16 v[188:191], a[252:255], a[24:27], v[188:191]\n"
"v_accvgpr_write_b32 a114, v98\n"
"v_accvgpr_write_b32 a115, v99\n"
"v_mfma_f32_16x16x32_bf16 v[196:199], a[248:251], a[12:15], 0\n"
"v_cvt_pk_bf16_f32 v108, v108, v109\n"
"v_cvt_pk_bf16_f32 v109, v110, v111\n"
"v_mfma_f32_16x16x32_bf16 v[196:199], a[252:255], a[28:31], v[196:199]\n"
"s_waitcnt lgkmcnt(2)\n"
"v_mul_f32 v116, s70, v116\n"
"v_mul_f32 v117, s70, v117\n"
"v_mul_f32 v118, s70, v118\n"
"v_mfma_f32_16x16x32_bf16 v[168:171], a[232:235], a[32:35], v[168:171]\n"
"ds_read_b128 a[248:251], v234 offset:8192\n"
"v_mul_f32 v119, s70, v119\n"
"v_mfma_f32_16x16x32_bf16 v[168:171], a[236:239], a[48:51], v[168:171]\n"
"v_cvt_pk_bf16_f32 v110, v116, v117\n"
"v_cvt_pk_bf16_f32 v111, v118, v119\n"
"v_mfma_f32_16x16x32_bf16 v[176:179], a[232:235], a[36:39], v[176:179]\n"
"v_mul_f32 v124, s70, v124\n"
"v_mul_f32 v125, s70, v125\n"
"v_mfma_f32_16x16x32_bf16 v[176:179], a[236:239], a[52:55], v[176:179]\n"
"v_permlane16_swap_b32 v108, v110\n"
"v_accvgpr_write_b32 a116, v108\n"
"v_mfma_f32_16x16x32_bf16 v[184:187], a[232:235], a[40:43], v[184:187]\n"
"ds_read_b128 a[252:255], v234 offset:8704\n"
"v_accvgpr_write_b32 a118, v110\n"
"v_mfma_f32_16x16x32_bf16 v[184:187], a[236:239], a[56:59], v[184:187]\n"
"v_permlane16_swap_b32 v109, v111\n"
"v_mfma_f32_16x16x32_bf16 v[192:195], a[232:235], a[44:47], v[192:195]\n"
"v_accvgpr_write_b32 a117, v109\n"
"v_accvgpr_write_b32 a119, v111\n"
"v_mfma_f32_16x16x32_bf16 v[192:195], a[236:239], a[60:63], v[192:195]\n"
"s_waitcnt lgkmcnt(2)\n"
"v_mul_f32 v126, s70, v126\n"
"v_mul_f32 v127, s70, v127\n"
"v_cvt_pk_bf16_f32 v124, v124, v125\n"
"v_mfma_f32_16x16x32_bf16 v[172:175], a[240:243], a[32:35], v[172:175]\n"
"ds_read_b128 a[232:235], v234 offset:10240\n"
"v_cvt_pk_bf16_f32 v125, v126, v127\n"
"v_mfma_f32_16x16x32_bf16 v[172:175], a[244:247], a[48:51], v[172:175]\n"
"v_mul_f32 v132, s70, v132\n"
"v_mul_f32 v133, s70, v133\n"
"v_mfma_f32_16x16x32_bf16 v[180:183], a[240:243], a[36:39], v[180:183]\n"
"s_barrier\n"
"v_mul_f32 v134, s70, v134\n"
"v_mfma_f32_16x16x32_bf16 v[180:183], a[244:247], a[52:55], v[180:183]\n"
"v_mul_f32 v135, s70, v135\n"
"v_cvt_pk_bf16_f32 v126, v132, v133\n"
"v_cvt_pk_bf16_f32 v127, v134, v135\n"
"v_mfma_f32_16x16x32_bf16 v[188:191], a[240:243], a[40:43], v[188:191]\n"
"ds_read_b128 a[236:239], v234 offset:10752\n"
"v_add_u32 v242, 65536, v241\n"
"s_add_u32 m0, s71, 81920\n"
"v_mfma_f32_16x16x32_bf16 v[188:191], a[244:247], a[56:59], v[188:191]\n"
"buffer_load_dwordx4 v242, s[40:43], 0 offen offset:0 sc0 sc1 nt lds\n"
"v_mfma_f32_16x16x32_bf16 v[196:199], a[240:243], a[44:47], v[196:199]\n"
"v_permlane16_swap_b32 v124, v126\n"
"v_mfma_f32_16x16x32_bf16 v[196:199], a[244:247], a[60:63], v[196:199]\n"
"s_waitcnt lgkmcnt(2)\n"
"v_permlane16_swap_b32 v125, v127\n"
"v_accvgpr_write_b32 a120, v124\n"
"v_mfma_f32_16x16x32_bf16 v[168:171], a[248:251], a[64:67], v[168:171]\n"
"ds_read_b128 a[240:243], v234 offset:12288\n"
"v_accvgpr_write_b32 a121, v125\n"
"v_mfma_f32_16x16x32_bf16 v[168:171], a[252:255], a[80:83], v[168:171]\n"
"v_accvgpr_write_b32 a122, v126\n"
"v_accvgpr_write_b32 a123, v127\n"
"v_mfma_f32_16x16x32_bf16 v[176:179], a[248:251], a[68:71], v[176:179]\n"
"v_mul_f32 v140, s70, v140\n"
"v_mul_f32 v141, s70, v141\n"
"v_mfma_f32_16x16x32_bf16 v[176:179], a[252:255], a[84:87], v[176:179]\n"
"v_mul_f32 v142, s70, v142\n"
"v_mul_f32 v143, s70, v143\n"
"v_cvt_pk_bf16_f32 v140, v140, v141\n"
"v_mfma_f32_16x16x32_bf16 v[184:187], a[248:251], a[72:75], v[184:187]\n"
"ds_read_b128 a[244:247], v234 offset:12800\n"
"v_cvt_pk_bf16_f32 v141, v142, v143\n"
"v_mfma_f32_16x16x32_bf16 v[184:187], a[252:255], a[88:91], v[184:187]\n"
"buffer_load_dwordx4 v242, s[40:43], s61 offen offset:1024 sc0 sc1 nt lds\n"
"v_mfma_f32_16x16x32_bf16 v[192:195], a[248:251], a[76:79], v[192:195]\n"
"v_mul_f32 v148, s70, v148\n"
"v_mul_f32 v149, s70, v149\n"
"v_mfma_f32_16x16x32_bf16 v[192:195], a[252:255], a[92:95], v[192:195]\n"
"s_waitcnt lgkmcnt(2)\n"
"v_mul_f32 v150, s70, v150\n"
"v_mul_f32 v151, s70, v151\n"
"v_cvt_pk_bf16_f32 v142, v148, v149\n"
"v_mfma_f32_16x16x32_bf16 v[172:175], a[232:235], a[64:67], v[172:175]\n"
"ds_read_b128 a[248:251], v234 offset:14336\n"
"v_cvt_pk_bf16_f32 v143, v150, v151\n"
"v_mfma_f32_16x16x32_bf16 v[172:175], a[236:239], a[80:83], v[172:175]\n"
"v_permlane16_swap_b32 v140, v142\n"
"v_mfma_f32_16x16x32_bf16 v[180:183], a[232:235], a[68:71], v[180:183]\n"
"v_permlane16_swap_b32 v141, v143\n"
"v_mfma_f32_16x16x32_bf16 v[180:183], a[236:239], a[84:87], v[180:183]\n"
"v_accvgpr_write_b32 a124, v140\n"
"v_accvgpr_write_b32 a125, v141\n"
"v_accvgpr_write_b32 a126, v142\n"
"v_mfma_f32_16x16x32_bf16 v[188:191], a[232:235], a[72:75], v[188:191]\n"
"ds_read_b128 a[252:255], v234 offset:14848\n"
"v_accvgpr_write_b32 a127, v143\n"
"v_mfma_f32_16x16x32_bf16 v[188:191], a[236:239], a[88:91], v[188:191]\n"
"buffer_load_dwordx4 v242, s[40:43], s62 offen offset:2048 sc0 sc1 nt lds\n"
"v_mfma_f32_16x16x32_bf16 v[196:199], a[232:235], a[76:79], v[196:199]\n"
"v_mfma_f32_16x16x32_bf16 v[196:199], a[236:239], a[92:95], v[196:199]\n"
"s_waitcnt lgkmcnt(2)\n"
"v_mfma_f32_16x16x32_bf16 v[168:171], a[240:243], a[96:99], v[168:171]\n"
"ds_read_b128 a[232:235], v234 offset:16384\n"
"v_mfma_f32_16x16x32_bf16 v[168:171], a[244:247], a[112:115], v[168:171]\n"
"v_mfma_f32_16x16x32_bf16 v[176:179], a[240:243], a[100:103], v[176:179]\n"
"v_mfma_f32_16x16x32_bf16 v[176:179], a[244:247], a[116:119], v[176:179]\n"
"v_mfma_f32_16x16x32_bf16 v[184:187], a[240:243], a[104:107], v[184:187]\n"
"ds_read_b128 a[236:239], v234 offset:16896\n"
"v_mfma_f32_16x16x32_bf16 v[184:187], a[244:247], a[120:123], v[184:187]\n"
"buffer_load_dwordx4 v242, s[40:43], s63 offen offset:3072 sc0 sc1 nt lds\n"
"v_mfma_f32_16x16x32_bf16 v[192:195], a[240:243], a[108:111], v[192:195]\n"
"v_mfma_f32_16x16x32_bf16 v[192:195], a[244:247], a[124:127], v[192:195]\n"
"s_waitcnt lgkmcnt(2)\n"
"v_mfma_f32_16x16x32_bf16 v[172:175], a[248:251], a[96:99], v[172:175]\n"
"ds_read_b128 a[240:243], v234 offset:18432\n"
"v_mfma_f32_16x16x32_bf16 v[172:175], a[252:255], a[112:115], v[172:175]\n"
"v_mfma_f32_16x16x32_bf16 v[180:183], a[248:251], a[100:103], v[180:183]\n"
"v_mfma_f32_16x16x32_bf16 v[180:183], a[252:255], a[116:119], v[180:183]\n"
"v_mfma_f32_16x16x32_bf16 v[188:191], a[248:251], a[104:107], v[188:191]\n"
"ds_read_b128 a[244:247], v234 offset:18944\n"
"v_add_u32 v242, 49152, v241\n"
"s_add_u32 m0, s71, 98304\n"
"v_mfma_f32_16x16x32_bf16 v[188:191], a[252:255], a[120:123], v[188:191]\n"
"buffer_load_dwordx4 v242, s[44:47], 0 offen offset:0 sc0 sc1 nt lds\n"
"v_mfma_f32_16x16x32_bf16 v[196:199], a[248:251], a[108:111], v[196:199]\n"
"v_mfma_f32_16x16x32_bf16 v[196:199], a[252:255], a[124:127], v[196:199]\n"
"s_waitcnt lgkmcnt(2)\n"
"v_mfma_f32_16x16x32_bf16 v[200:203], a[232:235], a[0:3], 0\n"
"ds_read_b128 a[248:251], v234 offset:20480\n"
"v_mfma_f32_16x16x32_bf16 v[200:203], a[236:239], a[16:19], v[200:203]\n"
"v_max3_f32 v152, v168, v169, v170\n"
"v_max3_f32 v152, v152, v171, v172\n"
"v_max3_f32 v152, v152, v173, v174\n"
"v_mfma_f32_16x16x32_bf16 v[208:211], a[232:235], a[4:7], 0\n"
"v_max_f32 v152, v152, v175\n"
"v_max3_f32 v156, v176, v177, v178\n"
"v_max3_f32 v156, v156, v179, v180\n"
"v_max3_f32 v156, v156, v181, v182\n"
"v_mfma_f32_16x16x32_bf16 v[208:211], a[236:239], a[20:23], v[208:211]\n"
"v_max_f32 v156, v156, v183\n"
"v_max3_f32 v160, v184, v185, v186\n"
"v_max3_f32 v160, v160, v187, v188\n"
"v_max3_f32 v160, v160, v189, v190\n"
"v_mfma_f32_16x16x32_bf16 v[216:219], a[232:235], a[8:11], 0\n"
"ds_read_b128 a[252:255], v234 offset:20992\n"
"v_max_f32 v160, v160, v191\n"
"v_max3_f32 v164, v192, v193, v194\n"
"v_mfma_f32_16x16x32_bf16 v[216:219], a[236:239], a[24:27], v[216:219]\n"
"buffer_load_dwordx4 v242, s[44:47], s61 offen offset:1024 sc0 sc1 nt lds\n"
"v_max3_f32 v164, v164, v195, v196\n"
"v_max3_f32 v164, v164, v197, v198\n"
"v_mfma_f32_16x16x32_bf16 v[224:227], a[232:235], a[12:15], 0\n"
"v_max_f32 v164, v164, v199\n"
"v_mfma_f32_16x16x32_bf16 v[224:227], a[236:239], a[28:31], v[224:227]\n"
"s_waitcnt lgkmcnt(2)\n"
"v_mfma_f32_16x16x32_bf16 v[204:207], a[240:243], a[0:3], 0\n"
"ds_read_b128 a[232:235], v234 offset:22528\n"
"v_permlane32_swap_b32 v152, v156\n"
"v_mfma_f32_16x16x32_bf16 v[204:207], a[244:247], a[16:19], v[204:207]\n"
"v_permlane32_swap_b32 v160, v164\n"
"v_max_f32 v152, v152, v156\n"
"v_max_f32 v160, v160, v164\n"
"v_mfma_f32_16x16x32_bf16 v[212:215], a[240:243], a[4:7], 0\n"
"s_nop 1\n"
"v_mfma_f32_16x16x32_bf16 v[212:215], a[244:247], a[20:23], v[212:215]\n"
"v_permlane16_swap_b32 v152, v160\n"
"v_min_f32 v152, -v152, -v160\n"
"v_mov_b32 v160, v152\n"
"v_mfma_f32_16x16x32_bf16 v[220:223], a[240:243], a[8:11], 0\n"
"ds_read_b128 a[236:239], v234 offset:23040\n"
"s_nop 1\n"
"v_mfma_f32_16x16x32_bf16 v[220:223], a[244:247], a[24:27], v[220:223]\n"
"buffer_load_dwordx4 v242, s[44:47], s62 offen offset:2048 sc0 sc1 nt lds\n"
"v_permlane16_swap_b32 v152, v160\n"
"v_mfma_f32_16x16x32_bf16 v[228:231], a[240:243], a[12:15], 0\n"
"v_mov_b32 v156, v152\n"
"v_mov_b32 v164, v160\n"
"s_nop 0\n"
"v_mfma_f32_16x16x32_bf16 v[228:231], a[244:247], a[28:31], v[228:231]\n"
"s_waitcnt lgkmcnt(2)\n"
"v_permlane32_swap_b32 v152, v156\n"
"v_permlane32_swap_b32 v160, v164\n"
"v_mfma_f32_16x16x32_bf16 v[200:203], a[248:251], a[32:35], v[200:203]\n"
"ds_read_b128 a[240:243], v234 offset:24576\n"
"v_add_f32 v168, v168, v152\n"
"v_exp_f32 v168, v168\n"
"v_mfma_f32_16x16x32_bf16 v[200:203], a[252:255], a[48:51], v[200:203]\n"
"v_add_f32 v169, v169, v152\n"
"v_exp_f32 v169, v169\n"
"v_mfma_f32_16x16x32_bf16 v[208:211], a[248:251], a[36:39], v[208:211]\n"
"v_add_f32 v244, v168, v169\n"
"v_cvt_pk_bf16_f32 v168, v168, v169\n"
"v_add_f32 v170, v170, v152\n"
"v_add_f32 v171, v171, v152\n"
"v_mfma_f32_16x16x32_bf16 v[208:211], a[252:255], a[52:55], v[208:211]\n"
"v_exp_f32 v170, v170\n"
"v_exp_f32 v171, v171\n"
"v_mfma_f32_16x16x32_bf16 v[216:219], a[248:251], a[40:43], v[216:219]\n"
"ds_read_b128 a[244:247], v234 offset:25088\n"
"v_add_f32 v244, v244, v170\n"
"v_add_f32 v244, v244, v171\n"
"v_mfma_f32_16x16x32_bf16 v[216:219], a[252:255], a[56:59], v[216:219]\n"
"buffer_load_dwordx4 v242, s[44:47], s63 offen offset:3072 sc0 sc1 nt lds\n"
"v_cvt_pk_bf16_f32 v169, v170, v171\n"
"v_add_f32 v172, v172, v152\n"
"v_mfma_f32_16x16x32_bf16 v[224:227], a[248:251], a[44:47], v[224:227]\n"
"v_exp_f32 v172, v172\n"
"v_add_f32 v173, v173, v152\n"
"v_add_f32 v174, v174, v152\n"
"v_mfma_f32_16x16x32_bf16 v[224:227], a[252:255], a[60:63], v[224:227]\n"
"s_waitcnt lgkmcnt(2)\n"
"v_add_f32 v244, v244, v172\n"
"v_exp_f32 v173, v173\n"
"v_add_f32 v175, v175, v152\n"
"v_mfma_f32_16x16x32_bf16 v[204:207], a[232:235], a[32:35], v[204:207]\n"
"ds_read_b128 a[248:251], v234 offset:26624\n"
"v_add_f32 v244, v244, v173\n"
"v_cvt_pk_bf16_f32 v170, v172, v173\n"
"v_mfma_f32_16x16x32_bf16 v[204:207], a[236:239], a[48:51], v[204:207]\n"
"v_exp_f32 v174, v174\n"
"v_exp_f32 v175, v175\n"
"v_mfma_f32_16x16x32_bf16 v[212:215], a[232:235], a[36:39], v[212:215]\n"
"s_barrier\n"
"v_add_f32 v244, v244, v174\n"
"v_add_f32 v244, v244, v175\n"
"v_cvt_pk_bf16_f32 v171, v174, v175\n"
"v_mfma_f32_16x16x32_bf16 v[212:215], a[236:239], a[52:55], v[212:215]\n"
"v_add_f32 v176, v176, v156\n"
"v_exp_f32 v176, v176\n"
"v_mfma_f32_16x16x32_bf16 v[220:223], a[232:235], a[40:43], v[220:223]\n"
"ds_read_b128 a[252:255], v234 offset:27136\n"
"v_add_u32 v242, 81920, v241\n"
"s_add_u32 m0, s71, 114688\n"
"v_add_f32 v177, v177, v156\n"
"v_mfma_f32_16x16x32_bf16 v[220:223], a[236:239], a[56:59], v[220:223]\n"
"buffer_load_dwordx4 v242, s[40:43], 0 offen offset:0 sc0 sc1 nt lds\n"
"v_exp_f32 v177, v177\n"
"v_mfma_f32_16x16x32_bf16 v[228:231], a[232:235], a[44:47], v[228:231]\n"
"v_add_f32 v245, v176, v177\n"
"v_cvt_pk_bf16_f32 v176, v176, v177\n"
"v_add_f32 v178, v178, v156\n"
"v_mfma_f32_16x16x32_bf16 v[228:231], a[236:239], a[60:63], v[228:231]\n"
"s_waitcnt lgkmcnt(2)\n"
"v_exp_f32 v178, v178\n"
"v_add_f32 v179, v179, v156\n"
"v_add_f32 v180, v180, v156\n"
"v_mfma_f32_16x16x32_bf16 v[200:203], a[240:243], a[64:67], v[200:203]\n"
"ds_read_b128 a[232:235], v234 offset:28672\n"
"v_add_f32 v245, v245, v178\n"
"v_exp_f32 v179, v179\n"
"v_mfma_f32_16x16x32_bf16 v[200:203], a[244:247], a[80:83], v[200:203]\n"
"v_add_f32 v245, v245, v179\n"
"v_cvt_pk_bf16_f32 v177, v178, v179\n"
"v_exp_f32 v180, v180\n"
"v_mfma_f32_16x16x32_bf16 v[208:211], a[240:243], a[68:71], v[208:211]\n"
"v_add_f32 v245, v245, v180\n"
"v_add_f32 v181, v181, v156\n"
"v_add_f32 v182, v182, v156\n"
"v_mfma_f32_16x16x32_bf16 v[208:211], a[244:247], a[84:87], v[208:211]\n"
"v_exp_f32 v181, v181\n"
"v_exp_f32 v182, v182\n"
"v_mfma_f32_16x16x32_bf16 v[216:219], a[240:243], a[72:75], v[216:219]\n"
"ds_read_b128 a[236:239], v234 offset:29184\n"
"v_add_f32 v245, v245, v181\n"
"v_cvt_pk_bf16_f32 v178, v180, v181\n"
"v_add_f32 v245, v245, v182\n"
"v_mfma_f32_16x16x32_bf16 v[216:219], a[244:247], a[88:91], v[216:219]\n"
"buffer_load_dwordx4 v242, s[40:43], s61 offen offset:1024 sc0 sc1 nt lds\n"
"v_add_f32 v183, v183, v156\n"
"v_mfma_f32_16x16x32_bf16 v[224:227], a[240:243], a[76:79], v[224:227]\n"
"v_exp_f32 v183, v183\n"
"v_add_f32 v184, v184, v160\n"
"v_add_f32 v185, v185, v160\n"
"v_mfma_f32_16x16x32_bf16 v[224:227], a[244:247], a[92:95], v[224:227]\n"
"s_waitcnt lgkmcnt(2)\n"
"v_add_f32 v245, v245, v183\n"
"v_cvt_pk_bf16_f32 v179, v182, v183\n"
"v_exp_f32 v184, v184\n"
"v_mfma_f32_16x16x32_bf16 v[204:207], a[248:251], a[64:67], v[204:207]\n"
"ds_read_b128 a[240:243], v234 offset:30720\n"
"v_exp_f32 v185, v185\n"
"v_mfma_f32_16x16x32_bf16 v[204:207], a[252:255], a[80:83], v[204:207]\n"
"v_add_f32 v246, v184, v185\n"
"v_cvt_pk_bf16_f32 v184, v184, v185\n"
"v_add_f32 v186, v186, v160\n"
"v_add_f32 v187, v187, v160\n"
"v_mfma_f32_16x16x32_bf16 v[212:215], a[248:251], a[68:71], v[212:215]\n"
"v_exp_f32 v186, v186\n"
"v_exp_f32 v187, v187\n"
"v_mfma_f32_16x16x32_bf16 v[212:215], a[252:255], a[84:87], v[212:215]\n"
"v_add_f32 v246, v246, v186\n"
"v_add_f32 v246, v246, v187\n"
"v_cvt_pk_bf16_f32 v185, v186, v187\n"
"v_add_f32 v188, v188, v160\n"
"v_mfma_f32_16x16x32_bf16 v[220:223], a[248:251], a[72:75], v[220:223]\n"
"ds_read_b128 a[244:247], v234 offset:31232\n"
"v_exp_f32 v188, v188\n"
"v_mfma_f32_16x16x32_bf16 v[220:223], a[252:255], a[88:91], v[220:223]\n"
"buffer_load_dwordx4 v242, s[40:43], s62 offen offset:2048 sc0 sc1 nt lds\n"
"v_add_f32 v246, v246, v188\n"
"v_add_f32 v189, v189, v160\n"
"v_mfma_f32_16x16x32_bf16 v[228:231], a[248:251], a[76:79], v[228:231]\n"
"v_exp_f32 v189, v189\n"
"v_add_f32 v190, v190, v160\n"
"v_add_f32 v191, v191, v160\n"
"v_mfma_f32_16x16x32_bf16 v[228:231], a[252:255], a[92:95], v[228:231]\n"
"s_waitcnt lgkmcnt(2)\n"
"v_add_f32 v246, v246, v189\n"
"v_cvt_pk_bf16_f32 v186, v188, v189\n"
"v_add_f32 v192, v192, v164\n"
"v_mfma_f32_16x16x32_bf16 v[200:203], a[232:235], a[96:99], v[200:203]\n"
"ds_read_b64_tr_b16 a[248:249], v235 offset:0\n"
"v_exp_f32 v190, v190\n"
"v_add_f32 v193, v193, v164\n"
"v_mfma_f32_16x16x32_bf16 v[200:203], a[236:239], a[112:115], v[200:203]\n"
"ds_read_b64_tr_b16 a[250:251], v235 offset:2048\n"
"v_add_f32 v246, v246, v190\n"
"v_exp_f32 v191, v191\n"
"v_mfma_f32_16x16x32_bf16 v[208:211], a[232:235], a[100:103], v[208:211]\n"
"v_add_f32 v246, v246, v191\n"
"v_cvt_pk_bf16_f32 v187, v190, v191\n"
"v_add_f32 v194, v194, v164\n"
"v_mfma_f32_16x16x32_bf16 v[208:211], a[236:239], a[116:119], v[208:211]\n"
"v_exp_f32 v192, v192\n"
"v_exp_f32 v193, v193\n"
"v_mfma_f32_16x16x32_bf16 v[216:219], a[232:235], a[104:107], v[216:219]\n"
"ds_read_b64_tr_b16 a[252:253], v236 offset:0\n"
"v_add_f32 v247, v192, v193\n"
"v_cvt_pk_bf16_f32 v192, v192, v193\n"
"v_add_f32 v195, v195, v164\n"
"v_mfma_f32_16x16x32_bf16 v[216:219], a[236:239], a[120:123], v[216:219]\n"
"ds_read_b64_tr_b16 a[254:255], v236 offset:2048\n"
"v_exp_f32 v194, v194\n"
"v_add_f32 v196, v196, v164\n"
"v_mfma_f32_16x16x32_bf16 v[224:227], a[232:235], a[108:111], v[224:227]\n"
"buffer_load_dwordx4 v242, s[40:43], s63 offen offset:3072 sc0 sc1 nt lds\n"
"v_add_f32 v247, v247, v194\n"
"v_mfma_f32_16x16x32_bf16 v[224:227], a[236:239], a[124:127], v[224:227]\n"
"s_waitcnt lgkmcnt(4)\n"
"v_exp_f32 v195, v195\n"
"v_exp_f32 v196, v196\n"
"v_mfma_f32_16x16x32_bf16 v[204:207], a[240:243], a[96:99], v[204:207]\n"
"ds_read_b64_tr_b16 a[232:233], v235 offset:512\n"
"v_add_f32 v247, v247, v195\n"
"v_cvt_pk_bf16_f32 v193, v194, v195\n"
"v_add_f32 v247, v247, v196\n"
"v_mfma_f32_16x16x32_bf16 v[204:207], a[244:247], a[112:115], v[204:207]\n"
"ds_read_b64_tr_b16 a[234:235], v235 offset:2560\n"
"v_add_f32 v197, v197, v164\n"
"v_add_f32 v198, v198, v164\n"
"v_mfma_f32_16x16x32_bf16 v[212:215], a[240:243], a[100:103], v[212:215]\n"
"v_exp_f32 v197, v197\n"
"v_exp_f32 v198, v198\n"
"v_mfma_f32_16x16x32_bf16 v[212:215], a[244:247], a[116:119], v[212:215]\n"
"v_add_f32 v247, v247, v197\n"
"v_cvt_pk_bf16_f32 v194, v196, v197\n"
"v_add_f32 v247, v247, v198\n"
"v_add_f32 v199, v199, v164\n"
"v_mfma_f32_16x16x32_bf16 v[220:223], a[240:243], a[104:107], v[220:223]\n"
"ds_read_b64_tr_b16 a[236:237], v236 offset:512\n"
"v_exp_f32 v199, v199\n"
"v_mfma_f32_16x16x32_bf16 v[220:223], a[244:247], a[120:123], v[220:223]\n"
"ds_read_b64_tr_b16 a[238:239], v236 offset:2560\n"
"v_add_u32 v242, 65536, v241\n"
"s_add_u32 m0, s71, 131072\n"
"v_add_f32 v247, v247, v199\n"
"v_mfma_f32_16x16x32_bf16 v[228:231], a[240:243], a[108:111], v[228:231]\n"
"buffer_load_dwordx4 v242, s[44:47], 0 offen offset:0 sc0 sc1 nt lds\n"
"v_cvt_pk_bf16_f32 v195, v198, v199\n"
"v_mfma_f32_16x16x32_bf16 v[228:231], a[244:247], a[124:127], v[228:231]\n"
"s_waitcnt lgkmcnt(4)\n"
"v_mfma_f32_16x16x32_bf16 v[0:3], a[248:251], v[168:171], 0\n"
"ds_read_b64_tr_b16 a[240:241], v235 offset:4096\n"
"v_mfma_f32_16x16x32_bf16 v[4:7], a[252:255], v[168:171], 0\n"
"ds_read_b64_tr_b16 a[242:243], v235 offset:6144\n"
"v_add_f32 v200, v200, v152\n"
"v_mfma_f32_16x16x32_bf16 v[8:11], a[248:251], v[176:179], 0\n"
"v_exp_f32 v200, v200\n"
"v_mfma_f32_16x16x32_bf16 v[12:15], a[252:255], v[176:179], 0\n"
"v_add_f32 v244, v244, v200\n"
"v_add_f32 v201, v201, v152\n"
"v_mfma_f32_16x16x32_bf16 v[16:19], a[248:251], v[184:187], 0\n"
"ds_read_b64_tr_b16 a[244:245], v236 offset:4096\n"
"v_add_f32 v202, v202, v152\n"
"v_mfma_f32_16x16x32_bf16 v[20:23], a[252:255], v[184:187], 0\n"
"ds_read_b64_tr_b16 a[246:247], v236 offset:6144\n"
"v_add_f32 v203, v203, v152\n"
"v_mfma_f32_16x16x32_bf16 v[24:27], a[248:251], v[192:195], 0\n"
"v_exp_f32 v201, v201\n"
"v_mfma_f32_16x16x32_bf16 v[28:31], a[252:255], v[192:195], 0\n"
"s_waitcnt lgkmcnt(4)\n"
"v_add_f32 v244, v244, v201\n"
"v_cvt_pk_bf16_f32 v200, v200, v201\n"
"v_mfma_f32_16x16x32_bf16 v[32:35], a[232:235], v[168:171], 0\n"
"ds_read_b64_tr_b16 a[248:249], v235 offset:4608\n"
"v_add_f32 v204, v204, v152\n"
"v_mfma_f32_16x16x32_bf16 v[36:39], a[236:239], v[168:171], 0\n"
"ds_read_b64_tr_b16 a[250:251], v235 offset:6656\n"
"v_add_f32 v205, v205, v152\n"
"v_mfma_f32_16x16x32_bf16 v[40:43], a[232:235], v[176:179], 0\n"
"v_exp_f32 v202, v202\n"
"v_mfma_f32_16x16x32_bf16 v[44:47], a[236:239], v[176:179], 0\n"
"v_add_f32 v244, v244, v202\n"
"v_add_f32 v206, v206, v152\n"
"v_mfma_f32_16x16x32_bf16 v[48:51], a[232:235], v[184:187], 0\n"
"ds_read_b64_tr_b16 a[252:253], v236 offset:4608\n"
"v_add_f32 v207, v207, v152\n"
"v_mfma_f32_16x16x32_bf16 v[52:55], a[236:239], v[184:187], 0\n"
"ds_read_b64_tr_b16 a[254:255], v236 offset:6656\n"
"v_add_f32 v208, v208, v156\n"
"v_mfma_f32_16x16x32_bf16 v[56:59], a[232:235], v[192:195], 0\n"
"buffer_load_dwordx4 v242, s[44:47], s61 offen offset:1024 sc0 sc1 nt lds\n"
"v_mfma_f32_16x16x32_bf16 v[60:63], a[236:239], v[192:195], 0\n"
"s_waitcnt lgkmcnt(4)\n"
"v_exp_f32 v203, v203\n"
"v_mfma_f32_16x16x32_bf16 v[64:67], a[240:243], v[168:171], 0\n"
"ds_read_b64_tr_b16 a[232:233], v235 offset:8192\n"
"v_add_f32 v244, v244, v203\n"
"v_mfma_f32_16x16x32_bf16 v[68:71], a[244:247], v[168:171], 0\n"
"ds_read_b64_tr_b16 a[234:235], v235 offset:10240\n"
"v_cvt_pk_bf16_f32 v201, v202, v203\n"
"v_mfma_f32_16x16x32_bf16 v[72:75], a[240:243], v[176:179], 0\n"
"v_exp_f32 v204, v204\n"
"v_mfma_f32_16x16x32_bf16 v[76:79], a[244:247], v[176:179], 0\n"
"v_add_f32 v244, v244, v204\n"
"v_add_f32 v209, v209, v156\n"
"v_mfma_f32_16x16x32_bf16 v[80:83], a[240:243], v[184:187], 0\n"
"ds_read_b64_tr_b16 a[236:237], v236 offset:8192\n"
"v_add_f32 v210, v210, v156\n"
"v_mfma_f32_16x16x32_bf16 v[84:87], a[244:247], v[184:187], 0\n"
"ds_read_b64_tr_b16 a[238:239], v236 offset:10240\n"
"v_add_f32 v211, v211, v156\n"
"v_mfma_f32_16x16x32_bf16 v[88:91], a[240:243], v[192:195], 0\n"
"v_exp_f32 v205, v205\n"
"v_mfma_f32_16x16x32_bf16 v[92:95], a[244:247], v[192:195], 0\n"
"s_waitcnt lgkmcnt(4)\n"
"v_add_f32 v244, v244, v205\n"
"v_cvt_pk_bf16_f32 v202, v204, v205\n"
"v_mfma_f32_16x16x32_bf16 v[96:99], a[248:251], v[168:171], 0\n"
"ds_read_b64_tr_b16 a[240:241], v235 offset:8704\n"
"v_add_f32 v212, v212, v156\n"
"v_mfma_f32_16x16x32_bf16 v[100:103], a[252:255], v[168:171], 0\n"
"ds_read_b64_tr_b16 a[242:243], v235 offset:10752\n"
"v_add_f32 v213, v213, v156\n"
"v_mfma_f32_16x16x32_bf16 v[104:107], a[248:251], v[176:179], 0\n"
"s_barrier\n"
"v_add_f32 v214, v214, v156\n"
"v_mfma_f32_16x16x32_bf16 v[108:111], a[252:255], v[176:179], 0\n"
"v_exp_f32 v206, v206\n"
"v_mfma_f32_16x16x32_bf16 v[112:115], a[248:251], v[184:187], 0\n"
"ds_read_b64_tr_b16 a[244:245], v236 offset:8704\n"
"v_add_f32 v244, v244, v206\n"
"v_mfma_f32_16x16x32_bf16 v[116:119], a[252:255], v[184:187], 0\n"
"ds_read_b64_tr_b16 a[246:247], v236 offset:10752\n"
"v_add_f32 v215, v215, v156\n"
"v_mfma_f32_16x16x32_bf16 v[120:123], a[248:251], v[192:195], 0\n"
"v_exp_f32 v207, v207\n"
"v_mfma_f32_16x16x32_bf16 v[124:127], a[252:255], v[192:195], 0\n"
"s_waitcnt lgkmcnt(4)\n"
"v_add_f32 v244, v244, v207\n"
"v_cvt_pk_bf16_f32 v203, v206, v207\n"
"v_mfma_f32_16x16x32_bf16 v[128:131], a[232:235], v[168:171], 0\n"
"ds_read_b64_tr_b16 a[248:249], v235 offset:12288\n"
"v_add_f32 v216, v216, v160\n"
"v_mfma_f32_16x16x32_bf16 v[132:135], a[236:239], v[168:171], 0\n"
"ds_read_b64_tr_b16 a[250:251], v235 offset:14336\n"
"v_add_f32 v217, v217, v160\n"
"v_mfma_f32_16x16x32_bf16 v[136:139], a[232:235], v[176:179], 0\n"
"buffer_load_dwordx4 v242, s[44:47], s62 offen offset:2048 sc0 sc1 nt lds\n"
"v_mfma_f32_16x16x32_bf16 v[140:143], a[236:239], v[176:179], 0\n"
"v_exp_f32 v208, v208\n"
"v_mfma_f32_16x16x32_bf16 v[144:147], a[232:235], v[184:187], 0\n"
"ds_read_b64_tr_b16 a[252:253], v236 offset:12288\n"
"v_add_f32 v245, v245, v208\n"
"v_mfma_f32_16x16x32_bf16 v[148:151], a[236:239], v[184:187], 0\n"
"ds_read_b64_tr_b16 a[254:255], v236 offset:14336\n"
"v_add_f32 v218, v218, v160\n"
"v_mfma_f32_16x16x32_bf16 a[128:131], a[232:235], v[192:195], 0\n"
"v_exp_f32 v209, v209\n"
"v_mfma_f32_16x16x32_bf16 a[132:135], a[236:239], v[192:195], 0\n"
"s_waitcnt lgkmcnt(4)\n"
"v_add_f32 v245, v245, v209\n"
"v_mfma_f32_16x16x32_bf16 a[136:139], a[240:243], v[168:171], 0\n"
"ds_read_b64_tr_b16 a[232:233], v235 offset:12800\n"
"v_mov_b32 v153, v152\n"
"v_mfma_f32_16x16x32_bf16 a[140:143], a[244:247], v[168:171], 0\n"
"ds_read_b64_tr_b16 a[234:235], v235 offset:14848\n"
"v_mov_b32 v154, v152\n"
"v_mfma_f32_16x16x32_bf16 a[144:147], a[240:243], v[176:179], 0\n"
"v_mov_b32 v155, v152\n"
"v_mov_b32 v157, v156\n"
"v_mfma_f32_16x16x32_bf16 a[148:151], a[244:247], v[176:179], 0\n"
"v_mov_b32 v158, v156\n"
"v_cvt_pk_bf16_f32 v208, v208, v209\n"
"v_mfma_f32_16x16x32_bf16 a[152:155], a[240:243], v[184:187], 0\n"
"ds_read_b64_tr_b16 a[236:237], v236 offset:12800\n"
"v_add_f32 v219, v219, v160\n"
"v_mfma_f32_16x16x32_bf16 a[156:159], a[244:247], v[184:187], 0\n"
"ds_read_b64_tr_b16 a[238:239], v236 offset:14848\n"
"v_mov_b32 v159, v156\n"
"v_mfma_f32_16x16x32_bf16 a[160:163], a[240:243], v[192:195], 0\n"
"v_mov_b32 v161, v160\n"
"v_mfma_f32_16x16x32_bf16 a[164:167], a[244:247], v[192:195], 0\n"
"s_waitcnt lgkmcnt(4)\n"
"v_exp_f32 v210, v210\n"
"v_mfma_f32_16x16x32_bf16 a[168:171], a[248:251], v[168:171], 0\n"
"ds_read_b128 a[240:243], v232 offset:16384\n"
"v_mov_b32 v162, v160\n"
"v_mfma_f32_16x16x32_bf16 a[172:175], a[252:255], v[168:171], 0\n"
"v_mov_b32 v163, v160\n"
"v_add_f32 v245, v245, v210\n"
"v_mfma_f32_16x16x32_bf16 a[176:179], a[248:251], v[176:179], 0\n"
"buffer_load_dwordx4 v242, s[44:47], s63 offen offset:3072 sc0 sc1 nt lds\n"
"v_mfma_f32_16x16x32_bf16 a[180:183], a[252:255], v[176:179], 0\n"
"v_exp_f32 v211, v211\n"
"v_mfma_f32_16x16x32_bf16 a[184:187], a[248:251], v[184:187], 0\n"
"ds_read_b128 a[244:247], v232 offset:16896\n"
"v_mov_b32 v165, v164\n"
"v_mfma_f32_16x16x32_bf16 a[188:191], a[252:255], v[184:187], 0\n"
"v_mov_b32 v166, v164\n"
"v_add_f32 v245, v245, v211\n"
"v_mfma_f32_16x16x32_bf16 a[192:195], a[248:251], v[192:195], 0\n"
"v_cvt_pk_bf16_f32 v209, v210, v211\n"
"v_add_f32 v220, v220, v160\n"
"v_mfma_f32_16x16x32_bf16 a[196:199], a[252:255], v[192:195], 0\n"
"s_waitcnt lgkmcnt(2)\n"
"v_mov_b32 v167, v164\n"
"v_add_f32 v221, v221, v160\n"
"v_mfma_f32_16x16x32_bf16 a[200:203], a[232:235], v[168:171], 0\n"
"ds_read_b128 a[248:251], v232 offset:18432\n"
"v_mfma_f32_16x16x32_bf16 a[204:207], a[236:239], v[168:171], 0\n"
"v_exp_f32 v212, v212\n"
"v_mfma_f32_16x16x32_bf16 a[208:211], a[232:235], v[176:179], 0\n"
"v_add_f32 v245, v245, v212\n"
"v_add_f32 v222, v222, v160\n"
"v_mfma_f32_16x16x32_bf16 a[212:215], a[236:239], v[176:179], 0\n"
"v_exp_f32 v213, v213\n"
"v_mfma_f32_16x16x32_bf16 a[216:219], a[232:235], v[184:187], 0\n"
"ds_read_b128 a[252:255], v232 offset:18944\n"
"v_add_f32 v245, v245, v213\n"
"v_mfma_f32_16x16x32_bf16 a[220:223], a[236:239], v[184:187], 0\n"
"v_cvt_pk_bf16_f32 v210, v212, v213\n"
"v_add_f32 v223, v223, v160\n"
"v_mfma_f32_16x16x32_bf16 a[224:227], a[232:235], v[192:195], 0\n"
"v_exp_f32 v214, v214\n"
"v_mfma_f32_16x16x32_bf16 a[228:231], a[236:239], v[192:195], 0\n"
"s_waitcnt lgkmcnt(2)\n"
"v_add_f32 v245, v245, v214\n"
"v_add_f32 v224, v224, v164\n"
"v_mfma_f32_16x16x32_bf16 v[168:171], a[240:243], a[0:3], v[152:155]\n"
"ds_read_b128 a[232:235], v232 offset:20480\n"
"v_mfma_f32_16x16x32_bf16 v[168:171], a[244:247], a[16:19], v[168:171]\n"
"v_exp_f32 v215, v215\n"
"v_mfma_f32_16x16x32_bf16 v[176:179], a[240:243], a[4:7], v[156:159]\n"
"v_add_f32 v245, v245, v215\n"
"v_cvt_pk_bf16_f32 v211, v214, v215\n"
"v_mfma_f32_16x16x32_bf16 v[176:179], a[244:247], a[20:23], v[176:179]\n"
"v_exp_f32 v216, v216\n"
"v_mfma_f32_16x16x32_bf16 v[184:187], a[240:243], a[8:11], v[160:163]\n"
"ds_read_b128 a[236:239], v232 offset:20992\n"
"v_add_u32 v242, 98304, v241\n"
"s_add_u32 m0, s71, 147456\n"
"v_mfma_f32_16x16x32_bf16 v[184:187], a[244:247], a[24:27], v[184:187]\n"
"buffer_load_dwordx4 v242, s[40:43], 0 offen offset:0 sc0 sc1 nt lds\n"
"v_mfma_f32_16x16x32_bf16 v[192:195], a[240:243], a[12:15], v[164:167]\n"
"v_add_f32 v246, v246, v216\n"
"v_add_f32 v225, v225, v164\n"
"v_mfma_f32_16x16x32_bf16 v[192:195], a[244:247], a[28:31], v[192:195]\n"
"s_waitcnt lgkmcnt(2)\n"
"v_exp_f32 v217, v217\n"
"v_mfma_f32_16x16x32_bf16 v[196:199], a[248:251], a[12:15], v[164:167]\n"
"ds_read_b128 a[240:243], v232 offset:22528\n"
"v_add_f32 v246, v246, v217\n"
"v_mfma_f32_16x16x32_bf16 v[196:199], a[252:255], a[28:31], v[196:199]\n"
"v_cvt_pk_bf16_f32 v216, v216, v217\n"
"v_add_f32 v226, v226, v164\n"
"v_mfma_f32_16x16x32_bf16 v[188:191], a[248:251], a[8:11], v[160:163]\n"
"v_exp_f32 v218, v218\n"
"v_mfma_f32_16x16x32_bf16 v[188:191], a[252:255], a[24:27], v[188:191]\n"
"v_add_f32 v246, v246, v218\n"
"v_add_f32 v227, v227, v164\n"
"v_mfma_f32_16x16x32_bf16 v[180:183], a[248:251], a[4:7], v[156:159]\n"
"ds_read_b128 a[244:247], v232 offset:23040\n"
"v_add_f32 v228, v228, v164\n"
"v_mfma_f32_16x16x32_bf16 v[180:183], a[252:255], a[20:23], v[180:183]\n"
"v_exp_f32 v219, v219\n"
"v_mfma_f32_16x16x32_bf16 v[172:175], a[248:251], a[0:3], v[152:155]\n"
"v_add_f32 v246, v246, v219\n"
"v_cvt_pk_bf16_f32 v217, v218, v219\n"
"v_mfma_f32_16x16x32_bf16 v[172:175], a[252:255], a[16:19], v[172:175]\n"
"s_waitcnt lgkmcnt(2)\n"
"v_exp_f32 v220, v220\n"
"v_mfma_f32_16x16x32_bf16 v[168:171], a[232:235], a[32:35], v[168:171]\n"
"ds_read_b128 a[248:251], v232 offset:24576\n"
"v_add_f32 v246, v246, v220\n"
"v_mfma_f32_16x16x32_bf16 v[168:171], a[236:239], a[48:51], v[168:171]\n"
"v_exp_f32 v221, v221\n"
"v_mfma_f32_16x16x32_bf16 v[176:179], a[232:235], a[36:39], v[176:179]\n"
"v_add_f32 v246, v246, v221\n"
"v_cvt_pk_bf16_f32 v218, v220, v221\n"
"v_mfma_f32_16x16x32_bf16 v[176:179], a[236:239], a[52:55], v[176:179]\n"
"v_exp_f32 v222, v222\n"
"v_mfma_f32_16x16x32_bf16 v[184:187], a[232:235], a[40:43], v[184:187]\n"
"ds_read_b128 a[252:255], v232 offset:25088\n"
"v_add_f32 v246, v246, v222\n"
"v_mfma_f32_16x16x32_bf16 v[184:187], a[236:239], a[56:59], v[184:187]\n"
"v_exp_f32 v223, v223\n"
"v_mfma_f32_16x16x32_bf16 v[192:195], a[232:235], a[44:47], v[192:195]\n"
"buffer_load_dwordx4 v242, s[40:43], s61 offen offset:1024 sc0 sc1 nt lds\n"
"v_mfma_f32_16x16x32_bf16 v[192:195], a[236:239], a[60:63], v[192:195]\n"
"s_waitcnt lgkmcnt(2)\n"
"v_add_f32 v246, v246, v223\n"
"v_cvt_pk_bf16_f32 v219, v222, v223\n"
"v_mfma_f32_16x16x32_bf16 v[196:199], a[240:243], a[44:47], v[196:199]\n"
"ds_read_b128 a[232:235], v232 offset:26624\n"
"v_add_f32 v229, v229, v164\n"
"v_mfma_f32_16x16x32_bf16 v[196:199], a[244:247], a[60:63], v[196:199]\n"
"v_exp_f32 v224, v224\n"
"v_mfma_f32_16x16x32_bf16 v[188:191], a[240:243], a[40:43], v[188:191]\n"
"s_barrier\n"
"v_mfma_f32_16x16x32_bf16 v[188:191], a[244:247], a[56:59], v[188:191]\n"
"v_add_f32 v247, v247, v224\n"
"v_add_f32 v230, v230, v164\n"
"v_mfma_f32_16x16x32_bf16 v[180:183], a[240:243], a[36:39], v[180:183]\n"
"ds_read_b128 a[236:239], v232 offset:27136\n"
"v_add_f32 v231, v231, v164\n"
"v_mfma_f32_16x16x32_bf16 v[180:183], a[244:247], a[52:55], v[180:183]\n"
"v_exp_f32 v225, v225\n"
"v_mfma_f32_16x16x32_bf16 v[172:175], a[240:243], a[32:35], v[172:175]\n"
"v_add_f32 v247, v247, v225\n"
"v_cvt_pk_bf16_f32 v224, v224, v225\n"
"v_mfma_f32_16x16x32_bf16 v[172:175], a[244:247], a[48:51], v[172:175]\n"
"s_waitcnt lgkmcnt(2)\n"
"v_exp_f32 v226, v226\n"
"v_mfma_f32_16x16x32_bf16 v[168:171], a[248:251], a[64:67], v[168:171]\n"
"ds_read_b128 a[240:243], v232 offset:28672\n"
"v_add_f32 v247, v247, v226\n"
"v_mfma_f32_16x16x32_bf16 v[168:171], a[252:255], a[80:83], v[168:171]\n"
"v_exp_f32 v227, v227\n"
"v_mfma_f32_16x16x32_bf16 v[176:179], a[248:251], a[68:71], v[176:179]\n"
"v_add_f32 v247, v247, v227\n"
"v_cvt_pk_bf16_f32 v225, v226, v227\n"
"v_mfma_f32_16x16x32_bf16 v[176:179], a[252:255], a[84:87], v[176:179]\n"
"v_exp_f32 v228, v228\n"
"v_mfma_f32_16x16x32_bf16 v[184:187], a[248:251], a[72:75], v[184:187]\n"
"ds_read_b128 a[244:247], v232 offset:29184\n"
"v_mfma_f32_16x16x32_bf16 v[184:187], a[252:255], a[88:91], v[184:187]\n"
"v_add_f32 v247, v247, v228\n"
"v_mfma_f32_16x16x32_bf16 v[192:195], a[248:251], a[76:79], v[192:195]\n"
"v_exp_f32 v229, v229\n"
"v_mfma_f32_16x16x32_bf16 v[192:195], a[252:255], a[92:95], v[192:195]\n"
"s_waitcnt lgkmcnt(2)\n"
"v_add_f32 v247, v247, v229\n"
"v_cvt_pk_bf16_f32 v226, v228, v229\n"
"v_mfma_f32_16x16x32_bf16 v[196:199], a[232:235], a[76:79], v[196:199]\n"
"ds_read_b128 a[248:251], v232 offset:30720\n"
"v_mfma_f32_16x16x32_bf16 v[196:199], a[236:239], a[92:95], v[196:199]\n"
"buffer_load_dwordx4 v242, s[40:43], s62 offen offset:2048 sc0 sc1 nt lds\n"
"v_mfma_f32_16x16x32_bf16 v[188:191], a[232:235], a[72:75], v[188:191]\n"
"v_exp_f32 v230, v230\n"
"v_mfma_f32_16x16x32_bf16 v[188:191], a[236:239], a[88:91], v[188:191]\n"
"v_add_f32 v247, v247, v230\n"
"v_mfma_f32_16x16x32_bf16 v[180:183], a[232:235], a[68:71], v[180:183]\n"
"ds_read_b128 a[252:255], v232 offset:31232\n"
"v_mfma_f32_16x16x32_bf16 v[180:183], a[236:239], a[84:87], v[180:183]\n"
"v_exp_f32 v231, v231\n"
"v_mfma_f32_16x16x32_bf16 v[172:175], a[232:235], a[64:67], v[172:175]\n"
"v_add_f32 v247, v247, v231\n"
"v_cvt_pk_bf16_f32 v227, v230, v231\n"
"v_mfma_f32_16x16x32_bf16 v[172:175], a[236:239], a[80:83], v[172:175]\n"
"s_waitcnt lgkmcnt(2)\n"
"v_mfma_f32_16x16x32_bf16 v[192:195], a[240:243], a[108:111], v[192:195]\n"
"ds_read_b64_tr_b16 a[232:233], v235 offset:32768\n"
"v_mfma_f32_16x16x32_bf16 v[192:195], a[244:247], a[124:127], v[192:195]\n"
"ds_read_b64_tr_b16 a[234:235], v235 offset:34816\n"
"v_mfma_f32_16x16x32_bf16 v[184:187], a[240:243], a[104:107], v[184:187]\n"
"v_mfma_f32_16x16x32_bf16 v[184:187], a[244:247], a[120:123], v[184:187]\n"
"v_mfma_f32_16x16x32_bf16 v[176:179], a[240:243], a[100:103], v[176:179]\n"
"ds_read_b64_tr_b16 a[236:237], v236 offset:32768\n"
"v_mfma_f32_16x16x32_bf16 v[176:179], a[244:247], a[116:119], v[176:179]\n"
"ds_read_b64_tr_b16 a[238:239], v236 offset:34816\n"
"v_mfma_f32_16x16x32_bf16 v[168:171], a[240:243], a[96:99], v[168:171]\n"
"v_mfma_f32_16x16x32_bf16 v[168:171], a[244:247], a[112:115], v[168:171]\n"
"s_waitcnt lgkmcnt(4)\n"
"v_mfma_f32_16x16x32_bf16 v[172:175], a[248:251], a[96:99], v[172:175]\n"
"ds_read_b64_tr_b16 a[240:241], v235 offset:33280\n"
"v_mfma_f32_16x16x32_bf16 v[172:175], a[252:255], a[112:115], v[172:175]\n"
"ds_read_b64_tr_b16 a[242:243], v235 offset:35328\n"
"v_mfma_f32_16x16x32_bf16 v[180:183], a[248:251], a[100:103], v[180:183]\n"
"buffer_load_dwordx4 v242, s[40:43], s63 offen offset:3072 sc0 sc1 nt lds\n"
"v_mfma_f32_16x16x32_bf16 v[180:183], a[252:255], a[116:119], v[180:183]\n"
"v_mfma_f32_16x16x32_bf16 v[188:191], a[248:251], a[104:107], v[188:191]\n"
"ds_read_b64_tr_b16 a[244:245], v236 offset:33280\n"
"v_mfma_f32_16x16x32_bf16 v[188:191], a[252:255], a[120:123], v[188:191]\n"
"ds_read_b64_tr_b16 a[246:247], v236 offset:35328\n"
"v_mfma_f32_16x16x32_bf16 v[196:199], a[248:251], a[108:111], v[196:199]\n"
"v_mfma_f32_16x16x32_bf16 v[196:199], a[252:255], a[124:127], v[196:199]\n"
"s_waitcnt lgkmcnt(4)\n"
"v_mfma_f32_16x16x32_bf16 v[0:3], a[232:235], v[200:203], v[0:3]\n"
"ds_read_b64_tr_b16 a[248:249], v235 offset:36864\n"
"v_mfma_f32_16x16x32_bf16 v[4:7], a[236:239], v[200:203], v[4:7]\n"
"ds_read_b64_tr_b16 a[250:251], v235 offset:38912\n"
"v_mfma_f32_16x16x32_bf16 v[8:11], a[232:235], v[208:211], v[8:11]\n"
"v_exp_f32 v168, v168\n"
"v_mfma_f32_16x16x32_bf16 v[12:15], a[236:239], v[208:211], v[12:15]\n"
"v_add_f32 v244, v244, v168\n"
"v_mfma_f32_16x16x32_bf16 v[16:19], a[232:235], v[216:219], v[16:19]\n"
"ds_read_b64_tr_b16 a[252:253], v236 offset:36864\n"
"v_mfma_f32_16x16x32_bf16 v[20:23], a[236:239], v[216:219], v[20:23]\n"
"ds_read_b64_tr_b16 a[254:255], v236 offset:38912\n"
"v_mfma_f32_16x16x32_bf16 v[24:27], a[232:235], v[224:227], v[24:27]\n"
"v_exp_f32 v169, v169\n"
"v_mfma_f32_16x16x32_bf16 v[28:31], a[236:239], v[224:227], v[28:31]\n"
"s_waitcnt lgkmcnt(4)\n"
"v_add_f32 v244, v244, v169\n"
"v_cvt_pk_bf16_f32 v168, v168, v169\n"
"v_mfma_f32_16x16x32_bf16 v[56:59], a[240:243], v[224:227], v[56:59]\n"
"ds_read_b64_tr_b16 a[232:233], v235 offset:37376\n"
"v_mfma_f32_16x16x32_bf16 v[60:63], a[244:247], v[224:227], v[60:63]\n"
"ds_read_b64_tr_b16 a[234:235], v235 offset:39424\n"
"v_mfma_f32_16x16x32_bf16 v[48:51], a[240:243], v[216:219], v[48:51]\n"
"v_exp_f32 v170, v170\n"
"v_mfma_f32_16x16x32_bf16 v[52:55], a[244:247], v[216:219], v[52:55]\n"
"v_add_f32 v244, v244, v170\n"
"v_mfma_f32_16x16x32_bf16 v[40:43], a[240:243], v[208:211], v[40:43]\n"
"ds_read_b64_tr_b16 a[236:237], v236 offset:37376\n"
"v_mfma_f32_16x16x32_bf16 v[44:47], a[244:247], v[208:211], v[44:47]\n"
"ds_read_b64_tr_b16 a[238:239], v236 offset:39424\n"
"v_add_u32 v242, 81920, v241\n"
"s_add_u32 m0, s71, 0\n"
"v_mfma_f32_16x16x32_bf16 v[32:35], a[240:243], v[200:203], v[32:35]\n"
"buffer_load_dwordx4 v242, s[44:47], 0 offen offset:0 sc0 sc1 nt lds\n"
"v_mfma_f32_16x16x32_bf16 v[36:39], a[244:247], v[200:203], v[36:39]\n"
"s_waitcnt lgkmcnt(4)\n"
"v_exp_f32 v171, v171\n"
"v_mfma_f32_16x16x32_bf16 v[64:67], a[248:251], v[200:203], v[64:67]\n"
"ds_read_b64_tr_b16 a[240:241], v235 offset:40960\n"
"v_add_f32 v244, v244, v171\n"
"v_mfma_f32_16x16x32_bf16 v[68:71], a[252:255], v[200:203], v[68:71]\n"
"ds_read_b64_tr_b16 a[242:243], v235 offset:43008\n"
"v_cvt_pk_bf16_f32 v169, v170, v171\n"
"v_mfma_f32_16x16x32_bf16 v[72:75], a[248:251], v[208:211], v[72:75]\n"
"v_exp_f32 v172, v172\n"
"v_mfma_f32_16x16x32_bf16 v[76:79], a[252:255], v[208:211], v[76:79]\n"
"v_add_f32 v244, v244, v172\n"
"v_mfma_f32_16x16x32_bf16 v[80:83], a[248:251], v[216:219], v[80:83]\n"
"ds_read_b64_tr_b16 a[244:245], v236 offset:40960\n"
"v_mfma_f32_16x16x32_bf16 v[84:87], a[252:255], v[216:219], v[84:87]\n"
"ds_read_b64_tr_b16 a[246:247], v236 offset:43008\n"
"v_mfma_f32_16x16x32_bf16 v[88:91], a[248:251], v[224:227], v[88:91]\n"
"v_exp_f32 v173, v173\n"
"v_mfma_f32_16x16x32_bf16 v[92:95], a[252:255], v[224:227], v[92:95]\n"
"s_waitcnt lgkmcnt(4)\n"
"v_add_f32 v244, v244, v173\n"
"v_cvt_pk_bf16_f32 v170, v172, v173\n"
"v_mfma_f32_16x16x32_bf16 v[120:123], a[232:235], v[224:227], v[120:123]\n"
"ds_read_b64_tr_b16 a[248:249], v235 offset:41472\n"
"v_mfma_f32_16x16x32_bf16 v[124:127], a[236:239], v[224:227], v[124:127]\n"
"ds_read_b64_tr_b16 a[250:251], v235 offset:43520\n"
"v_mfma_f32_16x16x32_bf16 v[112:115], a[232:235], v[216:219], v[112:115]\n"
"s_waitcnt vmcnt(30)\n"
"s_barrier\n"
"v_mfma_f32_16x16x32_bf16 v[116:119], a[236:239], v[216:219], v[116:119]\n"
"v_exp_f32 v174, v174\n"
"v_mfma_f32_16x16x32_bf16 v[104:107], a[232:235], v[208:211], v[104:107]\n"
"ds_read_b64_tr_b16 a[252:253], v236 offset:41472\n"
"v_add_f32 v244, v244, v174\n"
"v_mfma_f32_16x16x32_bf16 v[108:111], a[236:239], v[208:211], v[108:111]\n"
"ds_read_b64_tr_b16 a[254:255], v236 offset:43520\n"
"v_mfma_f32_16x16x32_bf16 v[96:99], a[232:235], v[200:203], v[96:99]\n"
"buffer_load_dwordx4 v242, s[44:47], s61 offen offset:1024 sc0 sc1 nt lds\n"
"v_mfma_f32_16x16x32_bf16 v[100:103], a[236:239], v[200:203], v[100:103]\n"
"s_waitcnt lgkmcnt(4)\n"
"v_exp_f32 v175, v175\n"
"v_mfma_f32_16x16x32_bf16 v[128:131], a[240:243], v[200:203], v[128:131]\n"
"ds_read_b64_tr_b16 a[232:233], v235 offset:45056\n"
"v_add_f32 v244, v244, v175\n"
"v_mfma_f32_16x16x32_bf16 v[132:135], a[244:247], v[200:203], v[132:135]\n"
"ds_read_b64_tr_b16 a[234:235], v235 offset:47104\n"
"v_cvt_pk_bf16_f32 v171, v174, v175\n"
"v_mfma_f32_16x16x32_bf16 v[136:139], a[240:243], v[208:211], v[136:139]\n"
"v_exp_f32 v176, v176\n"
"v_mfma_f32_16x16x32_bf16 v[140:143], a[244:247], v[208:211], v[140:143]\n"
"v_add_f32 v245, v245, v176\n"
"v_mfma_f32_16x16x32_bf16 v[144:147], a[240:243], v[216:219], v[144:147]\n"
"ds_read_b64_tr_b16 a[236:237], v236 offset:45056\n"
"v_mfma_f32_16x16x32_bf16 v[148:151], a[244:247], v[216:219], v[148:151]\n"
"ds_read_b64_tr_b16 a[238:239], v236 offset:47104\n"
"v_mfma_f32_16x16x32_bf16 a[128:131], a[240:243], v[224:227], a[128:131]\n"
"v_exp_f32 v177, v177\n"
"v_mfma_f32_16x16x32_bf16 a[132:135], a[244:247], v[224:227], a[132:135]\n"
"s_waitcnt lgkmcnt(4)\n"
"v_add_f32 v245, v245, v177\n"
"v_cvt_pk_bf16_f32 v176, v176, v177\n"
"v_mfma_f32_16x16x32_bf16 a[160:163], a[248:251], v[224:227], a[160:163]\n"
"ds_read_b64_tr_b16 a[240:241], v235 offset:45568\n"
"v_mfma_f32_16x16x32_bf16 a[164:167], a[252:255], v[224:227], a[164:167]\n"
"ds_read_b64_tr_b16 a[242:243], v235 offset:47616\n"
"v_mfma_f32_16x16x32_bf16 a[152:155], a[248:251], v[216:219], a[152:155]\n"
"v_exp_f32 v178, v178\n"
"v_mfma_f32_16x16x32_bf16 a[156:159], a[252:255], v[216:219], a[156:159]\n"
"v_add_f32 v245, v245, v178\n"
"v_mfma_f32_16x16x32_bf16 a[144:147], a[248:251], v[208:211], a[144:147]\n"
"ds_read_b64_tr_b16 a[244:245], v236 offset:45568\n"
"v_mfma_f32_16x16x32_bf16 a[148:151], a[252:255], v[208:211], a[148:151]\n"
"ds_read_b64_tr_b16 a[246:247], v236 offset:47616\n"
"v_mfma_f32_16x16x32_bf16 a[136:139], a[248:251], v[200:203], a[136:139]\n"
"v_exp_f32 v179, v179\n"
"v_mfma_f32_16x16x32_bf16 a[140:143], a[252:255], v[200:203], a[140:143]\n"
"s_waitcnt lgkmcnt(4)\n"
"v_add_f32 v245, v245, v179\n"
"v_cvt_pk_bf16_f32 v177, v178, v179\n"
"v_mfma_f32_16x16x32_bf16 a[168:171], a[232:235], v[200:203], a[168:171]\n"
"ds_read_b128 a[248:251], v232 offset:49152\n"
"v_mfma_f32_16x16x32_bf16 a[172:175], a[236:239], v[200:203], a[172:175]\n"
"buffer_load_dwordx4 v242, s[44:47], s62 offen offset:2048 sc0 sc1 nt lds\n"
"v_mfma_f32_16x16x32_bf16 a[176:179], a[232:235], v[208:211], a[176:179]\n"
"v_exp_f32 v180, v180\n"
"v_mfma_f32_16x16x32_bf16 a[180:183], a[236:239], v[208:211], a[180:183]\n"
"v_add_f32 v245, v245, v180\n"
"v_mfma_f32_16x16x32_bf16 a[184:187], a[232:235], v[216:219], a[184:187]\n"
"ds_read_b128 a[252:255], v232 offset:49664\n"
"v_mfma_f32_16x16x32_bf16 a[188:191], a[236:239], v[216:219], a[188:191]\n"
"v_exp_f32 v181, v181\n"
"v_mfma_f32_16x16x32_bf16 a[192:195], a[232:235], v[224:227], a[192:195]\n"
"v_add_f32 v245, v245, v181\n"
"v_cvt_pk_bf16_f32 v178, v180, v181\n"
"v_mfma_f32_16x16x32_bf16 a[196:199], a[236:239], v[224:227], a[196:199]\n"
"s_waitcnt lgkmcnt(2)\n"
"v_exp_f32 v182, v182\n"
"v_mfma_f32_16x16x32_bf16 a[200:203], a[240:243], v[200:203], a[200:203]\n"
"ds_read_b128 a[232:235], v232 offset:51200\n"
"v_add_f32 v245, v245, v182\n"
"v_mfma_f32_16x16x32_bf16 a[204:207], a[244:247], v[200:203], a[204:207]\n"
"v_exp_f32 v183, v183\n"
"v_mfma_f32_16x16x32_bf16 a[208:211], a[240:243], v[208:211], a[208:211]\n"
"v_add_f32 v245, v245, v183\n"
"v_cvt_pk_bf16_f32 v179, v182, v183\n"
"v_mfma_f32_16x16x32_bf16 a[212:215], a[244:247], v[208:211], a[212:215]\n"
"v_exp_f32 v184, v184\n"
"v_mfma_f32_16x16x32_bf16 a[216:219], a[240:243], v[216:219], a[216:219]\n"
"ds_read_b128 a[236:239], v232 offset:51712\n"
"v_add_f32 v246, v246, v184\n"
"v_mfma_f32_16x16x32_bf16 a[220:223], a[244:247], v[216:219], a[220:223]\n"
"v_exp_f32 v185, v185\n"
"v_mfma_f32_16x16x32_bf16 a[224:227], a[240:243], v[224:227], a[224:227]\n"
"v_add_f32 v246, v246, v185\n"
"v_cvt_pk_bf16_f32 v184, v184, v185\n"
"v_mfma_f32_16x16x32_bf16 a[228:231], a[244:247], v[224:227], a[228:231]\n"
"s_waitcnt lgkmcnt(2)\n"
"v_exp_f32 v186, v186\n"
"v_mfma_f32_16x16x32_bf16 v[200:203], a[248:251], a[0:3], v[152:155]\n"
"ds_read_b128 a[240:243], v232 offset:53248\n"
"v_add_f32 v246, v246, v186\n"
"v_mfma_f32_16x16x32_bf16 v[200:203], a[252:255], a[16:19], v[200:203]\n"
"v_exp_f32 v187, v187\n"
"v_mfma_f32_16x16x32_bf16 v[208:211], a[248:251], a[4:7], v[156:159]\n"
"buffer_load_dwordx4 v242, s[44:47], s63 offen offset:3072 sc0 sc1 nt lds\n"
"v_mfma_f32_16x16x32_bf16 v[208:211], a[252:255], a[20:23], v[208:211]\n"
"v_add_f32 v246, v246, v187\n"
"v_mfma_f32_16x16x32_bf16 v[216:219], a[248:251], a[8:11], v[160:163]\n"
"ds_read_b128 a[244:247], v232 offset:53760\n"
"v_mfma_f32_16x16x32_bf16 v[216:219], a[252:255], a[24:27], v[216:219]\n"
"v_cvt_pk_bf16_f32 v185, v186, v187\n"
"v_mfma_f32_16x16x32_bf16 v[224:227], a[248:251], a[12:15], v[164:167]\n"
"v_exp_f32 v188, v188\n"
"v_mfma_f32_16x16x32_bf16 v[224:227], a[252:255], a[28:31], v[224:227]\n"
"s_waitcnt lgkmcnt(2)\n"
"v_add_f32 v246, v246, v188\n"
"v_mfma_f32_16x16x32_bf16 v[228:231], a[232:235], a[12:15], v[164:167]\n"
"ds_read_b128 a[248:251], v232 offset:55296\n"
"v_mfma_f32_16x16x32_bf16 v[228:231], a[236:239], a[28:31], v[228:231]\n"
"v_exp_f32 v189, v189\n"
"v_mfma_f32_16x16x32_bf16 v[220:223], a[232:235], a[8:11], v[160:163]\n"
"v_add_f32 v246, v246, v189\n"
"v_cvt_pk_bf16_f32 v186, v188, v189\n"
"v_mfma_f32_16x16x32_bf16 v[220:223], a[236:239], a[24:27], v[220:223]\n"
"v_exp_f32 v190, v190\n"
"v_mfma_f32_16x16x32_bf16 v[212:215], a[232:235], a[4:7], v[156:159]\n"
"ds_read_b128 a[252:255], v232 offset:55808\n"
"v_mfma_f32_16x16x32_bf16 v[212:215], a[236:239], a[20:23], v[212:215]\n"
"v_add_f32 v246, v246, v190\n"
"v_mfma_f32_16x16x32_bf16 v[204:207], a[232:235], a[0:3], v[152:155]\n"
"v_exp_f32 v191, v191\n"
"v_mfma_f32_16x16x32_bf16 v[204:207], a[236:239], a[16:19], v[204:207]\n"
"s_waitcnt lgkmcnt(2)\n"
"v_add_f32 v246, v246, v191\n"
"v_mfma_f32_16x16x32_bf16 v[200:203], a[240:243], a[32:35], v[200:203]\n"
"ds_read_b128 a[232:235], v232 offset:57344\n"
"v_cvt_pk_bf16_f32 v187, v190, v191\n"
"v_mfma_f32_16x16x32_bf16 v[200:203], a[244:247], a[48:51], v[200:203]\n"
"v_exp_f32 v192, v192\n"
"v_mfma_f32_16x16x32_bf16 v[208:211], a[240:243], a[36:39], v[208:211]\n"
"v_add_f32 v247, v247, v192\n"
"v_mfma_f32_16x16x32_bf16 v[208:211], a[244:247], a[52:55], v[208:211]\n"
"v_exp_f32 v193, v193\n"
"v_mfma_f32_16x16x32_bf16 v[216:219], a[240:243], a[40:43], v[216:219]\n"
"ds_read_b128 a[236:239], v232 offset:57856\n"
"v_add_u32 v242, 114688, v241\n"
"s_add_u32 m0, s71, 16384\n"
"v_mfma_f32_16x16x32_bf16 v[216:219], a[244:247], a[56:59], v[216:219]\n"
"buffer_load_dwordx4 v242, s[40:43], 0 offen offset:0 sc0 sc1 nt lds\n"
"v_mfma_f32_16x16x32_bf16 v[224:227], a[240:243], a[44:47], v[224:227]\n"
"v_mfma_f32_16x16x32_bf16 v[224:227], a[244:247], a[60:63], v[224:227]\n"
"s_waitcnt lgkmcnt(2)\n"
"v_add_f32 v247, v247, v193\n"
"v_cvt_pk_bf16_f32 v192, v192, v193\n"
"v_mfma_f32_16x16x32_bf16 v[228:231], a[248:251], a[44:47], v[228:231]\n"
"ds_read_b128 a[240:243], v232 offset:59392\n"
"v_mfma_f32_16x16x32_bf16 v[228:231], a[252:255], a[60:63], v[228:231]\n"
"v_exp_f32 v194, v194\n"
"v_mfma_f32_16x16x32_bf16 v[220:223], a[248:251], a[40:43], v[220:223]\n"
"s_waitcnt vmcnt(26)\n"
"s_barrier\n"
"v_mfma_f32_16x16x32_bf16 v[220:223], a[252:255], a[56:59], v[220:223]\n"
"v_add_f32 v247, v247, v194\n"
"v_mfma_f32_16x16x32_bf16 v[212:215], a[248:251], a[36:39], v[212:215]\n"
"ds_read_b128 a[244:247], v232 offset:59904\n"
"v_mfma_f32_16x16x32_bf16 v[212:215], a[252:255], a[52:55], v[212:215]\n"
"v_exp_f32 v195, v195\n"
"v_mfma_f32_16x16x32_bf16 v[204:207], a[248:251], a[32:35], v[204:207]\n"
"v_add_f32 v247, v247, v195\n"
"v_cvt_pk_bf16_f32 v193, v194, v195\n"
"v_mfma_f32_16x16x32_bf16 v[204:207], a[252:255], a[48:51], v[204:207]\n"
"s_waitcnt lgkmcnt(2)\n"
"v_exp_f32 v196, v196\n"
"v_mfma_f32_16x16x32_bf16 v[200:203], a[232:235], a[64:67], v[200:203]\n"
"ds_read_b128 a[248:251], v232 offset:61440\n"
"v_mfma_f32_16x16x32_bf16 v[200:203], a[236:239], a[80:83], v[200:203]\n"
"v_add_f32 v247, v247, v196\n"
"v_mfma_f32_16x16x32_bf16 v[208:211], a[232:235], a[68:71], v[208:211]\n"
"v_exp_f32 v197, v197\n"
"v_mfma_f32_16x16x32_bf16 v[208:211], a[236:239], a[84:87], v[208:211]\n"
"v_add_f32 v247, v247, v197\n"
"v_mfma_f32_16x16x32_bf16 v[216:219], a[232:235], a[72:75], v[216:219]\n"
"ds_read_b128 a[252:255], v232 offset:61952\n"
"v_cvt_pk_bf16_f32 v194, v196, v197\n"
"v_mfma_f32_16x16x32_bf16 v[216:219], a[236:239], a[88:91], v[216:219]\n"
"v_exp_f32 v198, v198\n"
"v_mfma_f32_16x16x32_bf16 v[224:227], a[232:235], a[76:79], v[224:227]\n"
"buffer_load_dwordx4 v242, s[40:43], s61 offen offset:1024 sc0 sc1 nt lds\n"
"v_mfma_f32_16x16x32_bf16 v[224:227], a[236:239], a[92:95], v[224:227]\n"
"s_waitcnt lgkmcnt(2)\n"
"v_add_f32 v247, v247, v198\n"
"v_mfma_f32_16x16x32_bf16 v[228:231], a[240:243], a[76:79], v[228:231]\n"
"ds_read_b128 a[232:235], v232 offset:63488\n"
"v_mfma_f32_16x16x32_bf16 v[228:231], a[244:247], a[92:95], v[228:231]\n"
"v_exp_f32 v199, v199\n"
"v_mfma_f32_16x16x32_bf16 v[220:223], a[240:243], a[72:75], v[220:223]\n"
"v_add_f32 v247, v247, v199\n"
"v_mfma_f32_16x16x32_bf16 v[220:223], a[244:247], a[88:91], v[220:223]\n"
"v_cvt_pk_bf16_f32 v195, v198, v199\n"
"v_mfma_f32_16x16x32_bf16 v[212:215], a[240:243], a[68:71], v[212:215]\n"
"ds_read_b128 a[236:239], v232 offset:64000\n"
"v_mfma_f32_16x16x32_bf16 v[212:215], a[244:247], a[84:87], v[212:215]\n"
"v_mfma_f32_16x16x32_bf16 v[204:207], a[240:243], a[64:67], v[204:207]\n"
"v_mfma_f32_16x16x32_bf16 v[204:207], a[244:247], a[80:83], v[204:207]\n"
"s_waitcnt lgkmcnt(2)\n"
"v_mfma_f32_16x16x32_bf16 v[224:227], a[248:251], a[108:111], v[224:227]\n"
"ds_read_b64_tr_b16 a[240:241], v237 offset:0\n"
"v_mfma_f32_16x16x32_bf16 v[224:227], a[252:255], a[124:127], v[224:227]\n"
"ds_read_b64_tr_b16 a[242:243], v237 offset:2048\n"
"v_mfma_f32_16x16x32_bf16 v[216:219], a[248:251], a[104:107], v[216:219]\n"
"v_mfma_f32_16x16x32_bf16 v[216:219], a[252:255], a[120:123], v[216:219]\n"
"v_mfma_f32_16x16x32_bf16 v[208:211], a[248:251], a[100:103], v[208:211]\n"
"ds_read_b64_tr_b16 a[244:245], v238 offset:0\n"
"v_mfma_f32_16x16x32_bf16 v[208:211], a[252:255], a[116:119], v[208:211]\n"
"ds_read_b64_tr_b16 a[246:247], v238 offset:2048\n"
"v_mfma_f32_16x16x32_bf16 v[200:203], a[248:251], a[96:99], v[200:203]\n"
"v_mfma_f32_16x16x32_bf16 v[200:203], a[252:255], a[112:115], v[200:203]\n"
"s_waitcnt lgkmcnt(4)\n"
"v_mfma_f32_16x16x32_bf16 v[204:207], a[232:235], a[96:99], v[204:207]\n"
"ds_read_b64_tr_b16 a[248:249], v237 offset:512\n"
"v_mfma_f32_16x16x32_bf16 v[204:207], a[236:239], a[112:115], v[204:207]\n"
"ds_read_b64_tr_b16 a[250:251], v237 offset:2560\n"
"v_mfma_f32_16x16x32_bf16 v[212:215], a[232:235], a[100:103], v[212:215]\n"
"buffer_load_dwordx4 v242, s[40:43], s62 offen offset:2048 sc0 sc1 nt lds\n"
"v_mfma_f32_16x16x32_bf16 v[212:215], a[236:239], a[116:119], v[212:215]\n"
"v_mfma_f32_16x16x32_bf16 v[220:223], a[232:235], a[104:107], v[220:223]\n"
"ds_read_b64_tr_b16 a[252:253], v238 offset:512\n"
"v_mfma_f32_16x16x32_bf16 v[220:223], a[236:239], a[120:123], v[220:223]\n"
"ds_read_b64_tr_b16 a[254:255], v238 offset:2560\n"
"v_mfma_f32_16x16x32_bf16 v[228:231], a[232:235], a[108:111], v[228:231]\n"
"v_mfma_f32_16x16x32_bf16 v[228:231], a[236:239], a[124:127], v[228:231]\n"
"s_waitcnt lgkmcnt(4)\n"
"v_mfma_f32_16x16x32_bf16 v[0:3], a[240:243], v[168:171], v[0:3]\n"
"ds_read_b64_tr_b16 a[232:233], v237 offset:4096\n"
"v_mfma_f32_16x16x32_bf16 v[4:7], a[244:247], v[168:171], v[4:7]\n"
"ds_read_b64_tr_b16 a[234:235], v237 offset:6144\n"
"v_mfma_f32_16x16x32_bf16 v[8:11], a[240:243], v[176:179], v[8:11]\n"
"v_exp_f32 v200, v200\n"
"v_mfma_f32_16x16x32_bf16 v[12:15], a[244:247], v[176:179], v[12:15]\n"
"v_add_f32 v244, v244, v200\n"
"v_mfma_f32_16x16x32_bf16 v[16:19], a[240:243], v[184:187], v[16:19]\n"
"ds_read_b64_tr_b16 a[236:237], v238 offset:4096\n"
"v_mfma_f32_16x16x32_bf16 v[20:23], a[244:247], v[184:187], v[20:23]\n"
"ds_read_b64_tr_b16 a[238:239], v238 offset:6144\n"
"v_mfma_f32_16x16x32_bf16 v[24:27], a[240:243], v[192:195], v[24:27]\n"
"v_exp_f32 v201, v201\n"
"v_mfma_f32_16x16x32_bf16 v[28:31], a[244:247], v[192:195], v[28:31]\n"
"s_waitcnt lgkmcnt(4)\n"
"v_add_f32 v244, v244, v201\n"
"v_cvt_pk_bf16_f32 v200, v200, v201\n"
"v_mfma_f32_16x16x32_bf16 v[56:59], a[248:251], v[192:195], v[56:59]\n"
"ds_read_b64_tr_b16 a[240:241], v237 offset:4608\n"
"v_mfma_f32_16x16x32_bf16 v[60:63], a[252:255], v[192:195], v[60:63]\n"
"ds_read_b64_tr_b16 a[242:243], v237 offset:6656\n"
"v_mfma_f32_16x16x32_bf16 v[48:51], a[248:251], v[184:187], v[48:51]\n"
"buffer_load_dwordx4 v242, s[40:43], s63 offen offset:3072 sc0 sc1 nt lds\n"
"v_mfma_f32_16x16x32_bf16 v[52:55], a[252:255], v[184:187], v[52:55]\n"
"v_exp_f32 v202, v202\n"
"v_mfma_f32_16x16x32_bf16 v[40:43], a[248:251], v[176:179], v[40:43]\n"
"ds_read_b64_tr_b16 a[244:245], v238 offset:4608\n"
"v_add_f32 v244, v244, v202\n"
"v_mfma_f32_16x16x32_bf16 v[44:47], a[252:255], v[176:179], v[44:47]\n"
"ds_read_b64_tr_b16 a[246:247], v238 offset:6656\n"
"v_mfma_f32_16x16x32_bf16 v[32:35], a[248:251], v[168:171], v[32:35]\n"
"v_exp_f32 v203, v203\n"
"v_mfma_f32_16x16x32_bf16 v[36:39], a[252:255], v[168:171], v[36:39]\n"
"s_waitcnt lgkmcnt(4)\n"
"v_add_f32 v244, v244, v203\n"
"v_cvt_pk_bf16_f32 v201, v202, v203\n"
"v_mfma_f32_16x16x32_bf16 v[64:67], a[232:235], v[168:171], v[64:67]\n"
"ds_read_b64_tr_b16 a[248:249], v237 offset:8192\n"
"v_mfma_f32_16x16x32_bf16 v[68:71], a[236:239], v[168:171], v[68:71]\n"
"ds_read_b64_tr_b16 a[250:251], v237 offset:10240\n"
"v_mfma_f32_16x16x32_bf16 v[72:75], a[232:235], v[176:179], v[72:75]\n"
"v_exp_f32 v204, v204\n"
"v_mfma_f32_16x16x32_bf16 v[76:79], a[236:239], v[176:179], v[76:79]\n"
"v_add_f32 v244, v244, v204\n"
"v_mfma_f32_16x16x32_bf16 v[80:83], a[232:235], v[184:187], v[80:83]\n"
"ds_read_b64_tr_b16 a[252:253], v238 offset:8192\n"
"v_mfma_f32_16x16x32_bf16 v[84:87], a[236:239], v[184:187], v[84:87]\n"
"ds_read_b64_tr_b16 a[254:255], v238 offset:10240\n"
"v_mfma_f32_16x16x32_bf16 v[88:91], a[232:235], v[192:195], v[88:91]\n"
"v_exp_f32 v205, v205\n"
"v_mfma_f32_16x16x32_bf16 v[92:95], a[236:239], v[192:195], v[92:95]\n"
"s_waitcnt lgkmcnt(4)\n"
"v_add_f32 v244, v244, v205\n"
"v_cvt_pk_bf16_f32 v202, v204, v205\n"
"v_mfma_f32_16x16x32_bf16 v[120:123], a[240:243], v[192:195], v[120:123]\n"
"ds_read_b64_tr_b16 a[232:233], v237 offset:8704\n"
"v_mfma_f32_16x16x32_bf16 v[124:127], a[244:247], v[192:195], v[124:127]\n"
"ds_read_b64_tr_b16 a[234:235], v237 offset:10752\n"
"v_mfma_f32_16x16x32_bf16 v[112:115], a[240:243], v[184:187], v[112:115]\n"
"s_waitcnt vmcnt(24)\n"
"s_barrier\n"
"v_mfma_f32_16x16x32_bf16 v[116:119], a[244:247], v[184:187], v[116:119]\n"
"v_exp_f32 v206, v206\n"
"v_mfma_f32_16x16x32_bf16 v[104:107], a[240:243], v[176:179], v[104:107]\n"
"ds_read_b64_tr_b16 a[236:237], v238 offset:8704\n"
"v_add_f32 v244, v244, v206\n"
"v_mfma_f32_16x16x32_bf16 v[108:111], a[244:247], v[176:179], v[108:111]\n"
"ds_read_b64_tr_b16 a[238:239], v238 offset:10752\n"
"v_add_u32 v242, 98304, v241\n"
"s_add_u32 m0, s71, 32768\n"
"v_mfma_f32_16x16x32_bf16 v[96:99], a[240:243], v[168:171], v[96:99]\n"
"buffer_load_dwordx4 v242, s[44:47], 0 offen offset:0 sc0 sc1 nt lds\n"
"v_mfma_f32_16x16x32_bf16 v[100:103], a[244:247], v[168:171], v[100:103]\n"
"s_waitcnt lgkmcnt(4)\n"
"v_exp_f32 v207, v207\n"
"v_mfma_f32_16x16x32_bf16 v[128:131], a[248:251], v[168:171], v[128:131]\n"
"ds_read_b64_tr_b16 a[240:241], v237 offset:12288\n"
"v_add_f32 v244, v244, v207\n"
"v_mfma_f32_16x16x32_bf16 v[132:135], a[252:255], v[168:171], v[132:135]\n"
"ds_read_b64_tr_b16 a[242:243], v237 offset:14336\n"
"v_cvt_pk_bf16_f32 v203, v206, v207\n"
"v_mfma_f32_16x16x32_bf16 v[136:139], a[248:251], v[176:179], v[136:139]\n"
"v_exp_f32 v208, v208\n"
"v_mfma_f32_16x16x32_bf16 v[140:143], a[252:255], v[176:179], v[140:143]\n"
"v_add_f32 v245, v245, v208\n"
"v_mfma_f32_16x16x32_bf16 v[144:147], a[248:251], v[184:187], v[144:147]\n"
"ds_read_b64_tr_b16 a[244:245], v238 offset:12288\n"
"v_mfma_f32_16x16x32_bf16 v[148:151], a[252:255], v[184:187], v[148:151]\n"
"ds_read_b64_tr_b16 a[246:247], v238 offset:14336\n"
"v_mfma_f32_16x16x32_bf16 a[128:131], a[248:251], v[192:195], a[128:131]\n"
"v_exp_f32 v209, v209\n"
"v_mfma_f32_16x16x32_bf16 a[132:135], a[252:255], v[192:195], a[132:135]\n"
"s_waitcnt lgkmcnt(4)\n"
"v_add_f32 v245, v245, v209\n"
"v_cvt_pk_bf16_f32 v208, v208, v209\n"
"v_mfma_f32_16x16x32_bf16 a[160:163], a[232:235], v[192:195], a[160:163]\n"
"ds_read_b64_tr_b16 a[248:249], v237 offset:12800\n"
"v_mfma_f32_16x16x32_bf16 a[164:167], a[236:239], v[192:195], a[164:167]\n"
"ds_read_b64_tr_b16 a[250:251], v237 offset:14848\n"
"v_mfma_f32_16x16x32_bf16 a[152:155], a[232:235], v[184:187], a[152:155]\n"
"v_exp_f32 v210, v210\n"
"v_mfma_f32_16x16x32_bf16 a[156:159], a[236:239], v[184:187], a[156:159]\n"
"v_add_f32 v245, v245, v210\n"
"v_mfma_f32_16x16x32_bf16 a[144:147], a[232:235], v[176:179], a[144:147]\n"
"ds_read_b64_tr_b16 a[252:253], v238 offset:12800\n"
"v_mfma_f32_16x16x32_bf16 a[148:151], a[236:239], v[176:179], a[148:151]\n"
"ds_read_b64_tr_b16 a[254:255], v238 offset:14848\n"
"v_mfma_f32_16x16x32_bf16 a[136:139], a[232:235], v[168:171], a[136:139]\n"
"buffer_load_dwordx4 v242, s[44:47], s61 offen offset:1024 sc0 sc1 nt lds\n"
"v_mfma_f32_16x16x32_bf16 a[140:143], a[236:239], v[168:171], a[140:143]\n"
"s_waitcnt lgkmcnt(4)\n"
"v_exp_f32 v211, v211\n"
"v_mfma_f32_16x16x32_bf16 a[168:171], a[240:243], v[168:171], a[168:171]\n"
"ds_read_b128 a[232:235], v233 offset:16384\n"
"v_add_f32 v245, v245, v211\n"
"v_mfma_f32_16x16x32_bf16 a[172:175], a[244:247], v[168:171], a[172:175]\n"
"v_cvt_pk_bf16_f32 v209, v210, v211\n"
"v_mfma_f32_16x16x32_bf16 a[176:179], a[240:243], v[176:179], a[176:179]\n"
"v_exp_f32 v212, v212\n"
"v_mfma_f32_16x16x32_bf16 a[180:183], a[244:247], v[176:179], a[180:183]\n"
"v_add_f32 v245, v245, v212\n"
"v_mfma_f32_16x16x32_bf16 a[184:187], a[240:243], v[184:187], a[184:187]\n"
"ds_read_b128 a[236:239], v233 offset:16896\n"
"v_mfma_f32_16x16x32_bf16 a[188:191], a[244:247], v[184:187], a[188:191]\n"
"v_exp_f32 v213, v213\n"
"v_mfma_f32_16x16x32_bf16 a[192:195], a[240:243], v[192:195], a[192:195]\n"
"v_add_f32 v245, v245, v213\n"
"v_cvt_pk_bf16_f32 v210, v212, v213\n"
"v_mfma_f32_16x16x32_bf16 a[196:199], a[244:247], v[192:195], a[196:199]\n"
"s_waitcnt lgkmcnt(2)\n"
"v_exp_f32 v214, v214\n"
"v_mfma_f32_16x16x32_bf16 a[200:203], a[248:251], v[168:171], a[200:203]\n"
"ds_read_b128 a[240:243], v233 offset:18432\n"
"v_add_f32 v245, v245, v214\n"
"v_mfma_f32_16x16x32_bf16 a[204:207], a[252:255], v[168:171], a[204:207]\n"
"v_exp_f32 v215, v215\n"
"v_mfma_f32_16x16x32_bf16 a[208:211], a[248:251], v[176:179], a[208:211]\n"
"v_add_f32 v245, v245, v215\n"
"v_cvt_pk_bf16_f32 v211, v214, v215\n"
"v_mfma_f32_16x16x32_bf16 a[212:215], a[252:255], v[176:179], a[212:215]\n"
"v_exp_f32 v216, v216\n"
"v_mfma_f32_16x16x32_bf16 a[216:219], a[248:251], v[184:187], a[216:219]\n"
"ds_read_b128 a[244:247], v233 offset:18944\n"
"v_add_f32 v246, v246, v216\n"
"v_mfma_f32_16x16x32_bf16 a[220:223], a[252:255], v[184:187], a[220:223]\n"
"v_exp_f32 v217, v217\n"
"v_mfma_f32_16x16x32_bf16 a[224:227], a[248:251], v[192:195], a[224:227]\n"
"v_add_f32 v246, v246, v217\n"
"v_cvt_pk_bf16_f32 v216, v216, v217\n"
"v_mfma_f32_16x16x32_bf16 a[228:231], a[252:255], v[192:195], a[228:231]\n"
"s_waitcnt lgkmcnt(2)\n"
"v_exp_f32 v218, v218\n"
"v_mfma_f32_16x16x32_bf16 v[168:171], a[232:235], a[0:3], v[152:155]\n"
"ds_read_b128 a[248:251], v233 offset:20480\n"
"v_add_f32 v246, v246, v218\n"
"v_mfma_f32_16x16x32_bf16 v[168:171], a[236:239], a[16:19], v[168:171]\n"
"buffer_load_dwordx4 v242, s[44:47], s62 offen offset:2048 sc0 sc1 nt lds\n"
"v_mfma_f32_16x16x32_bf16 v[176:179], a[232:235], a[4:7], v[156:159]\n"
"v_exp_f32 v219, v219\n"
"v_mfma_f32_16x16x32_bf16 v[176:179], a[236:239], a[20:23], v[176:179]\n"
"v_add_f32 v246, v246, v219\n"
"v_mfma_f32_16x16x32_bf16 v[184:187], a[232:235], a[8:11], v[160:163]\n"
"ds_read_b128 a[252:255], v233 offset:20992\n"
"v_mfma_f32_16x16x32_bf16 v[184:187], a[236:239], a[24:27], v[184:187]\n"
"v_cvt_pk_bf16_f32 v217, v218, v219\n"
"v_mfma_f32_16x16x32_bf16 v[192:195], a[232:235], a[12:15], v[164:167]\n"
"v_exp_f32 v220, v220\n"
"v_mfma_f32_16x16x32_bf16 v[192:195], a[236:239], a[28:31], v[192:195]\n"
"s_waitcnt lgkmcnt(2)\n"
"v_add_f32 v246, v246, v220\n"
"v_mfma_f32_16x16x32_bf16 v[196:199], a[240:243], a[12:15], v[164:167]\n"
"ds_read_b128 a[232:235], v233 offset:22528\n"
"v_mfma_f32_16x16x32_bf16 v[196:199], a[244:247], a[28:31], v[196:199]\n"
"v_exp_f32 v221, v221\n"
"v_mfma_f32_16x16x32_bf16 v[188:191], a[240:243], a[8:11], v[160:163]\n"
"v_add_f32 v246, v246, v221\n"
"v_cvt_pk_bf16_f32 v218, v220, v221\n"
"v_mfma_f32_16x16x32_bf16 v[188:191], a[244:247], a[24:27], v[188:191]\n"
"v_exp_f32 v222, v222\n"
"v_mfma_f32_16x16x32_bf16 v[180:183], a[240:243], a[4:7], v[156:159]\n"
"ds_read_b128 a[236:239], v233 offset:23040\n"
"v_mfma_f32_16x16x32_bf16 v[180:183], a[244:247], a[20:23], v[180:183]\n"
"v_add_f32 v246, v246, v222\n"
"v_mfma_f32_16x16x32_bf16 v[172:175], a[240:243], a[0:3], v[152:155]\n"
"v_exp_f32 v223, v223\n"
"v_mfma_f32_16x16x32_bf16 v[172:175], a[244:247], a[16:19], v[172:175]\n"
"s_waitcnt lgkmcnt(2)\n"
"v_add_f32 v246, v246, v223\n"
"v_mfma_f32_16x16x32_bf16 v[168:171], a[248:251], a[32:35], v[168:171]\n"
"ds_read_b128 a[240:243], v233 offset:24576\n"
"v_cvt_pk_bf16_f32 v219, v222, v223\n"
"v_mfma_f32_16x16x32_bf16 v[168:171], a[252:255], a[48:51], v[168:171]\n"
"v_exp_f32 v224, v224\n"
"v_mfma_f32_16x16x32_bf16 v[176:179], a[248:251], a[36:39], v[176:179]\n"
"buffer_load_dwordx4 v242, s[44:47], s63 offen offset:3072 sc0 sc1 nt lds\n"
"v_mfma_f32_16x16x32_bf16 v[176:179], a[252:255], a[52:55], v[176:179]\n"
"v_add_f32 v247, v247, v224\n"
"v_mfma_f32_16x16x32_bf16 v[184:187], a[248:251], a[40:43], v[184:187]\n"
"ds_read_b128 a[244:247], v233 offset:25088\n"
"v_mfma_f32_16x16x32_bf16 v[184:187], a[252:255], a[56:59], v[184:187]\n"
"v_exp_f32 v225, v225\n"
"v_mfma_f32_16x16x32_bf16 v[192:195], a[248:251], a[44:47], v[192:195]\n"
"v_add_f32 v247, v247, v225\n"
"v_mfma_f32_16x16x32_bf16 v[192:195], a[252:255], a[60:63], v[192:195]\n"
"s_waitcnt lgkmcnt(2)\n"
"v_cvt_pk_bf16_f32 v224, v224, v225\n"
"v_mfma_f32_16x16x32_bf16 v[196:199], a[232:235], a[44:47], v[196:199]\n"
"ds_read_b128 a[248:251], v233 offset:26624\n"
"v_mfma_f32_16x16x32_bf16 v[196:199], a[236:239], a[60:63], v[196:199]\n"
"v_exp_f32 v226, v226\n"
"v_mfma_f32_16x16x32_bf16 v[188:191], a[232:235], a[40:43], v[188:191]\n"
"s_waitcnt vmcnt(24)\n"
"s_barrier\n"
"v_add_f32 v247, v247, v226\n"
"v_mfma_f32_16x16x32_bf16 v[188:191], a[236:239], a[56:59], v[188:191]\n"
"v_exp_f32 v227, v227\n"
"v_mfma_f32_16x16x32_bf16 v[180:183], a[232:235], a[36:39], v[180:183]\n"
"ds_read_b128 a[252:255], v233 offset:27136\n"
"v_mfma_f32_16x16x32_bf16 v[180:183], a[236:239], a[52:55], v[180:183]\n"
"v_add_f32 v247, v247, v227\n"
"v_cvt_pk_bf16_f32 v225, v226, v227\n"
"v_mfma_f32_16x16x32_bf16 v[172:175], a[232:235], a[32:35], v[172:175]\n"
"v_exp_f32 v228, v228\n"
"v_mfma_f32_16x16x32_bf16 v[172:175], a[236:239], a[48:51], v[172:175]\n"
"s_waitcnt lgkmcnt(2)\n"
"v_mfma_f32_16x16x32_bf16 v[168:171], a[240:243], a[64:67], v[168:171]\n"
"ds_read_b128 a[232:235], v233 offset:28672\n"
"v_add_f32 v247, v247, v228\n"
"v_mfma_f32_16x16x32_bf16 v[168:171], a[244:247], a[80:83], v[168:171]\n"
"v_exp_f32 v229, v229\n"
"v_mfma_f32_16x16x32_bf16 v[176:179], a[240:243], a[68:71], v[176:179]\n"
"v_add_f32 v247, v247, v229\n"
"v_mfma_f32_16x16x32_bf16 v[176:179], a[244:247], a[84:87], v[176:179]\n"
"v_cvt_pk_bf16_f32 v226, v228, v229\n"
"v_mfma_f32_16x16x32_bf16 v[184:187], a[240:243], a[72:75], v[184:187]\n"
"ds_read_b128 a[236:239], v233 offset:29184\n"
"v_add_u32 v242, 131072, v241\n"
"s_add_u32 m0, s71, 49152\n"
"v_mfma_f32_16x16x32_bf16 v[184:187], a[244:247], a[88:91], v[184:187]\n"
"buffer_load_dwordx4 v242, s[40:43], 0 offen offset:0 sc0 sc1 nt lds\n"
"v_mfma_f32_16x16x32_bf16 v[192:195], a[240:243], a[76:79], v[192:195]\n"
"v_exp_f32 v230, v230\n"
"v_mfma_f32_16x16x32_bf16 v[192:195], a[244:247], a[92:95], v[192:195]\n"
"s_waitcnt lgkmcnt(2)\n"
"v_add_f32 v247, v247, v230\n"
"v_mfma_f32_16x16x32_bf16 v[196:199], a[248:251], a[76:79], v[196:199]\n"
"ds_read_b128 a[240:243], v233 offset:30720\n"
"v_mfma_f32_16x16x32_bf16 v[196:199], a[252:255], a[92:95], v[196:199]\n"
"v_exp_f32 v231, v231\n"
"v_mfma_f32_16x16x32_bf16 v[188:191], a[248:251], a[72:75], v[188:191]\n"
"v_add_f32 v247, v247, v231\n"
"v_mfma_f32_16x16x32_bf16 v[188:191], a[252:255], a[88:91], v[188:191]\n"
"v_cvt_pk_bf16_f32 v227, v230, v231\n"
"v_mfma_f32_16x16x32_bf16 v[180:183], a[248:251], a[68:71], v[180:183]\n"
"ds_read_b128 a[244:247], v233 offset:31232\n"
"v_mfma_f32_16x16x32_bf16 v[180:183], a[252:255], a[84:87], v[180:183]\n"
"v_mfma_f32_16x16x32_bf16 v[172:175], a[248:251], a[64:67], v[172:175]\n"
"v_mfma_f32_16x16x32_bf16 v[172:175], a[252:255], a[80:83], v[172:175]\n"
"s_waitcnt lgkmcnt(2)\n"
"v_mfma_f32_16x16x32_bf16 v[192:195], a[232:235], a[108:111], v[192:195]\n"
"ds_read_b64_tr_b16 a[248:249], v237 offset:32768\n"
"v_mfma_f32_16x16x32_bf16 v[192:195], a[236:239], a[124:127], v[192:195]\n"
"ds_read_b64_tr_b16 a[250:251], v237 offset:34816\n"
"v_mfma_f32_16x16x32_bf16 v[184:187], a[232:235], a[104:107], v[184:187]\n"
"v_mfma_f32_16x16x32_bf16 v[184:187], a[236:239], a[120:123], v[184:187]\n"
"v_mfma_f32_16x16x32_bf16 v[176:179], a[232:235], a[100:103], v[176:179]\n"
"ds_read_b64_tr_b16 a[252:253], v238 offset:32768\n"
"v_mfma_f32_16x16x32_bf16 v[176:179], a[236:239], a[116:119], v[176:179]\n"
"ds_read_b64_tr_b16 a[254:255], v238 offset:34816\n"
"v_mfma_f32_16x16x32_bf16 v[168:171], a[232:235], a[96:99], v[168:171]\n"
"buffer_load_dwordx4 v242, s[40:43], s61 offen offset:1024 sc0 sc1 nt lds\n"
"v_mfma_f32_16x16x32_bf16 v[168:171], a[236:239], a[112:115], v[168:171]\n"
"s_waitcnt lgkmcnt(4)\n"
"v_mfma_f32_16x16x32_bf16 v[172:175], a[240:243], a[96:99], v[172:175]\n"
"ds_read_b64_tr_b16 a[232:233], v237 offset:33280\n"
"v_mfma_f32_16x16x32_bf16 v[172:175], a[244:247], a[112:115], v[172:175]\n"
"ds_read_b64_tr_b16 a[234:235], v237 offset:35328\n"
"v_mfma_f32_16x16x32_bf16 v[180:183], a[240:243], a[100:103], v[180:183]\n"
"v_mfma_f32_16x16x32_bf16 v[180:183], a[244:247], a[116:119], v[180:183]\n"
"v_mfma_f32_16x16x32_bf16 v[188:191], a[240:243], a[104:107], v[188:191]\n"
"ds_read_b64_tr_b16 a[236:237], v238 offset:33280\n"
"v_mfma_f32_16x16x32_bf16 v[188:191], a[244:247], a[120:123], v[188:191]\n"
"ds_read_b64_tr_b16 a[238:239], v238 offset:35328\n"
"v_mfma_f32_16x16x32_bf16 v[196:199], a[240:243], a[108:111], v[196:199]\n"
"v_mfma_f32_16x16x32_bf16 v[196:199], a[244:247], a[124:127], v[196:199]\n"
"s_waitcnt lgkmcnt(4)\n"
"v_mfma_f32_16x16x32_bf16 v[0:3], a[248:251], v[200:203], v[0:3]\n"
"ds_read_b64_tr_b16 a[240:241], v237 offset:36864\n"
"v_mfma_f32_16x16x32_bf16 v[4:7], a[252:255], v[200:203], v[4:7]\n"
"ds_read_b64_tr_b16 a[242:243], v237 offset:38912\n"
"v_mfma_f32_16x16x32_bf16 v[8:11], a[248:251], v[208:211], v[8:11]\n"
"v_exp_f32 v168, v168\n"
"v_mfma_f32_16x16x32_bf16 v[12:15], a[252:255], v[208:211], v[12:15]\n"
"v_add_f32 v244, v244, v168\n"
"v_mfma_f32_16x16x32_bf16 v[16:19], a[248:251], v[216:219], v[16:19]\n"
"ds_read_b64_tr_b16 a[244:245], v238 offset:36864\n"
"v_mfma_f32_16x16x32_bf16 v[20:23], a[252:255], v[216:219], v[20:23]\n"
"ds_read_b64_tr_b16 a[246:247], v238 offset:38912\n"
"v_mfma_f32_16x16x32_bf16 v[24:27], a[248:251], v[224:227], v[24:27]\n"
"v_exp_f32 v169, v169\n"
"v_mfma_f32_16x16x32_bf16 v[28:31], a[252:255], v[224:227], v[28:31]\n"
"s_waitcnt lgkmcnt(4)\n"
"v_add_f32 v244, v244, v169\n"
"v_cvt_pk_bf16_f32 v168, v168, v169\n"
"v_mfma_f32_16x16x32_bf16 v[56:59], a[232:235], v[224:227], v[56:59]\n"
"ds_read_b64_tr_b16 a[248:249], v237 offset:37376\n"
"v_mfma_f32_16x16x32_bf16 v[60:63], a[236:239], v[224:227], v[60:63]\n"
"ds_read_b64_tr_b16 a[250:251], v237 offset:39424\n"
"v_mfma_f32_16x16x32_bf16 v[48:51], a[232:235], v[216:219], v[48:51]\n"
"buffer_load_dwordx4 v242, s[40:43], s62 offen offset:2048 sc0 sc1 nt lds\n"
"v_mfma_f32_16x16x32_bf16 v[52:55], a[236:239], v[216:219], v[52:55]\n"
"v_exp_f32 v170, v170\n"
"v_mfma_f32_16x16x32_bf16 v[40:43], a[232:235], v[208:211], v[40:43]\n"
"ds_read_b64_tr_b16 a[252:253], v238 offset:37376\n"
"v_add_f32 v244, v244, v170\n"
"v_mfma_f32_16x16x32_bf16 v[44:47], a[236:239], v[208:211], v[44:47]\n"
"ds_read_b64_tr_b16 a[254:255], v238 offset:39424\n"
"v_mfma_f32_16x16x32_bf16 v[32:35], a[232:235], v[200:203], v[32:35]\n"
"v_exp_f32 v171, v171\n"
"v_mfma_f32_16x16x32_bf16 v[36:39], a[236:239], v[200:203], v[36:39]\n"
"s_waitcnt lgkmcnt(4)\n"
"v_add_f32 v244, v244, v171\n"
"v_cvt_pk_bf16_f32 v169, v170, v171\n"
"v_mfma_f32_16x16x32_bf16 v[64:67], a[240:243], v[200:203], v[64:67]\n"
"ds_read_b64_tr_b16 a[232:233], v237 offset:40960\n"
"v_mfma_f32_16x16x32_bf16 v[68:71], a[244:247], v[200:203], v[68:71]\n"
"ds_read_b64_tr_b16 a[234:235], v237 offset:43008\n"
"v_mfma_f32_16x16x32_bf16 v[72:75], a[240:243], v[208:211], v[72:75]\n"
"v_exp_f32 v172, v172\n"
"v_mfma_f32_16x16x32_bf16 v[76:79], a[244:247], v[208:211], v[76:79]\n"
"v_add_f32 v244, v244, v172\n"
"v_mfma_f32_16x16x32_bf16 v[80:83], a[240:243], v[216:219], v[80:83]\n"
"ds_read_b64_tr_b16 a[236:237], v238 offset:40960\n"
"v_mfma_f32_16x16x32_bf16 v[84:87], a[244:247], v[216:219], v[84:87]\n"
"ds_read_b64_tr_b16 a[238:239], v238 offset:43008\n"
"v_mfma_f32_16x16x32_bf16 v[88:91], a[240:243], v[224:227], v[88:91]\n"
"v_exp_f32 v173, v173\n"
"v_mfma_f32_16x16x32_bf16 v[92:95], a[244:247], v[224:227], v[92:95]\n"
"s_waitcnt lgkmcnt(4)\n"
"v_add_f32 v244, v244, v173\n"
"v_cvt_pk_bf16_f32 v170, v172, v173\n"
"v_mfma_f32_16x16x32_bf16 v[120:123], a[248:251], v[224:227], v[120:123]\n"
"ds_read_b64_tr_b16 a[240:241], v237 offset:41472\n"
"v_mfma_f32_16x16x32_bf16 v[124:127], a[252:255], v[224:227], v[124:127]\n"
"ds_read_b64_tr_b16 a[242:243], v237 offset:43520\n"
"v_mfma_f32_16x16x32_bf16 v[112:115], a[248:251], v[216:219], v[112:115]\n"
"s_waitcnt vmcnt(23)\n"
"s_barrier\n"
"v_mfma_f32_16x16x32_bf16 v[116:119], a[252:255], v[216:219], v[116:119]\n"
"buffer_load_dwordx4 v242, s[40:43], s63 offen offset:3072 sc0 sc1 nt lds\n"
"v_mfma_f32_16x16x32_bf16 v[104:107], a[248:251], v[208:211], v[104:107]\n"
"ds_read_b64_tr_b16 a[244:245], v238 offset:41472\n"
"v_mfma_f32_16x16x32_bf16 v[108:111], a[252:255], v[208:211], v[108:111]\n"
"ds_read_b64_tr_b16 a[246:247], v238 offset:43520\n"
"v_mfma_f32_16x16x32_bf16 v[96:99], a[248:251], v[200:203], v[96:99]\n"
"v_exp_f32 v174, v174\n"
"v_mfma_f32_16x16x32_bf16 v[100:103], a[252:255], v[200:203], v[100:103]\n"
"s_waitcnt lgkmcnt(4)\n"
"v_add_f32 v244, v244, v174\n"
"v_mfma_f32_16x16x32_bf16 v[128:131], a[232:235], v[200:203], v[128:131]\n"
"ds_read_b64_tr_b16 a[248:249], v237 offset:45056\n"
"v_mfma_f32_16x16x32_bf16 v[132:135], a[236:239], v[200:203], v[132:135]\n"
"ds_read_b64_tr_b16 a[250:251], v237 offset:47104\n"
"v_mfma_f32_16x16x32_bf16 v[136:139], a[232:235], v[208:211], v[136:139]\n"
"v_exp_f32 v175, v175\n"
"v_mfma_f32_16x16x32_bf16 v[140:143], a[236:239], v[208:211], v[140:143]\n"
"v_add_f32 v244, v244, v175\n"
"v_cvt_pk_bf16_f32 v171, v174, v175\n"
"v_mfma_f32_16x16x32_bf16 v[144:147], a[232:235], v[216:219], v[144:147]\n"
"ds_read_b64_tr_b16 a[252:253], v238 offset:45056\n"
"v_mfma_f32_16x16x32_bf16 v[148:151], a[236:239], v[216:219], v[148:151]\n"
"ds_read_b64_tr_b16 a[254:255], v238 offset:47104\n"
"v_mfma_f32_16x16x32_bf16 a[128:131], a[232:235], v[224:227], a[128:131]\n"
"v_exp_f32 v176, v176\n"
"v_mfma_f32_16x16x32_bf16 a[132:135], a[236:239], v[224:227], a[132:135]\n"
"s_waitcnt lgkmcnt(4)\n"
"v_add_f32 v245, v245, v176\n"
"v_mfma_f32_16x16x32_bf16 a[160:163], a[240:243], v[224:227], a[160:163]\n"
"ds_read_b64_tr_b16 a[232:233], v237 offset:45568\n"
"v_mfma_f32_16x16x32_bf16 a[164:167], a[244:247], v[224:227], a[164:167]\n"
"ds_read_b64_tr_b16 a[234:235], v237 offset:47616\n"
"v_mfma_f32_16x16x32_bf16 a[152:155], a[240:243], v[216:219], a[152:155]\n"
"v_exp_f32 v177, v177\n"
"v_mfma_f32_16x16x32_bf16 a[156:159], a[244:247], v[216:219], a[156:159]\n"
"v_add_f32 v245, v245, v177\n"
"v_cvt_pk_bf16_f32 v176, v176, v177\n"
"v_mfma_f32_16x16x32_bf16 a[144:147], a[240:243], v[208:211], a[144:147]\n"
"ds_read_b64_tr_b16 a[236:237], v238 offset:45568\n"
"v_mfma_f32_16x16x32_bf16 a[148:151], a[244:247], v[208:211], a[148:151]\n"
"ds_read_b64_tr_b16 a[238:239], v238 offset:47616\n"
"v_add_u32 v242, 114688, v241\n"
"s_add_u32 m0, s71, 65536\n"
"v_mfma_f32_16x16x32_bf16 a[136:139], a[240:243], v[200:203], a[136:139]\n"
"buffer_load_dwordx4 v242, s[44:47], 0 offen offset:0 sc0 sc1 nt lds\n"
"v_mfma_f32_16x16x32_bf16 a[140:143], a[244:247], v[200:203], a[140:143]\n"
"s_waitcnt lgkmcnt(4)\n"
"v_exp_f32 v178, v178\n"
"v_mfma_f32_16x16x32_bf16 a[168:171], a[248:251], v[200:203], a[168:171]\n"
"ds_read_b128 a[240:243], v233 offset:49152\n"
"v_add_f32 v245, v245, v178\n"
"v_mfma_f32_16x16x32_bf16 a[172:175], a[252:255], v[200:203], a[172:175]\n"
"v_exp_f32 v179, v179\n"
"v_mfma_f32_16x16x32_bf16 a[176:179], a[248:251], v[208:211], a[176:179]\n"
"v_add_f32 v245, v245, v179\n"
"v_cvt_pk_bf16_f32 v177, v178, v179\n"
"v_mfma_f32_16x16x32_bf16 a[180:183], a[252:255], v[208:211], a[180:183]\n"
"v_exp_f32 v180, v180\n"
"v_mfma_f32_16x16x32_bf16 a[184:187], a[248:251], v[216:219], a[184:187]\n"
"ds_read_b128 a[244:247], v233 offset:49664\n"
"v_add_f32 v245, v245, v180\n"
"v_mfma_f32_16x16x32_bf16 a[188:191], a[252:255], v[216:219], a[188:191]\n"
"v_exp_f32 v181, v181\n"
"v_mfma_f32_16x16x32_bf16 a[192:195], a[248:251], v[224:227], a[192:195]\n"
"v_add_f32 v245, v245, v181\n"
"v_cvt_pk_bf16_f32 v178, v180, v181\n"
"v_mfma_f32_16x16x32_bf16 a[196:199], a[252:255], v[224:227], a[196:199]\n"
"s_waitcnt lgkmcnt(2)\n"
"v_exp_f32 v182, v182\n"
"v_mfma_f32_16x16x32_bf16 a[200:203], a[232:235], v[200:203], a[200:203]\n"
"ds_read_b128 a[248:251], v233 offset:51200\n"
"v_add_f32 v245, v245, v182\n"
"v_mfma_f32_16x16x32_bf16 a[204:207], a[236:239], v[200:203], a[204:207]\n"
"v_exp_f32 v183, v183\n"
"v_mfma_f32_16x16x32_bf16 a[208:211], a[232:235], v[208:211], a[208:211]\n"
"v_add_f32 v245, v245, v183\n"
"v_cvt_pk_bf16_f32 v179, v182, v183\n"
"v_mfma_f32_16x16x32_bf16 a[212:215], a[236:239], v[208:211], a[212:215]\n"
"v_exp_f32 v184, v184\n"
"v_mfma_f32_16x16x32_bf16 a[216:219], a[232:235], v[216:219], a[216:219]\n"
"ds_read_b128 a[252:255], v233 offset:51712\n"
"v_add_f32 v246, v246, v184\n"
"v_mfma_f32_16x16x32_bf16 a[220:223], a[236:239], v[216:219], a[220:223]\n"
"v_exp_f32 v185, v185\n"
"v_mfma_f32_16x16x32_bf16 a[224:227], a[232:235], v[224:227], a[224:227]\n"
"buffer_load_dwordx4 v242, s[44:47], s61 offen offset:1024 sc0 sc1 nt lds\n"
"v_mfma_f32_16x16x32_bf16 a[228:231], a[236:239], v[224:227], a[228:231]\n"
"s_waitcnt lgkmcnt(2)\n"
"v_add_f32 v246, v246, v185\n"
"v_cvt_pk_bf16_f32 v184, v184, v185\n"
"v_mfma_f32_16x16x32_bf16 v[200:203], a[240:243], a[0:3], v[152:155]\n"
"ds_read_b128 a[232:235], v233 offset:53248\n"
"v_mfma_f32_16x16x32_bf16 v[200:203], a[244:247], a[16:19], v[200:203]\n"
"v_exp_f32 v186, v186\n"
"v_mfma_f32_16x16x32_bf16 v[208:211], a[240:243], a[4:7], v[156:159]\n"
"v_add_f32 v246, v246, v186\n"
"v_mfma_f32_16x16x32_bf16 v[208:211], a[244:247], a[20:23], v[208:211]\n"
"v_exp_f32 v187, v187\n"
"v_mfma_f32_16x16x32_bf16 v[216:219], a[240:243], a[8:11], v[160:163]\n"
"ds_read_b128 a[236:239], v233 offset:53760\n"
"v_add_f32 v246, v246, v187\n"
"v_mfma_f32_16x16x32_bf16 v[216:219], a[244:247], a[24:27], v[216:219]\n"
"v_cvt_pk_bf16_f32 v185, v186, v187\n"
"v_mfma_f32_16x16x32_bf16 v[224:227], a[240:243], a[12:15], v[164:167]\n"
"v_exp_f32 v188, v188\n"
"v_mfma_f32_16x16x32_bf16 v[224:227], a[244:247], a[28:31], v[224:227]\n"
"s_waitcnt lgkmcnt(2)\n"
"v_add_f32 v246, v246, v188\n"
"v_mfma_f32_16x16x32_bf16 v[228:231], a[248:251], a[12:15], v[164:167]\n"
"ds_read_b128 a[240:243], v233 offset:55296\n"
"v_mfma_f32_16x16x32_bf16 v[228:231], a[252:255], a[28:31], v[228:231]\n"
"v_exp_f32 v189, v189\n"
"v_mfma_f32_16x16x32_bf16 v[220:223], a[248:251], a[8:11], v[160:163]\n"
"v_add_f32 v246, v246, v189\n"
"v_cvt_pk_bf16_f32 v186, v188, v189\n"
"v_mfma_f32_16x16x32_bf16 v[220:223], a[252:255], a[24:27], v[220:223]\n"
"v_exp_f32 v190, v190\n"
"v_mfma_f32_16x16x32_bf16 v[212:215], a[248:251], a[4:7], v[156:159]\n"
"ds_read_b128 a[244:247], v233 offset:55808\n"
"v_mfma_f32_16x16x32_bf16 v[212:215], a[252:255], a[20:23], v[212:215]\n"
"v_add_f32 v246, v246, v190\n"
"v_mfma_f32_16x16x32_bf16 v[204:207], a[248:251], a[0:3], v[152:155]\n"
"v_exp_f32 v191, v191\n"
"v_mfma_f32_16x16x32_bf16 v[204:207], a[252:255], a[16:19], v[204:207]\n"
"s_waitcnt lgkmcnt(2)\n"
"v_add_f32 v246, v246, v191\n"
"v_mfma_f32_16x16x32_bf16 v[200:203], a[232:235], a[32:35], v[200:203]\n"
"ds_read_b128 a[248:251], v233 offset:57344\n"
"v_cvt_pk_bf16_f32 v187, v190, v191\n"
"v_mfma_f32_16x16x32_bf16 v[200:203], a[236:239], a[48:51], v[200:203]\n"
"buffer_load_dwordx4 v242, s[44:47], s62 offen offset:2048 sc0 sc1 nt lds\n"
"v_mfma_f32_16x16x32_bf16 v[208:211], a[232:235], a[36:39], v[208:211]\n"
"v_exp_f32 v192, v192\n"
"v_mfma_f32_16x16x32_bf16 v[208:211], a[236:239], a[52:55], v[208:211]\n"
"v_add_f32 v247, v247, v192\n"
"v_mfma_f32_16x16x32_bf16 v[216:219], a[232:235], a[40:43], v[216:219]\n"
"ds_read_b128 a[252:255], v233 offset:57856\n"
"v_mfma_f32_16x16x32_bf16 v[216:219], a[236:239], a[56:59], v[216:219]\n"
"v_exp_f32 v193, v193\n"
"v_mfma_f32_16x16x32_bf16 v[224:227], a[232:235], a[44:47], v[224:227]\n"
"v_add_f32 v247, v247, v193\n"
"v_mfma_f32_16x16x32_bf16 v[224:227], a[236:239], a[60:63], v[224:227]\n"
"s_waitcnt lgkmcnt(2)\n"
"v_cvt_pk_bf16_f32 v192, v192, v193\n"
"v_mfma_f32_16x16x32_bf16 v[228:231], a[240:243], a[44:47], v[228:231]\n"
"ds_read_b128 a[232:235], v233 offset:59392\n"
"v_mfma_f32_16x16x32_bf16 v[228:231], a[244:247], a[60:63], v[228:231]\n"
"v_exp_f32 v194, v194\n"
"v_mfma_f32_16x16x32_bf16 v[220:223], a[240:243], a[40:43], v[220:223]\n"
"s_waitcnt vmcnt(23)\n"
"s_barrier\n"
"v_add_f32 v247, v247, v194\n"
"v_mfma_f32_16x16x32_bf16 v[220:223], a[244:247], a[56:59], v[220:223]\n"
"v_exp_f32 v195, v195\n"
"v_mfma_f32_16x16x32_bf16 v[212:215], a[240:243], a[36:39], v[212:215]\n"
"ds_read_b128 a[236:239], v233 offset:59904\n"
"v_mfma_f32_16x16x32_bf16 v[212:215], a[244:247], a[52:55], v[212:215]\n"
"v_add_f32 v247, v247, v195\n"
"v_cvt_pk_bf16_f32 v193, v194, v195\n"
"v_mfma_f32_16x16x32_bf16 v[204:207], a[240:243], a[32:35], v[204:207]\n"
"v_exp_f32 v196, v196\n"
"v_mfma_f32_16x16x32_bf16 v[204:207], a[244:247], a[48:51], v[204:207]\n"
"s_waitcnt lgkmcnt(2)\n"
"v_mfma_f32_16x16x32_bf16 v[200:203], a[248:251], a[64:67], v[200:203]\n"
"ds_read_b128 a[240:243], v233 offset:61440\n"
"v_add_f32 v247, v247, v196\n"
"v_mfma_f32_16x16x32_bf16 v[200:203], a[252:255], a[80:83], v[200:203]\n"
"v_exp_f32 v197, v197\n"
"v_mfma_f32_16x16x32_bf16 v[208:211], a[248:251], a[68:71], v[208:211]\n"
"buffer_load_dwordx4 v242, s[44:47], s63 offen offset:3072 sc0 sc1 nt lds\n"
"v_mfma_f32_16x16x32_bf16 v[208:211], a[252:255], a[84:87], v[208:211]\n"
"v_add_f32 v247, v247, v197\n"
"v_mfma_f32_16x16x32_bf16 v[216:219], a[248:251], a[72:75], v[216:219]\n"
"ds_read_b128 a[244:247], v233 offset:61952\n"
"v_mfma_f32_16x16x32_bf16 v[216:219], a[252:255], a[88:91], v[216:219]\n"
"v_cvt_pk_bf16_f32 v194, v196, v197\n"
"v_mfma_f32_16x16x32_bf16 v[224:227], a[248:251], a[76:79], v[224:227]\n"
"v_exp_f32 v198, v198\n"
"v_mfma_f32_16x16x32_bf16 v[224:227], a[252:255], a[92:95], v[224:227]\n"
"s_waitcnt lgkmcnt(2)\n"
"v_add_f32 v247, v247, v198\n"
"v_mfma_f32_16x16x32_bf16 v[228:231], a[232:235], a[76:79], v[228:231]\n"
"ds_read_b128 a[248:251], v233 offset:63488\n"
"v_mfma_f32_16x16x32_bf16 v[228:231], a[236:239], a[92:95], v[228:231]\n"
"v_exp_f32 v199, v199\n"
"v_mfma_f32_16x16x32_bf16 v[220:223], a[232:235], a[72:75], v[220:223]\n"
"v_add_f32 v247, v247, v199\n"
"v_cvt_pk_bf16_f32 v195, v198, v199\n"
"v_mfma_f32_16x16x32_bf16 v[220:223], a[236:239], a[88:91], v[220:223]\n"
"v_mfma_f32_16x16x32_bf16 v[212:215], a[232:235], a[68:71], v[212:215]\n"
"ds_read_b128 a[252:255], v233 offset:64000\n"
"v_mfma_f32_16x16x32_bf16 v[212:215], a[236:239], a[84:87], v[212:215]\n"
"v_mfma_f32_16x16x32_bf16 v[204:207], a[232:235], a[64:67], v[204:207]\n"
"v_mfma_f32_16x16x32_bf16 v[204:207], a[236:239], a[80:83], v[204:207]\n"
"s_waitcnt lgkmcnt(2)\n"
"v_mfma_f32_16x16x32_bf16 v[224:227], a[240:243], a[108:111], v[224:227]\n"
"ds_read_b64_tr_b16 a[232:233], v239 offset:0\n"
"v_mfma_f32_16x16x32_bf16 v[224:227], a[244:247], a[124:127], v[224:227]\n"
"ds_read_b64_tr_b16 a[234:235], v239 offset:2048\n"
"v_mfma_f32_16x16x32_bf16 v[216:219], a[240:243], a[104:107], v[216:219]\n"
"v_mfma_f32_16x16x32_bf16 v[216:219], a[244:247], a[120:123], v[216:219]\n"
"v_mfma_f32_16x16x32_bf16 v[208:211], a[240:243], a[100:103], v[208:211]\n"
"ds_read_b64_tr_b16 a[236:237], v240 offset:0\n"
"v_mfma_f32_16x16x32_bf16 v[208:211], a[244:247], a[116:119], v[208:211]\n"
"ds_read_b64_tr_b16 a[238:239], v240 offset:2048\n"
"v_add_u32 v242, 147456, v241\n"
"s_add_u32 m0, s71, 81920\n"
"v_mfma_f32_16x16x32_bf16 v[200:203], a[240:243], a[96:99], v[200:203]\n"
"buffer_load_dwordx4 v242, s[40:43], 0 offen offset:0 sc0 sc1 nt lds\n"
"v_mfma_f32_16x16x32_bf16 v[200:203], a[244:247], a[112:115], v[200:203]\n"
"s_waitcnt lgkmcnt(4)\n"
"v_mfma_f32_16x16x32_bf16 v[204:207], a[248:251], a[96:99], v[204:207]\n"
"ds_read_b64_tr_b16 a[240:241], v239 offset:512\n"
"v_mfma_f32_16x16x32_bf16 v[204:207], a[252:255], a[112:115], v[204:207]\n"
"ds_read_b64_tr_b16 a[242:243], v239 offset:2560\n"
"v_mfma_f32_16x16x32_bf16 v[212:215], a[248:251], a[100:103], v[212:215]\n"
"v_mfma_f32_16x16x32_bf16 v[212:215], a[252:255], a[116:119], v[212:215]\n"
"v_mfma_f32_16x16x32_bf16 v[220:223], a[248:251], a[104:107], v[220:223]\n"
"ds_read_b64_tr_b16 a[244:245], v240 offset:512\n"
"v_mfma_f32_16x16x32_bf16 v[220:223], a[252:255], a[120:123], v[220:223]\n"
"ds_read_b64_tr_b16 a[246:247], v240 offset:2560\n"
"v_mfma_f32_16x16x32_bf16 v[228:231], a[248:251], a[108:111], v[228:231]\n"
"v_mfma_f32_16x16x32_bf16 v[228:231], a[252:255], a[124:127], v[228:231]\n"
"s_waitcnt lgkmcnt(4)\n"
"v_mfma_f32_16x16x32_bf16 v[0:3], a[232:235], v[168:171], v[0:3]\n"
"ds_read_b64_tr_b16 a[248:249], v239 offset:4096\n"
"v_mfma_f32_16x16x32_bf16 v[4:7], a[236:239], v[168:171], v[4:7]\n"
"ds_read_b64_tr_b16 a[250:251], v239 offset:6144\n"
"v_mfma_f32_16x16x32_bf16 v[8:11], a[232:235], v[176:179], v[8:11]\n"
"v_exp_f32 v200, v200\n"
"v_mfma_f32_16x16x32_bf16 v[12:15], a[236:239], v[176:179], v[12:15]\n"
"v_add_f32 v244, v244, v200\n"
"v_mfma_f32_16x16x32_bf16 v[16:19], a[232:235], v[184:187], v[16:19]\n"
"ds_read_b64_tr_b16 a[252:253], v240 offset:4096\n"
"v_mfma_f32_16x16x32_bf16 v[20:23], a[236:239], v[184:187], v[20:23]\n"
"ds_read_b64_tr_b16 a[254:255], v240 offset:6144\n"
"v_mfma_f32_16x16x32_bf16 v[24:27], a[232:235], v[192:195], v[24:27]\n"
"buffer_load_dwordx4 v242, s[40:43], s61 offen offset:1024 sc0 sc1 nt lds\n"
"v_mfma_f32_16x16x32_bf16 v[28:31], a[236:239], v[192:195], v[28:31]\n"
"s_waitcnt lgkmcnt(4)\n"
"v_exp_f32 v201, v201\n"
"v_mfma_f32_16x16x32_bf16 v[56:59], a[240:243], v[192:195], v[56:59]\n"
"ds_read_b64_tr_b16 a[232:233], v239 offset:4608\n"
"v_add_f32 v244, v244, v201\n"
"v_mfma_f32_16x16x32_bf16 v[60:63], a[244:247], v[192:195], v[60:63]\n"
"ds_read_b64_tr_b16 a[234:235], v239 offset:6656\n"
"v_cvt_pk_bf16_f32 v200, v200, v201\n"
"v_mfma_f32_16x16x32_bf16 v[48:51], a[240:243], v[184:187], v[48:51]\n"
"v_exp_f32 v202, v202\n"
"v_mfma_f32_16x16x32_bf16 v[52:55], a[244:247], v[184:187], v[52:55]\n"
"v_add_f32 v244, v244, v202\n"
"v_mfma_f32_16x16x32_bf16 v[40:43], a[240:243], v[176:179], v[40:43]\n"
"ds_read_b64_tr_b16 a[236:237], v240 offset:4608\n"
"v_mfma_f32_16x16x32_bf16 v[44:47], a[244:247], v[176:179], v[44:47]\n"
"ds_read_b64_tr_b16 a[238:239], v240 offset:6656\n"
"v_mfma_f32_16x16x32_bf16 v[32:35], a[240:243], v[168:171], v[32:35]\n"
"v_exp_f32 v203, v203\n"
"v_mfma_f32_16x16x32_bf16 v[36:39], a[244:247], v[168:171], v[36:39]\n"
"s_waitcnt lgkmcnt(4)\n"
"v_add_f32 v244, v244, v203\n"
"v_cvt_pk_bf16_f32 v201, v202, v203\n"
"v_mfma_f32_16x16x32_bf16 v[64:67], a[248:251], v[168:171], v[64:67]\n"
"ds_read_b64_tr_b16 a[240:241], v239 offset:8192\n"
"v_mfma_f32_16x16x32_bf16 v[68:71], a[252:255], v[168:171], v[68:71]\n"
"ds_read_b64_tr_b16 a[242:243], v239 offset:10240\n"
"v_mfma_f32_16x16x32_bf16 v[72:75], a[248:251], v[176:179], v[72:75]\n"
"v_exp_f32 v204, v204\n"
"v_mfma_f32_16x16x32_bf16 v[76:79], a[252:255], v[176:179], v[76:79]\n"
"v_add_f32 v244, v244, v204\n"
"v_mfma_f32_16x16x32_bf16 v[80:83], a[248:251], v[184:187], v[80:83]\n"
"ds_read_b64_tr_b16 a[244:245], v240 offset:8192\n"
"v_mfma_f32_16x16x32_bf16 v[84:87], a[252:255], v[184:187], v[84:87]\n"
"ds_read_b64_tr_b16 a[246:247], v240 offset:10240\n"
"v_mfma_f32_16x16x32_bf16 v[88:91], a[248:251], v[192:195], v[88:91]\n"
"v_exp_f32 v205, v205\n"
"v_mfma_f32_16x16x32_bf16 v[92:95], a[252:255], v[192:195], v[92:95]\n"
"s_waitcnt lgkmcnt(4)\n"
"v_add_f32 v244, v244, v205\n"
"v_cvt_pk_bf16_f32 v202, v204, v205\n"
"v_mfma_f32_16x16x32_bf16 v[120:123], a[232:235], v[192:195], v[120:123]\n"
"ds_read_b64_tr_b16 a[248:249], v239 offset:8704\n"
"v_mfma_f32_16x16x32_bf16 v[124:127], a[236:239], v[192:195], v[124:127]\n"
"ds_read_b64_tr_b16 a[250:251], v239 offset:10752\n"
"v_mfma_f32_16x16x32_bf16 v[112:115], a[232:235], v[184:187], v[112:115]\n"
"s_waitcnt vmcnt(22)\n"
"s_barrier\n"
"v_mfma_f32_16x16x32_bf16 v[116:119], a[236:239], v[184:187], v[116:119]\n"
"buffer_load_dwordx4 v242, s[40:43], s62 offen offset:2048 sc0 sc1 nt lds\n"
"v_mfma_f32_16x16x32_bf16 v[104:107], a[232:235], v[176:179], v[104:107]\n"
"ds_read_b64_tr_b16 a[252:253], v240 offset:8704\n"
"v_mfma_f32_16x16x32_bf16 v[108:111], a[236:239], v[176:179], v[108:111]\n"
"ds_read_b64_tr_b16 a[254:255], v240 offset:10752\n"
"v_mfma_f32_16x16x32_bf16 v[96:99], a[232:235], v[168:171], v[96:99]\n"
"v_exp_f32 v206, v206\n"
"v_mfma_f32_16x16x32_bf16 v[100:103], a[236:239], v[168:171], v[100:103]\n"
"s_waitcnt lgkmcnt(4)\n"
"v_add_f32 v244, v244, v206\n"
"v_mfma_f32_16x16x32_bf16 v[128:131], a[240:243], v[168:171], v[128:131]\n"
"ds_read_b64_tr_b16 a[232:233], v239 offset:12288\n"
"v_mfma_f32_16x16x32_bf16 v[132:135], a[244:247], v[168:171], v[132:135]\n"
"ds_read_b64_tr_b16 a[234:235], v239 offset:14336\n"
"v_mfma_f32_16x16x32_bf16 v[136:139], a[240:243], v[176:179], v[136:139]\n"
"v_exp_f32 v207, v207\n"
"v_mfma_f32_16x16x32_bf16 v[140:143], a[244:247], v[176:179], v[140:143]\n"
"v_add_f32 v244, v244, v207\n"
"v_cvt_pk_bf16_f32 v203, v206, v207\n"
"v_mfma_f32_16x16x32_bf16 v[144:147], a[240:243], v[184:187], v[144:147]\n"
"ds_read_b64_tr_b16 a[236:237], v240 offset:12288\n"
"v_mfma_f32_16x16x32_bf16 v[148:151], a[244:247], v[184:187], v[148:151]\n"
"ds_read_b64_tr_b16 a[238:239], v240 offset:14336\n"
"v_mfma_f32_16x16x32_bf16 a[128:131], a[240:243], v[192:195], a[128:131]\n"
"v_exp_f32 v208, v208\n"
"v_mfma_f32_16x16x32_bf16 a[132:135], a[244:247], v[192:195], a[132:135]\n"
"s_waitcnt lgkmcnt(4)\n"
"v_add_f32 v245, v245, v208\n"
"v_mfma_f32_16x16x32_bf16 a[160:163], a[248:251], v[192:195], a[160:163]\n"
"ds_read_b64_tr_b16 a[240:241], v239 offset:12800\n"
"v_mfma_f32_16x16x32_bf16 a[164:167], a[252:255], v[192:195], a[164:167]\n"
"ds_read_b64_tr_b16 a[242:243], v239 offset:14848\n"
"v_mfma_f32_16x16x32_bf16 a[152:155], a[248:251], v[184:187], a[152:155]\n"
"buffer_load_dwordx4 v242, s[40:43], s63 offen offset:3072 sc0 sc1 nt lds\n"
"v_mfma_f32_16x16x32_bf16 a[156:159], a[252:255], v[184:187], a[156:159]\n"
"v_exp_f32 v209, v209\n"
"v_mfma_f32_16x16x32_bf16 a[144:147], a[248:251], v[176:179], a[144:147]\n"
"ds_read_b64_tr_b16 a[244:245], v240 offset:12800\n"
"v_add_f32 v245, v245, v209\n"
"v_mfma_f32_16x16x32_bf16 a[148:151], a[252:255], v[176:179], a[148:151]\n"
"ds_read_b64_tr_b16 a[246:247], v240 offset:14848\n"
"v_cvt_pk_bf16_f32 v208, v208, v209\n"
"v_mfma_f32_16x16x32_bf16 a[136:139], a[248:251], v[168:171], a[136:139]\n"
"v_exp_f32 v210, v210\n"
"v_mfma_f32_16x16x32_bf16 a[140:143], a[252:255], v[168:171], a[140:143]\n"
"s_waitcnt lgkmcnt(4)\n"
"v_add_f32 v245, v245, v210\n"
"v_mfma_f32_16x16x32_bf16 a[168:171], a[232:235], v[168:171], a[168:171]\n"
"ds_read_b128 a[248:251], v234 offset:16384\n"
"v_mfma_f32_16x16x32_bf16 a[172:175], a[236:239], v[168:171], a[172:175]\n"
"v_exp_f32 v211, v211\n"
"v_mfma_f32_16x16x32_bf16 a[176:179], a[232:235], v[176:179], a[176:179]\n"
"v_add_f32 v245, v245, v211\n"
"v_cvt_pk_bf16_f32 v209, v210, v211\n"
"v_mfma_f32_16x16x32_bf16 a[180:183], a[236:239], v[176:179], a[180:183]\n"
"v_exp_f32 v212, v212\n"
"v_mfma_f32_16x16x32_bf16 a[184:187], a[232:235], v[184:187], a[184:187]\n"
"ds_read_b128 a[252:255], v234 offset:16896\n"
"v_add_f32 v245, v245, v212\n"
"v_mfma_f32_16x16x32_bf16 a[188:191], a[236:239], v[184:187], a[188:191]\n"
"v_exp_f32 v213, v213\n"
"v_mfma_f32_16x16x32_bf16 a[192:195], a[232:235], v[192:195], a[192:195]\n"
"v_add_f32 v245, v245, v213\n"
"v_cvt_pk_bf16_f32 v210, v212, v213\n"
"v_mfma_f32_16x16x32_bf16 a[196:199], a[236:239], v[192:195], a[196:199]\n"
"s_waitcnt lgkmcnt(2)\n"
"v_exp_f32 v214, v214\n"
"v_mfma_f32_16x16x32_bf16 a[200:203], a[240:243], v[168:171], a[200:203]\n"
"ds_read_b128 a[232:235], v234 offset:18432\n"
"v_add_f32 v245, v245, v214\n"
"v_mfma_f32_16x16x32_bf16 a[204:207], a[244:247], v[168:171], a[204:207]\n"
"v_exp_f32 v215, v215\n"
"v_mfma_f32_16x16x32_bf16 a[208:211], a[240:243], v[176:179], a[208:211]\n"
"v_add_f32 v245, v245, v215\n"
"v_cvt_pk_bf16_f32 v211, v214, v215\n"
"v_mfma_f32_16x16x32_bf16 a[212:215], a[244:247], v[176:179], a[212:215]\n"
"v_exp_f32 v216, v216\n"
"v_mfma_f32_16x16x32_bf16 a[216:219], a[240:243], v[184:187], a[216:219]\n"
"ds_read_b128 a[236:239], v234 offset:18944\n"
"v_add_u32 v242, 131072, v241\n"
"s_add_u32 m0, s71, 98304\n"
"v_mfma_f32_16x16x32_bf16 a[220:223], a[244:247], v[184:187], a[220:223]\n"
"buffer_load_dwordx4 v242, s[44:47], 0 offen offset:0 sc0 sc1 nt lds\n"
"v_mfma_f32_16x16x32_bf16 a[224:227], a[240:243], v[192:195], a[224:227]\n"
"v_add_f32 v246, v246, v216\n"
"v_mfma_f32_16x16x32_bf16 a[228:231], a[244:247], v[192:195], a[228:231]\n"
"s_waitcnt lgkmcnt(2)\n"
"v_exp_f32 v217, v217\n"
"v_mfma_f32_16x16x32_bf16 v[168:171], a[248:251], a[0:3], v[152:155]\n"
"ds_read_b128 a[240:243], v234 offset:20480\n"
"v_add_f32 v246, v246, v217\n"
"v_mfma_f32_16x16x32_bf16 v[168:171], a[252:255], a[16:19], v[168:171]\n"
"v_cvt_pk_bf16_f32 v216, v216, v217\n"
"v_mfma_f32_16x16x32_bf16 v[176:179], a[248:251], a[4:7], v[156:159]\n"
"v_exp_f32 v218, v218\n"
"v_mfma_f32_16x16x32_bf16 v[176:179], a[252:255], a[20:23], v[176:179]\n"
"v_add_f32 v246, v246, v218\n"
"v_mfma_f32_16x16x32_bf16 v[184:187], a[248:251], a[8:11], v[160:163]\n"
"ds_read_b128 a[244:247], v234 offset:20992\n"
"v_mfma_f32_16x16x32_bf16 v[184:187], a[252:255], a[24:27], v[184:187]\n"
"v_exp_f32 v219, v219\n"
"v_mfma_f32_16x16x32_bf16 v[192:195], a[248:251], a[12:15], v[164:167]\n"
"v_add_f32 v246, v246, v219\n"
"v_cvt_pk_bf16_f32 v217, v218, v219\n"
"v_mfma_f32_16x16x32_bf16 v[192:195], a[252:255], a[28:31], v[192:195]\n"
"s_waitcnt lgkmcnt(2)\n"
"v_exp_f32 v220, v220\n"
"v_mfma_f32_16x16x32_bf16 v[196:199], a[232:235], a[12:15], v[164:167]\n"
"ds_read_b128 a[248:251], v234 offset:22528\n"
"v_add_f32 v246, v246, v220\n"
"v_mfma_f32_16x16x32_bf16 v[196:199], a[236:239], a[28:31], v[196:199]\n"
"v_exp_f32 v221, v221\n"
"v_mfma_f32_16x16x32_bf16 v[188:191], a[232:235], a[8:11], v[160:163]\n"
"v_add_f32 v246, v246, v221\n"
"v_mfma_f32_16x16x32_bf16 v[188:191], a[236:239], a[24:27], v[188:191]\n"
"v_cvt_pk_bf16_f32 v218, v220, v221\n"
"v_mfma_f32_16x16x32_bf16 v[180:183], a[232:235], a[4:7], v[156:159]\n"
"ds_read_b128 a[252:255], v234 offset:23040\n"
"v_mfma_f32_16x16x32_bf16 v[180:183], a[236:239], a[20:23], v[180:183]\n"
"v_exp_f32 v222, v222\n"
"v_mfma_f32_16x16x32_bf16 v[172:175], a[232:235], a[0:3], v[152:155]\n"
"buffer_load_dwordx4 v242, s[44:47], s61 offen offset:1024 sc0 sc1 nt lds\n"
"v_mfma_f32_16x16x32_bf16 v[172:175], a[236:239], a[16:19], v[172:175]\n"
"s_waitcnt lgkmcnt(2)\n"
"v_add_f32 v246, v246, v222\n"
"v_mfma_f32_16x16x32_bf16 v[168:171], a[240:243], a[32:35], v[168:171]\n"
"ds_read_b128 a[232:235], v234 offset:24576\n"
"v_mfma_f32_16x16x32_bf16 v[168:171], a[244:247], a[48:51], v[168:171]\n"
"v_exp_f32 v223, v223\n"
"v_mfma_f32_16x16x32_bf16 v[176:179], a[240:243], a[36:39], v[176:179]\n"
"v_add_f32 v246, v246, v223\n"
"v_cvt_pk_bf16_f32 v219, v222, v223\n"
"v_mfma_f32_16x16x32_bf16 v[176:179], a[244:247], a[52:55], v[176:179]\n"
"v_exp_f32 v224, v224\n"
"v_mfma_f32_16x16x32_bf16 v[184:187], a[240:243], a[40:43], v[184:187]\n"
"ds_read_b128 a[236:239], v234 offset:25088\n"
"v_add_f32 v247, v247, v224\n"
"v_mfma_f32_16x16x32_bf16 v[184:187], a[244:247], a[56:59], v[184:187]\n"
"v_exp_f32 v225, v225\n"
"v_mfma_f32_16x16x32_bf16 v[192:195], a[240:243], a[44:47], v[192:195]\n"
"v_add_f32 v247, v247, v225\n"
"v_mfma_f32_16x16x32_bf16 v[192:195], a[244:247], a[60:63], v[192:195]\n"
"s_waitcnt lgkmcnt(2)\n"
"v_cvt_pk_bf16_f32 v224, v224, v225\n"
"v_mfma_f32_16x16x32_bf16 v[196:199], a[248:251], a[44:47], v[196:199]\n"
"ds_read_b128 a[240:243], v234 offset:26624\n"
"v_mfma_f32_16x16x32_bf16 v[196:199], a[252:255], a[60:63], v[196:199]\n"
"v_exp_f32 v226, v226\n"
"v_mfma_f32_16x16x32_bf16 v[188:191], a[248:251], a[40:43], v[188:191]\n"
"s_waitcnt vmcnt(22)\n"
"s_barrier\n"
"v_add_f32 v247, v247, v226\n"
"v_mfma_f32_16x16x32_bf16 v[188:191], a[252:255], a[56:59], v[188:191]\n"
"v_exp_f32 v227, v227\n"
"v_mfma_f32_16x16x32_bf16 v[180:183], a[248:251], a[36:39], v[180:183]\n"
"ds_read_b128 a[244:247], v234 offset:27136\n"
"v_mfma_f32_16x16x32_bf16 v[180:183], a[252:255], a[52:55], v[180:183]\n"
"v_add_f32 v247, v247, v227\n"
"v_mfma_f32_16x16x32_bf16 v[172:175], a[248:251], a[32:35], v[172:175]\n"
"v_cvt_pk_bf16_f32 v225, v226, v227\n"
"v_mfma_f32_16x16x32_bf16 v[172:175], a[252:255], a[48:51], v[172:175]\n"
"s_waitcnt lgkmcnt(2)\n"
"v_exp_f32 v228, v228\n"
"v_mfma_f32_16x16x32_bf16 v[168:171], a[232:235], a[64:67], v[168:171]\n"
"ds_read_b128 a[248:251], v234 offset:28672\n"
"v_mfma_f32_16x16x32_bf16 v[168:171], a[236:239], a[80:83], v[168:171]\n"
"buffer_load_dwordx4 v242, s[44:47], s62 offen offset:2048 sc0 sc1 nt lds\n"
"v_mfma_f32_16x16x32_bf16 v[176:179], a[232:235], a[68:71], v[176:179]\n"
"v_add_f32 v247, v247, v228\n"
"v_mfma_f32_16x16x32_bf16 v[176:179], a[236:239], a[84:87], v[176:179]\n"
"v_exp_f32 v229, v229\n"
"v_mfma_f32_16x16x32_bf16 v[184:187], a[232:235], a[72:75], v[184:187]\n"
"ds_read_b128 a[252:255], v234 offset:29184\n"
"v_add_f32 v247, v247, v229\n"
"v_mfma_f32_16x16x32_bf16 v[184:187], a[236:239], a[88:91], v[184:187]\n"
"v_cvt_pk_bf16_f32 v226, v228, v229\n"
"v_mfma_f32_16x16x32_bf16 v[192:195], a[232:235], a[76:79], v[192:195]\n"
"v_exp_f32 v230, v230\n"
"v_mfma_f32_16x16x32_bf16 v[192:195], a[236:239], a[92:95], v[192:195]\n"
"s_waitcnt lgkmcnt(2)\n"
"v_add_f32 v247, v247, v230\n"
"v_mfma_f32_16x16x32_bf16 v[196:199], a[240:243], a[76:79], v[196:199]\n"
"ds_read_b128 a[232:235], v234 offset:30720\n"
"v_mfma_f32_16x16x32_bf16 v[196:199], a[244:247], a[92:95], v[196:199]\n"
"v_exp_f32 v231, v231\n"
"v_mfma_f32_16x16x32_bf16 v[188:191], a[240:243], a[72:75], v[188:191]\n"
"v_add_f32 v247, v247, v231\n"
"v_mfma_f32_16x16x32_bf16 v[188:191], a[244:247], a[88:91], v[188:191]\n"
"v_cvt_pk_bf16_f32 v227, v230, v231\n"
"v_mfma_f32_16x16x32_bf16 v[180:183], a[240:243], a[68:71], v[180:183]\n"
"ds_read_b128 a[236:239], v234 offset:31232\n"
"v_mfma_f32_16x16x32_bf16 v[180:183], a[244:247], a[84:87], v[180:183]\n"
"v_mfma_f32_16x16x32_bf16 v[172:175], a[240:243], a[64:67], v[172:175]\n"
"v_mfma_f32_16x16x32_bf16 v[172:175], a[244:247], a[80:83], v[172:175]\n"
"s_waitcnt lgkmcnt(2)\n"
"v_mfma_f32_16x16x32_bf16 v[192:195], a[248:251], a[108:111], v[192:195]\n"
"ds_read_b64_tr_b16 a[240:241], v235 offset:0\n"
"v_mfma_f32_16x16x32_bf16 v[192:195], a[252:255], a[124:127], v[192:195]\n"
"ds_read_b64_tr_b16 a[242:243], v235 offset:2048\n"
"v_mfma_f32_16x16x32_bf16 v[184:187], a[248:251], a[104:107], v[184:187]\n"
"buffer_load_dwordx4 v242, s[44:47], s63 offen offset:3072 sc0 sc1 nt lds\n"
"v_mfma_f32_16x16x32_bf16 v[184:187], a[252:255], a[120:123], v[184:187]\n"
"v_mfma_f32_16x16x32_bf16 v[176:179], a[248:251], a[100:103], v[176:179]\n"
"ds_read_b64_tr_b16 a[244:245], v236 offset:0\n"
"v_mfma_f32_16x16x32_bf16 v[176:179], a[252:255], a[116:119], v[176:179]\n"
"ds_read_b64_tr_b16 a[246:247], v236 offset:2048\n"
"v_mfma_f32_16x16x32_bf16 v[168:171], a[248:251], a[96:99], v[168:171]\n"
"v_mfma_f32_16x16x32_bf16 v[168:171], a[252:255], a[112:115], v[168:171]\n"
"s_waitcnt lgkmcnt(4)\n"
"v_mfma_f32_16x16x32_bf16 v[172:175], a[232:235], a[96:99], v[172:175]\n"
"ds_read_b64_tr_b16 a[248:249], v235 offset:512\n"
"v_mfma_f32_16x16x32_bf16 v[172:175], a[236:239], a[112:115], v[172:175]\n"
"ds_read_b64_tr_b16 a[250:251], v235 offset:2560\n"
"v_mfma_f32_16x16x32_bf16 v[180:183], a[232:235], a[100:103], v[180:183]\n"
"v_mfma_f32_16x16x32_bf16 v[180:183], a[236:239], a[116:119], v[180:183]\n"
"v_mfma_f32_16x16x32_bf16 v[188:191], a[232:235], a[104:107], v[188:191]\n"
"ds_read_b64_tr_b16 a[252:253], v236 offset:512\n"
"v_mfma_f32_16x16x32_bf16 v[188:191], a[236:239], a[120:123], v[188:191]\n"
"ds_read_b64_tr_b16 a[254:255], v236 offset:2560\n"
"v_mfma_f32_16x16x32_bf16 v[196:199], a[232:235], a[108:111], v[196:199]\n"
"v_mfma_f32_16x16x32_bf16 v[196:199], a[236:239], a[124:127], v[196:199]\n"
"s_waitcnt lgkmcnt(4)\n"
"v_mfma_f32_16x16x32_bf16 v[0:3], a[240:243], v[200:203], v[0:3]\n"
"ds_read_b64_tr_b16 a[232:233], v235 offset:4096\n"
"v_mfma_f32_16x16x32_bf16 v[4:7], a[244:247], v[200:203], v[4:7]\n"
"ds_read_b64_tr_b16 a[234:235], v235 offset:6144\n"
"v_mfma_f32_16x16x32_bf16 v[8:11], a[240:243], v[208:211], v[8:11]\n"
"v_exp_f32 v168, v168\n"
"v_mfma_f32_16x16x32_bf16 v[12:15], a[244:247], v[208:211], v[12:15]\n"
"v_add_f32 v244, v244, v168\n"
"v_mfma_f32_16x16x32_bf16 v[16:19], a[240:243], v[216:219], v[16:19]\n"
"ds_read_b64_tr_b16 a[236:237], v236 offset:4096\n"
"v_mfma_f32_16x16x32_bf16 v[20:23], a[244:247], v[216:219], v[20:23]\n"
"ds_read_b64_tr_b16 a[238:239], v236 offset:6144\n"
"v_add_u32 v242, 163840, v241\n"
"s_add_u32 m0, s71, 114688\n"
"v_mfma_f32_16x16x32_bf16 v[24:27], a[240:243], v[224:227], v[24:27]\n"
"buffer_load_dwordx4 v242, s[40:43], 0 offen offset:0 sc0 sc1 nt lds\n"
"v_mfma_f32_16x16x32_bf16 v[28:31], a[244:247], v[224:227], v[28:31]\n"
"s_waitcnt lgkmcnt(4)\n"
"v_exp_f32 v169, v169\n"
"v_mfma_f32_16x16x32_bf16 v[56:59], a[248:251], v[224:227], v[56:59]\n"
"ds_read_b64_tr_b16 a[240:241], v235 offset:4608\n"
"v_add_f32 v244, v244, v169\n"
"v_mfma_f32_16x16x32_bf16 v[60:63], a[252:255], v[224:227], v[60:63]\n"
"ds_read_b64_tr_b16 a[242:243], v235 offset:6656\n"
"v_cvt_pk_bf16_f32 v168, v168, v169\n"
"v_mfma_f32_16x16x32_bf16 v[48:51], a[248:251], v[216:219], v[48:51]\n"
"v_exp_f32 v170, v170\n"
"v_mfma_f32_16x16x32_bf16 v[52:55], a[252:255], v[216:219], v[52:55]\n"
"v_add_f32 v244, v244, v170\n"
"v_mfma_f32_16x16x32_bf16 v[40:43], a[248:251], v[208:211], v[40:43]\n"
"ds_read_b64_tr_b16 a[244:245], v236 offset:4608\n"
"v_mfma_f32_16x16x32_bf16 v[44:47], a[252:255], v[208:211], v[44:47]\n"
"ds_read_b64_tr_b16 a[246:247], v236 offset:6656\n"
"v_mfma_f32_16x16x32_bf16 v[32:35], a[248:251], v[200:203], v[32:35]\n"
"v_exp_f32 v171, v171\n"
"v_mfma_f32_16x16x32_bf16 v[36:39], a[252:255], v[200:203], v[36:39]\n"
"s_waitcnt lgkmcnt(4)\n"
"v_add_f32 v244, v244, v171\n"
"v_cvt_pk_bf16_f32 v169, v170, v171\n"
"v_mfma_f32_16x16x32_bf16 v[64:67], a[232:235], v[200:203], v[64:67]\n"
"ds_read_b64_tr_b16 a[248:249], v235 offset:8192\n"
"v_mfma_f32_16x16x32_bf16 v[68:71], a[236:239], v[200:203], v[68:71]\n"
"ds_read_b64_tr_b16 a[250:251], v235 offset:10240\n"
"v_mfma_f32_16x16x32_bf16 v[72:75], a[232:235], v[208:211], v[72:75]\n"
"v_exp_f32 v172, v172\n"
"v_mfma_f32_16x16x32_bf16 v[76:79], a[236:239], v[208:211], v[76:79]\n"
"v_add_f32 v244, v244, v172\n"
"v_mfma_f32_16x16x32_bf16 v[80:83], a[232:235], v[216:219], v[80:83]\n"
"ds_read_b64_tr_b16 a[252:253], v236 offset:8192\n"
"v_mfma_f32_16x16x32_bf16 v[84:87], a[236:239], v[216:219], v[84:87]\n"
"ds_read_b64_tr_b16 a[254:255], v236 offset:10240\n"
"v_mfma_f32_16x16x32_bf16 v[88:91], a[232:235], v[224:227], v[88:91]\n"
"buffer_load_dwordx4 v242, s[40:43], s61 offen offset:1024 sc0 sc1 nt lds\n"
"v_mfma_f32_16x16x32_bf16 v[92:95], a[236:239], v[224:227], v[92:95]\n"
"s_waitcnt lgkmcnt(4)\n"
"v_exp_f32 v173, v173\n"
"v_mfma_f32_16x16x32_bf16 v[120:123], a[240:243], v[224:227], v[120:123]\n"
"ds_read_b64_tr_b16 a[232:233], v235 offset:8704\n"
"v_add_f32 v244, v244, v173\n"
"v_mfma_f32_16x16x32_bf16 v[124:127], a[244:247], v[224:227], v[124:127]\n"
"ds_read_b64_tr_b16 a[234:235], v235 offset:10752\n"
"v_cvt_pk_bf16_f32 v170, v172, v173\n"
"v_mfma_f32_16x16x32_bf16 v[112:115], a[240:243], v[216:219], v[112:115]\n"
"s_waitcnt vmcnt(22)\n"
"s_barrier\n"
"v_mfma_f32_16x16x32_bf16 v[116:119], a[244:247], v[216:219], v[116:119]\n"
"v_exp_f32 v174, v174\n"
"v_mfma_f32_16x16x32_bf16 v[104:107], a[240:243], v[208:211], v[104:107]\n"
"ds_read_b64_tr_b16 a[236:237], v236 offset:8704\n"
"v_add_f32 v244, v244, v174\n"
"v_mfma_f32_16x16x32_bf16 v[108:111], a[244:247], v[208:211], v[108:111]\n"
"ds_read_b64_tr_b16 a[238:239], v236 offset:10752\n"
"v_mfma_f32_16x16x32_bf16 v[96:99], a[240:243], v[200:203], v[96:99]\n"
"v_exp_f32 v175, v175\n"
"v_mfma_f32_16x16x32_bf16 v[100:103], a[244:247], v[200:203], v[100:103]\n"
"s_waitcnt lgkmcnt(4)\n"
"v_add_f32 v244, v244, v175\n"
"v_cvt_pk_bf16_f32 v171, v174, v175\n"
"v_mfma_f32_16x16x32_bf16 v[128:131], a[248:251], v[200:203], v[128:131]\n"
"ds_read_b64_tr_b16 a[240:241], v235 offset:12288\n"
"v_mfma_f32_16x16x32_bf16 v[132:135], a[252:255], v[200:203], v[132:135]\n"
"ds_read_b64_tr_b16 a[242:243], v235 offset:14336\n"
"v_mfma_f32_16x16x32_bf16 v[136:139], a[248:251], v[208:211], v[136:139]\n"
"v_exp_f32 v176, v176\n"
"v_mfma_f32_16x16x32_bf16 v[140:143], a[252:255], v[208:211], v[140:143]\n"
"v_add_f32 v245, v245, v176\n"
"v_mfma_f32_16x16x32_bf16 v[144:147], a[248:251], v[216:219], v[144:147]\n"
"ds_read_b64_tr_b16 a[244:245], v236 offset:12288\n"
"v_mfma_f32_16x16x32_bf16 v[148:151], a[252:255], v[216:219], v[148:151]\n"
"ds_read_b64_tr_b16 a[246:247], v236 offset:14336\n"
"v_mfma_f32_16x16x32_bf16 a[128:131], a[248:251], v[224:227], a[128:131]\n"
"v_exp_f32 v177, v177\n"
"v_mfma_f32_16x16x32_bf16 a[132:135], a[252:255], v[224:227], a[132:135]\n"
"s_waitcnt lgkmcnt(4)\n"
"v_add_f32 v245, v245, v177\n"
"v_cvt_pk_bf16_f32 v176, v176, v177\n"
"v_mfma_f32_16x16x32_bf16 a[160:163], a[232:235], v[224:227], a[160:163]\n"
"ds_read_b64_tr_b16 a[248:249], v235 offset:12800\n"
"v_mfma_f32_16x16x32_bf16 a[164:167], a[236:239], v[224:227], a[164:167]\n"
"ds_read_b64_tr_b16 a[250:251], v235 offset:14848\n"
"v_mfma_f32_16x16x32_bf16 a[152:155], a[232:235], v[216:219], a[152:155]\n"
"buffer_load_dwordx4 v242, s[40:43], s62 offen offset:2048 sc0 sc1 nt lds\n"
"v_mfma_f32_16x16x32_bf16 a[156:159], a[236:239], v[216:219], a[156:159]\n"
"v_exp_f32 v178, v178\n"
"v_mfma_f32_16x16x32_bf16 a[144:147], a[232:235], v[208:211], a[144:147]\n"
"ds_read_b64_tr_b16 a[252:253], v236 offset:12800\n"
"v_add_f32 v245, v245, v178\n"
"v_mfma_f32_16x16x32_bf16 a[148:151], a[236:239], v[208:211], a[148:151]\n"
"ds_read_b64_tr_b16 a[254:255], v236 offset:14848\n"
"v_mfma_f32_16x16x32_bf16 a[136:139], a[232:235], v[200:203], a[136:139]\n"
"v_exp_f32 v179, v179\n"
"v_mfma_f32_16x16x32_bf16 a[140:143], a[236:239], v[200:203], a[140:143]\n"
"s_waitcnt lgkmcnt(4)\n"
"v_add_f32 v245, v245, v179\n"
"v_cvt_pk_bf16_f32 v177, v178, v179\n"
"v_mfma_f32_16x16x32_bf16 a[168:171], a[240:243], v[200:203], a[168:171]\n"
"ds_read_b128 a[232:235], v232 offset:16384\n"
"v_mfma_f32_16x16x32_bf16 a[172:175], a[244:247], v[200:203], a[172:175]\n"
"v_exp_f32 v180, v180\n"
"v_mfma_f32_16x16x32_bf16 a[176:179], a[240:243], v[208:211], a[176:179]\n"
"v_add_f32 v245, v245, v180\n"
"v_mfma_f32_16x16x32_bf16 a[180:183], a[244:247], v[208:211], a[180:183]\n"
"v_exp_f32 v181, v181\n"
"v_mfma_f32_16x16x32_bf16 a[184:187], a[240:243], v[216:219], a[184:187]\n"
"ds_read_b128 a[236:239], v232 offset:16896\n"
"v_add_f32 v245, v245, v181\n"
"v_mfma_f32_16x16x32_bf16 a[188:191], a[244:247], v[216:219], a[188:191]\n"
"v_cvt_pk_bf16_f32 v178, v180, v181\n"
"v_mfma_f32_16x16x32_bf16 a[192:195], a[240:243], v[224:227], a[192:195]\n"
"v_exp_f32 v182, v182\n"
"v_mfma_f32_16x16x32_bf16 a[196:199], a[244:247], v[224:227], a[196:199]\n"
"s_waitcnt lgkmcnt(2)\n"
"v_add_f32 v245, v245, v182\n"
"v_mfma_f32_16x16x32_bf16 a[200:203], a[248:251], v[200:203], a[200:203]\n"
"ds_read_b128 a[240:243], v232 offset:18432\n"
"v_mfma_f32_16x16x32_bf16 a[204:207], a[252:255], v[200:203], a[204:207]\n"
"v_exp_f32 v183, v183\n"
"v_mfma_f32_16x16x32_bf16 a[208:211], a[248:251], v[208:211], a[208:211]\n"
"buffer_load_dwordx4 v242, s[40:43], s63 offen offset:3072 sc0 sc1 nt lds\n"
"v_mfma_f32_16x16x32_bf16 a[212:215], a[252:255], v[208:211], a[212:215]\n"
"v_add_f32 v245, v245, v183\n"
"v_cvt_pk_bf16_f32 v179, v182, v183\n"
"v_mfma_f32_16x16x32_bf16 a[216:219], a[248:251], v[216:219], a[216:219]\n"
"ds_read_b128 a[244:247], v232 offset:18944\n"
"v_mfma_f32_16x16x32_bf16 a[220:223], a[252:255], v[216:219], a[220:223]\n"
"v_exp_f32 v184, v184\n"
"v_mfma_f32_16x16x32_bf16 a[224:227], a[248:251], v[224:227], a[224:227]\n"
"v_add_f32 v246, v246, v184\n"
"v_mfma_f32_16x16x32_bf16 a[228:231], a[252:255], v[224:227], a[228:231]\n"
"s_waitcnt lgkmcnt(2)\n"
"v_exp_f32 v185, v185\n"
"v_mfma_f32_16x16x32_bf16 v[200:203], a[232:235], a[0:3], v[152:155]\n"
"ds_read_b128 a[248:251], v232 offset:20480\n"
"v_add_f32 v246, v246, v185\n"
"v_mfma_f32_16x16x32_bf16 v[200:203], a[236:239], a[16:19], v[200:203]\n"
"v_cvt_pk_bf16_f32 v184, v184, v185\n"
"v_mfma_f32_16x16x32_bf16 v[208:211], a[232:235], a[4:7], v[156:159]\n"
"v_exp_f32 v186, v186\n"
"v_mfma_f32_16x16x32_bf16 v[208:211], a[236:239], a[20:23], v[208:211]\n"
"v_add_f32 v246, v246, v186\n"
"v_mfma_f32_16x16x32_bf16 v[216:219], a[232:235], a[8:11], v[160:163]\n"
"ds_read_b128 a[252:255], v232 offset:20992\n"
"v_mfma_f32_16x16x32_bf16 v[216:219], a[236:239], a[24:27], v[216:219]\n"
"v_exp_f32 v187, v187\n"
"v_mfma_f32_16x16x32_bf16 v[224:227], a[232:235], a[12:15], v[164:167]\n"
"v_add_f32 v246, v246, v187\n"
"v_cvt_pk_bf16_f32 v185, v186, v187\n"
"v_mfma_f32_16x16x32_bf16 v[224:227], a[236:239], a[28:31], v[224:227]\n"
"s_waitcnt lgkmcnt(2)\n"
"v_exp_f32 v188, v188\n"
"v_mfma_f32_16x16x32_bf16 v[228:231], a[240:243], a[12:15], v[164:167]\n"
"ds_read_b128 a[232:235], v232 offset:22528\n"
"v_add_f32 v246, v246, v188\n"
"v_mfma_f32_16x16x32_bf16 v[228:231], a[244:247], a[28:31], v[228:231]\n"
"v_exp_f32 v189, v189\n"
"v_mfma_f32_16x16x32_bf16 v[220:223], a[240:243], a[8:11], v[160:163]\n"
"v_add_f32 v246, v246, v189\n"
"v_cvt_pk_bf16_f32 v186, v188, v189\n"
"v_mfma_f32_16x16x32_bf16 v[220:223], a[244:247], a[24:27], v[220:223]\n"
"v_exp_f32 v190, v190\n"
"v_mfma_f32_16x16x32_bf16 v[212:215], a[240:243], a[4:7], v[156:159]\n"
"ds_read_b128 a[236:239], v232 offset:23040\n"
"v_add_u32 v242, 147456, v241\n"
"s_add_u32 m0, s71, 131072\n"
"v_mfma_f32_16x16x32_bf16 v[212:215], a[244:247], a[20:23], v[212:215]\n"
"buffer_load_dwordx4 v242, s[44:47], 0 offen offset:0 sc0 sc1 nt lds\n"
"v_mfma_f32_16x16x32_bf16 v[204:207], a[240:243], a[0:3], v[152:155]\n"
"v_add_f32 v246, v246, v190\n"
"v_mfma_f32_16x16x32_bf16 v[204:207], a[244:247], a[16:19], v[204:207]\n"
"s_waitcnt lgkmcnt(2)\n"
"v_exp_f32 v191, v191\n"
"v_mfma_f32_16x16x32_bf16 v[200:203], a[248:251], a[32:35], v[200:203]\n"
"ds_read_b128 a[240:243], v232 offset:24576\n"
"v_mfma_f32_16x16x32_bf16 v[200:203], a[252:255], a[48:51], v[200:203]\n"
"v_add_f32 v246, v246, v191\n"
"v_mfma_f32_16x16x32_bf16 v[208:211], a[248:251], a[36:39], v[208:211]\n"
"v_cvt_pk_bf16_f32 v187, v190, v191\n"
"v_mfma_f32_16x16x32_bf16 v[208:211], a[252:255], a[52:55], v[208:211]\n"
"v_exp_f32 v192, v192\n"
"v_mfma_f32_16x16x32_bf16 v[216:219], a[248:251], a[40:43], v[216:219]\n"
"ds_read_b128 a[244:247], v232 offset:25088\n"
"v_add_f32 v247, v247, v192\n"
"v_mfma_f32_16x16x32_bf16 v[216:219], a[252:255], a[56:59], v[216:219]\n"
"v_exp_f32 v193, v193\n"
"v_mfma_f32_16x16x32_bf16 v[224:227], a[248:251], a[44:47], v[224:227]\n"
"v_mfma_f32_16x16x32_bf16 v[224:227], a[252:255], a[60:63], v[224:227]\n"
"s_waitcnt lgkmcnt(2)\n"
"v_add_f32 v247, v247, v193\n"
"v_cvt_pk_bf16_f32 v192, v192, v193\n"
"v_mfma_f32_16x16x32_bf16 v[228:231], a[232:235], a[44:47], v[228:231]\n"
"ds_read_b128 a[248:251], v232 offset:26624\n"
"v_mfma_f32_16x16x32_bf16 v[228:231], a[236:239], a[60:63], v[228:231]\n"
"v_exp_f32 v194, v194\n"
"v_mfma_f32_16x16x32_bf16 v[220:223], a[232:235], a[40:43], v[220:223]\n"
"s_waitcnt vmcnt(21)\n"
"s_barrier\n"
"v_mfma_f32_16x16x32_bf16 v[220:223], a[236:239], a[56:59], v[220:223]\n"
"v_add_f32 v247, v247, v194\n"
"v_mfma_f32_16x16x32_bf16 v[212:215], a[232:235], a[36:39], v[212:215]\n"
"ds_read_b128 a[252:255], v232 offset:27136\n"
"v_mfma_f32_16x16x32_bf16 v[212:215], a[236:239], a[52:55], v[212:215]\n"
"v_exp_f32 v195, v195\n"
"v_mfma_f32_16x16x32_bf16 v[204:207], a[232:235], a[32:35], v[204:207]\n"
"buffer_load_dwordx4 v242, s[44:47], s61 offen offset:1024 sc0 sc1 nt lds\n"
"v_mfma_f32_16x16x32_bf16 v[204:207], a[236:239], a[48:51], v[204:207]\n"
"s_waitcnt lgkmcnt(2)\n"
"v_add_f32 v247, v247, v195\n"
"v_cvt_pk_bf16_f32 v193, v194, v195\n"
"v_mfma_f32_16x16x32_bf16 v[200:203], a[240:243], a[64:67], v[200:203]\n"
"ds_read_b128 a[232:235], v232 offset:28672\n"
"v_mfma_f32_16x16x32_bf16 v[200:203], a[244:247], a[80:83], v[200:203]\n"
"v_exp_f32 v196, v196\n"
"v_mfma_f32_16x16x32_bf16 v[208:211], a[240:243], a[68:71], v[208:211]\n"
"v_add_f32 v247, v247, v196\n"
"v_mfma_f32_16x16x32_bf16 v[208:211], a[244:247], a[84:87], v[208:211]\n"
"v_exp_f32 v197, v197\n"
"v_mfma_f32_16x16x32_bf16 v[216:219], a[240:243], a[72:75], v[216:219]\n"
"ds_read_b128 a[236:239], v232 offset:29184\n"
"v_add_f32 v247, v247, v197\n"
"v_mfma_f32_16x16x32_bf16 v[216:219], a[244:247], a[88:91], v[216:219]\n"
"v_cvt_pk_bf16_f32 v194, v196, v197\n"
"v_mfma_f32_16x16x32_bf16 v[224:227], a[240:243], a[76:79], v[224:227]\n"
"v_exp_f32 v198, v198\n"
"v_mfma_f32_16x16x32_bf16 v[224:227], a[244:247], a[92:95], v[224:227]\n"
"s_waitcnt lgkmcnt(2)\n"
"v_add_f32 v247, v247, v198\n"
"v_mfma_f32_16x16x32_bf16 v[228:231], a[248:251], a[76:79], v[228:231]\n"
"ds_read_b128 a[240:243], v232 offset:30720\n"
"v_mfma_f32_16x16x32_bf16 v[228:231], a[252:255], a[92:95], v[228:231]\n"
"v_exp_f32 v199, v199\n"
"v_mfma_f32_16x16x32_bf16 v[220:223], a[248:251], a[72:75], v[220:223]\n"
"v_add_f32 v247, v247, v199\n"
"v_mfma_f32_16x16x32_bf16 v[220:223], a[252:255], a[88:91], v[220:223]\n"
"v_cvt_pk_bf16_f32 v195, v198, v199\n"
"v_mfma_f32_16x16x32_bf16 v[212:215], a[248:251], a[68:71], v[212:215]\n"
"ds_read_b128 a[244:247], v232 offset:31232\n"
"v_mfma_f32_16x16x32_bf16 v[212:215], a[252:255], a[84:87], v[212:215]\n"
"v_mfma_f32_16x16x32_bf16 v[204:207], a[248:251], a[64:67], v[204:207]\n"
"v_mfma_f32_16x16x32_bf16 v[204:207], a[252:255], a[80:83], v[204:207]\n"
"s_waitcnt lgkmcnt(2)\n"
"v_mfma_f32_16x16x32_bf16 v[224:227], a[232:235], a[108:111], v[224:227]\n"
"ds_read_b64_tr_b16 a[248:249], v235 offset:32768\n"
"v_mfma_f32_16x16x32_bf16 v[224:227], a[236:239], a[124:127], v[224:227]\n"
"ds_read_b64_tr_b16 a[250:251], v235 offset:34816\n"
"v_mfma_f32_16x16x32_bf16 v[216:219], a[232:235], a[104:107], v[216:219]\n"
"buffer_load_dwordx4 v242, s[44:47], s62 offen offset:2048 sc0 sc1 nt lds\n"
"v_mfma_f32_16x16x32_bf16 v[216:219], a[236:239], a[120:123], v[216:219]\n"
"v_mfma_f32_16x16x32_bf16 v[208:211], a[232:235], a[100:103], v[208:211]\n"
"ds_read_b64_tr_b16 a[252:253], v236 offset:32768\n"
"v_mfma_f32_16x16x32_bf16 v[208:211], a[236:239], a[116:119], v[208:211]\n"
"ds_read_b64_tr_b16 a[254:255], v236 offset:34816\n"
"v_mfma_f32_16x16x32_bf16 v[200:203], a[232:235], a[96:99], v[200:203]\n"
"v_mfma_f32_16x16x32_bf16 v[200:203], a[236:239], a[112:115], v[200:203]\n"
"s_waitcnt lgkmcnt(4)\n"
"v_mfma_f32_16x16x32_bf16 v[204:207], a[240:243], a[96:99], v[204:207]\n"
"ds_read_b64_tr_b16 a[232:233], v235 offset:33280\n"
"v_mfma_f32_16x16x32_bf16 v[204:207], a[244:247], a[112:115], v[204:207]\n"
"ds_read_b64_tr_b16 a[234:235], v235 offset:35328\n"
"v_mfma_f32_16x16x32_bf16 v[212:215], a[240:243], a[100:103], v[212:215]\n"
"v_mfma_f32_16x16x32_bf16 v[212:215], a[244:247], a[116:119], v[212:215]\n"
"v_mfma_f32_16x16x32_bf16 v[220:223], a[240:243], a[104:107], v[220:223]\n"
"ds_read_b64_tr_b16 a[236:237], v236 offset:33280\n"
"v_mfma_f32_16x16x32_bf16 v[220:223], a[244:247], a[120:123], v[220:223]\n"
"ds_read_b64_tr_b16 a[238:239], v236 offset:35328\n"
"v_mfma_f32_16x16x32_bf16 v[228:231], a[240:243], a[108:111], v[228:231]\n"
"v_mfma_f32_16x16x32_bf16 v[228:231], a[244:247], a[124:127], v[228:231]\n"
"s_waitcnt lgkmcnt(4)\n"
"v_mfma_f32_16x16x32_bf16 v[0:3], a[248:251], v[168:171], v[0:3]\n"
"ds_read_b64_tr_b16 a[240:241], v235 offset:36864\n"
"v_mfma_f32_16x16x32_bf16 v[4:7], a[252:255], v[168:171], v[4:7]\n"
"ds_read_b64_tr_b16 a[242:243], v235 offset:38912\n"
"v_mfma_f32_16x16x32_bf16 v[8:11], a[248:251], v[176:179], v[8:11]\n"
"buffer_load_dwordx4 v242, s[44:47], s63 offen offset:3072 sc0 sc1 nt lds\n"
"v_mfma_f32_16x16x32_bf16 v[12:15], a[252:255], v[176:179], v[12:15]\n"
"v_exp_f32 v200, v200\n"
"v_mfma_f32_16x16x32_bf16 v[16:19], a[248:251], v[184:187], v[16:19]\n"
"ds_read_b64_tr_b16 a[244:245], v236 offset:36864\n"
"v_add_f32 v244, v244, v200\n"
"v_mfma_f32_16x16x32_bf16 v[20:23], a[252:255], v[184:187], v[20:23]\n"
"ds_read_b64_tr_b16 a[246:247], v236 offset:38912\n"
"v_mfma_f32_16x16x32_bf16 v[24:27], a[248:251], v[192:195], v[24:27]\n"
"v_exp_f32 v201, v201\n"
"v_mfma_f32_16x16x32_bf16 v[28:31], a[252:255], v[192:195], v[28:31]\n"
"s_waitcnt lgkmcnt(4)\n"
"v_add_f32 v244, v244, v201\n"
"v_cvt_pk_bf16_f32 v200, v200, v201\n"
"v_mfma_f32_16x16x32_bf16 v[56:59], a[232:235], v[192:195], v[56:59]\n"
"ds_read_b64_tr_b16 a[248:249], v235 offset:37376\n"
"v_mfma_f32_16x16x32_bf16 v[60:63], a[236:239], v[192:195], v[60:63]\n"
"ds_read_b64_tr_b16 a[250:251], v235 offset:39424\n"
"v_mfma_f32_16x16x32_bf16 v[48:51], a[232:235], v[184:187], v[48:51]\n"
"v_exp_f32 v202, v202\n"
"v_mfma_f32_16x16x32_bf16 v[52:55], a[236:239], v[184:187], v[52:55]\n"
"v_add_f32 v244, v244, v202\n"
"v_mfma_f32_16x16x32_bf16 v[40:43], a[232:235], v[176:179], v[40:43]\n"
"ds_read_b64_tr_b16 a[252:253], v236 offset:37376\n"
"v_mfma_f32_16x16x32_bf16 v[44:47], a[236:239], v[176:179], v[44:47]\n"
"ds_read_b64_tr_b16 a[254:255], v236 offset:39424\n"
"v_mfma_f32_16x16x32_bf16 v[32:35], a[232:235], v[168:171], v[32:35]\n"
"v_exp_f32 v203, v203\n"
"v_mfma_f32_16x16x32_bf16 v[36:39], a[236:239], v[168:171], v[36:39]\n"
"s_waitcnt lgkmcnt(4)\n"
"v_add_f32 v244, v244, v203\n"
"v_cvt_pk_bf16_f32 v201, v202, v203\n"
"v_mfma_f32_16x16x32_bf16 v[64:67], a[240:243], v[168:171], v[64:67]\n"
"ds_read_b64_tr_b16 a[232:233], v235 offset:40960\n"
"v_mfma_f32_16x16x32_bf16 v[68:71], a[244:247], v[168:171], v[68:71]\n"
"ds_read_b64_tr_b16 a[234:235], v235 offset:43008\n"
"v_mfma_f32_16x16x32_bf16 v[72:75], a[240:243], v[176:179], v[72:75]\n"
"v_exp_f32 v204, v204\n"
"v_mfma_f32_16x16x32_bf16 v[76:79], a[244:247], v[176:179], v[76:79]\n"
"v_add_f32 v244, v244, v204\n"
"v_mfma_f32_16x16x32_bf16 v[80:83], a[240:243], v[184:187], v[80:83]\n"
"ds_read_b64_tr_b16 a[236:237], v236 offset:40960\n"
"v_mfma_f32_16x16x32_bf16 v[84:87], a[244:247], v[184:187], v[84:87]\n"
"ds_read_b64_tr_b16 a[238:239], v236 offset:43008\n"
"v_add_u32 v242, 180224, v241\n"
"s_add_u32 m0, s71, 147456\n"
"v_mfma_f32_16x16x32_bf16 v[88:91], a[240:243], v[192:195], v[88:91]\n"
"buffer_load_dwordx4 v242, s[40:43], 0 offen offset:0 sc0 sc1 nt lds\n"
"v_mfma_f32_16x16x32_bf16 v[92:95], a[244:247], v[192:195], v[92:95]\n"
"s_waitcnt lgkmcnt(4)\n"
"v_exp_f32 v205, v205\n"
"v_mfma_f32_16x16x32_bf16 v[120:123], a[248:251], v[192:195], v[120:123]\n"
"ds_read_b64_tr_b16 a[240:241], v235 offset:41472\n"
"v_add_f32 v244, v244, v205\n"
"v_mfma_f32_16x16x32_bf16 v[124:127], a[252:255], v[192:195], v[124:127]\n"
"ds_read_b64_tr_b16 a[242:243], v235 offset:43520\n"
"v_cvt_pk_bf16_f32 v202, v204, v205\n"
"v_mfma_f32_16x16x32_bf16 v[112:115], a[248:251], v[184:187], v[112:115]\n"
"s_waitcnt vmcnt(21)\n"
"s_barrier\n"
"v_mfma_f32_16x16x32_bf16 v[116:119], a[252:255], v[184:187], v[116:119]\n"
"v_exp_f32 v206, v206\n"
"v_mfma_f32_16x16x32_bf16 v[104:107], a[248:251], v[176:179], v[104:107]\n"
"ds_read_b64_tr_b16 a[244:245], v236 offset:41472\n"
"v_add_f32 v244, v244, v206\n"
"v_mfma_f32_16x16x32_bf16 v[108:111], a[252:255], v[176:179], v[108:111]\n"
"ds_read_b64_tr_b16 a[246:247], v236 offset:43520\n"
"v_mfma_f32_16x16x32_bf16 v[96:99], a[248:251], v[168:171], v[96:99]\n"
"v_exp_f32 v207, v207\n"
"v_mfma_f32_16x16x32_bf16 v[100:103], a[252:255], v[168:171], v[100:103]\n"
"s_waitcnt lgkmcnt(4)\n"
"v_add_f32 v244, v244, v207\n"
"v_cvt_pk_bf16_f32 v203, v206, v207\n"
"v_mfma_f32_16x16x32_bf16 v[128:131], a[232:235], v[168:171], v[128:131]\n"
"ds_read_b64_tr_b16 a[248:249], v235 offset:45056\n"
"v_mfma_f32_16x16x32_bf16 v[132:135], a[236:239], v[168:171], v[132:135]\n"
"ds_read_b64_tr_b16 a[250:251], v235 offset:47104\n"
"v_mfma_f32_16x16x32_bf16 v[136:139], a[232:235], v[176:179], v[136:139]\n"
"v_exp_f32 v208, v208\n"
"v_mfma_f32_16x16x32_bf16 v[140:143], a[236:239], v[176:179], v[140:143]\n"
"v_add_f32 v245, v245, v208\n"
"v_mfma_f32_16x16x32_bf16 v[144:147], a[232:235], v[184:187], v[144:147]\n"
"ds_read_b64_tr_b16 a[252:253], v236 offset:45056\n"
"v_mfma_f32_16x16x32_bf16 v[148:151], a[236:239], v[184:187], v[148:151]\n"
"ds_read_b64_tr_b16 a[254:255], v236 offset:47104\n"
"v_mfma_f32_16x16x32_bf16 a[128:131], a[232:235], v[192:195], a[128:131]\n"
"buffer_load_dwordx4 v242, s[40:43], s61 offen offset:1024 sc0 sc1 nt lds\n"
"v_mfma_f32_16x16x32_bf16 a[132:135], a[236:239], v[192:195], a[132:135]\n"
"s_waitcnt lgkmcnt(4)\n"
"v_exp_f32 v209, v209\n"
"v_mfma_f32_16x16x32_bf16 a[160:163], a[240:243], v[192:195], a[160:163]\n"
"ds_read_b64_tr_b16 a[232:233], v235 offset:45568\n"
"v_add_f32 v245, v245, v209\n"
"v_mfma_f32_16x16x32_bf16 a[164:167], a[244:247], v[192:195], a[164:167]\n"
"ds_read_b64_tr_b16 a[234:235], v235 offset:47616\n"
"v_cvt_pk_bf16_f32 v208, v208, v209\n"
"v_mfma_f32_16x16x32_bf16 a[152:155], a[240:243], v[184:187], a[152:155]\n"
"v_exp_f32 v210, v210\n"
"v_mfma_f32_16x16x32_bf16 a[156:159], a[244:247], v[184:187], a[156:159]\n"
"v_add_f32 v245, v245, v210\n"
"v_mfma_f32_16x16x32_bf16 a[144:147], a[240:243], v[176:179], a[144:147]\n"
"ds_read_b64_tr_b16 a[236:237], v236 offset:45568\n"
"v_mfma_f32_16x16x32_bf16 a[148:151], a[244:247], v[176:179], a[148:151]\n"
"ds_read_b64_tr_b16 a[238:239], v236 offset:47616\n"
"v_mfma_f32_16x16x32_bf16 a[136:139], a[240:243], v[168:171], a[136:139]\n"
"v_exp_f32 v211, v211\n"
"v_mfma_f32_16x16x32_bf16 a[140:143], a[244:247], v[168:171], a[140:143]\n"
"s_waitcnt lgkmcnt(4)\n"
"v_add_f32 v245, v245, v211\n"
"v_cvt_pk_bf16_f32 v209, v210, v211\n"
"v_mfma_f32_16x16x32_bf16 a[168:171], a[248:251], v[168:171], a[168:171]\n"
"ds_read_b128 a[240:243], v232 offset:49152\n"
"v_mfma_f32_16x16x32_bf16 a[172:175], a[252:255], v[168:171], a[172:175]\n"
"v_exp_f32 v212, v212\n"
"v_mfma_f32_16x16x32_bf16 a[176:179], a[248:251], v[176:179], a[176:179]\n"
"v_add_f32 v245, v245, v212\n"
"v_mfma_f32_16x16x32_bf16 a[180:183], a[252:255], v[176:179], a[180:183]\n"
"v_exp_f32 v213, v213\n"
"v_mfma_f32_16x16x32_bf16 a[184:187], a[248:251], v[184:187], a[184:187]\n"
"ds_read_b128 a[244:247], v232 offset:49664\n"
"v_add_f32 v245, v245, v213\n"
"v_mfma_f32_16x16x32_bf16 a[188:191], a[252:255], v[184:187], a[188:191]\n"
"v_cvt_pk_bf16_f32 v210, v212, v213\n"
"v_mfma_f32_16x16x32_bf16 a[192:195], a[248:251], v[192:195], a[192:195]\n"
"v_exp_f32 v214, v214\n"
"v_mfma_f32_16x16x32_bf16 a[196:199], a[252:255], v[192:195], a[196:199]\n"
"s_waitcnt lgkmcnt(2)\n"
"v_add_f32 v245, v245, v214\n"
"v_mfma_f32_16x16x32_bf16 a[200:203], a[232:235], v[168:171], a[200:203]\n"
"ds_read_b128 a[248:251], v232 offset:51200\n"
"v_mfma_f32_16x16x32_bf16 a[204:207], a[236:239], v[168:171], a[204:207]\n"
"buffer_load_dwordx4 v242, s[40:43], s62 offen offset:2048 sc0 sc1 nt lds\n"
"v_mfma_f32_16x16x32_bf16 a[208:211], a[232:235], v[176:179], a[208:211]\n"
"v_exp_f32 v215, v215\n"
"v_mfma_f32_16x16x32_bf16 a[212:215], a[236:239], v[176:179], a[212:215]\n"
"v_add_f32 v245, v245, v215\n"
"v_cvt_pk_bf16_f32 v211, v214, v215\n"
"v_mfma_f32_16x16x32_bf16 a[216:219], a[232:235], v[184:187], a[216:219]\n"
"ds_read_b128 a[252:255], v232 offset:51712\n"
"v_mfma_f32_16x16x32_bf16 a[220:223], a[236:239], v[184:187], a[220:223]\n"
"v_exp_f32 v216, v216\n"
"v_mfma_f32_16x16x32_bf16 a[224:227], a[232:235], v[192:195], a[224:227]\n"
"v_add_f32 v246, v246, v216\n"
"v_mfma_f32_16x16x32_bf16 a[228:231], a[236:239], v[192:195], a[228:231]\n"
"s_waitcnt lgkmcnt(2)\n"
"v_exp_f32 v217, v217\n"
"v_mfma_f32_16x16x32_bf16 v[168:171], a[240:243], a[0:3], v[152:155]\n"
"ds_read_b128 a[232:235], v232 offset:53248\n"
"v_add_f32 v246, v246, v217\n"
"v_mfma_f32_16x16x32_bf16 v[168:171], a[244:247], a[16:19], v[168:171]\n"
"v_cvt_pk_bf16_f32 v216, v216, v217\n"
"v_mfma_f32_16x16x32_bf16 v[176:179], a[240:243], a[4:7], v[156:159]\n"
"v_exp_f32 v218, v218\n"
"v_mfma_f32_16x16x32_bf16 v[176:179], a[244:247], a[20:23], v[176:179]\n"
"v_add_f32 v246, v246, v218\n"
"v_mfma_f32_16x16x32_bf16 v[184:187], a[240:243], a[8:11], v[160:163]\n"
"ds_read_b128 a[236:239], v232 offset:53760\n"
"v_mfma_f32_16x16x32_bf16 v[184:187], a[244:247], a[24:27], v[184:187]\n"
"v_exp_f32 v219, v219\n"
"v_mfma_f32_16x16x32_bf16 v[192:195], a[240:243], a[12:15], v[164:167]\n"
"v_add_f32 v246, v246, v219\n"
"v_cvt_pk_bf16_f32 v217, v218, v219\n"
"v_mfma_f32_16x16x32_bf16 v[192:195], a[244:247], a[28:31], v[192:195]\n"
"s_waitcnt lgkmcnt(2)\n"
"v_exp_f32 v220, v220\n"
"v_mfma_f32_16x16x32_bf16 v[196:199], a[248:251], a[12:15], v[164:167]\n"
"ds_read_b128 a[240:243], v232 offset:55296\n"
"v_add_f32 v246, v246, v220\n"
"v_mfma_f32_16x16x32_bf16 v[196:199], a[252:255], a[28:31], v[196:199]\n"
"v_exp_f32 v221, v221\n"
"v_mfma_f32_16x16x32_bf16 v[188:191], a[248:251], a[8:11], v[160:163]\n"
"buffer_load_dwordx4 v242, s[40:43], s63 offen offset:3072 sc0 sc1 nt lds\n"
"v_mfma_f32_16x16x32_bf16 v[188:191], a[252:255], a[24:27], v[188:191]\n"
"v_add_f32 v246, v246, v221\n"
"v_cvt_pk_bf16_f32 v218, v220, v221\n"
"v_mfma_f32_16x16x32_bf16 v[180:183], a[248:251], a[4:7], v[156:159]\n"
"ds_read_b128 a[244:247], v232 offset:55808\n"
"v_mfma_f32_16x16x32_bf16 v[180:183], a[252:255], a[20:23], v[180:183]\n"
"v_exp_f32 v222, v222\n"
"v_mfma_f32_16x16x32_bf16 v[172:175], a[248:251], a[0:3], v[152:155]\n"
"v_add_f32 v246, v246, v222\n"
"v_mfma_f32_16x16x32_bf16 v[172:175], a[252:255], a[16:19], v[172:175]\n"
"s_waitcnt lgkmcnt(2)\n"
"v_exp_f32 v223, v223\n"
"v_mfma_f32_16x16x32_bf16 v[168:171], a[232:235], a[32:35], v[168:171]\n"
"ds_read_b128 a[248:251], v232 offset:57344\n"
"v_mfma_f32_16x16x32_bf16 v[168:171], a[236:239], a[48:51], v[168:171]\n"
"v_add_f32 v246, v246, v223\n"
"v_cvt_pk_bf16_f32 v219, v222, v223\n"
"v_mfma_f32_16x16x32_bf16 v[176:179], a[232:235], a[36:39], v[176:179]\n"
"v_exp_f32 v224, v224\n"
"v_mfma_f32_16x16x32_bf16 v[176:179], a[236:239], a[52:55], v[176:179]\n"
"v_add_f32 v247, v247, v224\n"
"v_mfma_f32_16x16x32_bf16 v[184:187], a[232:235], a[40:43], v[184:187]\n"
"ds_read_b128 a[252:255], v232 offset:57856\n"
"v_mfma_f32_16x16x32_bf16 v[184:187], a[236:239], a[56:59], v[184:187]\n"
"v_exp_f32 v225, v225\n"
"v_mfma_f32_16x16x32_bf16 v[192:195], a[232:235], a[44:47], v[192:195]\n"
"v_add_f32 v247, v247, v225\n"
"v_mfma_f32_16x16x32_bf16 v[192:195], a[236:239], a[60:63], v[192:195]\n"
"s_waitcnt lgkmcnt(2)\n"
"v_cvt_pk_bf16_f32 v224, v224, v225\n"
"v_mfma_f32_16x16x32_bf16 v[196:199], a[240:243], a[44:47], v[196:199]\n"
"ds_read_b128 a[232:235], v232 offset:59392\n"
"v_mfma_f32_16x16x32_bf16 v[196:199], a[244:247], a[60:63], v[196:199]\n"
"v_exp_f32 v226, v226\n"
"v_mfma_f32_16x16x32_bf16 v[188:191], a[240:243], a[40:43], v[188:191]\n"
"s_waitcnt vmcnt(20)\n"
"s_barrier\n"
"v_add_f32 v247, v247, v226\n"
"v_mfma_f32_16x16x32_bf16 v[188:191], a[244:247], a[56:59], v[188:191]\n"
"v_exp_f32 v227, v227\n"
"v_mfma_f32_16x16x32_bf16 v[180:183], a[240:243], a[36:39], v[180:183]\n"
"ds_read_b128 a[236:239], v232 offset:59904\n"
"v_add_u32 v242, 163840, v241\n"
"s_add_u32 m0, s71, 0\n"
"v_mfma_f32_16x16x32_bf16 v[180:183], a[244:247], a[52:55], v[180:183]\n"
"buffer_load_dwordx4 v242, s[44:47], 0 offen offset:0 sc0 sc1 nt lds\n"
"v_mfma_f32_16x16x32_bf16 v[172:175], a[240:243], a[32:35], v[172:175]\n"
"v_add_f32 v247, v247, v227\n"
"v_mfma_f32_16x16x32_bf16 v[172:175], a[244:247], a[48:51], v[172:175]\n"
"s_waitcnt lgkmcnt(2)\n"
"v_cvt_pk_bf16_f32 v225, v226, v227\n"
"v_mfma_f32_16x16x32_bf16 v[168:171], a[248:251], a[64:67], v[168:171]\n"
"ds_read_b128 a[240:243], v232 offset:61440\n"
"v_mfma_f32_16x16x32_bf16 v[168:171], a[252:255], a[80:83], v[168:171]\n"
"v_exp_f32 v228, v228\n"
"v_mfma_f32_16x16x32_bf16 v[176:179], a[248:251], a[68:71], v[176:179]\n"
"v_add_f32 v247, v247, v228\n"
"v_mfma_f32_16x16x32_bf16 v[176:179], a[252:255], a[84:87], v[176:179]\n"
"v_exp_f32 v229, v229\n"
"v_mfma_f32_16x16x32_bf16 v[184:187], a[248:251], a[72:75], v[184:187]\n"
"ds_read_b128 a[244:247], v232 offset:61952\n"
"v_add_f32 v247, v247, v229\n"
"v_mfma_f32_16x16x32_bf16 v[184:187], a[252:255], a[88:91], v[184:187]\n"
"v_cvt_pk_bf16_f32 v226, v228, v229\n"
"v_mfma_f32_16x16x32_bf16 v[192:195], a[248:251], a[76:79], v[192:195]\n"
"v_exp_f32 v230, v230\n"
"v_mfma_f32_16x16x32_bf16 v[192:195], a[252:255], a[92:95], v[192:195]\n"
"s_waitcnt lgkmcnt(2)\n"
"v_add_f32 v247, v247, v230\n"
"v_mfma_f32_16x16x32_bf16 v[196:199], a[232:235], a[76:79], v[196:199]\n"
"ds_read_b128 a[248:251], v232 offset:63488\n"
"v_mfma_f32_16x16x32_bf16 v[196:199], a[236:239], a[92:95], v[196:199]\n"
"v_exp_f32 v231, v231\n"
"v_mfma_f32_16x16x32_bf16 v[188:191], a[232:235], a[72:75], v[188:191]\n"
"v_add_f32 v247, v247, v231\n"
"v_mfma_f32_16x16x32_bf16 v[188:191], a[236:239], a[88:91], v[188:191]\n"
"v_cvt_pk_bf16_f32 v227, v230, v231\n"
"v_mfma_f32_16x16x32_bf16 v[180:183], a[232:235], a[68:71], v[180:183]\n"
"ds_read_b128 a[252:255], v232 offset:64000\n"
"v_mfma_f32_16x16x32_bf16 v[180:183], a[236:239], a[84:87], v[180:183]\n"
"v_mfma_f32_16x16x32_bf16 v[172:175], a[232:235], a[64:67], v[172:175]\n"
"buffer_load_dwordx4 v242, s[44:47], s61 offen offset:1024 sc0 sc1 nt lds\n"
"v_mfma_f32_16x16x32_bf16 v[172:175], a[236:239], a[80:83], v[172:175]\n"
"s_waitcnt lgkmcnt(2)\n"
"v_mfma_f32_16x16x32_bf16 v[192:195], a[240:243], a[108:111], v[192:195]\n"
"ds_read_b64_tr_b16 a[232:233], v237 offset:0\n"
"v_mfma_f32_16x16x32_bf16 v[192:195], a[244:247], a[124:127], v[192:195]\n"
"ds_read_b64_tr_b16 a[234:235], v237 offset:2048\n"
"v_mfma_f32_16x16x32_bf16 v[184:187], a[240:243], a[104:107], v[184:187]\n"
"v_mfma_f32_16x16x32_bf16 v[184:187], a[244:247], a[120:123], v[184:187]\n"
"v_mfma_f32_16x16x32_bf16 v[176:179], a[240:243], a[100:103], v[176:179]\n"
"ds_read_b64_tr_b16 a[236:237], v238 offset:0\n"
"v_mfma_f32_16x16x32_bf16 v[176:179], a[244:247], a[116:119], v[176:179]\n"
"ds_read_b64_tr_b16 a[238:239], v238 offset:2048\n"
"v_mfma_f32_16x16x32_bf16 v[168:171], a[240:243], a[96:99], v[168:171]\n"
"v_mfma_f32_16x16x32_bf16 v[168:171], a[244:247], a[112:115], v[168:171]\n"
"s_waitcnt lgkmcnt(4)\n"
"v_mfma_f32_16x16x32_bf16 v[172:175], a[248:251], a[96:99], v[172:175]\n"
"ds_read_b64_tr_b16 a[240:241], v237 offset:512\n"
"v_mfma_f32_16x16x32_bf16 v[172:175], a[252:255], a[112:115], v[172:175]\n"
"ds_read_b64_tr_b16 a[242:243], v237 offset:2560\n"
"v_mfma_f32_16x16x32_bf16 v[180:183], a[248:251], a[100:103], v[180:183]\n"
"v_mfma_f32_16x16x32_bf16 v[180:183], a[252:255], a[116:119], v[180:183]\n"
"v_mfma_f32_16x16x32_bf16 v[188:191], a[248:251], a[104:107], v[188:191]\n"
"ds_read_b64_tr_b16 a[244:245], v238 offset:512\n"
"v_mfma_f32_16x16x32_bf16 v[188:191], a[252:255], a[120:123], v[188:191]\n"
"ds_read_b64_tr_b16 a[246:247], v238 offset:2560\n"
"v_mfma_f32_16x16x32_bf16 v[196:199], a[248:251], a[108:111], v[196:199]\n"
"v_mfma_f32_16x16x32_bf16 v[196:199], a[252:255], a[124:127], v[196:199]\n"
"s_waitcnt lgkmcnt(4)\n"
"v_mfma_f32_16x16x32_bf16 v[0:3], a[232:235], v[200:203], v[0:3]\n"
"ds_read_b64_tr_b16 a[248:249], v237 offset:4096\n"
"v_mfma_f32_16x16x32_bf16 v[4:7], a[236:239], v[200:203], v[4:7]\n"
"ds_read_b64_tr_b16 a[250:251], v237 offset:6144\n"
"v_mfma_f32_16x16x32_bf16 v[8:11], a[232:235], v[208:211], v[8:11]\n"
"buffer_load_dwordx4 v242, s[44:47], s62 offen offset:2048 sc0 sc1 nt lds\n"
"v_mfma_f32_16x16x32_bf16 v[12:15], a[236:239], v[208:211], v[12:15]\n"
"v_exp_f32 v168, v168\n"
"v_mfma_f32_16x16x32_bf16 v[16:19], a[232:235], v[216:219], v[16:19]\n"
"ds_read_b64_tr_b16 a[252:253], v238 offset:4096\n"
"v_add_f32 v244, v244, v168\n"
"v_mfma_f32_16x16x32_bf16 v[20:23], a[236:239], v[216:219], v[20:23]\n"
"ds_read_b64_tr_b16 a[254:255], v238 offset:6144\n"
"v_mfma_f32_16x16x32_bf16 v[24:27], a[232:235], v[224:227], v[24:27]\n"
"v_exp_f32 v169, v169\n"
"v_mfma_f32_16x16x32_bf16 v[28:31], a[236:239], v[224:227], v[28:31]\n"
"s_waitcnt lgkmcnt(4)\n"
"v_add_f32 v244, v244, v169\n"
"v_cvt_pk_bf16_f32 v168, v168, v169\n"
"v_mfma_f32_16x16x32_bf16 v[56:59], a[240:243], v[224:227], v[56:59]\n"
"ds_read_b64_tr_b16 a[232:233], v237 offset:4608\n"
"v_mfma_f32_16x16x32_bf16 v[60:63], a[244:247], v[224:227], v[60:63]\n"
"ds_read_b64_tr_b16 a[234:235], v237 offset:6656\n"
"v_mfma_f32_16x16x32_bf16 v[48:51], a[240:243], v[216:219], v[48:51]\n"
"v_exp_f32 v170, v170\n"
"v_mfma_f32_16x16x32_bf16 v[52:55], a[244:247], v[216:219], v[52:55]\n"
"v_add_f32 v244, v244, v170\n"
"v_mfma_f32_16x16x32_bf16 v[40:43], a[240:243], v[208:211], v[40:43]\n"
"ds_read_b64_tr_b16 a[236:237], v238 offset:4608\n"
"v_mfma_f32_16x16x32_bf16 v[44:47], a[244:247], v[208:211], v[44:47]\n"
"ds_read_b64_tr_b16 a[238:239], v238 offset:6656\n"
"v_mfma_f32_16x16x32_bf16 v[32:35], a[240:243], v[200:203], v[32:35]\n"
"v_exp_f32 v171, v171\n"
"v_mfma_f32_16x16x32_bf16 v[36:39], a[244:247], v[200:203], v[36:39]\n"
"s_waitcnt lgkmcnt(4)\n"
"v_add_f32 v244, v244, v171\n"
"v_cvt_pk_bf16_f32 v169, v170, v171\n"
"v_mfma_f32_16x16x32_bf16 v[64:67], a[248:251], v[200:203], v[64:67]\n"
"ds_read_b64_tr_b16 a[240:241], v237 offset:8192\n"
"v_mfma_f32_16x16x32_bf16 v[68:71], a[252:255], v[200:203], v[68:71]\n"
"ds_read_b64_tr_b16 a[242:243], v237 offset:10240\n"
"v_mfma_f32_16x16x32_bf16 v[72:75], a[248:251], v[208:211], v[72:75]\n"
"buffer_load_dwordx4 v242, s[44:47], s63 offen offset:3072 sc0 sc1 nt lds\n"
"v_mfma_f32_16x16x32_bf16 v[76:79], a[252:255], v[208:211], v[76:79]\n"
"v_exp_f32 v172, v172\n"
"v_mfma_f32_16x16x32_bf16 v[80:83], a[248:251], v[216:219], v[80:83]\n"
"ds_read_b64_tr_b16 a[244:245], v238 offset:8192\n"
"v_add_f32 v244, v244, v172\n"
"v_mfma_f32_16x16x32_bf16 v[84:87], a[252:255], v[216:219], v[84:87]\n"
"ds_read_b64_tr_b16 a[246:247], v238 offset:10240\n"
"v_mfma_f32_16x16x32_bf16 v[88:91], a[248:251], v[224:227], v[88:91]\n"
"v_exp_f32 v173, v173\n"
"v_mfma_f32_16x16x32_bf16 v[92:95], a[252:255], v[224:227], v[92:95]\n"
"s_waitcnt lgkmcnt(4)\n"
"v_add_f32 v244, v244, v173\n"
"v_cvt_pk_bf16_f32 v170, v172, v173\n"
"v_mfma_f32_16x16x32_bf16 v[120:123], a[232:235], v[224:227], v[120:123]\n"
"ds_read_b64_tr_b16 a[248:249], v237 offset:8704\n"
"v_mfma_f32_16x16x32_bf16 v[124:127], a[236:239], v[224:227], v[124:127]\n"
"ds_read_b64_tr_b16 a[250:251], v237 offset:10752\n"
"v_mfma_f32_16x16x32_bf16 v[112:115], a[232:235], v[216:219], v[112:115]\n"
"s_waitcnt vmcnt(20)\n"
"s_barrier\n"
"v_mfma_f32_16x16x32_bf16 v[116:119], a[236:239], v[216:219], v[116:119]\n"
"v_exp_f32 v174, v174\n"
"v_mfma_f32_16x16x32_bf16 v[104:107], a[232:235], v[208:211], v[104:107]\n"
"ds_read_b64_tr_b16 a[252:253], v238 offset:8704\n"
"v_add_f32 v244, v244, v174\n"
"v_mfma_f32_16x16x32_bf16 v[108:111], a[236:239], v[208:211], v[108:111]\n"
"ds_read_b64_tr_b16 a[254:255], v238 offset:10752\n"
"v_mfma_f32_16x16x32_bf16 v[96:99], a[232:235], v[200:203], v[96:99]\n"
"v_exp_f32 v175, v175\n"
"v_mfma_f32_16x16x32_bf16 v[100:103], a[236:239], v[200:203], v[100:103]\n"
"s_waitcnt lgkmcnt(4)\n"
"v_add_f32 v244, v244, v175\n"
"v_cvt_pk_bf16_f32 v171, v174, v175\n"
"v_mfma_f32_16x16x32_bf16 v[128:131], a[240:243], v[200:203], v[128:131]\n"
"ds_read_b64_tr_b16 a[232:233], v237 offset:12288\n"
"v_mfma_f32_16x16x32_bf16 v[132:135], a[244:247], v[200:203], v[132:135]\n"
"ds_read_b64_tr_b16 a[234:235], v237 offset:14336\n"
"v_mfma_f32_16x16x32_bf16 v[136:139], a[240:243], v[208:211], v[136:139]\n"
"v_exp_f32 v176, v176\n"
"v_mfma_f32_16x16x32_bf16 v[140:143], a[244:247], v[208:211], v[140:143]\n"
"v_add_f32 v245, v245, v176\n"
"v_mfma_f32_16x16x32_bf16 v[144:147], a[240:243], v[216:219], v[144:147]\n"
"ds_read_b64_tr_b16 a[236:237], v238 offset:12288\n"
"v_mfma_f32_16x16x32_bf16 v[148:151], a[244:247], v[216:219], v[148:151]\n"
"ds_read_b64_tr_b16 a[238:239], v238 offset:14336\n"
"v_add_u32 v242, 196608, v241\n"
"s_add_u32 m0, s71, 16384\n"
"v_mfma_f32_16x16x32_bf16 a[128:131], a[240:243], v[224:227], a[128:131]\n"
"buffer_load_dwordx4 v242, s[40:43], 0 offen offset:0 sc0 sc1 nt lds\n"
"v_mfma_f32_16x16x32_bf16 a[132:135], a[244:247], v[224:227], a[132:135]\n"
"s_waitcnt lgkmcnt(4)\n"
"v_exp_f32 v177, v177\n"
"v_mfma_f32_16x16x32_bf16 a[160:163], a[248:251], v[224:227], a[160:163]\n"
"ds_read_b64_tr_b16 a[240:241], v237 offset:12800\n"
"v_add_f32 v245, v245, v177\n"
"v_mfma_f32_16x16x32_bf16 a[164:167], a[252:255], v[224:227], a[164:167]\n"
"ds_read_b64_tr_b16 a[242:243], v237 offset:14848\n"
"v_cvt_pk_bf16_f32 v176, v176, v177\n"
"v_mfma_f32_16x16x32_bf16 a[152:155], a[248:251], v[216:219], a[152:155]\n"
"v_exp_f32 v178, v178\n"
"v_mfma_f32_16x16x32_bf16 a[156:159], a[252:255], v[216:219], a[156:159]\n"
"v_add_f32 v245, v245, v178\n"
"v_mfma_f32_16x16x32_bf16 a[144:147], a[248:251], v[208:211], a[144:147]\n"
"ds_read_b64_tr_b16 a[244:245], v238 offset:12800\n"
"v_mfma_f32_16x16x32_bf16 a[148:151], a[252:255], v[208:211], a[148:151]\n"
"ds_read_b64_tr_b16 a[246:247], v238 offset:14848\n"
"v_mfma_f32_16x16x32_bf16 a[136:139], a[248:251], v[200:203], a[136:139]\n"
"v_exp_f32 v179, v179\n"
"v_mfma_f32_16x16x32_bf16 a[140:143], a[252:255], v[200:203], a[140:143]\n"
"s_waitcnt lgkmcnt(4)\n"
"v_add_f32 v245, v245, v179\n"
"v_cvt_pk_bf16_f32 v177, v178, v179\n"
"v_mfma_f32_16x16x32_bf16 a[168:171], a[232:235], v[200:203], a[168:171]\n"
"ds_read_b128 a[248:251], v233 offset:16384\n"
"v_mfma_f32_16x16x32_bf16 a[172:175], a[236:239], v[200:203], a[172:175]\n"
"v_exp_f32 v180, v180\n"
"v_mfma_f32_16x16x32_bf16 a[176:179], a[232:235], v[208:211], a[176:179]\n"
"v_add_f32 v245, v245, v180\n"
"v_mfma_f32_16x16x32_bf16 a[180:183], a[236:239], v[208:211], a[180:183]\n"
"v_exp_f32 v181, v181\n"
"v_mfma_f32_16x16x32_bf16 a[184:187], a[232:235], v[216:219], a[184:187]\n"
"ds_read_b128 a[252:255], v233 offset:16896\n"
"v_add_f32 v245, v245, v181\n"
"v_mfma_f32_16x16x32_bf16 a[188:191], a[236:239], v[216:219], a[188:191]\n"
"v_cvt_pk_bf16_f32 v178, v180, v181\n"
"v_mfma_f32_16x16x32_bf16 a[192:195], a[232:235], v[224:227], a[192:195]\n"
"buffer_load_dwordx4 v242, s[40:43], s61 offen offset:1024 sc0 sc1 nt lds\n"
"v_mfma_f32_16x16x32_bf16 a[196:199], a[236:239], v[224:227], a[196:199]\n"
"s_waitcnt lgkmcnt(2)\n"
"v_exp_f32 v182, v182\n"
"v_mfma_f32_16x16x32_bf16 a[200:203], a[240:243], v[200:203], a[200:203]\n"
"ds_read_b128 a[232:235], v233 offset:18432\n"
"v_add_f32 v245, v245, v182\n"
"v_mfma_f32_16x16x32_bf16 a[204:207], a[244:247], v[200:203], a[204:207]\n"
"v_exp_f32 v183, v183\n"
"v_mfma_f32_16x16x32_bf16 a[208:211], a[240:243], v[208:211], a[208:211]\n"
"v_add_f32 v245, v245, v183\n"
"v_cvt_pk_bf16_f32 v179, v182, v183\n"
"v_mfma_f32_16x16x32_bf16 a[212:215], a[244:247], v[208:211], a[212:215]\n"
"v_exp_f32 v184, v184\n"
"v_mfma_f32_16x16x32_bf16 a[216:219], a[240:243], v[216:219], a[216:219]\n"
"ds_read_b128 a[236:239], v233 offset:18944\n"
"v_add_f32 v246, v246, v184\n"
"v_mfma_f32_16x16x32_bf16 a[220:223], a[244:247], v[216:219], a[220:223]\n"
"v_exp_f32 v185, v185\n"
"v_mfma_f32_16x16x32_bf16 a[224:227], a[240:243], v[224:227], a[224:227]\n"
"v_add_f32 v246, v246, v185\n"
"v_cvt_pk_bf16_f32 v184, v184, v185\n"
"v_mfma_f32_16x16x32_bf16 a[228:231], a[244:247], v[224:227], a[228:231]\n"
"s_waitcnt lgkmcnt(2)\n"
"v_exp_f32 v186, v186\n"
"v_mfma_f32_16x16x32_bf16 v[200:203], a[248:251], a[0:3], v[152:155]\n"
"ds_read_b128 a[240:243], v233 offset:20480\n"
"v_mfma_f32_16x16x32_bf16 v[200:203], a[252:255], a[16:19], v[200:203]\n"
"v_add_f32 v246, v246, v186\n"
"v_mfma_f32_16x16x32_bf16 v[208:211], a[248:251], a[4:7], v[156:159]\n"
"v_exp_f32 v187, v187\n"
"v_mfma_f32_16x16x32_bf16 v[208:211], a[252:255], a[20:23], v[208:211]\n"
"v_add_f32 v246, v246, v187\n"
"v_mfma_f32_16x16x32_bf16 v[216:219], a[248:251], a[8:11], v[160:163]\n"
"ds_read_b128 a[244:247], v233 offset:20992\n"
"v_cvt_pk_bf16_f32 v185, v186, v187\n"
"v_mfma_f32_16x16x32_bf16 v[216:219], a[252:255], a[24:27], v[216:219]\n"
"v_exp_f32 v188, v188\n"
"v_mfma_f32_16x16x32_bf16 v[224:227], a[248:251], a[12:15], v[164:167]\n"
"v_add_f32 v246, v246, v188\n"
"v_mfma_f32_16x16x32_bf16 v[224:227], a[252:255], a[28:31], v[224:227]\n"
"s_waitcnt lgkmcnt(2)\n"
"v_exp_f32 v189, v189\n"
"v_mfma_f32_16x16x32_bf16 v[228:231], a[232:235], a[12:15], v[164:167]\n"
"ds_read_b128 a[248:251], v233 offset:22528\n"
"v_mfma_f32_16x16x32_bf16 v[228:231], a[236:239], a[28:31], v[228:231]\n"
"buffer_load_dwordx4 v242, s[40:43], s62 offen offset:2048 sc0 sc1 nt lds\n"
"v_mfma_f32_16x16x32_bf16 v[220:223], a[232:235], a[8:11], v[160:163]\n"
"v_add_f32 v246, v246, v189\n"
"v_mfma_f32_16x16x32_bf16 v[220:223], a[236:239], a[24:27], v[220:223]\n"
"v_cvt_pk_bf16_f32 v186, v188, v189\n"
"v_mfma_f32_16x16x32_bf16 v[212:215], a[232:235], a[4:7], v[156:159]\n"
"ds_read_b128 a[252:255], v233 offset:23040\n"
"v_mfma_f32_16x16x32_bf16 v[212:215], a[236:239], a[20:23], v[212:215]\n"
"v_exp_f32 v190, v190\n"
"v_mfma_f32_16x16x32_bf16 v[204:207], a[232:235], a[0:3], v[152:155]\n"
"v_add_f32 v246, v246, v190\n"
"v_mfma_f32_16x16x32_bf16 v[204:207], a[236:239], a[16:19], v[204:207]\n"
"s_waitcnt lgkmcnt(2)\n"
"v_exp_f32 v191, v191\n"
"v_mfma_f32_16x16x32_bf16 v[200:203], a[240:243], a[32:35], v[200:203]\n"
"ds_read_b128 a[232:235], v233 offset:24576\n"
"v_add_f32 v246, v246, v191\n"
"v_mfma_f32_16x16x32_bf16 v[200:203], a[244:247], a[48:51], v[200:203]\n"
"v_cvt_pk_bf16_f32 v187, v190, v191\n"
"v_mfma_f32_16x16x32_bf16 v[208:211], a[240:243], a[36:39], v[208:211]\n"
"v_exp_f32 v192, v192\n"
"v_mfma_f32_16x16x32_bf16 v[208:211], a[244:247], a[52:55], v[208:211]\n"
"v_add_f32 v247, v247, v192\n"
"v_mfma_f32_16x16x32_bf16 v[216:219], a[240:243], a[40:43], v[216:219]\n"
"ds_read_b128 a[236:239], v233 offset:25088\n"
"v_mfma_f32_16x16x32_bf16 v[216:219], a[244:247], a[56:59], v[216:219]\n"
"v_exp_f32 v193, v193\n"
"v_mfma_f32_16x16x32_bf16 v[224:227], a[240:243], a[44:47], v[224:227]\n"
"v_add_f32 v247, v247, v193\n"
"v_cvt_pk_bf16_f32 v192, v192, v193\n"
"v_mfma_f32_16x16x32_bf16 v[224:227], a[244:247], a[60:63], v[224:227]\n"
"s_waitcnt lgkmcnt(2)\n"
"v_exp_f32 v194, v194\n"
"v_mfma_f32_16x16x32_bf16 v[228:231], a[248:251], a[44:47], v[228:231]\n"
"ds_read_b128 a[240:243], v233 offset:26624\n"
"v_mfma_f32_16x16x32_bf16 v[228:231], a[252:255], a[60:63], v[228:231]\n"
"v_add_f32 v247, v247, v194\n"
"v_mfma_f32_16x16x32_bf16 v[220:223], a[248:251], a[40:43], v[220:223]\n"
"s_waitcnt vmcnt(19)\n"
"s_barrier\n"
"v_mfma_f32_16x16x32_bf16 v[220:223], a[252:255], a[56:59], v[220:223]\n"
"buffer_load_dwordx4 v242, s[40:43], s63 offen offset:3072 sc0 sc1 nt lds\n"
"v_mfma_f32_16x16x32_bf16 v[212:215], a[248:251], a[36:39], v[212:215]\n"
"ds_read_b128 a[244:247], v233 offset:27136\n"
"v_mfma_f32_16x16x32_bf16 v[212:215], a[252:255], a[52:55], v[212:215]\n"
"v_exp_f32 v195, v195\n"
"v_mfma_f32_16x16x32_bf16 v[204:207], a[248:251], a[32:35], v[204:207]\n"
"v_add_f32 v247, v247, v195\n"
"v_cvt_pk_bf16_f32 v193, v194, v195\n"
"v_mfma_f32_16x16x32_bf16 v[204:207], a[252:255], a[48:51], v[204:207]\n"
"s_waitcnt lgkmcnt(2)\n"
"v_exp_f32 v196, v196\n"
"v_mfma_f32_16x16x32_bf16 v[200:203], a[232:235], a[64:67], v[200:203]\n"
"ds_read_b128 a[248:251], v233 offset:28672\n"
"v_add_f32 v247, v247, v196\n"
"v_mfma_f32_16x16x32_bf16 v[200:203], a[236:239], a[80:83], v[200:203]\n"
"v_exp_f32 v197, v197\n"
"v_mfma_f32_16x16x32_bf16 v[208:211], a[232:235], a[68:71], v[208:211]\n"
"v_mfma_f32_16x16x32_bf16 v[208:211], a[236:239], a[84:87], v[208:211]\n"
"v_add_f32 v247, v247, v197\n"
"v_cvt_pk_bf16_f32 v194, v196, v197\n"
"v_mfma_f32_16x16x32_bf16 v[216:219], a[232:235], a[72:75], v[216:219]\n"
"ds_read_b128 a[252:255], v233 offset:29184\n"
"v_mfma_f32_16x16x32_bf16 v[216:219], a[236:239], a[88:91], v[216:219]\n"
"v_exp_f32 v198, v198\n"
"v_mfma_f32_16x16x32_bf16 v[224:227], a[232:235], a[76:79], v[224:227]\n"
"v_add_f32 v247, v247, v198\n"
"v_mfma_f32_16x16x32_bf16 v[224:227], a[236:239], a[92:95], v[224:227]\n"
"s_waitcnt lgkmcnt(2)\n"
"v_exp_f32 v199, v199\n"
"v_mfma_f32_16x16x32_bf16 v[228:231], a[240:243], a[76:79], v[228:231]\n"
"ds_read_b128 a[232:235], v233 offset:30720\n"
"v_mfma_f32_16x16x32_bf16 v[228:231], a[244:247], a[92:95], v[228:231]\n"
"v_add_f32 v247, v247, v199\n"
"v_cvt_pk_bf16_f32 v195, v198, v199\n"
"v_mfma_f32_16x16x32_bf16 v[220:223], a[240:243], a[72:75], v[220:223]\n"
"v_mfma_f32_16x16x32_bf16 v[220:223], a[244:247], a[88:91], v[220:223]\n"
"v_mfma_f32_16x16x32_bf16 v[212:215], a[240:243], a[68:71], v[212:215]\n"
"ds_read_b128 a[236:239], v233 offset:31232\n"
"v_add_u32 v242, 180224, v241\n"
"s_add_u32 m0, s71, 32768\n"
"v_mfma_f32_16x16x32_bf16 v[212:215], a[244:247], a[84:87], v[212:215]\n"
"buffer_load_dwordx4 v242, s[44:47], 0 offen offset:0 sc0 sc1 nt lds\n"
"v_mfma_f32_16x16x32_bf16 v[204:207], a[240:243], a[64:67], v[204:207]\n"
"v_mfma_f32_16x16x32_bf16 v[204:207], a[244:247], a[80:83], v[204:207]\n"
"s_waitcnt lgkmcnt(2)\n"
"v_mfma_f32_16x16x32_bf16 v[224:227], a[248:251], a[108:111], v[224:227]\n"
"ds_read_b64_tr_b16 a[240:241], v237 offset:32768\n"
"v_mfma_f32_16x16x32_bf16 v[224:227], a[252:255], a[124:127], v[224:227]\n"
"ds_read_b64_tr_b16 a[242:243], v237 offset:34816\n"
"v_mfma_f32_16x16x32_bf16 v[216:219], a[248:251], a[104:107], v[216:219]\n"
"v_mfma_f32_16x16x32_bf16 v[216:219], a[252:255], a[120:123], v[216:219]\n"
"v_mfma_f32_16x16x32_bf16 v[208:211], a[248:251], a[100:103], v[208:211]\n"
"ds_read_b64_tr_b16 a[244:245], v238 offset:32768\n"
"v_mfma_f32_16x16x32_bf16 v[208:211], a[252:255], a[116:119], v[208:211]\n"
"ds_read_b64_tr_b16 a[246:247], v238 offset:34816\n"
"v_mfma_f32_16x16x32_bf16 v[200:203], a[248:251], a[96:99], v[200:203]\n"
"v_mfma_f32_16x16x32_bf16 v[200:203], a[252:255], a[112:115], v[200:203]\n"
"s_waitcnt lgkmcnt(4)\n"
"v_mfma_f32_16x16x32_bf16 v[204:207], a[232:235], a[96:99], v[204:207]\n"
"ds_read_b64_tr_b16 a[248:249], v237 offset:33280\n"
"v_mfma_f32_16x16x32_bf16 v[204:207], a[236:239], a[112:115], v[204:207]\n"
"ds_read_b64_tr_b16 a[250:251], v237 offset:35328\n"
"v_mfma_f32_16x16x32_bf16 v[212:215], a[232:235], a[100:103], v[212:215]\n"
"v_mfma_f32_16x16x32_bf16 v[212:215], a[236:239], a[116:119], v[212:215]\n"
"v_mfma_f32_16x16x32_bf16 v[220:223], a[232:235], a[104:107], v[220:223]\n"
"ds_read_b64_tr_b16 a[252:253], v238 offset:33280\n"
"v_mfma_f32_16x16x32_bf16 v[220:223], a[236:239], a[120:123], v[220:223]\n"
"ds_read_b64_tr_b16 a[254:255], v238 offset:35328\n"
"v_mfma_f32_16x16x32_bf16 v[228:231], a[232:235], a[108:111], v[228:231]\n"
"buffer_load_dwordx4 v242, s[44:47], s61 offen offset:1024 sc0 sc1 nt lds\n"
"v_mfma_f32_16x16x32_bf16 v[228:231], a[236:239], a[124:127], v[228:231]\n"
"s_waitcnt lgkmcnt(4)\n"
"v_mfma_f32_16x16x32_bf16 v[0:3], a[240:243], v[168:171], v[0:3]\n"
"ds_read_b64_tr_b16 a[232:233], v237 offset:36864\n"
"v_mfma_f32_16x16x32_bf16 v[4:7], a[244:247], v[168:171], v[4:7]\n"
"ds_read_b64_tr_b16 a[234:235], v237 offset:38912\n"
"v_mfma_f32_16x16x32_bf16 v[8:11], a[240:243], v[176:179], v[8:11]\n"
"v_exp_f32 v200, v200\n"
"v_mfma_f32_16x16x32_bf16 v[12:15], a[244:247], v[176:179], v[12:15]\n"
"v_add_f32 v244, v244, v200\n"
"v_mfma_f32_16x16x32_bf16 v[16:19], a[240:243], v[184:187], v[16:19]\n"
"ds_read_b64_tr_b16 a[236:237], v238 offset:36864\n"
"v_mfma_f32_16x16x32_bf16 v[20:23], a[244:247], v[184:187], v[20:23]\n"
"ds_read_b64_tr_b16 a[238:239], v238 offset:38912\n"
"v_mfma_f32_16x16x32_bf16 v[24:27], a[240:243], v[192:195], v[24:27]\n"
"v_exp_f32 v201, v201\n"
"v_mfma_f32_16x16x32_bf16 v[28:31], a[244:247], v[192:195], v[28:31]\n"
"s_waitcnt lgkmcnt(4)\n"
"v_add_f32 v244, v244, v201\n"
"v_cvt_pk_bf16_f32 v200, v200, v201\n"
"v_mfma_f32_16x16x32_bf16 v[56:59], a[248:251], v[192:195], v[56:59]\n"
"ds_read_b64_tr_b16 a[240:241], v237 offset:37376\n"
"v_mfma_f32_16x16x32_bf16 v[60:63], a[252:255], v[192:195], v[60:63]\n"
"ds_read_b64_tr_b16 a[242:243], v237 offset:39424\n"
"v_mfma_f32_16x16x32_bf16 v[48:51], a[248:251], v[184:187], v[48:51]\n"
"v_exp_f32 v202, v202\n"
"v_mfma_f32_16x16x32_bf16 v[52:55], a[252:255], v[184:187], v[52:55]\n"
"v_add_f32 v244, v244, v202\n"
"v_mfma_f32_16x16x32_bf16 v[40:43], a[248:251], v[176:179], v[40:43]\n"
"ds_read_b64_tr_b16 a[244:245], v238 offset:37376\n"
"v_mfma_f32_16x16x32_bf16 v[44:47], a[252:255], v[176:179], v[44:47]\n"
"ds_read_b64_tr_b16 a[246:247], v238 offset:39424\n"
"v_mfma_f32_16x16x32_bf16 v[32:35], a[248:251], v[168:171], v[32:35]\n"
"v_exp_f32 v203, v203\n"
"v_mfma_f32_16x16x32_bf16 v[36:39], a[252:255], v[168:171], v[36:39]\n"
"s_waitcnt lgkmcnt(4)\n"
"v_add_f32 v244, v244, v203\n"
"v_cvt_pk_bf16_f32 v201, v202, v203\n"
"v_mfma_f32_16x16x32_bf16 v[64:67], a[232:235], v[168:171], v[64:67]\n"
"ds_read_b64_tr_b16 a[248:249], v237 offset:40960\n"
"v_mfma_f32_16x16x32_bf16 v[68:71], a[236:239], v[168:171], v[68:71]\n"
"ds_read_b64_tr_b16 a[250:251], v237 offset:43008\n"
"v_mfma_f32_16x16x32_bf16 v[72:75], a[232:235], v[176:179], v[72:75]\n"
"buffer_load_dwordx4 v242, s[44:47], s62 offen offset:2048 sc0 sc1 nt lds\n"
"v_mfma_f32_16x16x32_bf16 v[76:79], a[236:239], v[176:179], v[76:79]\n"
"v_exp_f32 v204, v204\n"
"v_mfma_f32_16x16x32_bf16 v[80:83], a[232:235], v[184:187], v[80:83]\n"
"ds_read_b64_tr_b16 a[252:253], v238 offset:40960\n"
"v_add_f32 v244, v244, v204\n"
"v_mfma_f32_16x16x32_bf16 v[84:87], a[236:239], v[184:187], v[84:87]\n"
"ds_read_b64_tr_b16 a[254:255], v238 offset:43008\n"
"v_mfma_f32_16x16x32_bf16 v[88:91], a[232:235], v[192:195], v[88:91]\n"
"v_exp_f32 v205, v205\n"
"v_mfma_f32_16x16x32_bf16 v[92:95], a[236:239], v[192:195], v[92:95]\n"
"s_waitcnt lgkmcnt(4)\n"
"v_add_f32 v244, v244, v205\n"
"v_cvt_pk_bf16_f32 v202, v204, v205\n"
"v_mfma_f32_16x16x32_bf16 v[120:123], a[240:243], v[192:195], v[120:123]\n"
"ds_read_b64_tr_b16 a[232:233], v237 offset:41472\n"
"v_mfma_f32_16x16x32_bf16 v[124:127], a[244:247], v[192:195], v[124:127]\n"
"ds_read_b64_tr_b16 a[234:235], v237 offset:43520\n"
"v_mfma_f32_16x16x32_bf16 v[112:115], a[240:243], v[184:187], v[112:115]\n"
"s_waitcnt vmcnt(19)\n"
"s_barrier\n"
"v_mfma_f32_16x16x32_bf16 v[116:119], a[244:247], v[184:187], v[116:119]\n"
"v_exp_f32 v206, v206\n"
"v_mfma_f32_16x16x32_bf16 v[104:107], a[240:243], v[176:179], v[104:107]\n"
"ds_read_b64_tr_b16 a[236:237], v238 offset:41472\n"
"v_add_f32 v244, v244, v206\n"
"v_mfma_f32_16x16x32_bf16 v[108:111], a[244:247], v[176:179], v[108:111]\n"
"ds_read_b64_tr_b16 a[238:239], v238 offset:43520\n"
"v_mfma_f32_16x16x32_bf16 v[96:99], a[240:243], v[168:171], v[96:99]\n"
"v_exp_f32 v207, v207\n"
"v_mfma_f32_16x16x32_bf16 v[100:103], a[244:247], v[168:171], v[100:103]\n"
"s_waitcnt lgkmcnt(4)\n"
"v_add_f32 v244, v244, v207\n"
"v_cvt_pk_bf16_f32 v203, v206, v207\n"
"v_mfma_f32_16x16x32_bf16 v[128:131], a[248:251], v[168:171], v[128:131]\n"
"ds_read_b64_tr_b16 a[240:241], v237 offset:45056\n"
"v_mfma_f32_16x16x32_bf16 v[132:135], a[252:255], v[168:171], v[132:135]\n"
"ds_read_b64_tr_b16 a[242:243], v237 offset:47104\n"
"v_mfma_f32_16x16x32_bf16 v[136:139], a[248:251], v[176:179], v[136:139]\n"
"buffer_load_dwordx4 v242, s[44:47], s63 offen offset:3072 sc0 sc1 nt lds\n"
"v_mfma_f32_16x16x32_bf16 v[140:143], a[252:255], v[176:179], v[140:143]\n"
"v_exp_f32 v208, v208\n"
"v_mfma_f32_16x16x32_bf16 v[144:147], a[248:251], v[184:187], v[144:147]\n"
"ds_read_b64_tr_b16 a[244:245], v238 offset:45056\n"
"v_add_f32 v245, v245, v208\n"
"v_mfma_f32_16x16x32_bf16 v[148:151], a[252:255], v[184:187], v[148:151]\n"
"ds_read_b64_tr_b16 a[246:247], v238 offset:47104\n"
"v_mfma_f32_16x16x32_bf16 a[128:131], a[248:251], v[192:195], a[128:131]\n"
"v_exp_f32 v209, v209\n"
"v_mfma_f32_16x16x32_bf16 a[132:135], a[252:255], v[192:195], a[132:135]\n"
"s_waitcnt lgkmcnt(4)\n"
"v_add_f32 v245, v245, v209\n"
"v_cvt_pk_bf16_f32 v208, v208, v209\n"
"v_mfma_f32_16x16x32_bf16 a[160:163], a[232:235], v[192:195], a[160:163]\n"
"ds_read_b64_tr_b16 a[248:249], v237 offset:45568\n"
"v_mfma_f32_16x16x32_bf16 a[164:167], a[236:239], v[192:195], a[164:167]\n"
"ds_read_b64_tr_b16 a[250:251], v237 offset:47616\n"
"v_mfma_f32_16x16x32_bf16 a[152:155], a[232:235], v[184:187], a[152:155]\n"
"v_exp_f32 v210, v210\n"
"v_mfma_f32_16x16x32_bf16 a[156:159], a[236:239], v[184:187], a[156:159]\n"
"v_add_f32 v245, v245, v210\n"
"v_mfma_f32_16x16x32_bf16 a[144:147], a[232:235], v[176:179], a[144:147]\n"
"ds_read_b64_tr_b16 a[252:253], v238 offset:45568\n"
"v_mfma_f32_16x16x32_bf16 a[148:151], a[236:239], v[176:179], a[148:151]\n"
"ds_read_b64_tr_b16 a[254:255], v238 offset:47616\n"
"v_mfma_f32_16x16x32_bf16 a[136:139], a[232:235], v[168:171], a[136:139]\n"
"v_exp_f32 v211, v211\n"
"v_mfma_f32_16x16x32_bf16 a[140:143], a[236:239], v[168:171], a[140:143]\n"
"s_waitcnt lgkmcnt(4)\n"
"v_add_f32 v245, v245, v211\n"
"v_cvt_pk_bf16_f32 v209, v210, v211\n"
"v_mfma_f32_16x16x32_bf16 a[168:171], a[240:243], v[168:171], a[168:171]\n"
"ds_read_b128 a[232:235], v233 offset:49152\n"
"v_mfma_f32_16x16x32_bf16 a[172:175], a[244:247], v[168:171], a[172:175]\n"
"v_exp_f32 v212, v212\n"
"v_mfma_f32_16x16x32_bf16 a[176:179], a[240:243], v[176:179], a[176:179]\n"
"v_add_f32 v245, v245, v212\n"
"v_mfma_f32_16x16x32_bf16 a[180:183], a[244:247], v[176:179], a[180:183]\n"
"v_exp_f32 v213, v213\n"
"v_mfma_f32_16x16x32_bf16 a[184:187], a[240:243], v[184:187], a[184:187]\n"
"ds_read_b128 a[236:239], v233 offset:49664\n"
"v_add_u32 v242, 212992, v241\n"
"s_add_u32 m0, s71, 49152\n"
"v_mfma_f32_16x16x32_bf16 a[188:191], a[244:247], v[184:187], a[188:191]\n"
"buffer_load_dwordx4 v242, s[40:43], 0 offen offset:0 sc0 sc1 nt lds\n"
"v_mfma_f32_16x16x32_bf16 a[192:195], a[240:243], v[192:195], a[192:195]\n"
"v_add_f32 v245, v245, v213\n"
"v_cvt_pk_bf16_f32 v210, v212, v213\n"
"v_mfma_f32_16x16x32_bf16 a[196:199], a[244:247], v[192:195], a[196:199]\n"
"s_waitcnt lgkmcnt(2)\n"
"v_exp_f32 v214, v214\n"
"v_mfma_f32_16x16x32_bf16 a[200:203], a[248:251], v[168:171], a[200:203]\n"
"ds_read_b128 a[240:243], v233 offset:51200\n"
"v_add_f32 v245, v245, v214\n"
"v_mfma_f32_16x16x32_bf16 a[204:207], a[252:255], v[168:171], a[204:207]\n"
"v_exp_f32 v215, v215\n"
"v_mfma_f32_16x16x32_bf16 a[208:211], a[248:251], v[176:179], a[208:211]\n"
"v_add_f32 v245, v245, v215\n"
"v_cvt_pk_bf16_f32 v211, v214, v215\n"
"v_mfma_f32_16x16x32_bf16 a[212:215], a[252:255], v[176:179], a[212:215]\n"
"v_exp_f32 v216, v216\n"
"v_mfma_f32_16x16x32_bf16 a[216:219], a[248:251], v[184:187], a[216:219]\n"
"ds_read_b128 a[244:247], v233 offset:51712\n"
"v_add_f32 v246, v246, v216\n"
"v_mfma_f32_16x16x32_bf16 a[220:223], a[252:255], v[184:187], a[220:223]\n"
"v_exp_f32 v217, v217\n"
"v_mfma_f32_16x16x32_bf16 a[224:227], a[248:251], v[192:195], a[224:227]\n"
"v_add_f32 v246, v246, v217\n"
"v_cvt_pk_bf16_f32 v216, v216, v217\n"
"v_mfma_f32_16x16x32_bf16 a[228:231], a[252:255], v[192:195], a[228:231]\n"
"s_waitcnt lgkmcnt(2)\n"
"v_exp_f32 v218, v218\n"
"v_mfma_f32_16x16x32_bf16 v[168:171], a[232:235], a[0:3], v[152:155]\n"
"ds_read_b128 a[248:251], v233 offset:53248\n"
"v_add_f32 v246, v246, v218\n"
"v_mfma_f32_16x16x32_bf16 v[168:171], a[236:239], a[16:19], v[168:171]\n"
"s_min_u32 s83, %[g], 6\n"
"s_lshl_b32 s38, s83, 7\n"
"v_mfma_f32_16x16x32_bf16 v[176:179], a[232:235], a[4:7], v[156:159]\n"
"s_add_u32 s32, s38, 128\n"
"s_cmp_eq_u32 s32, 896\n"
"s_cselect_b32 s32, 0, s32\n"
"v_mfma_f32_16x16x32_bf16 v[176:179], a[236:239], a[20:23], v[176:179]\n"
"s_add_u32 s33, s32, 128\n"
"s_cmp_eq_u32 s33, 896\n"
"s_cselect_b32 s33, 0, s33\n"
"v_mfma_f32_16x16x32_bf16 v[184:187], a[232:235], a[8:11], v[160:163]\n"
"ds_read_b128 a[252:255], v233 offset:53760\n"
"v_mfma_f32_16x16x32_bf16 v[184:187], a[236:239], a[24:27], v[184:187]\n"
"v_exp_f32 v219, v219\n"
"v_mfma_f32_16x16x32_bf16 v[192:195], a[232:235], a[12:15], v[164:167]\n"
"buffer_load_dwordx4 v242, s[40:43], s61 offen offset:1024 sc0 sc1 nt lds\n"
"v_mfma_f32_16x16x32_bf16 v[192:195], a[236:239], a[28:31], v[192:195]\n"
"s_waitcnt lgkmcnt(2)\n"
"s_add_u32 s34, s33, 128\n"
"s_cmp_eq_u32 s34, 896\n"
"s_cselect_b32 s34, 0, s34\n"
"v_mfma_f32_16x16x32_bf16 v[196:199], a[240:243], a[12:15], v[164:167]\n"
"ds_read_b128 a[232:235], v233 offset:55296\n"
"v_add_f32 v246, v246, v219\n"
"v_mfma_f32_16x16x32_bf16 v[196:199], a[244:247], a[28:31], v[196:199]\n"
"v_cvt_pk_bf16_f32 v217, v218, v219\n"
"v_mfma_f32_16x16x32_bf16 v[188:191], a[240:243], a[8:11], v[160:163]\n"
"s_add_u32 s35, s34, 128\n"
"s_cmp_eq_u32 s35, 896\n"
"s_cselect_b32 s35, 0, s35\n"
"v_mfma_f32_16x16x32_bf16 v[188:191], a[244:247], a[24:27], v[188:191]\n"
"v_exp_f32 v220, v220\n"
"v_mfma_f32_16x16x32_bf16 v[180:183], a[240:243], a[4:7], v[156:159]\n"
"ds_read_b128 a[236:239], v233 offset:55808\n"
"v_add_f32 v246, v246, v220\n"
"v_mfma_f32_16x16x32_bf16 v[180:183], a[244:247], a[20:23], v[180:183]\n"
"s_add_u32 s36, s35, 128\n"
"s_cmp_eq_u32 s36, 896\n"
"s_cselect_b32 s36, 0, s36\n"
"v_mfma_f32_16x16x32_bf16 v[172:175], a[240:243], a[0:3], v[152:155]\n"
"v_exp_f32 v221, v221\n"
"v_mfma_f32_16x16x32_bf16 v[172:175], a[244:247], a[16:19], v[172:175]\n"
"s_waitcnt lgkmcnt(2)\n"
"v_add_f32 v246, v246, v221\n"
"v_cvt_pk_bf16_f32 v218, v220, v221\n"
"v_mfma_f32_16x16x32_bf16 v[168:171], a[248:251], a[32:35], v[168:171]\n"
"ds_read_b128 a[240:243], v233 offset:57344\n"
"s_add_u32 s37, s36, 128\n"
"s_cmp_eq_u32 s37, 896\n"
"s_cselect_b32 s37, 0, s37\n"
"v_mfma_f32_16x16x32_bf16 v[168:171], a[252:255], a[48:51], v[168:171]\n"
"v_exp_f32 v222, v222\n"
"v_mfma_f32_16x16x32_bf16 v[176:179], a[248:251], a[36:39], v[176:179]\n"
"v_add_f32 v246, v246, v222\n"
"v_mfma_f32_16x16x32_bf16 v[176:179], a[252:255], a[52:55], v[176:179]\n"
"v_exp_f32 v223, v223\n"
"v_mfma_f32_16x16x32_bf16 v[184:187], a[248:251], a[40:43], v[184:187]\n"
"ds_read_b128 a[244:247], v233 offset:57856\n"
"v_add_f32 v246, v246, v223\n"
"v_mfma_f32_16x16x32_bf16 v[184:187], a[252:255], a[56:59], v[184:187]\n"
"v_cvt_pk_bf16_f32 v219, v222, v223\n"
"v_mfma_f32_16x16x32_bf16 v[192:195], a[248:251], a[44:47], v[192:195]\n"
"v_exp_f32 v224, v224\n"
"v_mfma_f32_16x16x32_bf16 v[192:195], a[252:255], a[60:63], v[192:195]\n"
"s_waitcnt lgkmcnt(2)\n"
"v_add_f32 v247, v247, v224\n"
"v_mfma_f32_16x16x32_bf16 v[196:199], a[232:235], a[44:47], v[196:199]\n"
"ds_read_b128 a[248:251], v233 offset:59392\n"
"v_mfma_f32_16x16x32_bf16 v[196:199], a[236:239], a[60:63], v[196:199]\n"
"buffer_load_dwordx4 v242, s[40:43], s62 offen offset:2048 sc0 sc1 nt lds\n"
"v_mfma_f32_16x16x32_bf16 v[188:191], a[232:235], a[40:43], v[188:191]\n"
"s_waitcnt vmcnt(19)\n"
"s_barrier\n"
"v_mfma_f32_16x16x32_bf16 v[188:191], a[236:239], a[56:59], v[188:191]\n"
"v_exp_f32 v225, v225\n"
"v_mfma_f32_16x16x32_bf16 v[180:183], a[232:235], a[36:39], v[180:183]\n"
"ds_read_b128 a[252:255], v233 offset:59904\n"
"v_add_f32 v247, v247, v225\n"
"v_mfma_f32_16x16x32_bf16 v[180:183], a[236:239], a[52:55], v[180:183]\n"
"v_cvt_pk_bf16_f32 v224, v224, v225\n"
"v_mfma_f32_16x16x32_bf16 v[172:175], a[232:235], a[32:35], v[172:175]\n"
"v_exp_f32 v226, v226\n"
"v_mfma_f32_16x16x32_bf16 v[172:175], a[236:239], a[48:51], v[172:175]\n"
"s_waitcnt lgkmcnt(2)\n"
"v_add_f32 v247, v247, v226\n"
"v_mfma_f32_16x16x32_bf16 v[168:171], a[240:243], a[64:67], v[168:171]\n"
"ds_read_b128 a[232:235], v233 offset:61440\n"
"v_mfma_f32_16x16x32_bf16 v[168:171], a[244:247], a[80:83], v[168:171]\n"
"v_exp_f32 v227, v227\n"
"v_mfma_f32_16x16x32_bf16 v[176:179], a[240:243], a[68:71], v[176:179]\n"
"v_add_f32 v247, v247, v227\n"
"v_cvt_pk_bf16_f32 v225, v226, v227\n"
"v_mfma_f32_16x16x32_bf16 v[176:179], a[244:247], a[84:87], v[176:179]\n"
"v_exp_f32 v228, v228\n"
"v_mfma_f32_16x16x32_bf16 v[184:187], a[240:243], a[72:75], v[184:187]\n"
"ds_read_b128 a[236:239], v233 offset:61952\n"
"v_add_f32 v247, v247, v228\n"
"v_mfma_f32_16x16x32_bf16 v[184:187], a[244:247], a[88:91], v[184:187]\n"
"v_exp_f32 v229, v229\n"
"v_mfma_f32_16x16x32_bf16 v[192:195], a[240:243], a[76:79], v[192:195]\n"
"v_add_f32 v247, v247, v229\n"
"v_cvt_pk_bf16_f32 v226, v228, v229\n"
"v_mfma_f32_16x16x32_bf16 v[192:195], a[244:247], a[92:95], v[192:195]\n"
"s_waitcnt lgkmcnt(2)\n"
"v_exp_f32 v230, v230\n"
"v_mfma_f32_16x16x32_bf16 v[196:199], a[248:251], a[76:79], v[196:199]\n"
"ds_read_b128 a[240:243], v233 offset:63488\n"
"v_add_f32 v247, v247, v230\n"
"v_mfma_f32_16x16x32_bf16 v[196:199], a[252:255], a[92:95], v[196:199]\n"
"v_exp_f32 v231, v231\n"
"v_mfma_f32_16x16x32_bf16 v[188:191], a[248:251], a[72:75], v[188:191]\n"
"buffer_load_dwordx4 v242, s[40:43], s63 offen offset:3072 sc0 sc1 nt lds\n"
"v_mfma_f32_16x16x32_bf16 v[188:191], a[252:255], a[88:91], v[188:191]\n"
"v_add_f32 v247, v247, v231\n"
"v_cvt_pk_bf16_f32 v227, v230, v231\n"
"v_mfma_f32_16x16x32_bf16 v[180:183], a[248:251], a[68:71], v[180:183]\n"
"ds_read_b128 a[244:247], v233 offset:64000\n"
"v_mfma_f32_16x16x32_bf16 v[180:183], a[252:255], a[84:87], v[180:183]\n"
"v_mfma_f32_16x16x32_bf16 v[172:175], a[248:251], a[64:67], v[172:175]\n"
"v_mfma_f32_16x16x32_bf16 v[172:175], a[252:255], a[80:83], v[172:175]\n"
"s_waitcnt lgkmcnt(2)\n"
"v_mfma_f32_16x16x32_bf16 v[192:195], a[232:235], a[108:111], v[192:195]\n"
"ds_read_b64_tr_b16 a[248:249], v239 offset:0\n"
"v_mfma_f32_16x16x32_bf16 v[192:195], a[236:239], a[124:127], v[192:195]\n"
"ds_read_b64_tr_b16 a[250:251], v239 offset:2048\n"
"v_mfma_f32_16x16x32_bf16 v[184:187], a[232:235], a[104:107], v[184:187]\n"
"v_mfma_f32_16x16x32_bf16 v[184:187], a[236:239], a[120:123], v[184:187]\n"
"v_mfma_f32_16x16x32_bf16 v[176:179], a[232:235], a[100:103], v[176:179]\n"
"ds_read_b64_tr_b16 a[252:253], v240 offset:0\n"
"v_mfma_f32_16x16x32_bf16 v[176:179], a[236:239], a[116:119], v[176:179]\n"
"ds_read_b64_tr_b16 a[254:255], v240 offset:2048\n"
"v_mfma_f32_16x16x32_bf16 v[168:171], a[232:235], a[96:99], v[168:171]\n"
"v_mfma_f32_16x16x32_bf16 v[168:171], a[236:239], a[112:115], v[168:171]\n"
"s_waitcnt lgkmcnt(4)\n"
"v_mfma_f32_16x16x32_bf16 v[172:175], a[240:243], a[96:99], v[172:175]\n"
"ds_read_b64_tr_b16 a[232:233], v239 offset:512\n"
"v_mfma_f32_16x16x32_bf16 v[172:175], a[244:247], a[112:115], v[172:175]\n"
"ds_read_b64_tr_b16 a[234:235], v239 offset:2560\n"
"v_mfma_f32_16x16x32_bf16 v[180:183], a[240:243], a[100:103], v[180:183]\n"
"v_mfma_f32_16x16x32_bf16 v[180:183], a[244:247], a[116:119], v[180:183]\n"
"v_mfma_f32_16x16x32_bf16 v[188:191], a[240:243], a[104:107], v[188:191]\n"
"ds_read_b64_tr_b16 a[236:237], v240 offset:512\n"
"v_mfma_f32_16x16x32_bf16 v[188:191], a[244:247], a[120:123], v[188:191]\n"
"ds_read_b64_tr_b16 a[238:239], v240 offset:2560\n"
"v_add_u32 v242, 196608, v241\n"
"s_add_u32 m0, s71, 65536\n"
"v_mfma_f32_16x16x32_bf16 v[196:199], a[240:243], a[108:111], v[196:199]\n"
"buffer_load_dwordx4 v242, s[44:47], 0 offen offset:0 sc0 sc1 nt lds\n"
"v_mfma_f32_16x16x32_bf16 v[196:199], a[244:247], a[124:127], v[196:199]\n"
"s_waitcnt lgkmcnt(4)\n"
"v_mfma_f32_16x16x32_bf16 v[0:3], a[248:251], v[200:203], v[0:3]\n"
"ds_read_b64_tr_b16 a[240:241], v239 offset:4096\n"
"v_mfma_f32_16x16x32_bf16 v[4:7], a[252:255], v[200:203], v[4:7]\n"
"ds_read_b64_tr_b16 a[242:243], v239 offset:6144\n"
"v_mfma_f32_16x16x32_bf16 v[8:11], a[248:251], v[208:211], v[8:11]\n"
"v_exp_f32 v168, v168\n"
"v_mfma_f32_16x16x32_bf16 v[12:15], a[252:255], v[208:211], v[12:15]\n"
"v_add_f32 v244, v244, v168\n"
"v_mfma_f32_16x16x32_bf16 v[16:19], a[248:251], v[216:219], v[16:19]\n"
"ds_read_b64_tr_b16 a[244:245], v240 offset:4096\n"
"v_mfma_f32_16x16x32_bf16 v[20:23], a[252:255], v[216:219], v[20:23]\n"
"ds_read_b64_tr_b16 a[246:247], v240 offset:6144\n"
"v_mfma_f32_16x16x32_bf16 v[24:27], a[248:251], v[224:227], v[24:27]\n"
"v_exp_f32 v169, v169\n"
"v_mfma_f32_16x16x32_bf16 v[28:31], a[252:255], v[224:227], v[28:31]\n"
"s_waitcnt lgkmcnt(4)\n"
"v_add_f32 v244, v244, v169\n"
"v_cvt_pk_bf16_f32 v168, v168, v169\n"
"v_mfma_f32_16x16x32_bf16 v[56:59], a[232:235], v[224:227], v[56:59]\n"
"ds_read_b64_tr_b16 a[248:249], v239 offset:4608\n"
"v_mfma_f32_16x16x32_bf16 v[60:63], a[236:239], v[224:227], v[60:63]\n"
"ds_read_b64_tr_b16 a[250:251], v239 offset:6656\n"
"v_mfma_f32_16x16x32_bf16 v[48:51], a[232:235], v[216:219], v[48:51]\n"
"v_exp_f32 v170, v170\n"
"v_mfma_f32_16x16x32_bf16 v[52:55], a[236:239], v[216:219], v[52:55]\n"
"v_add_f32 v244, v244, v170\n"
"v_mfma_f32_16x16x32_bf16 v[40:43], a[232:235], v[208:211], v[40:43]\n"
"ds_read_b64_tr_b16 a[252:253], v240 offset:4608\n"
"v_mfma_f32_16x16x32_bf16 v[44:47], a[236:239], v[208:211], v[44:47]\n"
"ds_read_b64_tr_b16 a[254:255], v240 offset:6656\n"
"v_mfma_f32_16x16x32_bf16 v[32:35], a[232:235], v[200:203], v[32:35]\n"
"buffer_load_dwordx4 v242, s[44:47], s61 offen offset:1024 sc0 sc1 nt lds\n"
"v_mfma_f32_16x16x32_bf16 v[36:39], a[236:239], v[200:203], v[36:39]\n"
"s_waitcnt lgkmcnt(4)\n"
"v_exp_f32 v171, v171\n"
"v_mfma_f32_16x16x32_bf16 v[64:67], a[240:243], v[200:203], v[64:67]\n"
"ds_read_b64_tr_b16 a[232:233], v239 offset:8192\n"
"v_add_f32 v244, v244, v171\n"
"v_mfma_f32_16x16x32_bf16 v[68:71], a[244:247], v[200:203], v[68:71]\n"
"ds_read_b64_tr_b16 a[234:235], v239 offset:10240\n"
"v_cvt_pk_bf16_f32 v169, v170, v171\n"
"v_mfma_f32_16x16x32_bf16 v[72:75], a[240:243], v[208:211], v[72:75]\n"
"v_exp_f32 v172, v172\n"
"v_mfma_f32_16x16x32_bf16 v[76:79], a[244:247], v[208:211], v[76:79]\n"
"v_add_f32 v244, v244, v172\n"
"v_mfma_f32_16x16x32_bf16 v[80:83], a[240:243], v[216:219], v[80:83]\n"
"ds_read_b64_tr_b16 a[236:237], v240 offset:8192\n"
"v_mfma_f32_16x16x32_bf16 v[84:87], a[244:247], v[216:219], v[84:87]\n"
"ds_read_b64_tr_b16 a[238:239], v240 offset:10240\n"
"v_mfma_f32_16x16x32_bf16 v[88:91], a[240:243], v[224:227], v[88:91]\n"
"v_exp_f32 v173, v173\n"
"v_mfma_f32_16x16x32_bf16 v[92:95], a[244:247], v[224:227], v[92:95]\n"
"s_waitcnt lgkmcnt(4)\n"
"v_add_f32 v244, v244, v173\n"
"v_cvt_pk_bf16_f32 v170, v172, v173\n"
"v_mfma_f32_16x16x32_bf16 v[120:123], a[248:251], v[224:227], v[120:123]\n"
"ds_read_b64_tr_b16 a[240:241], v239 offset:8704\n"
"v_mfma_f32_16x16x32_bf16 v[124:127], a[252:255], v[224:227], v[124:127]\n"
"ds_read_b64_tr_b16 a[242:243], v239 offset:10752\n"
"v_mfma_f32_16x16x32_bf16 v[112:115], a[248:251], v[216:219], v[112:115]\n"
"s_waitcnt vmcnt(18)\n"
"s_barrier\n"
"v_mfma_f32_16x16x32_bf16 v[116:119], a[252:255], v[216:219], v[116:119]\n"
"v_exp_f32 v174, v174\n"
"v_mfma_f32_16x16x32_bf16 v[104:107], a[248:251], v[208:211], v[104:107]\n"
"ds_read_b64_tr_b16 a[244:245], v240 offset:8704\n"
"v_add_f32 v244, v244, v174\n"
"v_mfma_f32_16x16x32_bf16 v[108:111], a[252:255], v[208:211], v[108:111]\n"
"ds_read_b64_tr_b16 a[246:247], v240 offset:10752\n"
"v_mfma_f32_16x16x32_bf16 v[96:99], a[248:251], v[200:203], v[96:99]\n"
"v_exp_f32 v175, v175\n"
"v_mfma_f32_16x16x32_bf16 v[100:103], a[252:255], v[200:203], v[100:103]\n"
"s_waitcnt lgkmcnt(4)\n"
"v_add_f32 v244, v244, v175\n"
"v_cvt_pk_bf16_f32 v171, v174, v175\n"
"v_mfma_f32_16x16x32_bf16 v[128:131], a[232:235], v[200:203], v[128:131]\n"
"ds_read_b64_tr_b16 a[248:249], v239 offset:12288\n"
"v_mfma_f32_16x16x32_bf16 v[132:135], a[236:239], v[200:203], v[132:135]\n"
"ds_read_b64_tr_b16 a[250:251], v239 offset:14336\n"
"v_mfma_f32_16x16x32_bf16 v[136:139], a[232:235], v[208:211], v[136:139]\n"
"buffer_load_dwordx4 v242, s[44:47], s62 offen offset:2048 sc0 sc1 nt lds\n"
"v_mfma_f32_16x16x32_bf16 v[140:143], a[236:239], v[208:211], v[140:143]\n"
"v_exp_f32 v176, v176\n"
"v_mfma_f32_16x16x32_bf16 v[144:147], a[232:235], v[216:219], v[144:147]\n"
"ds_read_b64_tr_b16 a[252:253], v240 offset:12288\n"
"v_add_f32 v245, v245, v176\n"
"v_mfma_f32_16x16x32_bf16 v[148:151], a[236:239], v[216:219], v[148:151]\n"
"ds_read_b64_tr_b16 a[254:255], v240 offset:14336\n"
"v_mfma_f32_16x16x32_bf16 a[128:131], a[232:235], v[224:227], a[128:131]\n"
"v_exp_f32 v177, v177\n"
"v_mfma_f32_16x16x32_bf16 a[132:135], a[236:239], v[224:227], a[132:135]\n"
"s_waitcnt lgkmcnt(4)\n"
"v_add_f32 v245, v245, v177\n"
"v_cvt_pk_bf16_f32 v176, v176, v177\n"
"v_mfma_f32_16x16x32_bf16 a[160:163], a[240:243], v[224:227], a[160:163]\n"
"ds_read_b64_tr_b16 a[232:233], v239 offset:12800\n"
"v_mfma_f32_16x16x32_bf16 a[164:167], a[244:247], v[224:227], a[164:167]\n"
"ds_read_b64_tr_b16 a[234:235], v239 offset:14848\n"
"v_mfma_f32_16x16x32_bf16 a[152:155], a[240:243], v[216:219], a[152:155]\n"
"v_exp_f32 v178, v178\n"
"v_mfma_f32_16x16x32_bf16 a[156:159], a[244:247], v[216:219], a[156:159]\n"
"v_add_f32 v245, v245, v178\n"
"v_mfma_f32_16x16x32_bf16 a[144:147], a[240:243], v[208:211], a[144:147]\n"
"ds_read_b64_tr_b16 a[236:237], v240 offset:12800\n"
"v_mfma_f32_16x16x32_bf16 a[148:151], a[244:247], v[208:211], a[148:151]\n"
"ds_read_b64_tr_b16 a[238:239], v240 offset:14848\n"
"v_mfma_f32_16x16x32_bf16 a[136:139], a[240:243], v[200:203], a[136:139]\n"
"v_exp_f32 v179, v179\n"
"v_mfma_f32_16x16x32_bf16 a[140:143], a[244:247], v[200:203], a[140:143]\n"
"s_waitcnt lgkmcnt(4)\n"
"v_add_f32 v245, v245, v179\n"
"v_cvt_pk_bf16_f32 v177, v178, v179\n"
"v_mfma_f32_16x16x32_bf16 a[168:171], a[248:251], v[200:203], a[168:171]\n"
"ds_read_b128 a[240:243], v234 offset:16384\n"
"v_mfma_f32_16x16x32_bf16 a[172:175], a[252:255], v[200:203], a[172:175]\n"
"v_exp_f32 v180, v180\n"
"v_mfma_f32_16x16x32_bf16 a[176:179], a[248:251], v[208:211], a[176:179]\n"
"buffer_load_dwordx4 v242, s[44:47], s63 offen offset:3072 sc0 sc1 nt lds\n"
"v_mfma_f32_16x16x32_bf16 a[180:183], a[252:255], v[208:211], a[180:183]\n"
"v_add_f32 v245, v245, v180\n"
"v_mfma_f32_16x16x32_bf16 a[184:187], a[248:251], v[216:219], a[184:187]\n"
"ds_read_b128 a[244:247], v234 offset:16896\n"
"v_mfma_f32_16x16x32_bf16 a[188:191], a[252:255], v[216:219], a[188:191]\n"
"v_exp_f32 v181, v181\n"
"v_mfma_f32_16x16x32_bf16 a[192:195], a[248:251], v[224:227], a[192:195]\n"
"v_add_f32 v245, v245, v181\n"
"v_cvt_pk_bf16_f32 v178, v180, v181\n"
"v_mfma_f32_16x16x32_bf16 a[196:199], a[252:255], v[224:227], a[196:199]\n"
"s_waitcnt lgkmcnt(2)\n"
"v_exp_f32 v182, v182\n"
"v_mfma_f32_16x16x32_bf16 a[200:203], a[232:235], v[200:203], a[200:203]\n"
"ds_read_b128 a[248:251], v234 offset:18432\n"
"v_add_f32 v245, v245, v182\n"
"v_mfma_f32_16x16x32_bf16 a[204:207], a[236:239], v[200:203], a[204:207]\n"
"v_exp_f32 v183, v183\n"
"v_mfma_f32_16x16x32_bf16 a[208:211], a[232:235], v[208:211], a[208:211]\n"
"v_add_f32 v245, v245, v183\n"
"v_cvt_pk_bf16_f32 v179, v182, v183\n"
"v_mfma_f32_16x16x32_bf16 a[212:215], a[236:239], v[208:211], a[212:215]\n"
"v_exp_f32 v184, v184\n"
"v_mfma_f32_16x16x32_bf16 a[216:219], a[232:235], v[216:219], a[216:219]\n"
"ds_read_b128 a[252:255], v234 offset:18944\n"
"v_add_f32 v246, v246, v184\n"
"v_mfma_f32_16x16x32_bf16 a[220:223], a[236:239], v[216:219], a[220:223]\n"
"v_exp_f32 v185, v185\n"
"v_mfma_f32_16x16x32_bf16 a[224:227], a[232:235], v[224:227], a[224:227]\n"
"v_add_f32 v246, v246, v185\n"
"v_cvt_pk_bf16_f32 v184, v184, v185\n"
"v_mfma_f32_16x16x32_bf16 a[228:231], a[236:239], v[224:227], a[228:231]\n"
"s_waitcnt lgkmcnt(2)\n"
"v_exp_f32 v186, v186\n"
"v_mfma_f32_16x16x32_bf16 v[200:203], a[240:243], a[0:3], v[152:155]\n"
"ds_read_b128 a[232:235], v234 offset:20480\n"
"s_lshl_b32 s83, s60, 6\n"
"s_add_u32 s83, s83, 1839104\n"
"s_lshl_b32 s84, %[g], 11\n"
"s_add_u32 s39, s83, s84\n"
"s_lshl_b32 s84, s38, 1\n"
"s_add_u32 s67, s83, s84\n"
"v_mfma_f32_16x16x32_bf16 v[200:203], a[244:247], a[16:19], v[200:203]\n"
"v_add_f32 v246, v246, v186\n"
"v_mfma_f32_16x16x32_bf16 v[208:211], a[240:243], a[4:7], v[156:159]\n"
"v_exp_f32 v187, v187\n"
"v_mfma_f32_16x16x32_bf16 v[208:211], a[244:247], a[20:23], v[208:211]\n"
"v_add_f32 v246, v246, v187\n"
"v_cvt_pk_bf16_f32 v185, v186, v187\n"
"v_mfma_f32_16x16x32_bf16 v[216:219], a[240:243], a[8:11], v[160:163]\n"
"ds_read_b128 a[236:239], v234 offset:20992\n"
"v_add_u32 v242, 229376, v241\n"
"s_add_u32 m0, s71, 81920\n"
"v_mfma_f32_16x16x32_bf16 v[216:219], a[244:247], a[24:27], v[216:219]\n"
"buffer_load_dwordx4 v242, s[40:43], 0 offen offset:0 sc0 sc1 nt lds\n"
"v_mfma_f32_16x16x32_bf16 v[224:227], a[240:243], a[12:15], v[164:167]\n"
"v_exp_f32 v188, v188\n"
"v_mfma_f32_16x16x32_bf16 v[224:227], a[244:247], a[28:31], v[224:227]\n"
"s_waitcnt lgkmcnt(2)\n"
"v_add_f32 v246, v246, v188\n"
"v_mfma_f32_16x16x32_bf16 v[228:231], a[248:251], a[12:15], v[164:167]\n"
"ds_read_b128 a[240:243], v234 offset:22528\n"
"v_mfma_f32_16x16x32_bf16 v[228:231], a[252:255], a[28:31], v[228:231]\n"
"s_cmp_eq_u32 %[g], 6\n"
"s_cselect_b32 s83, 0, 4096\n"
"s_cselect_b32 s85, 32, 0\n"
"s_lshl_b32 s84, s38, 11\n"
"s_add_u32 s69, s84, s83\n"
"s_cmp_ge_u32 %[g], 6\n"
"s_cselect_b32 s68, 1073741824, s84\n"
"s_lshr_b32 s83, s83, 7\n"
"s_add_u32 s74, s38, s83\n"
"v_mfma_f32_16x16x32_bf16 v[220:223], a[248:251], a[8:11], v[160:163]\n"
"v_exp_f32 v189, v189\n"
"v_mfma_f32_16x16x32_bf16 v[220:223], a[252:255], a[24:27], v[220:223]\n"
"v_add_f32 v246, v246, v189\n"
"v_cvt_pk_bf16_f32 v186, v188, v189\n"
"v_mfma_f32_16x16x32_bf16 v[212:215], a[248:251], a[4:7], v[156:159]\n"
"ds_read_b128 a[244:247], v234 offset:23040\n"
"v_mfma_f32_16x16x32_bf16 v[212:215], a[252:255], a[20:23], v[212:215]\n"
"v_exp_f32 v190, v190\n"
"v_mfma_f32_16x16x32_bf16 v[204:207], a[248:251], a[0:3], v[152:155]\n"
"v_add_f32 v246, v246, v190\n"
"v_mfma_f32_16x16x32_bf16 v[204:207], a[252:255], a[16:19], v[204:207]\n"
"s_waitcnt lgkmcnt(2)\n"
"v_exp_f32 v191, v191\n"
"v_mfma_f32_16x16x32_bf16 v[200:203], a[232:235], a[32:35], v[200:203]\n"
"ds_read_b128 a[248:251], v234 offset:24576\n"
"v_add_f32 v246, v246, v191\n"
"v_mfma_f32_16x16x32_bf16 v[200:203], a[236:239], a[48:51], v[200:203]\n"
"v_cvt_pk_bf16_f32 v187, v190, v191\n"
"v_mfma_f32_16x16x32_bf16 v[208:211], a[232:235], a[36:39], v[208:211]\n"
"v_exp_f32 v192, v192\n"
"v_mfma_f32_16x16x32_bf16 v[208:211], a[236:239], a[52:55], v[208:211]\n"
"v_add_f32 v247, v247, v192\n"
"v_mfma_f32_16x16x32_bf16 v[216:219], a[232:235], a[40:43], v[216:219]\n"
"ds_read_b128 a[252:255], v234 offset:25088\n"
"v_mfma_f32_16x16x32_bf16 v[216:219], a[236:239], a[56:59], v[216:219]\n"
"v_exp_f32 v193, v193\n"
"v_mfma_f32_16x16x32_bf16 v[224:227], a[232:235], a[44:47], v[224:227]\n"
"buffer_load_dwordx4 v242, s[40:43], s61 offen offset:1024 sc0 sc1 nt lds\n"
"v_mfma_f32_16x16x32_bf16 v[224:227], a[236:239], a[60:63], v[224:227]\n"
"s_waitcnt lgkmcnt(2)\n"
"v_add_f32 v247, v247, v193\n"
"v_cvt_pk_bf16_f32 v192, v192, v193\n"
"v_mfma_f32_16x16x32_bf16 v[228:231], a[240:243], a[44:47], v[228:231]\n"
"ds_read_b128 a[232:235], v234 offset:26624\n"
"v_mfma_f32_16x16x32_bf16 v[228:231], a[244:247], a[60:63], v[228:231]\n"
"s_xor_b32 s70, s69, 4096\n"
"s_cmp_ge_u32 %[g], 6\n"
"s_cselect_b32 s70, s70, 1073741824\n"
"s_cselect_b32 s89, 1073741824, s38\n"
"v_mfma_f32_16x16x32_bf16 v[220:223], a[240:243], a[40:43], v[220:223]\n"
"s_waitcnt vmcnt(18)\n"
"s_barrier\n"
"v_mfma_f32_16x16x32_bf16 v[220:223], a[244:247], a[56:59], v[220:223]\n"
"v_exp_f32 v194, v194\n"
"v_mfma_f32_16x16x32_bf16 v[212:215], a[240:243], a[36:39], v[212:215]\n"
"ds_read_b128 a[236:239], v234 offset:27136\n"
"v_add_f32 v247, v247, v194\n"
"v_mfma_f32_16x16x32_bf16 v[212:215], a[244:247], a[52:55], v[212:215]\n"
"v_exp_f32 v195, v195\n"
"v_mfma_f32_16x16x32_bf16 v[204:207], a[240:243], a[32:35], v[204:207]\n"
"v_add_f32 v247, v247, v195\n"
"v_cvt_pk_bf16_f32 v193, v194, v195\n"
"v_mfma_f32_16x16x32_bf16 v[204:207], a[244:247], a[48:51], v[204:207]\n"
"s_waitcnt lgkmcnt(2)\n"
"v_exp_f32 v196, v196\n"
"v_mfma_f32_16x16x32_bf16 v[200:203], a[248:251], a[64:67], v[200:203]\n"
"ds_read_b128 a[240:243], v234 offset:28672\n"
"v_add_f32 v247, v247, v196\n"
"v_mfma_f32_16x16x32_bf16 v[200:203], a[252:255], a[80:83], v[200:203]\n"
"s_cmp_eq_u32 %[g], 7\n"
"s_cselect_b32 s88, 6, 7\n"
"s_lshl_b32 s88, s88, 15\n"
"v_mfma_f32_16x16x32_bf16 v[208:211], a[248:251], a[68:71], v[208:211]\n"
"v_exp_f32 v197, v197\n"
"v_mfma_f32_16x16x32_bf16 v[208:211], a[252:255], a[84:87], v[208:211]\n"
"v_add_f32 v247, v247, v197\n"
"v_cvt_pk_bf16_f32 v194, v196, v197\n"
"v_mfma_f32_16x16x32_bf16 v[216:219], a[248:251], a[72:75], v[216:219]\n"
"ds_read_b128 a[244:247], v234 offset:29184\n"
"v_mfma_f32_16x16x32_bf16 v[216:219], a[252:255], a[88:91], v[216:219]\n"
"v_exp_f32 v198, v198\n"
"v_mfma_f32_16x16x32_bf16 v[224:227], a[248:251], a[76:79], v[224:227]\n"
"s_lshr_b32 s83, s88, 4\n"
"s_lshl_b32 s84, s33, 4\n"
"s_cmp_le_u32 %[g], 4\n"
"s_cselect_b32 s76, s88, 1073741824\n"
"s_cselect_b32 s83, s83, s84\n"
"s_add_u32 s77, s67, s83\n"
"v_mfma_f32_16x16x32_bf16 v[224:227], a[252:255], a[92:95], v[224:227]\n"
"s_waitcnt lgkmcnt(2)\n"
"v_add_f32 v247, v247, v198\n"
"v_mfma_f32_16x16x32_bf16 v[228:231], a[232:235], a[76:79], v[228:231]\n"
"ds_read_b128 a[248:251], v234 offset:30720\n"
"v_mfma_f32_16x16x32_bf16 v[228:231], a[236:239], a[92:95], v[228:231]\n"
"buffer_load_dwordx4 v242, s[40:43], s62 offen offset:2048 sc0 sc1 nt lds\n"
"v_mfma_f32_16x16x32_bf16 v[220:223], a[232:235], a[72:75], v[220:223]\n"
"v_exp_f32 v199, v199\n"
"v_mfma_f32_16x16x32_bf16 v[220:223], a[236:239], a[88:91], v[220:223]\n"
"v_add_f32 v247, v247, v199\n"
"v_cvt_pk_bf16_f32 v195, v198, v199\n"
"v_mfma_f32_16x16x32_bf16 v[212:215], a[232:235], a[68:71], v[212:215]\n"
"ds_read_b128 a[252:255], v234 offset:31232\n"
"v_mfma_f32_16x16x32_bf16 v[212:215], a[236:239], a[84:87], v[212:215]\n"
"v_mfma_f32_16x16x32_bf16 v[204:207], a[232:235], a[64:67], v[204:207]\n"
"v_mfma_f32_16x16x32_bf16 v[204:207], a[236:239], a[80:83], v[204:207]\n"
"s_waitcnt lgkmcnt(2)\n"
"v_mfma_f32_16x16x32_bf16 v[224:227], a[240:243], a[108:111], v[224:227]\n"
"ds_read_b64_tr_b16 a[232:233], v235 offset:0\n"
"v_mfma_f32_16x16x32_bf16 v[224:227], a[244:247], a[124:127], v[224:227]\n"
"ds_read_b64_tr_b16 a[234:235], v235 offset:2048\n"
"v_mfma_f32_16x16x32_bf16 v[216:219], a[240:243], a[104:107], v[216:219]\n"
"v_mfma_f32_16x16x32_bf16 v[216:219], a[244:247], a[120:123], v[216:219]\n"
"v_mfma_f32_16x16x32_bf16 v[208:211], a[240:243], a[100:103], v[208:211]\n"
"ds_read_b64_tr_b16 a[236:237], v236 offset:0\n"
"v_mfma_f32_16x16x32_bf16 v[208:211], a[244:247], a[116:119], v[208:211]\n"
"ds_read_b64_tr_b16 a[238:239], v236 offset:2048\n"
"v_mfma_f32_16x16x32_bf16 v[200:203], a[240:243], a[96:99], v[200:203]\n"
"v_mfma_f32_16x16x32_bf16 v[200:203], a[244:247], a[112:115], v[200:203]\n"
"s_waitcnt lgkmcnt(4)\n"
"v_mfma_f32_16x16x32_bf16 v[204:207], a[248:251], a[96:99], v[204:207]\n"
"ds_read_b64_tr_b16 a[240:241], v235 offset:512\n"
"v_mfma_f32_16x16x32_bf16 v[204:207], a[252:255], a[112:115], v[204:207]\n"
"ds_read_b64_tr_b16 a[242:243], v235 offset:2560\n"
"v_mfma_f32_16x16x32_bf16 v[212:215], a[248:251], a[100:103], v[212:215]\n"
"buffer_load_dwordx4 v242, s[40:43], s63 offen offset:3072 sc0 sc1 nt lds\n"
"v_mfma_f32_16x16x32_bf16 v[212:215], a[252:255], a[116:119], v[212:215]\n"
"v_mfma_f32_16x16x32_bf16 v[220:223], a[248:251], a[104:107], v[220:223]\n"
"ds_read_b64_tr_b16 a[244:245], v236 offset:512\n"
"v_mfma_f32_16x16x32_bf16 v[220:223], a[252:255], a[120:123], v[220:223]\n"
"ds_read_b64_tr_b16 a[246:247], v236 offset:2560\n"
"v_mfma_f32_16x16x32_bf16 v[228:231], a[248:251], a[108:111], v[228:231]\n"
"v_mfma_f32_16x16x32_bf16 v[228:231], a[252:255], a[124:127], v[228:231]\n"
"s_waitcnt lgkmcnt(4)\n"
"v_mfma_f32_16x16x32_bf16 v[0:3], a[232:235], v[168:171], v[0:3]\n"
"ds_read_b64_tr_b16 a[248:249], v235 offset:4096\n"
"v_mfma_f32_16x16x32_bf16 v[4:7], a[236:239], v[168:171], v[4:7]\n"
"ds_read_b64_tr_b16 a[250:251], v235 offset:6144\n"
"v_mfma_f32_16x16x32_bf16 v[8:11], a[232:235], v[176:179], v[8:11]\n"
"v_exp_f32 v200, v200\n"
"v_mfma_f32_16x16x32_bf16 v[12:15], a[236:239], v[176:179], v[12:15]\n"
"v_add_f32 v244, v244, v200\n"
"v_mfma_f32_16x16x32_bf16 v[16:19], a[232:235], v[184:187], v[16:19]\n"
"ds_read_b64_tr_b16 a[252:253], v236 offset:4096\n"
"v_mfma_f32_16x16x32_bf16 v[20:23], a[236:239], v[184:187], v[20:23]\n"
"ds_read_b64_tr_b16 a[254:255], v236 offset:6144\n"
"v_mfma_f32_16x16x32_bf16 v[24:27], a[232:235], v[192:195], v[24:27]\n"
"v_exp_f32 v201, v201\n"
"v_mfma_f32_16x16x32_bf16 v[28:31], a[236:239], v[192:195], v[28:31]\n"
"s_waitcnt lgkmcnt(4)\n"
"v_add_f32 v244, v244, v201\n"
"v_cvt_pk_bf16_f32 v200, v200, v201\n"
"v_mfma_f32_16x16x32_bf16 v[56:59], a[240:243], v[192:195], v[56:59]\n"
"ds_read_b64_tr_b16 a[232:233], v235 offset:4608\n"
"v_mfma_f32_16x16x32_bf16 v[60:63], a[244:247], v[192:195], v[60:63]\n"
"ds_read_b64_tr_b16 a[234:235], v235 offset:6656\n"
"v_mfma_f32_16x16x32_bf16 v[48:51], a[240:243], v[184:187], v[48:51]\n"
"v_exp_f32 v202, v202\n"
"v_mfma_f32_16x16x32_bf16 v[52:55], a[244:247], v[184:187], v[52:55]\n"
"v_add_f32 v244, v244, v202\n"
"v_mfma_f32_16x16x32_bf16 v[40:43], a[240:243], v[176:179], v[40:43]\n"
"ds_read_b64_tr_b16 a[236:237], v236 offset:4608\n"
"v_mfma_f32_16x16x32_bf16 v[44:47], a[244:247], v[176:179], v[44:47]\n"
"ds_read_b64_tr_b16 a[238:239], v236 offset:6656\n"
"v_add_u32 v242, 212992, v241\n"
"s_add_u32 m0, s71, 98304\n"
"v_mfma_f32_16x16x32_bf16 v[32:35], a[240:243], v[168:171], v[32:35]\n"
"buffer_load_dwordx4 v242, s[44:47], 0 offen offset:0 sc0 sc1 nt lds\n"
"v_mfma_f32_16x16x32_bf16 v[36:39], a[244:247], v[168:171], v[36:39]\n"
"s_waitcnt lgkmcnt(4)\n"
"v_exp_f32 v203, v203\n"
"v_mfma_f32_16x16x32_bf16 v[64:67], a[248:251], v[168:171], v[64:67]\n"
"ds_read_b64_tr_b16 a[240:241], v235 offset:8192\n"
"v_add_f32 v244, v244, v203\n"
"v_mfma_f32_16x16x32_bf16 v[68:71], a[252:255], v[168:171], v[68:71]\n"
"ds_read_b64_tr_b16 a[242:243], v235 offset:10240\n"
"v_cvt_pk_bf16_f32 v201, v202, v203\n"
"v_mfma_f32_16x16x32_bf16 v[72:75], a[248:251], v[176:179], v[72:75]\n"
"v_exp_f32 v204, v204\n"
"v_mfma_f32_16x16x32_bf16 v[76:79], a[252:255], v[176:179], v[76:79]\n"
"v_add_f32 v244, v244, v204\n"
"v_mfma_f32_16x16x32_bf16 v[80:83], a[248:251], v[184:187], v[80:83]\n"
"ds_read_b64_tr_b16 a[244:245], v236 offset:8192\n"
"v_mfma_f32_16x16x32_bf16 v[84:87], a[252:255], v[184:187], v[84:87]\n"
"ds_read_b64_tr_b16 a[246:247], v236 offset:10240\n"
"v_mfma_f32_16x16x32_bf16 v[88:91], a[248:251], v[192:195], v[88:91]\n"
"v_exp_f32 v205, v205\n"
"v_mfma_f32_16x16x32_bf16 v[92:95], a[252:255], v[192:195], v[92:95]\n"
"s_waitcnt lgkmcnt(4)\n"
"v_add_f32 v244, v244, v205\n"
"v_cvt_pk_bf16_f32 v202, v204, v205\n"
"v_mfma_f32_16x16x32_bf16 v[120:123], a[232:235], v[192:195], v[120:123]\n"
"ds_read_b64_tr_b16 a[248:249], v235 offset:8704\n"
"v_mfma_f32_16x16x32_bf16 v[124:127], a[236:239], v[192:195], v[124:127]\n"
"ds_read_b64_tr_b16 a[250:251], v235 offset:10752\n"
"v_mfma_f32_16x16x32_bf16 v[112:115], a[232:235], v[184:187], v[112:115]\n"
"s_waitcnt vmcnt(17)\n"
"s_barrier\n"
"v_mfma_f32_16x16x32_bf16 v[116:119], a[236:239], v[184:187], v[116:119]\n"
"v_exp_f32 v206, v206\n"
"v_mfma_f32_16x16x32_bf16 v[104:107], a[232:235], v[176:179], v[104:107]\n"
"ds_read_b64_tr_b16 a[252:253], v236 offset:8704\n"
"v_add_f32 v244, v244, v206\n"
"v_mfma_f32_16x16x32_bf16 v[108:111], a[236:239], v[176:179], v[108:111]\n"
"ds_read_b64_tr_b16 a[254:255], v236 offset:10752\n"
"v_mfma_f32_16x16x32_bf16 v[96:99], a[232:235], v[168:171], v[96:99]\n"
"buffer_load_dwordx4 v242, s[44:47], s61 offen offset:1024 sc0 sc1 nt lds\n"
"v_mfma_f32_16x16x32_bf16 v[100:103], a[236:239], v[168:171], v[100:103]\n"
"s_waitcnt lgkmcnt(4)\n"
"v_exp_f32 v207, v207\n"
"v_mfma_f32_16x16x32_bf16 v[128:131], a[240:243], v[168:171], v[128:131]\n"
"ds_read_b64_tr_b16 a[232:233], v235 offset:12288\n"
"v_add_f32 v244, v244, v207\n"
"v_mfma_f32_16x16x32_bf16 v[132:135], a[244:247], v[168:171], v[132:135]\n"
"ds_read_b64_tr_b16 a[234:235], v235 offset:14336\n"
"v_cvt_pk_bf16_f32 v203, v206, v207\n"
"v_mfma_f32_16x16x32_bf16 v[136:139], a[240:243], v[176:179], v[136:139]\n"
"v_exp_f32 v208, v208\n"
"v_mfma_f32_16x16x32_bf16 v[140:143], a[244:247], v[176:179], v[140:143]\n"
"v_add_f32 v245, v245, v208\n"
"v_mfma_f32_16x16x32_bf16 v[144:147], a[240:243], v[184:187], v[144:147]\n"
"ds_read_b64_tr_b16 a[236:237], v236 offset:12288\n"
"v_mfma_f32_16x16x32_bf16 v[148:151], a[244:247], v[184:187], v[148:151]\n"
"ds_read_b64_tr_b16 a[238:239], v236 offset:14336\n"
"v_mfma_f32_16x16x32_bf16 a[128:131], a[240:243], v[192:195], a[128:131]\n"
"v_exp_f32 v209, v209\n"
"v_mfma_f32_16x16x32_bf16 a[132:135], a[244:247], v[192:195], a[132:135]\n"
"s_waitcnt lgkmcnt(4)\n"
"v_add_f32 v245, v245, v209\n"
"v_cvt_pk_bf16_f32 v208, v208, v209\n"
"v_mfma_f32_16x16x32_bf16 a[160:163], a[248:251], v[192:195], a[160:163]\n"
"ds_read_b64_tr_b16 a[240:241], v235 offset:12800\n"
"v_mfma_f32_16x16x32_bf16 a[164:167], a[252:255], v[192:195], a[164:167]\n"
"ds_read_b64_tr_b16 a[242:243], v235 offset:14848\n"
"v_mfma_f32_16x16x32_bf16 a[152:155], a[248:251], v[184:187], a[152:155]\n"
"v_exp_f32 v210, v210\n"
"v_mfma_f32_16x16x32_bf16 a[156:159], a[252:255], v[184:187], a[156:159]\n"
"v_add_f32 v245, v245, v210\n"
"v_mfma_f32_16x16x32_bf16 a[144:147], a[248:251], v[176:179], a[144:147]\n"
"ds_read_b64_tr_b16 a[244:245], v236 offset:12800\n"
"v_mfma_f32_16x16x32_bf16 a[148:151], a[252:255], v[176:179], a[148:151]\n"
"ds_read_b64_tr_b16 a[246:247], v236 offset:14848\n"
"v_mfma_f32_16x16x32_bf16 a[136:139], a[248:251], v[168:171], a[136:139]\n"
"v_exp_f32 v211, v211\n"
"v_mfma_f32_16x16x32_bf16 a[140:143], a[252:255], v[168:171], a[140:143]\n"
"s_waitcnt lgkmcnt(4)\n"
"v_add_f32 v245, v245, v211\n"
"v_cvt_pk_bf16_f32 v209, v210, v211\n"
"v_mfma_f32_16x16x32_bf16 a[168:171], a[232:235], v[168:171], a[168:171]\n"
"ds_read_b128 a[248:251], v232 offset:16384\n"
"v_mfma_f32_16x16x32_bf16 a[172:175], a[236:239], v[168:171], a[172:175]\n"
"buffer_load_dwordx4 v242, s[44:47], s62 offen offset:2048 sc0 sc1 nt lds\n"
"v_mfma_f32_16x16x32_bf16 a[176:179], a[232:235], v[176:179], a[176:179]\n"
"v_exp_f32 v212, v212\n"
"v_mfma_f32_16x16x32_bf16 a[180:183], a[236:239], v[176:179], a[180:183]\n"
"v_add_f32 v245, v245, v212\n"
"v_mfma_f32_16x16x32_bf16 a[184:187], a[232:235], v[184:187], a[184:187]\n"
"ds_read_b128 a[252:255], v232 offset:16896\n"
"v_mfma_f32_16x16x32_bf16 a[188:191], a[236:239], v[184:187], a[188:191]\n"
"v_exp_f32 v213, v213\n"
"v_mfma_f32_16x16x32_bf16 a[192:195], a[232:235], v[192:195], a[192:195]\n"
"v_add_f32 v245, v245, v213\n"
"v_cvt_pk_bf16_f32 v210, v212, v213\n"
"v_mfma_f32_16x16x32_bf16 a[196:199], a[236:239], v[192:195], a[196:199]\n"
"s_waitcnt lgkmcnt(2)\n"
"v_exp_f32 v214, v214\n"
"v_mfma_f32_16x16x32_bf16 a[200:203], a[240:243], v[168:171], a[200:203]\n"
"ds_read_b128 a[232:235], v232 offset:18432\n"
"v_add_f32 v245, v245, v214\n"
"v_mfma_f32_16x16x32_bf16 a[204:207], a[244:247], v[168:171], a[204:207]\n"
"v_exp_f32 v215, v215\n"
"v_mfma_f32_16x16x32_bf16 a[208:211], a[240:243], v[176:179], a[208:211]\n"
"v_add_f32 v245, v245, v215\n"
"v_cvt_pk_bf16_f32 v211, v214, v215\n"
"v_mfma_f32_16x16x32_bf16 a[212:215], a[244:247], v[176:179], a[212:215]\n"
"v_exp_f32 v216, v216\n"
"v_mfma_f32_16x16x32_bf16 a[216:219], a[240:243], v[184:187], a[216:219]\n"
"ds_read_b128 a[236:239], v232 offset:18944\n"
"v_add_f32 v246, v246, v216\n"
"v_mfma_f32_16x16x32_bf16 a[220:223], a[244:247], v[184:187], a[220:223]\n"
"v_exp_f32 v217, v217\n"
"v_mfma_f32_16x16x32_bf16 a[224:227], a[240:243], v[192:195], a[224:227]\n"
"v_add_f32 v246, v246, v217\n"
"v_cvt_pk_bf16_f32 v216, v216, v217\n"
"v_mfma_f32_16x16x32_bf16 a[228:231], a[244:247], v[192:195], a[228:231]\n"
"s_waitcnt lgkmcnt(2)\n"
"v_exp_f32 v218, v218\n"
"v_mfma_f32_16x16x32_bf16 v[168:171], a[248:251], a[0:3], v[152:155]\n"
"ds_read_b128 a[240:243], v232 offset:20480\n"
"v_add_f32 v246, v246, v218\n"
"v_mfma_f32_16x16x32_bf16 v[168:171], a[252:255], a[16:19], v[168:171]\n"
"v_exp_f32 v219, v219\n"
"v_mfma_f32_16x16x32_bf16 v[176:179], a[248:251], a[4:7], v[156:159]\n"
"buffer_load_dwordx4 v242, s[44:47], s63 offen offset:3072 sc0 sc1 nt lds\n"
"v_mfma_f32_16x16x32_bf16 v[176:179], a[252:255], a[20:23], v[176:179]\n"
"v_add_f32 v246, v246, v219\n"
"v_cvt_pk_bf16_f32 v217, v218, v219\n"
"v_mfma_f32_16x16x32_bf16 v[184:187], a[248:251], a[8:11], v[160:163]\n"
"ds_read_b128 a[244:247], v232 offset:20992\n"
"v_mfma_f32_16x16x32_bf16 v[184:187], a[252:255], a[24:27], v[184:187]\n"
"v_exp_f32 v220, v220\n"
"v_mfma_f32_16x16x32_bf16 v[192:195], a[248:251], a[12:15], v[164:167]\n"
"v_add_f32 v246, v246, v220\n"
"v_mfma_f32_16x16x32_bf16 v[192:195], a[252:255], a[28:31], v[192:195]\n"
"s_waitcnt lgkmcnt(2)\n"
"v_exp_f32 v221, v221\n"
"v_mfma_f32_16x16x32_bf16 v[196:199], a[232:235], a[12:15], v[164:167]\n"
"ds_read_b128 a[248:251], v232 offset:22528\n"
"v_add_f32 v246, v246, v221\n"
"v_mfma_f32_16x16x32_bf16 v[196:199], a[236:239], a[28:31], v[196:199]\n"
"v_cvt_pk_bf16_f32 v218, v220, v221\n"
"v_mfma_f32_16x16x32_bf16 v[188:191], a[232:235], a[8:11], v[160:163]\n"
"v_exp_f32 v222, v222\n"
"v_mfma_f32_16x16x32_bf16 v[188:191], a[236:239], a[24:27], v[188:191]\n"
"v_add_f32 v246, v246, v222\n"
"v_mfma_f32_16x16x32_bf16 v[180:183], a[232:235], a[4:7], v[156:159]\n"
"ds_read_b128 a[252:255], v232 offset:23040\n"
"v_mfma_f32_16x16x32_bf16 v[180:183], a[236:239], a[20:23], v[180:183]\n"
"v_exp_f32 v223, v223\n"
"v_mfma_f32_16x16x32_bf16 v[172:175], a[232:235], a[0:3], v[152:155]\n"
"v_add_f32 v246, v246, v223\n"
"v_cvt_pk_bf16_f32 v219, v222, v223\n"
"v_mfma_f32_16x16x32_bf16 v[172:175], a[236:239], a[16:19], v[172:175]\n"
"s_waitcnt lgkmcnt(2)\n"
"v_exp_f32 v224, v224\n"
"v_mfma_f32_16x16x32_bf16 v[168:171], a[240:243], a[32:35], v[168:171]\n"
"ds_read_b128 a[232:235], v232 offset:24576\n"
"v_add_f32 v247, v247, v224\n"
"v_mfma_f32_16x16x32_bf16 v[168:171], a[244:247], a[48:51], v[168:171]\n"
"v_exp_f32 v225, v225\n"
"v_mfma_f32_16x16x32_bf16 v[176:179], a[240:243], a[36:39], v[176:179]\n"
"v_add_f32 v247, v247, v225\n"
"v_mfma_f32_16x16x32_bf16 v[176:179], a[244:247], a[52:55], v[176:179]\n"
"v_cvt_pk_bf16_f32 v224, v224, v225\n"
"v_mfma_f32_16x16x32_bf16 v[184:187], a[240:243], a[40:43], v[184:187]\n"
"ds_read_b128 a[236:239], v232 offset:25088\n"
"v_add_u32 v242, 245760, v241\n"
"s_add_u32 m0, s71, 114688\n"
"v_mfma_f32_16x16x32_bf16 v[184:187], a[244:247], a[56:59], v[184:187]\n"
"buffer_load_dwordx4 v242, s[40:43], 0 offen offset:0 sc0 sc1 nt lds\n"
"v_mfma_f32_16x16x32_bf16 v[192:195], a[240:243], a[44:47], v[192:195]\n"
"v_exp_f32 v226, v226\n"
"v_mfma_f32_16x16x32_bf16 v[192:195], a[244:247], a[60:63], v[192:195]\n"
"s_waitcnt lgkmcnt(2)\n"
"v_add_f32 v247, v247, v226\n"
"v_mfma_f32_16x16x32_bf16 v[196:199], a[248:251], a[44:47], v[196:199]\n"
"ds_read_b128 a[240:243], v232 offset:26624\n"
"v_mfma_f32_16x16x32_bf16 v[196:199], a[252:255], a[60:63], v[196:199]\n"
"buffer_load_dwordx4 v242, s[40:43], s61 offen offset:1024 sc0 sc1 nt lds\n"
"v_mfma_f32_16x16x32_bf16 v[188:191], a[248:251], a[40:43], v[188:191]\n"
"s_waitcnt vmcnt(18)\n"
"s_barrier\n"
"v_mfma_f32_16x16x32_bf16 v[188:191], a[252:255], a[56:59], v[188:191]\n"
"v_exp_f32 v227, v227\n"
"v_mfma_f32_16x16x32_bf16 v[180:183], a[248:251], a[36:39], v[180:183]\n"
"ds_read_b128 a[244:247], v232 offset:27136\n"
"v_add_f32 v247, v247, v227\n"
"v_mfma_f32_16x16x32_bf16 v[180:183], a[252:255], a[52:55], v[180:183]\n"
"buffer_load_dwordx4 v242, s[40:43], s62 offen offset:2048 sc0 sc1 nt lds\n"
"v_mfma_f32_16x16x32_bf16 v[172:175], a[248:251], a[32:35], v[172:175]\n"
"v_cvt_pk_bf16_f32 v225, v226, v227\n"
"v_mfma_f32_16x16x32_bf16 v[172:175], a[252:255], a[48:51], v[172:175]\n"
"s_waitcnt lgkmcnt(2)\n"
"v_exp_f32 v228, v228\n"
"v_mfma_f32_16x16x32_bf16 v[168:171], a[232:235], a[64:67], v[168:171]\n"
"ds_read_b128 a[248:251], v232 offset:28672\n"
"v_add_f32 v247, v247, v228\n"
"v_mfma_f32_16x16x32_bf16 v[168:171], a[236:239], a[80:83], v[168:171]\n"
"buffer_load_dwordx4 v242, s[40:43], s63 offen offset:3072 sc0 sc1 nt lds\n"
"v_mfma_f32_16x16x32_bf16 v[176:179], a[232:235], a[68:71], v[176:179]\n"
"s_add_u32 s82, s32, 0\n"
"v_add_u32 v242, s82, v243\n"
"s_add_u32 m0, s71, 0\n"
"v_mfma_f32_16x16x32_bf16 v[176:179], a[236:239], a[84:87], v[176:179]\n"
"buffer_load_dwordx4 v242, s[48:51], 0 offen offset:0  lds\n"
"v_mfma_f32_16x16x32_bf16 v[184:187], a[232:235], a[72:75], v[184:187]\n"
"ds_read_b128 a[252:255], v232 offset:29184\n"
"v_mfma_f32_16x16x32_bf16 v[184:187], a[236:239], a[88:91], v[184:187]\n"
"v_exp_f32 v229, v229\n"
"v_mfma_f32_16x16x32_bf16 v[192:195], a[232:235], a[76:79], v[192:195]\n"
"v_add_f32 v247, v247, v229\n"
"v_cvt_pk_bf16_f32 v226, v228, v229\n"
"v_mfma_f32_16x16x32_bf16 v[192:195], a[236:239], a[92:95], v[192:195]\n"
"s_waitcnt lgkmcnt(2)\n"
"v_exp_f32 v230, v230\n"
"v_mfma_f32_16x16x32_bf16 v[196:199], a[240:243], a[76:79], v[196:199]\n"
"ds_read_b128 a[232:235], v232 offset:30720\n"
"v_add_f32 v247, v247, v230\n"
"v_mfma_f32_16x16x32_bf16 v[196:199], a[244:247], a[92:95], v[196:199]\n"
"v_exp_f32 v231, v231\n"
"v_mfma_f32_16x16x32_bf16 v[188:191], a[240:243], a[72:75], v[188:191]\n"
"v_mfma_f32_16x16x32_bf16 v[188:191], a[244:247], a[88:91], v[188:191]\n"
"v_add_f32 v247, v247, v231\n"
"v_cvt_pk_bf16_f32 v227, v230, v231\n"
"v_mfma_f32_16x16x32_bf16 v[180:183], a[240:243], a[68:71], v[180:183]\n"
"ds_read_b128 a[236:239], v232 offset:31232\n"
"v_mfma_f32_16x16x32_bf16 v[180:183], a[244:247], a[84:87], v[180:183]\n"
"v_mfma_f32_16x16x32_bf16 v[172:175], a[240:243], a[64:67], v[172:175]\n"
"v_mfma_f32_16x16x32_bf16 v[172:175], a[244:247], a[80:83], v[172:175]\n"
"s_waitcnt lgkmcnt(2)\n"
"v_mfma_f32_16x16x32_bf16 v[192:195], a[248:251], a[108:111], v[192:195]\n"
"ds_read_b64_tr_b16 a[240:241], v235 offset:32768\n"
"v_mfma_f32_16x16x32_bf16 v[192:195], a[252:255], a[124:127], v[192:195]\n"
"ds_read_b64_tr_b16 a[242:243], v235 offset:34816\n"
"v_mfma_f32_16x16x32_bf16 v[184:187], a[248:251], a[104:107], v[184:187]\n"
"buffer_load_dwordx4 v242, s[48:51], s64 offen offset:1024  lds\n"
"v_mfma_f32_16x16x32_bf16 v[184:187], a[252:255], a[120:123], v[184:187]\n"
"v_mfma_f32_16x16x32_bf16 v[176:179], a[248:251], a[100:103], v[176:179]\n"
"ds_read_b64_tr_b16 a[244:245], v236 offset:32768\n"
"v_mfma_f32_16x16x32_bf16 v[176:179], a[252:255], a[116:119], v[176:179]\n"
"ds_read_b64_tr_b16 a[246:247], v236 offset:34816\n"
"v_mfma_f32_16x16x32_bf16 v[168:171], a[248:251], a[96:99], v[168:171]\n"
"v_mfma_f32_16x16x32_bf16 v[168:171], a[252:255], a[112:115], v[168:171]\n"
"s_waitcnt lgkmcnt(4)\n"
"v_mfma_f32_16x16x32_bf16 v[172:175], a[232:235], a[96:99], v[172:175]\n"
"ds_read_b64_tr_b16 a[248:249], v235 offset:33280\n"
"v_mfma_f32_16x16x32_bf16 v[172:175], a[236:239], a[112:115], v[172:175]\n"
"ds_read_b64_tr_b16 a[250:251], v235 offset:35328\n"
"v_mfma_f32_16x16x32_bf16 v[180:183], a[232:235], a[100:103], v[180:183]\n"
"v_mfma_f32_16x16x32_bf16 v[180:183], a[236:239], a[116:119], v[180:183]\n"
"v_mfma_f32_16x16x32_bf16 v[188:191], a[232:235], a[104:107], v[188:191]\n"
"ds_read_b64_tr_b16 a[252:253], v236 offset:33280\n"
"v_mfma_f32_16x16x32_bf16 v[188:191], a[236:239], a[120:123], v[188:191]\n"
"ds_read_b64_tr_b16 a[254:255], v236 offset:35328\n"
"v_mfma_f32_16x16x32_bf16 v[196:199], a[232:235], a[108:111], v[196:199]\n"
"v_mfma_f32_16x16x32_bf16 v[196:199], a[236:239], a[124:127], v[196:199]\n"
"s_waitcnt lgkmcnt(4)\n"
"buffer_load_dwordx4 v242, s[48:51], s65 offen offset:2048  lds\n"
"v_mfma_f32_16x16x32_bf16 v[0:3], a[240:243], v[200:203], v[0:3]\n"
"ds_read_b64_tr_b16 a[232:233], v235 offset:36864\n"
"v_mfma_f32_16x16x32_bf16 v[4:7], a[244:247], v[200:203], v[4:7]\n"
"ds_read_b64_tr_b16 a[234:235], v235 offset:38912\n"
"v_mfma_f32_16x16x32_bf16 v[8:11], a[240:243], v[208:211], v[8:11]\n"
"v_exp_f32 v168, v168\n"
"v_mfma_f32_16x16x32_bf16 v[12:15], a[244:247], v[208:211], v[12:15]\n"
"v_add_f32 v244, v244, v168\n"
"v_mfma_f32_16x16x32_bf16 v[16:19], a[240:243], v[216:219], v[16:19]\n"
"ds_read_b64_tr_b16 a[236:237], v236 offset:36864\n"
"v_mfma_f32_16x16x32_bf16 v[20:23], a[244:247], v[216:219], v[20:23]\n"
"ds_read_b64_tr_b16 a[238:239], v236 offset:38912\n"
"v_mfma_f32_16x16x32_bf16 v[24:27], a[240:243], v[224:227], v[24:27]\n"
"v_exp_f32 v169, v169\n"
"v_mfma_f32_16x16x32_bf16 v[28:31], a[244:247], v[224:227], v[28:31]\n"
"s_waitcnt lgkmcnt(4)\n"
"v_add_f32 v244, v244, v169\n"
"v_cvt_pk_bf16_f32 v168, v168, v169\n"
"v_mfma_f32_16x16x32_bf16 v[56:59], a[248:251], v[224:227], v[56:59]\n"
"ds_read_b64_tr_b16 a[240:241], v235 offset:37376\n"
"v_mfma_f32_16x16x32_bf16 v[60:63], a[252:255], v[224:227], v[60:63]\n"
"ds_read_b64_tr_b16 a[242:243], v235 offset:39424\n"
"v_mfma_f32_16x16x32_bf16 v[48:51], a[248:251], v[216:219], v[48:51]\n"
"v_exp_f32 v170, v170\n"
"v_mfma_f32_16x16x32_bf16 v[52:55], a[252:255], v[216:219], v[52:55]\n"
"v_add_f32 v244, v244, v170\n"
"v_mfma_f32_16x16x32_bf16 v[40:43], a[248:251], v[208:211], v[40:43]\n"
"ds_read_b64_tr_b16 a[244:245], v236 offset:37376\n"
"v_mfma_f32_16x16x32_bf16 v[44:47], a[252:255], v[208:211], v[44:47]\n"
"ds_read_b64_tr_b16 a[246:247], v236 offset:39424\n"
"v_mfma_f32_16x16x32_bf16 v[32:35], a[248:251], v[200:203], v[32:35]\n"
"buffer_load_dwordx4 v242, s[48:51], s66 offen offset:3072  lds\n"
"v_mfma_f32_16x16x32_bf16 v[36:39], a[252:255], v[200:203], v[36:39]\n"
"s_waitcnt lgkmcnt(4)\n"
"v_exp_f32 v171, v171\n"
"v_mfma_f32_16x16x32_bf16 v[64:67], a[232:235], v[200:203], v[64:67]\n"
"ds_read_b64_tr_b16 a[248:249], v235 offset:40960\n"
"v_add_f32 v244, v244, v171\n"
"v_mfma_f32_16x16x32_bf16 v[68:71], a[236:239], v[200:203], v[68:71]\n"
"ds_read_b64_tr_b16 a[250:251], v235 offset:43008\n"
"v_cvt_pk_bf16_f32 v169, v170, v171\n"
"v_mfma_f32_16x16x32_bf16 v[72:75], a[232:235], v[208:211], v[72:75]\n"
"v_exp_f32 v172, v172\n"
"v_mfma_f32_16x16x32_bf16 v[76:79], a[236:239], v[208:211], v[76:79]\n"
"v_add_f32 v244, v244, v172\n"
"v_mfma_f32_16x16x32_bf16 v[80:83], a[232:235], v[216:219], v[80:83]\n"
"ds_read_b64_tr_b16 a[252:253], v236 offset:40960\n"
"v_mfma_f32_16x16x32_bf16 v[84:87], a[236:239], v[216:219], v[84:87]\n"
"ds_read_b64_tr_b16 a[254:255], v236 offset:43008\n"
"v_mfma_f32_16x16x32_bf16 v[88:91], a[232:235], v[224:227], v[88:91]\n"
"v_exp_f32 v173, v173\n"
"v_mfma_f32_16x16x32_bf16 v[92:95], a[236:239], v[224:227], v[92:95]\n"
"s_waitcnt lgkmcnt(4)\n"
"v_add_f32 v244, v244, v173\n"
"v_cvt_pk_bf16_f32 v170, v172, v173\n"
"v_mfma_f32_16x16x32_bf16 v[120:123], a[240:243], v[224:227], v[120:123]\n"
"ds_read_b64_tr_b16 a[232:233], v235 offset:41472\n"
"v_mfma_f32_16x16x32_bf16 v[124:127], a[244:247], v[224:227], v[124:127]\n"
"ds_read_b64_tr_b16 a[234:235], v235 offset:43520\n"
"v_mfma_f32_16x16x32_bf16 v[112:115], a[240:243], v[216:219], v[112:115]\n"
"s_waitcnt vmcnt(20)\n"
"s_barrier\n"
"v_mfma_f32_16x16x32_bf16 v[116:119], a[244:247], v[216:219], v[116:119]\n"
"v_exp_f32 v174, v174\n"
"v_mfma_f32_16x16x32_bf16 v[104:107], a[240:243], v[208:211], v[104:107]\n"
"ds_read_b64_tr_b16 a[236:237], v236 offset:41472\n"
"v_add_f32 v244, v244, v174\n"
"v_mfma_f32_16x16x32_bf16 v[108:111], a[244:247], v[208:211], v[108:111]\n"
"ds_read_b64_tr_b16 a[238:239], v236 offset:43520\n"
"v_add_u32 v242, 229376, v241\n"
"s_add_u32 m0, s71, 131072\n"
"v_mfma_f32_16x16x32_bf16 v[96:99], a[240:243], v[200:203], v[96:99]\n"
"buffer_load_dwordx4 v242, s[44:47], 0 offen offset:0 sc0 sc1 nt lds\n"
"v_mfma_f32_16x16x32_bf16 v[100:103], a[244:247], v[200:203], v[100:103]\n"
"s_waitcnt lgkmcnt(4)\n"
"v_exp_f32 v175, v175\n"
"v_mfma_f32_16x16x32_bf16 v[128:131], a[248:251], v[200:203], v[128:131]\n"
"ds_read_b64_tr_b16 a[240:241], v235 offset:45056\n"
"v_add_f32 v244, v244, v175\n"
"v_mfma_f32_16x16x32_bf16 v[132:135], a[252:255], v[200:203], v[132:135]\n"
"ds_read_b64_tr_b16 a[242:243], v235 offset:47104\n"
"v_cvt_pk_bf16_f32 v171, v174, v175\n"
"v_mfma_f32_16x16x32_bf16 v[136:139], a[248:251], v[208:211], v[136:139]\n"
"buffer_load_dwordx4 v242, s[44:47], s61 offen offset:1024 sc0 sc1 nt lds\n"
"v_mfma_f32_16x16x32_bf16 v[140:143], a[252:255], v[208:211], v[140:143]\n"
"v_exp_f32 v176, v176\n"
"v_mfma_f32_16x16x32_bf16 v[144:147], a[248:251], v[216:219], v[144:147]\n"
"ds_read_b64_tr_b16 a[244:245], v236 offset:45056\n"
"v_add_f32 v245, v245, v176\n"
"v_mfma_f32_16x16x32_bf16 v[148:151], a[252:255], v[216:219], v[148:151]\n"
"ds_read_b64_tr_b16 a[246:247], v236 offset:47104\n"
"v_mfma_f32_16x16x32_bf16 a[128:131], a[248:251], v[224:227], a[128:131]\n"
"buffer_load_dwordx4 v242, s[44:47], s62 offen offset:2048 sc0 sc1 nt lds\n"
"v_mfma_f32_16x16x32_bf16 a[132:135], a[252:255], v[224:227], a[132:135]\n"
"s_waitcnt lgkmcnt(4)\n"
"v_exp_f32 v177, v177\n"
"v_mfma_f32_16x16x32_bf16 a[160:163], a[232:235], v[224:227], a[160:163]\n"
"ds_read_b64_tr_b16 a[248:249], v235 offset:45568\n"
"v_add_f32 v245, v245, v177\n"
"v_mfma_f32_16x16x32_bf16 a[164:167], a[236:239], v[224:227], a[164:167]\n"
"ds_read_b64_tr_b16 a[250:251], v235 offset:47616\n"
"v_cvt_pk_bf16_f32 v176, v176, v177\n"
"v_mfma_f32_16x16x32_bf16 a[152:155], a[232:235], v[216:219], a[152:155]\n"
"buffer_load_dwordx4 v242, s[44:47], s63 offen offset:3072 sc0 sc1 nt lds\n"
"v_mfma_f32_16x16x32_bf16 a[156:159], a[236:239], v[216:219], a[156:159]\n"
"v_exp_f32 v178, v178\n"
"v_mfma_f32_16x16x32_bf16 a[144:147], a[232:235], v[208:211], a[144:147]\n"
"ds_read_b64_tr_b16 a[252:253], v236 offset:45568\n"
"v_add_f32 v245, v245, v178\n"
"v_mfma_f32_16x16x32_bf16 a[148:151], a[236:239], v[208:211], a[148:151]\n"
"ds_read_b64_tr_b16 a[254:255], v236 offset:47616\n"
"s_add_u32 s82, s32, 229376\n"
"v_add_u32 v242, s82, v243\n"
"v_xor_b32 v242, 64, v242\n"
"s_add_u32 m0, s71, 16384\n"
"v_mfma_f32_16x16x32_bf16 a[136:139], a[232:235], v[200:203], a[136:139]\n"
"buffer_load_dwordx4 v242, s[48:51], 0 offen offset:0  lds\n"
"v_mfma_f32_16x16x32_bf16 a[140:143], a[236:239], v[200:203], a[140:143]\n"
"s_waitcnt lgkmcnt(4)\n"
"v_exp_f32 v179, v179\n"
"v_mfma_f32_16x16x32_bf16 a[168:171], a[240:243], v[200:203], a[168:171]\n"
"ds_read_b128 a[232:235], v232 offset:49152\n"
"v_add_f32 v245, v245, v179\n"
"v_mfma_f32_16x16x32_bf16 a[172:175], a[244:247], v[200:203], a[172:175]\n"
"v_cvt_pk_bf16_f32 v177, v178, v179\n"
"v_mfma_f32_16x16x32_bf16 a[176:179], a[240:243], v[208:211], a[176:179]\n"
"v_exp_f32 v180, v180\n"
"v_mfma_f32_16x16x32_bf16 a[180:183], a[244:247], v[208:211], a[180:183]\n"
"v_add_f32 v245, v245, v180\n"
"v_mfma_f32_16x16x32_bf16 a[184:187], a[240:243], v[216:219], a[184:187]\n"
"ds_read_b128 a[236:239], v232 offset:49664\n"
"v_mfma_f32_16x16x32_bf16 a[188:191], a[244:247], v[216:219], a[188:191]\n"
"v_exp_f32 v181, v181\n"
"v_mfma_f32_16x16x32_bf16 a[192:195], a[240:243], v[224:227], a[192:195]\n"
"v_add_f32 v245, v245, v181\n"
"v_cvt_pk_bf16_f32 v178, v180, v181\n"
"v_mfma_f32_16x16x32_bf16 a[196:199], a[244:247], v[224:227], a[196:199]\n"
"s_waitcnt lgkmcnt(2)\n"
"v_exp_f32 v182, v182\n"
"v_mfma_f32_16x16x32_bf16 a[200:203], a[248:251], v[200:203], a[200:203]\n"
"ds_read_b128 a[240:243], v232 offset:51200\n"
"v_add_f32 v245, v245, v182\n"
"v_mfma_f32_16x16x32_bf16 a[204:207], a[252:255], v[200:203], a[204:207]\n"
"buffer_load_dwordx4 v242, s[48:51], s64 offen offset:1024  lds\n"
"v_mfma_f32_16x16x32_bf16 a[208:211], a[248:251], v[208:211], a[208:211]\n"
"v_exp_f32 v183, v183\n"
"v_mfma_f32_16x16x32_bf16 a[212:215], a[252:255], v[208:211], a[212:215]\n"
"v_add_f32 v245, v245, v183\n"
"v_cvt_pk_bf16_f32 v179, v182, v183\n"
"v_mfma_f32_16x16x32_bf16 a[216:219], a[248:251], v[216:219], a[216:219]\n"
"ds_read_b128 a[244:247], v232 offset:51712\n"
"v_mfma_f32_16x16x32_bf16 a[220:223], a[252:255], v[216:219], a[220:223]\n"
"v_exp_f32 v184, v184\n"
"v_mfma_f32_16x16x32_bf16 a[224:227], a[248:251], v[224:227], a[224:227]\n"
"v_add_f32 v246, v246, v184\n"
"v_mfma_f32_16x16x32_bf16 a[228:231], a[252:255], v[224:227], a[228:231]\n"
"s_waitcnt lgkmcnt(2)\n"
"v_exp_f32 v185, v185\n"
"v_mfma_f32_16x16x32_bf16 v[200:203], a[232:235], a[0:3], v[152:155]\n"
"ds_read_b128 a[248:251], v232 offset:53248\n"
"v_add_f32 v246, v246, v185\n"
"v_mfma_f32_16x16x32_bf16 v[200:203], a[236:239], a[16:19], v[200:203]\n"
"v_cvt_pk_bf16_f32 v184, v184, v185\n"
"v_mfma_f32_16x16x32_bf16 v[208:211], a[232:235], a[4:7], v[156:159]\n"
"v_exp_f32 v186, v186\n"
"v_mfma_f32_16x16x32_bf16 v[208:211], a[236:239], a[20:23], v[208:211]\n"
"v_add_f32 v246, v246, v186\n"
"v_mfma_f32_16x16x32_bf16 v[216:219], a[232:235], a[8:11], v[160:163]\n"
"ds_read_b128 a[252:255], v232 offset:53760\n"
"v_mfma_f32_16x16x32_bf16 v[216:219], a[236:239], a[24:27], v[216:219]\n"
"buffer_load_dwordx4 v242, s[48:51], s65 offen offset:2048  lds\n"
"v_mfma_f32_16x16x32_bf16 v[224:227], a[232:235], a[12:15], v[164:167]\n"
"v_exp_f32 v187, v187\n"
"v_mfma_f32_16x16x32_bf16 v[224:227], a[236:239], a[28:31], v[224:227]\n"
"s_waitcnt lgkmcnt(2)\n"
"v_add_f32 v246, v246, v187\n"
"v_cvt_pk_bf16_f32 v185, v186, v187\n"
"v_mfma_f32_16x16x32_bf16 v[228:231], a[240:243], a[12:15], v[164:167]\n"
"ds_read_b128 a[232:235], v232 offset:55296\n"
"v_mfma_f32_16x16x32_bf16 v[228:231], a[244:247], a[28:31], v[228:231]\n"
"v_exp_f32 v188, v188\n"
"v_mfma_f32_16x16x32_bf16 v[220:223], a[240:243], a[8:11], v[160:163]\n"
"v_add_f32 v246, v246, v188\n"
"v_mfma_f32_16x16x32_bf16 v[220:223], a[244:247], a[24:27], v[220:223]\n"
"v_exp_f32 v189, v189\n"
"v_mfma_f32_16x16x32_bf16 v[212:215], a[240:243], a[4:7], v[156:159]\n"
"ds_read_b128 a[236:239], v232 offset:55808\n"
"v_add_f32 v246, v246, v189\n"
"v_mfma_f32_16x16x32_bf16 v[212:215], a[244:247], a[20:23], v[212:215]\n"
"v_cvt_pk_bf16_f32 v186, v188, v189\n"
"v_mfma_f32_16x16x32_bf16 v[204:207], a[240:243], a[0:3], v[152:155]\n"
"v_exp_f32 v190, v190\n"
"v_mfma_f32_16x16x32_bf16 v[204:207], a[244:247], a[16:19], v[204:207]\n"
"s_waitcnt lgkmcnt(2)\n"
"v_add_f32 v246, v246, v190\n"
"v_mfma_f32_16x16x32_bf16 v[200:203], a[248:251], a[32:35], v[200:203]\n"
"ds_read_b128 a[240:243], v232 offset:57344\n"
"v_mfma_f32_16x16x32_bf16 v[200:203], a[252:255], a[48:51], v[200:203]\n"
"buffer_load_dwordx4 v242, s[48:51], s66 offen offset:3072  lds\n"
"v_mfma_f32_16x16x32_bf16 v[208:211], a[248:251], a[36:39], v[208:211]\n"
"v_exp_f32 v191, v191\n"
"v_mfma_f32_16x16x32_bf16 v[208:211], a[252:255], a[52:55], v[208:211]\n"
"v_add_f32 v246, v246, v191\n"
"v_cvt_pk_bf16_f32 v187, v190, v191\n"
"v_mfma_f32_16x16x32_bf16 v[216:219], a[248:251], a[40:43], v[216:219]\n"
"ds_read_b128 a[244:247], v232 offset:57856\n"
"v_mfma_f32_16x16x32_bf16 v[216:219], a[252:255], a[56:59], v[216:219]\n"
"v_exp_f32 v192, v192\n"
"v_mfma_f32_16x16x32_bf16 v[224:227], a[248:251], a[44:47], v[224:227]\n"
"v_add_f32 v247, v247, v192\n"
"v_mfma_f32_16x16x32_bf16 v[224:227], a[252:255], a[60:63], v[224:227]\n"
"s_waitcnt lgkmcnt(2)\n"
"v_exp_f32 v193, v193\n"
"v_mfma_f32_16x16x32_bf16 v[228:231], a[232:235], a[44:47], v[228:231]\n"
"ds_read_b128 a[248:251], v232 offset:59392\n"
"v_add_f32 v247, v247, v193\n"
"v_mfma_f32_16x16x32_bf16 v[228:231], a[236:239], a[60:63], v[228:231]\n"
"v_cvt_pk_bf16_f32 v192, v192, v193\n"
"v_mfma_f32_16x16x32_bf16 v[220:223], a[232:235], a[40:43], v[220:223]\n"
"s_waitcnt vmcnt(24)\n"
"s_barrier\n"
"v_mfma_f32_16x16x32_bf16 v[220:223], a[236:239], a[56:59], v[220:223]\n"
"v_exp_f32 v194, v194\n"
"v_mfma_f32_16x16x32_bf16 v[212:215], a[232:235], a[36:39], v[212:215]\n"
"ds_read_b128 a[252:255], v232 offset:59904\n"
"s_add_u32 s82, s32, 114688\n"
"v_add_u32 v242, s82, v243\n"
"s_add_u32 m0, s71, 32768\n"
"v_mfma_f32_16x16x32_bf16 v[212:215], a[236:239], a[52:55], v[212:215]\n"
"buffer_load_dwordx4 v242, s[48:51], 0 offen offset:0  lds\n"
"v_mfma_f32_16x16x32_bf16 v[204:207], a[232:235], a[32:35], v[204:207]\n"
"v_add_f32 v247, v247, v194\n"
"v_mfma_f32_16x16x32_bf16 v[204:207], a[236:239], a[48:51], v[204:207]\n"
"s_waitcnt lgkmcnt(2)\n"
"v_exp_f32 v195, v195\n"
"v_mfma_f32_16x16x32_bf16 v[200:203], a[240:243], a[64:67], v[200:203]\n"
"ds_read_b128 a[232:235], v232 offset:61440\n"
"v_add_f32 v247, v247, v195\n"
"v_mfma_f32_16x16x32_bf16 v[200:203], a[244:247], a[80:83], v[200:203]\n"
"buffer_load_dwordx4 v242, s[48:51], s64 offen offset:1024  lds\n"
"v_mfma_f32_16x16x32_bf16 v[208:211], a[240:243], a[68:71], v[208:211]\n"
"v_cvt_pk_bf16_f32 v193, v194, v195\n"
"v_mfma_f32_16x16x32_bf16 v[208:211], a[244:247], a[84:87], v[208:211]\n"
"v_exp_f32 v196, v196\n"
"v_mfma_f32_16x16x32_bf16 v[216:219], a[240:243], a[72:75], v[216:219]\n"
"ds_read_b128 a[236:239], v232 offset:61952\n"
"v_add_f32 v247, v247, v196\n"
"v_mfma_f32_16x16x32_bf16 v[216:219], a[244:247], a[88:91], v[216:219]\n"
"buffer_load_dwordx4 v242, s[48:51], s65 offen offset:2048  lds\n"
"v_mfma_f32_16x16x32_bf16 v[224:227], a[240:243], a[76:79], v[224:227]\n"
"v_exp_f32 v197, v197\n"
"v_mfma_f32_16x16x32_bf16 v[224:227], a[244:247], a[92:95], v[224:227]\n"
"s_waitcnt lgkmcnt(2)\n"
"v_add_f32 v247, v247, v197\n"
"v_cvt_pk_bf16_f32 v194, v196, v197\n"
"v_mfma_f32_16x16x32_bf16 v[228:231], a[248:251], a[76:79], v[228:231]\n"
"ds_read_b128 a[240:243], v232 offset:63488\n"
"v_mfma_f32_16x16x32_bf16 v[228:231], a[252:255], a[92:95], v[228:231]\n"
"buffer_load_dwordx4 v242, s[48:51], s66 offen offset:3072  lds\n"
"v_mfma_f32_16x16x32_bf16 v[220:223], a[248:251], a[72:75], v[220:223]\n"
"v_add_u32 v242, 245760, v241\n"
"s_add_u32 m0, s71, 147456\n"
"v_mfma_f32_16x16x32_bf16 v[220:223], a[252:255], a[88:91], v[220:223]\n"
"buffer_load_dwordx4 v242, s[44:47], 0 offen offset:0 sc0 sc1 nt lds\n"
"v_mfma_f32_16x16x32_bf16 v[212:215], a[248:251], a[68:71], v[212:215]\n"
"ds_read_b128 a[244:247], v232 offset:64000\n"
"v_mfma_f32_16x16x32_bf16 v[212:215], a[252:255], a[84:87], v[212:215]\n"
"v_exp_f32 v198, v198\n"
"v_mfma_f32_16x16x32_bf16 v[204:207], a[248:251], a[64:67], v[204:207]\n"
"v_add_f32 v247, v247, v198\n"
"v_mfma_f32_16x16x32_bf16 v[204:207], a[252:255], a[80:83], v[204:207]\n"
"s_waitcnt lgkmcnt(2)\n"
"v_exp_f32 v199, v199\n"
"v_mfma_f32_16x16x32_bf16 v[224:227], a[232:235], a[108:111], v[224:227]\n"
"ds_read_b64_tr_b16 a[248:249], v237 offset:0\n"
"v_add_f32 v247, v247, v199\n"
"v_mfma_f32_16x16x32_bf16 v[224:227], a[236:239], a[124:127], v[224:227]\n"
"ds_read_b64_tr_b16 a[250:251], v237 offset:2048\n"
"v_cvt_pk_bf16_f32 v195, v198, v199\n"
"v_mfma_f32_16x16x32_bf16 v[216:219], a[232:235], a[104:107], v[216:219]\n"
"v_mfma_f32_16x16x32_bf16 v[216:219], a[236:239], a[120:123], v[216:219]\n"
"v_mfma_f32_16x16x32_bf16 v[208:211], a[232:235], a[100:103], v[208:211]\n"
"ds_read_b64_tr_b16 a[252:253], v238 offset:0\n"
"v_mfma_f32_16x16x32_bf16 v[208:211], a[236:239], a[116:119], v[208:211]\n"
"ds_read_b64_tr_b16 a[254:255], v238 offset:2048\n"
"v_mfma_f32_16x16x32_bf16 v[200:203], a[232:235], a[96:99], v[200:203]\n"
"v_mfma_f32_16x16x32_bf16 v[200:203], a[236:239], a[112:115], v[200:203]\n"
"s_waitcnt lgkmcnt(4)\n"
"buffer_load_dwordx4 v242, s[44:47], s61 offen offset:1024 sc0 sc1 nt lds\n"
"v_mfma_f32_16x16x32_bf16 v[204:207], a[240:243], a[96:99], v[204:207]\n"
"ds_read_b64_tr_b16 a[232:233], v237 offset:512\n"
"v_mfma_f32_16x16x32_bf16 v[204:207], a[244:247], a[112:115], v[204:207]\n"
"ds_read_b64_tr_b16 a[234:235], v237 offset:2560\n"
"v_mfma_f32_16x16x32_bf16 v[212:215], a[240:243], a[100:103], v[212:215]\n"
"v_mfma_f32_16x16x32_bf16 v[212:215], a[244:247], a[116:119], v[212:215]\n"
"v_mfma_f32_16x16x32_bf16 v[220:223], a[240:243], a[104:107], v[220:223]\n"
"ds_read_b64_tr_b16 a[236:237], v238 offset:512\n"
"v_mfma_f32_16x16x32_bf16 v[220:223], a[244:247], a[120:123], v[220:223]\n"
"ds_read_b64_tr_b16 a[238:239], v238 offset:2560\n"
"v_mfma_f32_16x16x32_bf16 v[228:231], a[240:243], a[108:111], v[228:231]\n"
"v_mfma_f32_16x16x32_bf16 v[228:231], a[244:247], a[124:127], v[228:231]\n"
"s_waitcnt lgkmcnt(4)\n"
"v_mfma_f32_16x16x32_bf16 v[0:3], a[248:251], v[168:171], v[0:3]\n"
"ds_read_b64_tr_b16 a[240:241], v237 offset:4096\n"
"v_mfma_f32_16x16x32_bf16 v[4:7], a[252:255], v[168:171], v[4:7]\n"
"ds_read_b64_tr_b16 a[242:243], v237 offset:6144\n"
"v_mfma_f32_16x16x32_bf16 v[8:11], a[248:251], v[176:179], v[8:11]\n"
"buffer_load_dword v248, v249, s[48:51], s87 offen\n"
"v_mfma_f32_16x16x32_bf16 v[12:15], a[252:255], v[176:179], v[12:15]\n"
"buffer_load_dwordx4 v242, s[44:47], s62 offen offset:2048 sc0 sc1 nt lds\n"
"v_mfma_f32_16x16x32_bf16 v[16:19], a[248:251], v[184:187], v[16:19]\n"
"ds_read_b64_tr_b16 a[244:245], v238 offset:4096\n"
"v_mfma_f32_16x16x32_bf16 v[20:23], a[252:255], v[184:187], v[20:23]\n"
"ds_read_b64_tr_b16 a[246:247], v238 offset:6144\n"
"v_mfma_f32_16x16x32_bf16 v[24:27], a[248:251], v[192:195], v[24:27]\n"
"v_exp_f32 v200, v200\n"
"v_mfma_f32_16x16x32_bf16 v[28:31], a[252:255], v[192:195], v[28:31]\n"
"s_waitcnt lgkmcnt(4)\n"
"v_add_f32 v244, v244, v200\n"
"v_mfma_f32_16x16x32_bf16 v[56:59], a[232:235], v[192:195], v[56:59]\n"
"ds_read_b64_tr_b16 a[248:249], v237 offset:4608\n"
"v_mfma_f32_16x16x32_bf16 v[60:63], a[236:239], v[192:195], v[60:63]\n"
"ds_read_b64_tr_b16 a[250:251], v237 offset:6656\n"
"v_mfma_f32_16x16x32_bf16 v[48:51], a[232:235], v[184:187], v[48:51]\n"
"v_exp_f32 v201, v201\n"
"v_mfma_f32_16x16x32_bf16 v[52:55], a[236:239], v[184:187], v[52:55]\n"
"v_add_f32 v244, v244, v201\n"
"v_cvt_pk_bf16_f32 v200, v200, v201\n"
"v_mfma_f32_16x16x32_bf16 v[40:43], a[232:235], v[176:179], v[40:43]\n"
"ds_read_b64_tr_b16 a[252:253], v238 offset:4608\n"
"v_mfma_f32_16x16x32_bf16 v[44:47], a[236:239], v[176:179], v[44:47]\n"
"ds_read_b64_tr_b16 a[254:255], v238 offset:6656\n"
"v_mfma_f32_16x16x32_bf16 v[32:35], a[232:235], v[168:171], v[32:35]\n"
"v_exp_f32 v202, v202\n"
"v_mfma_f32_16x16x32_bf16 v[36:39], a[236:239], v[168:171], v[36:39]\n"
"s_waitcnt lgkmcnt(4)\n"
"buffer_load_dwordx4 v242, s[44:47], s63 offen offset:3072 sc0 sc1 nt lds\n"
"v_mfma_f32_16x16x32_bf16 v[64:67], a[240:243], v[168:171], v[64:67]\n"
"ds_read_b64_tr_b16 a[232:233], v237 offset:8192\n"
"v_add_f32 v244, v244, v202\n"
"v_mfma_f32_16x16x32_bf16 v[68:71], a[244:247], v[168:171], v[68:71]\n"
"ds_read_b64_tr_b16 a[234:235], v237 offset:10240\n"
"v_mfma_f32_16x16x32_bf16 v[72:75], a[240:243], v[176:179], v[72:75]\n"
"v_exp_f32 v203, v203\n"
"v_mfma_f32_16x16x32_bf16 v[76:79], a[244:247], v[176:179], v[76:79]\n"
"v_add_f32 v244, v244, v203\n"
"v_cvt_pk_bf16_f32 v201, v202, v203\n"
"v_mfma_f32_16x16x32_bf16 v[80:83], a[240:243], v[184:187], v[80:83]\n"
"ds_read_b64_tr_b16 a[236:237], v238 offset:8192\n"
"v_mfma_f32_16x16x32_bf16 v[84:87], a[244:247], v[184:187], v[84:87]\n"
"ds_read_b64_tr_b16 a[238:239], v238 offset:10240\n"
"v_mfma_f32_16x16x32_bf16 v[88:91], a[240:243], v[192:195], v[88:91]\n"
"v_exp_f32 v204, v204\n"
"v_mfma_f32_16x16x32_bf16 v[92:95], a[244:247], v[192:195], v[92:95]\n"
"s_waitcnt lgkmcnt(4)\n"
"v_add_f32 v244, v244, v204\n"
"v_mfma_f32_16x16x32_bf16 v[120:123], a[248:251], v[192:195], v[120:123]\n"
"ds_read_b64_tr_b16 a[240:241], v237 offset:8704\n"
"v_mfma_f32_16x16x32_bf16 v[124:127], a[252:255], v[192:195], v[124:127]\n"
"ds_read_b64_tr_b16 a[242:243], v237 offset:10752\n"
"v_mfma_f32_16x16x32_bf16 v[112:115], a[248:251], v[184:187], v[112:115]\n"
"s_waitcnt vmcnt(29)\n"
"s_barrier\n"
"v_mfma_f32_16x16x32_bf16 v[116:119], a[252:255], v[184:187], v[116:119]\n"
"v_exp_f32 v205, v205\n"
"v_mfma_f32_16x16x32_bf16 v[104:107], a[248:251], v[176:179], v[104:107]\n"
"ds_read_b64_tr_b16 a[244:245], v238 offset:8704\n"
"v_add_f32 v244, v244, v205\n"
"v_mfma_f32_16x16x32_bf16 v[108:111], a[252:255], v[176:179], v[108:111]\n"
"ds_read_b64_tr_b16 a[246:247], v238 offset:10752\n"
"s_add_u32 s82, s32, 344064\n"
"v_add_u32 v242, s82, v243\n"
"v_xor_b32 v242, 64, v242\n"
"s_add_u32 m0, s71, 49152\n"
"v_mfma_f32_16x16x32_bf16 v[96:99], a[248:251], v[168:171], v[96:99]\n"
"buffer_load_dwordx4 v242, s[48:51], 0 offen offset:0  lds\n"
"v_mfma_f32_16x16x32_bf16 v[100:103], a[252:255], v[168:171], v[100:103]\n"
"s_waitcnt lgkmcnt(4)\n"
"v_cvt_pk_bf16_f32 v202, v204, v205\n"
"v_mfma_f32_16x16x32_bf16 v[128:131], a[232:235], v[168:171], v[128:131]\n"
"ds_read_b64_tr_b16 a[248:249], v237 offset:12288\n"
"v_mfma_f32_16x16x32_bf16 v[132:135], a[236:239], v[168:171], v[132:135]\n"
"ds_read_b64_tr_b16 a[250:251], v237 offset:14336\n"
"v_mfma_f32_16x16x32_bf16 v[136:139], a[232:235], v[176:179], v[136:139]\n"
"v_exp_f32 v206, v206\n"
"v_mfma_f32_16x16x32_bf16 v[140:143], a[236:239], v[176:179], v[140:143]\n"
"v_add_f32 v244, v244, v206\n"
"v_mfma_f32_16x16x32_bf16 v[144:147], a[232:235], v[184:187], v[144:147]\n"
"ds_read_b64_tr_b16 a[252:253], v238 offset:12288\n"
"v_mfma_f32_16x16x32_bf16 v[148:151], a[236:239], v[184:187], v[148:151]\n"
"ds_read_b64_tr_b16 a[254:255], v238 offset:14336\n"
"v_mfma_f32_16x16x32_bf16 a[128:131], a[232:235], v[192:195], a[128:131]\n"
"v_exp_f32 v207, v207\n"
"v_mfma_f32_16x16x32_bf16 a[132:135], a[236:239], v[192:195], a[132:135]\n"
"s_waitcnt lgkmcnt(4)\n"
"v_add_f32 v244, v244, v207\n"
"v_cvt_pk_bf16_f32 v203, v206, v207\n"
"v_mfma_f32_16x16x32_bf16 a[160:163], a[240:243], v[192:195], a[160:163]\n"
"ds_read_b64_tr_b16 a[232:233], v237 offset:12800\n"
"v_mfma_f32_16x16x32_bf16 a[164:167], a[244:247], v[192:195], a[164:167]\n"
"ds_read_b64_tr_b16 a[234:235], v237 offset:14848\n"
"v_mfma_f32_16x16x32_bf16 a[152:155], a[240:243], v[184:187], a[152:155]\n"
"v_exp_f32 v208, v208\n"
"v_mfma_f32_16x16x32_bf16 a[156:159], a[244:247], v[184:187], a[156:159]\n"
"v_add_f32 v245, v245, v208\n"
"v_mfma_f32_16x16x32_bf16 a[144:147], a[240:243], v[176:179], a[144:147]\n"
"ds_read_b64_tr_b16 a[236:237], v238 offset:12800\n"
"v_mfma_f32_16x16x32_bf16 a[148:151], a[244:247], v[176:179], a[148:151]\n"
"ds_read_b64_tr_b16 a[238:239], v238 offset:14848\n"
"v_mfma_f32_16x16x32_bf16 a[136:139], a[240:243], v[168:171], a[136:139]\n"
"buffer_load_dwordx4 v242, s[48:51], s64 offen offset:1024  lds\n"
"v_mfma_f32_16x16x32_bf16 a[140:143], a[244:247], v[168:171], a[140:143]\n"
"s_waitcnt lgkmcnt(4)\n"
"v_exp_f32 v209, v209\n"
"v_mfma_f32_16x16x32_bf16 a[168:171], a[248:251], v[168:171], a[168:171]\n"
"ds_read_b128 a[240:243], v233 offset:16384\n"
"v_add_f32 v245, v245, v209\n"
"v_mfma_f32_16x16x32_bf16 a[172:175], a[252:255], v[168:171], a[172:175]\n"
"v_cvt_pk_bf16_f32 v208, v208, v209\n"
"v_mfma_f32_16x16x32_bf16 a[176:179], a[248:251], v[176:179], a[176:179]\n"
"v_exp_f32 v210, v210\n"
"v_mfma_f32_16x16x32_bf16 a[180:183], a[252:255], v[176:179], a[180:183]\n"
"v_add_f32 v245, v245, v210\n"
"v_mfma_f32_16x16x32_bf16 a[184:187], a[248:251], v[184:187], a[184:187]\n"
"ds_read_b128 a[244:247], v233 offset:16896\n"
"v_mfma_f32_16x16x32_bf16 a[188:191], a[252:255], v[184:187], a[188:191]\n"
"v_exp_f32 v211, v211\n"
"v_mfma_f32_16x16x32_bf16 a[192:195], a[248:251], v[192:195], a[192:195]\n"
"v_add_f32 v245, v245, v211\n"
"v_cvt_pk_bf16_f32 v209, v210, v211\n"
"v_mfma_f32_16x16x32_bf16 a[196:199], a[252:255], v[192:195], a[196:199]\n"
"s_waitcnt lgkmcnt(2)\n"
"v_exp_f32 v212, v212\n"
"v_mfma_f32_16x16x32_bf16 a[200:203], a[232:235], v[168:171], a[200:203]\n"
"ds_read_b128 a[248:251], v233 offset:18432\n"
"v_add_f32 v245, v245, v212\n"
"v_mfma_f32_16x16x32_bf16 a[204:207], a[236:239], v[168:171], a[204:207]\n"
"v_exp_f32 v213, v213\n"
"v_mfma_f32_16x16x32_bf16 a[208:211], a[232:235], v[176:179], a[208:211]\n"
"v_add_f32 v245, v245, v213\n"
"v_cvt_pk_bf16_f32 v210, v212, v213\n"
"v_mfma_f32_16x16x32_bf16 a[212:215], a[236:239], v[176:179], a[212:215]\n"
"v_exp_f32 v214, v214\n"
"v_mfma_f32_16x16x32_bf16 a[216:219], a[232:235], v[184:187], a[216:219]\n"
"ds_read_b128 a[252:255], v233 offset:18944\n"
"v_add_f32 v245, v245, v214\n"
"v_mfma_f32_16x16x32_bf16 a[220:223], a[236:239], v[184:187], a[220:223]\n"
"buffer_load_dwordx4 v242, s[48:51], s65 offen offset:2048  lds\n"
"v_mfma_f32_16x16x32_bf16 a[224:227], a[232:235], v[192:195], a[224:227]\n"
"v_exp_f32 v215, v215\n"
"v_mfma_f32_16x16x32_bf16 a[228:231], a[236:239], v[192:195], a[228:231]\n"
"s_waitcnt lgkmcnt(2)\n"
"v_add_f32 v245, v245, v215\n"
"v_cvt_pk_bf16_f32 v211, v214, v215\n"
"v_mfma_f32_16x16x32_bf16 v[168:171], a[240:243], a[0:3], v[152:155]\n"
"ds_read_b128 a[232:235], v233 offset:20480\n"
"v_mfma_f32_16x16x32_bf16 v[168:171], a[244:247], a[16:19], v[168:171]\n"
"v_exp_f32 v216, v216\n"
"v_mfma_f32_16x16x32_bf16 v[176:179], a[240:243], a[4:7], v[156:159]\n"
"v_add_f32 v246, v246, v216\n"
"v_mfma_f32_16x16x32_bf16 v[176:179], a[244:247], a[20:23], v[176:179]\n"
"v_exp_f32 v217, v217\n"
"v_mfma_f32_16x16x32_bf16 v[184:187], a[240:243], a[8:11], v[160:163]\n"
"ds_read_b128 a[236:239], v233 offset:20992\n"
"v_add_f32 v246, v246, v217\n"
"v_mfma_f32_16x16x32_bf16 v[184:187], a[244:247], a[24:27], v[184:187]\n"
"v_cvt_pk_bf16_f32 v216, v216, v217\n"
"v_mfma_f32_16x16x32_bf16 v[192:195], a[240:243], a[12:15], v[164:167]\n"
"v_exp_f32 v218, v218\n"
"v_mfma_f32_16x16x32_bf16 v[192:195], a[244:247], a[28:31], v[192:195]\n"
"s_waitcnt lgkmcnt(2)\n"
"v_add_f32 v246, v246, v218\n"
"v_mfma_f32_16x16x32_bf16 v[196:199], a[248:251], a[12:15], v[164:167]\n"
"ds_read_b128 a[240:243], v233 offset:22528\n"
"v_mfma_f32_16x16x32_bf16 v[196:199], a[252:255], a[28:31], v[196:199]\n"
"v_exp_f32 v219, v219\n"
"v_mfma_f32_16x16x32_bf16 v[188:191], a[248:251], a[8:11], v[160:163]\n"
"v_add_f32 v246, v246, v219\n"
"v_cvt_pk_bf16_f32 v217, v218, v219\n"
"v_mfma_f32_16x16x32_bf16 v[188:191], a[252:255], a[24:27], v[188:191]\n"
"v_exp_f32 v220, v220\n"
"v_mfma_f32_16x16x32_bf16 v[180:183], a[248:251], a[4:7], v[156:159]\n"
"ds_read_b128 a[244:247], v233 offset:23040\n"
"v_add_f32 v246, v246, v220\n"
"v_mfma_f32_16x16x32_bf16 v[180:183], a[252:255], a[20:23], v[180:183]\n"
"buffer_load_dwordx4 v242, s[48:51], s66 offen offset:3072  lds\n"
"v_mfma_f32_16x16x32_bf16 v[172:175], a[248:251], a[0:3], v[152:155]\n"
"v_exp_f32 v221, v221\n"
"v_mfma_f32_16x16x32_bf16 v[172:175], a[252:255], a[16:19], v[172:175]\n"
"s_waitcnt lgkmcnt(2)\n"
"v_add_f32 v246, v246, v221\n"
"v_cvt_pk_bf16_f32 v218, v220, v221\n"
"v_mfma_f32_16x16x32_bf16 v[168:171], a[232:235], a[32:35], v[168:171]\n"
"ds_read_b128 a[248:251], v233 offset:24576\n"
"v_mfma_f32_16x16x32_bf16 v[168:171], a[236:239], a[48:51], v[168:171]\n"
"v_exp_f32 v222, v222\n"
"v_mfma_f32_16x16x32_bf16 v[176:179], a[232:235], a[36:39], v[176:179]\n"
"v_add_f32 v246, v246, v222\n"
"v_mfma_f32_16x16x32_bf16 v[176:179], a[236:239], a[52:55], v[176:179]\n"
"v_exp_f32 v223, v223\n"
"v_mfma_f32_16x16x32_bf16 v[184:187], a[232:235], a[40:43], v[184:187]\n"
"ds_read_b128 a[252:255], v233 offset:25088\n"
"v_add_f32 v246, v246, v223\n"
"v_mfma_f32_16x16x32_bf16 v[184:187], a[236:239], a[56:59], v[184:187]\n"
"v_cvt_pk_bf16_f32 v219, v222, v223\n"
"v_mfma_f32_16x16x32_bf16 v[192:195], a[232:235], a[44:47], v[192:195]\n"
"v_exp_f32 v224, v224\n"
"v_mfma_f32_16x16x32_bf16 v[192:195], a[236:239], a[60:63], v[192:195]\n"
"s_waitcnt lgkmcnt(2)\n"
"v_add_f32 v247, v247, v224\n"
"v_mfma_f32_16x16x32_bf16 v[196:199], a[240:243], a[44:47], v[196:199]\n"
"ds_read_b128 a[232:235], v233 offset:26624\n"
"v_mfma_f32_16x16x32_bf16 v[196:199], a[244:247], a[60:63], v[196:199]\n"
"v_exp_f32 v225, v225\n"
"v_mfma_f32_16x16x32_bf16 v[188:191], a[240:243], a[40:43], v[188:191]\n"
"s_waitcnt vmcnt(29)\n"
"s_barrier\n"
"v_add_f32 v247, v247, v225\n"
"v_mfma_f32_16x16x32_bf16 v[188:191], a[244:247], a[56:59], v[188:191]\n"
"v_cvt_pk_bf16_f32 v224, v224, v225\n"
"v_mfma_f32_16x16x32_bf16 v[180:183], a[240:243], a[36:39], v[180:183]\n"
"ds_read_b128 a[236:239], v233 offset:27136\n"
"s_add_u32 s82, s33, 0\n"
"v_add_u32 v242, s82, v243\n"
"s_add_u32 m0, s71, 65536\n"
"v_mfma_f32_16x16x32_bf16 v[180:183], a[244:247], a[52:55], v[180:183]\n"
"buffer_load_dwordx4 v242, s[48:51], 0 offen offset:0  lds\n"
"v_mfma_f32_16x16x32_bf16 v[172:175], a[240:243], a[32:35], v[172:175]\n"
"v_exp_f32 v226, v226\n"
"v_mfma_f32_16x16x32_bf16 v[172:175], a[244:247], a[48:51], v[172:175]\n"
"s_waitcnt lgkmcnt(2)\n"
"v_add_f32 v247, v247, v226\n"
"v_mfma_f32_16x16x32_bf16 v[168:171], a[248:251], a[64:67], v[168:171]\n"
"ds_read_b128 a[240:243], v233 offset:28672\n"
"v_mfma_f32_16x16x32_bf16 v[168:171], a[252:255], a[80:83], v[168:171]\n"
"v_exp_f32 v227, v227\n"
"v_mfma_f32_16x16x32_bf16 v[176:179], a[248:251], a[68:71], v[176:179]\n"
"v_add_f32 v247, v247, v227\n"
"v_cvt_pk_bf16_f32 v225, v226, v227\n"
"v_mfma_f32_16x16x32_bf16 v[176:179], a[252:255], a[84:87], v[176:179]\n"
"v_exp_f32 v228, v228\n"
"v_mfma_f32_16x16x32_bf16 v[184:187], a[248:251], a[72:75], v[184:187]\n"
"ds_read_b128 a[244:247], v233 offset:29184\n"
"v_add_f32 v247, v247, v228\n"
"v_mfma_f32_16x16x32_bf16 v[184:187], a[252:255], a[88:91], v[184:187]\n"
"v_exp_f32 v229, v229\n"
"v_mfma_f32_16x16x32_bf16 v[192:195], a[248:251], a[76:79], v[192:195]\n"
"v_add_f32 v247, v247, v229\n"
"v_cvt_pk_bf16_f32 v226, v228, v229\n"
"v_mfma_f32_16x16x32_bf16 v[192:195], a[252:255], a[92:95], v[192:195]\n"
"s_waitcnt lgkmcnt(2)\n"
"v_exp_f32 v230, v230\n"
"v_mfma_f32_16x16x32_bf16 v[196:199], a[232:235], a[76:79], v[196:199]\n"
"ds_read_b128 a[248:251], v233 offset:30720\n"
"v_add_f32 v247, v247, v230\n"
"v_mfma_f32_16x16x32_bf16 v[196:199], a[236:239], a[92:95], v[196:199]\n"
"v_exp_f32 v231, v231\n"
"v_mfma_f32_16x16x32_bf16 v[188:191], a[232:235], a[72:75], v[188:191]\n"
"v_add_f32 v247, v247, v231\n"
"v_cvt_pk_bf16_f32 v227, v230, v231\n"
"v_mfma_f32_16x16x32_bf16 v[188:191], a[236:239], a[88:91], v[188:191]\n"
"v_mfma_f32_16x16x32_bf16 v[180:183], a[232:235], a[68:71], v[180:183]\n"
"ds_read_b128 a[252:255], v233 offset:31232\n"
"v_mfma_f32_16x16x32_bf16 v[180:183], a[236:239], a[84:87], v[180:183]\n"
"buffer_load_dwordx4 v242, s[48:51], s64 offen offset:1024  lds\n"
"v_mfma_f32_16x16x32_bf16 v[172:175], a[232:235], a[64:67], v[172:175]\n"
"v_mfma_f32_16x16x32_bf16 v[172:175], a[236:239], a[80:83], v[172:175]\n"
"s_waitcnt lgkmcnt(2)\n"
"v_mfma_f32_16x16x32_bf16 v[192:195], a[240:243], a[108:111], v[192:195]\n"
"ds_read_b64_tr_b16 a[232:233], v237 offset:32768\n"
"v_mfma_f32_16x16x32_bf16 v[192:195], a[244:247], a[124:127], v[192:195]\n"
"ds_read_b64_tr_b16 a[234:235], v237 offset:34816\n"
"v_mfma_f32_16x16x32_bf16 v[184:187], a[240:243], a[104:107], v[184:187]\n"
"v_mfma_f32_16x16x32_bf16 v[184:187], a[244:247], a[120:123], v[184:187]\n"
"v_mfma_f32_16x16x32_bf16 v[176:179], a[240:243], a[100:103], v[176:179]\n"
"ds_read_b64_tr_b16 a[236:237], v238 offset:32768\n"
"v_mfma_f32_16x16x32_bf16 v[176:179], a[244:247], a[116:119], v[176:179]\n"
"ds_read_b64_tr_b16 a[238:239], v238 offset:34816\n"
"v_mfma_f32_16x16x32_bf16 v[168:171], a[240:243], a[96:99], v[168:171]\n"
"v_mfma_f32_16x16x32_bf16 v[168:171], a[244:247], a[112:115], v[168:171]\n"
"s_waitcnt lgkmcnt(4)\n"
"v_mfma_f32_16x16x32_bf16 v[172:175], a[248:251], a[96:99], v[172:175]\n"
"ds_read_b64_tr_b16 a[240:241], v237 offset:33280\n"
"v_mfma_f32_16x16x32_bf16 v[172:175], a[252:255], a[112:115], v[172:175]\n"
"ds_read_b64_tr_b16 a[242:243], v237 offset:35328\n"
"v_mfma_f32_16x16x32_bf16 v[180:183], a[248:251], a[100:103], v[180:183]\n"
"v_mfma_f32_16x16x32_bf16 v[180:183], a[252:255], a[116:119], v[180:183]\n"
"v_mfma_f32_16x16x32_bf16 v[188:191], a[248:251], a[104:107], v[188:191]\n"
"ds_read_b64_tr_b16 a[244:245], v238 offset:33280\n"
"v_mfma_f32_16x16x32_bf16 v[188:191], a[252:255], a[120:123], v[188:191]\n"
"ds_read_b64_tr_b16 a[246:247], v238 offset:35328\n"
"v_mfma_f32_16x16x32_bf16 v[196:199], a[248:251], a[108:111], v[196:199]\n"
"buffer_load_dwordx4 v242, s[48:51], s65 offen offset:2048  lds\n"
"v_mfma_f32_16x16x32_bf16 v[196:199], a[252:255], a[124:127], v[196:199]\n"
"s_waitcnt lgkmcnt(4)\n"
"v_mfma_f32_16x16x32_bf16 v[0:3], a[232:235], v[200:203], v[0:3]\n"
"ds_read_b64_tr_b16 a[248:249], v237 offset:36864\n"
"v_mfma_f32_16x16x32_bf16 v[4:7], a[236:239], v[200:203], v[4:7]\n"
"ds_read_b64_tr_b16 a[250:251], v237 offset:38912\n"
"v_mfma_f32_16x16x32_bf16 v[8:11], a[232:235], v[208:211], v[8:11]\n"
"v_exp_f32 v168, v168\n"
"v_mfma_f32_16x16x32_bf16 v[12:15], a[236:239], v[208:211], v[12:15]\n"
"v_add_f32 v244, v244, v168\n"
"v_mfma_f32_16x16x32_bf16 v[16:19], a[232:235], v[216:219], v[16:19]\n"
"ds_read_b64_tr_b16 a[252:253], v238 offset:36864\n"
"v_mfma_f32_16x16x32_bf16 v[20:23], a[236:239], v[216:219], v[20:23]\n"
"ds_read_b64_tr_b16 a[254:255], v238 offset:38912\n"
"v_mfma_f32_16x16x32_bf16 v[24:27], a[232:235], v[224:227], v[24:27]\n"
"v_exp_f32 v169, v169\n"
"v_mfma_f32_16x16x32_bf16 v[28:31], a[236:239], v[224:227], v[28:31]\n"
"s_waitcnt lgkmcnt(4)\n"
"v_add_f32 v244, v244, v169\n"
"v_cvt_pk_bf16_f32 v168, v168, v169\n"
"v_mfma_f32_16x16x32_bf16 v[56:59], a[240:243], v[224:227], v[56:59]\n"
"ds_read_b64_tr_b16 a[232:233], v237 offset:37376\n"
"v_mfma_f32_16x16x32_bf16 v[60:63], a[244:247], v[224:227], v[60:63]\n"
"ds_read_b64_tr_b16 a[234:235], v237 offset:39424\n"
"v_mfma_f32_16x16x32_bf16 v[48:51], a[240:243], v[216:219], v[48:51]\n"
"v_exp_f32 v170, v170\n"
"v_mfma_f32_16x16x32_bf16 v[52:55], a[244:247], v[216:219], v[52:55]\n"
"v_add_f32 v244, v244, v170\n"
"v_mfma_f32_16x16x32_bf16 v[40:43], a[240:243], v[208:211], v[40:43]\n"
"ds_read_b64_tr_b16 a[236:237], v238 offset:37376\n"
"v_mfma_f32_16x16x32_bf16 v[44:47], a[244:247], v[208:211], v[44:47]\n"
"ds_read_b64_tr_b16 a[238:239], v238 offset:39424\n"
"v_mfma_f32_16x16x32_bf16 v[32:35], a[240:243], v[200:203], v[32:35]\n"
"buffer_load_dwordx4 v242, s[48:51], s66 offen offset:3072  lds\n"
"v_mfma_f32_16x16x32_bf16 v[36:39], a[244:247], v[200:203], v[36:39]\n"
"s_waitcnt lgkmcnt(4)\n"
"v_exp_f32 v171, v171\n"
"v_mfma_f32_16x16x32_bf16 v[64:67], a[248:251], v[200:203], v[64:67]\n"
"ds_read_b64_tr_b16 a[240:241], v237 offset:40960\n"
"v_add_f32 v244, v244, v171\n"
"v_mfma_f32_16x16x32_bf16 v[68:71], a[252:255], v[200:203], v[68:71]\n"
"ds_read_b64_tr_b16 a[242:243], v237 offset:43008\n"
"v_cvt_pk_bf16_f32 v169, v170, v171\n"
"v_mfma_f32_16x16x32_bf16 v[72:75], a[248:251], v[208:211], v[72:75]\n"
"v_exp_f32 v172, v172\n"
"v_mfma_f32_16x16x32_bf16 v[76:79], a[252:255], v[208:211], v[76:79]\n"
"v_add_f32 v244, v244, v172\n"
"v_mfma_f32_16x16x32_bf16 v[80:83], a[248:251], v[216:219], v[80:83]\n"
"ds_read_b64_tr_b16 a[244:245], v238 offset:40960\n"
"v_mfma_f32_16x16x32_bf16 v[84:87], a[252:255], v[216:219], v[84:87]\n"
"ds_read_b64_tr_b16 a[246:247], v238 offset:43008\n"
"v_mfma_f32_16x16x32_bf16 v[88:91], a[248:251], v[224:227], v[88:91]\n"
"v_exp_f32 v173, v173\n"
"v_mfma_f32_16x16x32_bf16 v[92:95], a[252:255], v[224:227], v[92:95]\n"
"s_waitcnt lgkmcnt(4)\n"
"v_add_f32 v244, v244, v173\n"
"v_cvt_pk_bf16_f32 v170, v172, v173\n"
"v_mfma_f32_16x16x32_bf16 v[120:123], a[232:235], v[224:227], v[120:123]\n"
"ds_read_b64_tr_b16 a[248:249], v237 offset:41472\n"
"v_mfma_f32_16x16x32_bf16 v[124:127], a[236:239], v[224:227], v[124:127]\n"
"ds_read_b64_tr_b16 a[250:251], v237 offset:43520\n"
"v_mfma_f32_16x16x32_bf16 v[112:115], a[232:235], v[216:219], v[112:115]\n"
"s_waitcnt vmcnt(29)\n"
"s_barrier\n"
"v_mfma_f32_16x16x32_bf16 v[116:119], a[236:239], v[216:219], v[116:119]\n"
"v_exp_f32 v174, v174\n"
"v_mfma_f32_16x16x32_bf16 v[104:107], a[232:235], v[208:211], v[104:107]\n"
"ds_read_b64_tr_b16 a[252:253], v238 offset:41472\n"
"v_add_f32 v244, v244, v174\n"
"v_mfma_f32_16x16x32_bf16 v[108:111], a[236:239], v[208:211], v[108:111]\n"
"ds_read_b64_tr_b16 a[254:255], v238 offset:43520\n"
"s_add_u32 s82, s33, 229376\n"
"v_add_u32 v242, s82, v243\n"
"v_xor_b32 v242, 64, v242\n"
"s_add_u32 m0, s71, 81920\n"
"v_mfma_f32_16x16x32_bf16 v[96:99], a[232:235], v[200:203], v[96:99]\n"
"buffer_load_dwordx4 v242, s[48:51], 0 offen offset:0  lds\n"
"v_mfma_f32_16x16x32_bf16 v[100:103], a[236:239], v[200:203], v[100:103]\n"
"s_waitcnt lgkmcnt(4)\n"
"v_exp_f32 v175, v175\n"
"v_mfma_f32_16x16x32_bf16 v[128:131], a[240:243], v[200:203], v[128:131]\n"
"ds_read_b64_tr_b16 a[232:233], v237 offset:45056\n"
"v_add_f32 v244, v244, v175\n"
"v_mfma_f32_16x16x32_bf16 v[132:135], a[244:247], v[200:203], v[132:135]\n"
"ds_read_b64_tr_b16 a[234:235], v237 offset:47104\n"
"v_cvt_pk_bf16_f32 v171, v174, v175\n"
"v_mfma_f32_16x16x32_bf16 v[136:139], a[240:243], v[208:211], v[136:139]\n"
"v_exp_f32 v176, v176\n"
"v_mfma_f32_16x16x32_bf16 v[140:143], a[244:247], v[208:211], v[140:143]\n"
"v_add_f32 v245, v245, v176\n"
"v_mfma_f32_16x16x32_bf16 v[144:147], a[240:243], v[216:219], v[144:147]\n"
"ds_read_b64_tr_b16 a[236:237], v238 offset:45056\n"
"v_mfma_f32_16x16x32_bf16 v[148:151], a[244:247], v[216:219], v[148:151]\n"
"ds_read_b64_tr_b16 a[238:239], v238 offset:47104\n"
"v_mfma_f32_16x16x32_bf16 a[128:131], a[240:243], v[224:227], a[128:131]\n"
"v_exp_f32 v177, v177\n"
"v_mfma_f32_16x16x32_bf16 a[132:135], a[244:247], v[224:227], a[132:135]\n"
"s_waitcnt lgkmcnt(4)\n"
"v_add_f32 v245, v245, v177\n"
"v_cvt_pk_bf16_f32 v176, v176, v177\n"
"v_mfma_f32_16x16x32_bf16 a[160:163], a[248:251], v[224:227], a[160:163]\n"
"ds_read_b64_tr_b16 a[240:241], v237 offset:45568\n"
"v_mfma_f32_16x16x32_bf16 a[164:167], a[252:255], v[224:227], a[164:167]\n"
"ds_read_b64_tr_b16 a[242:243], v237 offset:47616\n"
"v_mfma_f32_16x16x32_bf16 a[152:155], a[248:251], v[216:219], a[152:155]\n"
"v_exp_f32 v178, v178\n"
"v_mfma_f32_16x16x32_bf16 a[156:159], a[252:255], v[216:219], a[156:159]\n"
"v_add_f32 v245, v245, v178\n"
"v_mfma_f32_16x16x32_bf16 a[144:147], a[248:251], v[208:211], a[144:147]\n"
"ds_read_b64_tr_b16 a[244:245], v238 offset:45568\n"
"v_mfma_f32_16x16x32_bf16 a[148:151], a[252:255], v[208:211], a[148:151]\n"
"ds_read_b64_tr_b16 a[246:247], v238 offset:47616\n"
"v_mfma_f32_16x16x32_bf16 a[136:139], a[248:251], v[200:203], a[136:139]\n"
"buffer_load_dwordx4 v242, s[48:51], s64 offen offset:1024  lds\n"
"v_mfma_f32_16x16x32_bf16 a[140:143], a[252:255], v[200:203], a[140:143]\n"
"s_waitcnt lgkmcnt(4)\n"
"v_exp_f32 v179, v179\n"
"v_mfma_f32_16x16x32_bf16 a[168:171], a[232:235], v[200:203], a[168:171]\n"
"ds_read_b128 a[248:251], v233 offset:49152\n"
"v_add_f32 v245, v245, v179\n"
"v_mfma_f32_16x16x32_bf16 a[172:175], a[236:239], v[200:203], a[172:175]\n"
"v_cvt_pk_bf16_f32 v177, v178, v179\n"
"v_mfma_f32_16x16x32_bf16 a[176:179], a[232:235], v[208:211], a[176:179]\n"
"v_exp_f32 v180, v180\n"
"v_mfma_f32_16x16x32_bf16 a[180:183], a[236:239], v[208:211], a[180:183]\n"
"v_add_f32 v245, v245, v180\n"
"v_mfma_f32_16x16x32_bf16 a[184:187], a[232:235], v[216:219], a[184:187]\n"
"ds_read_b128 a[252:255], v233 offset:49664\n"
"v_mfma_f32_16x16x32_bf16 a[188:191], a[236:239], v[216:219], a[188:191]\n"
"v_exp_f32 v181, v181\n"
"v_mfma_f32_16x16x32_bf16 a[192:195], a[232:235], v[224:227], a[192:195]\n"
"v_add_f32 v245, v245, v181\n"
"v_cvt_pk_bf16_f32 v178, v180, v181\n"
"v_mfma_f32_16x16x32_bf16 a[196:199], a[236:239], v[224:227], a[196:199]\n"
"s_waitcnt lgkmcnt(2)\n"
"v_exp_f32 v182, v182\n"
"v_mfma_f32_16x16x32_bf16 a[200:203], a[240:243], v[200:203], a[200:203]\n"
"ds_read_b128 a[232:235], v233 offset:51200\n"
"v_add_f32 v245, v245, v182\n"
"v_mfma_f32_16x16x32_bf16 a[204:207], a[244:247], v[200:203], a[204:207]\n"
"v_exp_f32 v183, v183\n"
"v_mfma_f32_16x16x32_bf16 a[208:211], a[240:243], v[208:211], a[208:211]\n"
"v_add_f32 v245, v245, v183\n"
"v_cvt_pk_bf16_f32 v179, v182, v183\n"
"v_mfma_f32_16x16x32_bf16 a[212:215], a[244:247], v[208:211], a[212:215]\n"
"v_exp_f32 v184, v184\n"
"v_mfma_f32_16x16x32_bf16 a[216:219], a[240:243], v[216:219], a[216:219]\n"
"ds_read_b128 a[236:239], v233 offset:51712\n"
"v_add_f32 v246, v246, v184\n"
"v_mfma_f32_16x16x32_bf16 a[220:223], a[244:247], v[216:219], a[220:223]\n"
"buffer_load_dwordx4 v242, s[48:51], s65 offen offset:2048  lds\n"
"v_mfma_f32_16x16x32_bf16 a[224:227], a[240:243], v[224:227], a[224:227]\n"
"v_exp_f32 v185, v185\n"
"v_mfma_f32_16x16x32_bf16 a[228:231], a[244:247], v[224:227], a[228:231]\n"
"s_waitcnt lgkmcnt(2)\n"
"v_add_f32 v246, v246, v185\n"
"v_cvt_pk_bf16_f32 v184, v184, v185\n"
"v_mfma_f32_16x16x32_bf16 v[200:203], a[248:251], a[0:3], v[152:155]\n"
"ds_read_b128 a[240:243], v233 offset:53248\n"
"v_mfma_f32_16x16x32_bf16 v[200:203], a[252:255], a[16:19], v[200:203]\n"
"v_exp_f32 v186, v186\n"
"v_mfma_f32_16x16x32_bf16 v[208:211], a[248:251], a[4:7], v[156:159]\n"
"v_add_f32 v246, v246, v186\n"
"v_mfma_f32_16x16x32_bf16 v[208:211], a[252:255], a[20:23], v[208:211]\n"
"v_exp_f32 v187, v187\n"
"v_mfma_f32_16x16x32_bf16 v[216:219], a[248:251], a[8:11], v[160:163]\n"
"ds_read_b128 a[244:247], v233 offset:53760\n"
"v_add_f32 v246, v246, v187\n"
"v_mfma_f32_16x16x32_bf16 v[216:219], a[252:255], a[24:27], v[216:219]\n"
"v_cvt_pk_bf16_f32 v185, v186, v187\n"
"v_mfma_f32_16x16x32_bf16 v[224:227], a[248:251], a[12:15], v[164:167]\n"
"v_exp_f32 v188, v188\n"
"v_mfma_f32_16x16x32_bf16 v[224:227], a[252:255], a[28:31], v[224:227]\n"
"s_waitcnt lgkmcnt(2)\n"
"v_add_f32 v246, v246, v188\n"
"v_mfma_f32_16x16x32_bf16 v[228:231], a[232:235], a[12:15], v[164:167]\n"
"ds_read_b128 a[248:251], v233 offset:55296\n"
"v_mfma_f32_16x16x32_bf16 v[228:231], a[236:239], a[28:31], v[228:231]\n"
"v_exp_f32 v189, v189\n"
"v_mfma_f32_16x16x32_bf16 v[220:223], a[232:235], a[8:11], v[160:163]\n"
"v_add_f32 v246, v246, v189\n"
"v_cvt_pk_bf16_f32 v186, v188, v189\n"
"v_mfma_f32_16x16x32_bf16 v[220:223], a[236:239], a[24:27], v[220:223]\n"
"v_exp_f32 v190, v190\n"
"v_mfma_f32_16x16x32_bf16 v[212:215], a[232:235], a[4:7], v[156:159]\n"
"ds_read_b128 a[252:255], v233 offset:55808\n"
"v_add_f32 v246, v246, v190\n"
"v_mfma_f32_16x16x32_bf16 v[212:215], a[236:239], a[20:23], v[212:215]\n"
"buffer_load_dwordx4 v242, s[48:51], s66 offen offset:3072  lds\n"
"v_mfma_f32_16x16x32_bf16 v[204:207], a[232:235], a[0:3], v[152:155]\n"
"v_exp_f32 v191, v191\n"
"v_mfma_f32_16x16x32_bf16 v[204:207], a[236:239], a[16:19], v[204:207]\n"
"s_waitcnt lgkmcnt(2)\n"
"v_add_f32 v246, v246, v191\n"
"v_cvt_pk_bf16_f32 v187, v190, v191\n"
"v_mfma_f32_16x16x32_bf16 v[200:203], a[240:243], a[32:35], v[200:203]\n"
"ds_read_b128 a[232:235], v233 offset:57344\n"
"v_mfma_f32_16x16x32_bf16 v[200:203], a[244:247], a[48:51], v[200:203]\n"
"s_min_u32 s75, %[g], 6\n"
"s_sub_u32 s75, s75, 1\n"
"s_cselect_b32 s75, 6, s75\n"
"s_lshl_b32 s75, s75, 3\n"
"s_add_u32 s75, s75, %[g]\n"
"s_lshl_b32 s75, s75, 15\n"
"v_mfma_f32_16x16x32_bf16 v[208:211], a[240:243], a[36:39], v[208:211]\n"
"buffer_load_dword v249, v255, s[52:55], s75 offen sc1\n"
"v_mfma_f32_16x16x32_bf16 v[208:211], a[244:247], a[52:55], v[208:211]\n"
"v_exp_f32 v192, v192\n"
"v_mfma_f32_16x16x32_bf16 v[216:219], a[240:243], a[40:43], v[216:219]\n"
"ds_read_b128 a[236:239], v233 offset:57856\n"
"v_add_f32 v247, v247, v192\n"
"v_mfma_f32_16x16x32_bf16 v[216:219], a[244:247], a[56:59], v[216:219]\n"
"v_exp_f32 v193, v193\n"
"v_mfma_f32_16x16x32_bf16 v[224:227], a[240:243], a[44:47], v[224:227]\n"
"v_add_f32 v247, v247, v193\n"
"v_mfma_f32_16x16x32_bf16 v[224:227], a[244:247], a[60:63], v[224:227]\n"
"s_waitcnt lgkmcnt(2)\n"
"v_cvt_pk_bf16_f32 v192, v192, v193\n"
"v_mfma_f32_16x16x32_bf16 v[228:231], a[248:251], a[44:47], v[228:231]\n"
"ds_read_b128 a[240:243], v233 offset:59392\n"
"v_mfma_f32_16x16x32_bf16 v[228:231], a[252:255], a[60:63], v[228:231]\n"
"v_exp_f32 v194, v194\n"
"v_mfma_f32_16x16x32_bf16 v[220:223], a[248:251], a[40:43], v[220:223]\n"
"s_waitcnt vmcnt(26)\n"
"s_barrier\n"
"v_add_f32 v247, v247, v194\n"
"v_mfma_f32_16x16x32_bf16 v[220:223], a[252:255], a[56:59], v[220:223]\n"
"v_exp_f32 v195, v195\n"
"v_mfma_f32_16x16x32_bf16 v[212:215], a[248:251], a[36:39], v[212:215]\n"
"ds_read_b128 a[244:247], v233 offset:59904\n"
"s_add_u32 s82, s33, 114688\n"
"v_add_u32 v242, s82, v243\n"
"s_add_u32 m0, s71, 98304\n"
"v_mfma_f32_16x16x32_bf16 v[212:215], a[252:255], a[52:55], v[212:215]\n"
"buffer_load_dwordx4 v242, s[48:51], 0 offen offset:0  lds\n"
"v_mfma_f32_16x16x32_bf16 v[204:207], a[248:251], a[32:35], v[204:207]\n"
"v_add_f32 v247, v247, v195\n"
"v_mfma_f32_16x16x32_bf16 v[204:207], a[252:255], a[48:51], v[204:207]\n"
"s_waitcnt lgkmcnt(2)\n"
"v_cvt_pk_bf16_f32 v193, v194, v195\n"
"v_mfma_f32_16x16x32_bf16 v[200:203], a[232:235], a[64:67], v[200:203]\n"
"ds_read_b128 a[248:251], v233 offset:61440\n"
"v_mfma_f32_16x16x32_bf16 v[200:203], a[236:239], a[80:83], v[200:203]\n"
"v_exp_f32 v196, v196\n"
"v_mfma_f32_16x16x32_bf16 v[208:211], a[232:235], a[68:71], v[208:211]\n"
"v_add_f32 v247, v247, v196\n"
"v_mfma_f32_16x16x32_bf16 v[208:211], a[236:239], a[84:87], v[208:211]\n"
"v_exp_f32 v197, v197\n"
"v_mfma_f32_16x16x32_bf16 v[216:219], a[232:235], a[72:75], v[216:219]\n"
"ds_read_b128 a[252:255], v233 offset:61952\n"
"v_add_f32 v247, v247, v197\n"
"v_mfma_f32_16x16x32_bf16 v[216:219], a[236:239], a[88:91], v[216:219]\n"
"v_cvt_pk_bf16_f32 v194, v196, v197\n"
"v_mfma_f32_16x16x32_bf16 v[224:227], a[232:235], a[76:79], v[224:227]\n"
"v_exp_f32 v198, v198\n"
"v_mfma_f32_16x16x32_bf16 v[224:227], a[236:239], a[92:95], v[224:227]\n"
"s_waitcnt lgkmcnt(2)\n"
"v_add_f32 v247, v247, v198\n"
"v_mfma_f32_16x16x32_bf16 v[228:231], a[240:243], a[76:79], v[228:231]\n"
"ds_read_b128 a[232:235], v233 offset:63488\n"
"v_mfma_f32_16x16x32_bf16 v[228:231], a[244:247], a[92:95], v[228:231]\n"
"v_exp_f32 v199, v199\n"
"v_mfma_f32_16x16x32_bf16 v[220:223], a[240:243], a[72:75], v[220:223]\n"
"v_add_f32 v247, v247, v199\n"
"v_cvt_pk_bf16_f32 v195, v198, v199\n"
"v_mfma_f32_16x16x32_bf16 v[220:223], a[244:247], a[88:91], v[220:223]\n"
"v_mfma_f32_16x16x32_bf16 v[212:215], a[240:243], a[68:71], v[212:215]\n"
"ds_read_b128 a[236:239], v233 offset:64000\n"
"v_mfma_f32_16x16x32_bf16 v[212:215], a[244:247], a[84:87], v[212:215]\n"
"buffer_load_dwordx4 v242, s[48:51], s64 offen offset:1024  lds\n"
"v_mfma_f32_16x16x32_bf16 v[204:207], a[240:243], a[64:67], v[204:207]\n"
"v_mfma_f32_16x16x32_bf16 v[204:207], a[244:247], a[80:83], v[204:207]\n"
"s_waitcnt lgkmcnt(2)\n"
"v_mfma_f32_16x16x32_bf16 v[224:227], a[248:251], a[108:111], v[224:227]\n"
"ds_read_b64_tr_b16 a[240:241], v239 offset:0\n"
"v_mfma_f32_16x16x32_bf16 v[224:227], a[252:255], a[124:127], v[224:227]\n"
"ds_read_b64_tr_b16 a[242:243], v239 offset:2048\n"
"v_mfma_f32_16x16x32_bf16 v[216:219], a[248:251], a[104:107], v[216:219]\n"
"v_mfma_f32_16x16x32_bf16 v[216:219], a[252:255], a[120:123], v[216:219]\n"
"v_mfma_f32_16x16x32_bf16 v[208:211], a[248:251], a[100:103], v[208:211]\n"
"ds_read_b64_tr_b16 a[244:245], v240 offset:0\n"
"v_mfma_f32_16x16x32_bf16 v[208:211], a[252:255], a[116:119], v[208:211]\n"
"ds_read_b64_tr_b16 a[246:247], v240 offset:2048\n"
"v_mfma_f32_16x16x32_bf16 v[200:203], a[248:251], a[96:99], v[200:203]\n"
"v_mfma_f32_16x16x32_bf16 v[200:203], a[252:255], a[112:115], v[200:203]\n"
"s_waitcnt lgkmcnt(4)\n"
"v_mfma_f32_16x16x32_bf16 v[204:207], a[232:235], a[96:99], v[204:207]\n"
"ds_read_b64_tr_b16 a[248:249], v239 offset:512\n"
"v_mfma_f32_16x16x32_bf16 v[204:207], a[236:239], a[112:115], v[204:207]\n"
"ds_read_b64_tr_b16 a[250:251], v239 offset:2560\n"
"v_mfma_f32_16x16x32_bf16 v[212:215], a[232:235], a[100:103], v[212:215]\n"
"v_mfma_f32_16x16x32_bf16 v[212:215], a[236:239], a[116:119], v[212:215]\n"
"v_mfma_f32_16x16x32_bf16 v[220:223], a[232:235], a[104:107], v[220:223]\n"
"ds_read_b64_tr_b16 a[252:253], v240 offset:512\n"
"v_mfma_f32_16x16x32_bf16 v[220:223], a[236:239], a[120:123], v[220:223]\n"
"ds_read_b64_tr_b16 a[254:255], v240 offset:2560\n"
"v_mfma_f32_16x16x32_bf16 v[228:231], a[232:235], a[108:111], v[228:231]\n"
"buffer_load_dwordx4 v242, s[48:51], s65 offen offset:2048  lds\n"
"v_mfma_f32_16x16x32_bf16 v[228:231], a[236:239], a[124:127], v[228:231]\n"
"s_waitcnt lgkmcnt(4)\n"
"v_mfma_f32_16x16x32_bf16 v[0:3], a[240:243], v[168:171], v[0:3]\n"
"ds_read_b64_tr_b16 a[232:233], v239 offset:4096\n"
"v_mfma_f32_16x16x32_bf16 v[4:7], a[244:247], v[168:171], v[4:7]\n"
"ds_read_b64_tr_b16 a[234:235], v239 offset:6144\n"
"v_exp_f32 v200, v200\n"
"v_mfma_f32_16x16x32_bf16 v[8:11], a[240:243], v[176:179], v[8:11]\n"
"v_add_f32 v244, v244, v200\n"
"v_exp_f32 v201, v201\n"
"v_mfma_f32_16x16x32_bf16 v[12:15], a[244:247], v[176:179], v[12:15]\n"
"v_add_f32 v244, v244, v201\n"
"v_cvt_pk_bf16_f32 v200, v200, v201\n"
"v_mfma_f32_16x16x32_bf16 v[16:19], a[240:243], v[184:187], v[16:19]\n"
"ds_read_b64_tr_b16 a[236:237], v240 offset:4096\n"
"v_exp_f32 v202, v202\n"
"v_mfma_f32_16x16x32_bf16 v[20:23], a[244:247], v[184:187], v[20:23]\n"
"ds_read_b64_tr_b16 a[238:239], v240 offset:6144\n"
"v_add_f32 v244, v244, v202\n"
"v_mfma_f32_16x16x32_bf16 v[24:27], a[240:243], v[192:195], v[24:27]\n"
"v_exp_f32 v203, v203\n"
"v_mfma_f32_16x16x32_bf16 v[28:31], a[244:247], v[192:195], v[28:31]\n"
"s_waitcnt lgkmcnt(4)\n"
"v_add_f32 v244, v244, v203\n"
"v_cvt_pk_bf16_f32 v201, v202, v203\n"
"v_mfma_f32_16x16x32_bf16 v[56:59], a[248:251], v[192:195], v[56:59]\n"
"ds_read_b64_tr_b16 a[240:241], v239 offset:4608\n"
"v_exp_f32 v204, v204\n"
"v_mfma_f32_16x16x32_bf16 v[60:63], a[252:255], v[192:195], v[60:63]\n"
"ds_read_b64_tr_b16 a[242:243], v239 offset:6656\n"
"v_add_f32 v244, v244, v204\n"
"v_mfma_f32_16x16x32_bf16 v[48:51], a[248:251], v[184:187], v[48:51]\n"
"v_exp_f32 v205, v205\n"
"v_exp_f32 v206, v206\n"
"v_mfma_f32_16x16x32_bf16 v[52:55], a[252:255], v[184:187], v[52:55]\n"
"v_add_f32 v244, v244, v205\n"
"v_cvt_pk_bf16_f32 v202, v204, v205\n"
"v_add_f32 v244, v244, v206\n"
"v_mfma_f32_16x16x32_bf16 v[40:43], a[248:251], v[176:179], v[40:43]\n"
"ds_read_b64_tr_b16 a[244:245], v240 offset:4608\n"
"v_exp_f32 v207, v207\n"
"v_mfma_f32_16x16x32_bf16 v[44:47], a[252:255], v[176:179], v[44:47]\n"
"ds_read_b64_tr_b16 a[246:247], v240 offset:6656\n"
"v_add_f32 v244, v244, v207\n"
"v_cvt_pk_bf16_f32 v203, v206, v207\n"
"v_mfma_f32_16x16x32_bf16 v[32:35], a[248:251], v[168:171], v[32:35]\n"
"buffer_load_dwordx4 v242, s[48:51], s66 offen offset:3072  lds\n"
"v_mfma_f32_16x16x32_bf16 v[36:39], a[252:255], v[168:171], v[36:39]\n"
"s_waitcnt lgkmcnt(4)\n"
"v_exp_f32 v208, v208\n"
"v_mfma_f32_16x16x32_bf16 v[64:67], a[232:235], v[168:171], v[64:67]\n"
"ds_read_b64_tr_b16 a[248:249], v239 offset:8192\n"
"v_add_f32 v245, v245, v208\n"
"v_mfma_f32_16x16x32_bf16 v[68:71], a[236:239], v[168:171], v[68:71]\n"
"ds_read_b64_tr_b16 a[250:251], v239 offset:10240\n"
"s_lshl_b32 s75, %[g], 15\n"
"s_add_u32 s75, s75, 1572864\n"
"s_cmp_ge_u32 %[g], 6\n"
"s_cselect_b32 s75, s75, 1073741824\n"
"v_mfma_f32_16x16x32_bf16 v[72:75], a[232:235], v[176:179], v[72:75]\n"
"buffer_load_dword v249, v255, s[52:55], s75 offen sc1\n"
"v_mfma_f32_16x16x32_bf16 v[76:79], a[236:239], v[176:179], v[76:79]\n"
"v_exp_f32 v209, v209\n"
"v_mfma_f32_16x16x32_bf16 v[80:83], a[232:235], v[184:187], v[80:83]\n"
"ds_read_b64_tr_b16 a[252:253], v240 offset:8192\n"
"v_add_f32 v245, v245, v209\n"
"v_cvt_pk_bf16_f32 v208, v208, v209\n"
"v_mfma_f32_16x16x32_bf16 v[84:87], a[236:239], v[184:187], v[84:87]\n"
"ds_read_b64_tr_b16 a[254:255], v240 offset:10240\n"
"v_exp_f32 v210, v210\n"
"v_mfma_f32_16x16x32_bf16 v[88:91], a[232:235], v[192:195], v[88:91]\n"
"v_add_f32 v245, v245, v210\n"
"v_exp_f32 v211, v211\n"
"v_mfma_f32_16x16x32_bf16 v[92:95], a[236:239], v[192:195], v[92:95]\n"
"s_waitcnt lgkmcnt(4)\n"
"v_add_f32 v245, v245, v211\n"
"v_cvt_pk_bf16_f32 v209, v210, v211\n"
"v_mfma_f32_16x16x32_bf16 v[120:123], a[240:243], v[192:195], v[120:123]\n"
"ds_read_b64_tr_b16 a[232:233], v239 offset:8704\n"
"v_exp_f32 v212, v212\n"
"v_mfma_f32_16x16x32_bf16 v[124:127], a[244:247], v[192:195], v[124:127]\n"
"ds_read_b64_tr_b16 a[234:235], v239 offset:10752\n"
"v_add_f32 v245, v245, v212\n"
"v_mfma_f32_16x16x32_bf16 v[112:115], a[240:243], v[184:187], v[112:115]\n"
"s_waitcnt vmcnt(18)\n"
"s_barrier\n"
"v_exp_f32 v213, v213\n"
"v_mfma_f32_16x16x32_bf16 v[116:119], a[244:247], v[184:187], v[116:119]\n"
"v_add_f32 v245, v245, v213\n"
"v_cvt_pk_bf16_f32 v210, v212, v213\n"
"v_mfma_f32_16x16x32_bf16 v[104:107], a[240:243], v[176:179], v[104:107]\n"
"ds_read_b64_tr_b16 a[236:237], v240 offset:8704\n"
"v_exp_f32 v214, v214\n"
"v_mfma_f32_16x16x32_bf16 v[108:111], a[244:247], v[176:179], v[108:111]\n"
"ds_read_b64_tr_b16 a[238:239], v240 offset:10752\n"
"v_add_f32 v245, v245, v214\n"
"v_mfma_f32_16x16x32_bf16 v[96:99], a[240:243], v[168:171], v[96:99]\n"
"v_exp_f32 v215, v215\n"
"v_exp_f32 v216, v216\n"
"v_mfma_f32_16x16x32_bf16 v[100:103], a[244:247], v[168:171], v[100:103]\n"
"s_waitcnt lgkmcnt(4)\n"
"v_add_f32 v245, v245, v215\n"
"v_cvt_pk_bf16_f32 v211, v214, v215\n"
"v_add_f32 v246, v246, v216\n"
"v_mfma_f32_16x16x32_bf16 v[128:131], a[248:251], v[168:171], v[128:131]\n"
"ds_read_b64_tr_b16 a[240:241], v239 offset:12288\n"
"v_exp_f32 v217, v217\n"
"v_mfma_f32_16x16x32_bf16 v[132:135], a[252:255], v[168:171], v[132:135]\n"
"ds_read_b64_tr_b16 a[242:243], v239 offset:14336\n"
"v_add_f32 v246, v246, v217\n"
"v_cvt_pk_bf16_f32 v216, v216, v217\n"
"v_mfma_f32_16x16x32_bf16 v[136:139], a[248:251], v[176:179], v[136:139]\n"
"v_exp_f32 v218, v218\n"
"v_mfma_f32_16x16x32_bf16 v[140:143], a[252:255], v[176:179], v[140:143]\n"
"v_add_f32 v246, v246, v218\n"
"v_exp_f32 v219, v219\n"
"v_mfma_f32_16x16x32_bf16 v[144:147], a[248:251], v[184:187], v[144:147]\n"
"ds_read_b64_tr_b16 a[244:245], v240 offset:12288\n"
"v_add_f32 v246, v246, v219\n"
"v_cvt_pk_bf16_f32 v217, v218, v219\n"
"v_mfma_f32_16x16x32_bf16 v[148:151], a[252:255], v[184:187], v[148:151]\n"
"ds_read_b64_tr_b16 a[246:247], v240 offset:14336\n"
"v_exp_f32 v220, v220\n"
"v_mfma_f32_16x16x32_bf16 a[128:131], a[248:251], v[192:195], a[128:131]\n"
"v_add_f32 v246, v246, v220\n"
"v_exp_f32 v221, v221\n"
"v_mfma_f32_16x16x32_bf16 a[132:135], a[252:255], v[192:195], a[132:135]\n"
"s_waitcnt lgkmcnt(4)\n"
"v_add_f32 v246, v246, v221\n"
"v_cvt_pk_bf16_f32 v218, v220, v221\n"
"v_mfma_f32_16x16x32_bf16 a[160:163], a[232:235], v[192:195], a[160:163]\n"
"ds_read_b64_tr_b16 a[248:249], v239 offset:12800\n"
"v_exp_f32 v222, v222\n"
"v_mfma_f32_16x16x32_bf16 a[164:167], a[236:239], v[192:195], a[164:167]\n"
"ds_read_b64_tr_b16 a[250:251], v239 offset:14848\n"
"v_add_f32 v246, v246, v222\n"
"v_mfma_f32_16x16x32_bf16 a[152:155], a[232:235], v[184:187], a[152:155]\n"
"v_exp_f32 v223, v223\n"
"v_mfma_f32_16x16x32_bf16 a[156:159], a[236:239], v[184:187], a[156:159]\n"
"v_add_f32 v246, v246, v223\n"
"v_cvt_pk_bf16_f32 v219, v222, v223\n"
"v_mfma_f32_16x16x32_bf16 a[144:147], a[232:235], v[176:179], a[144:147]\n"
"ds_read_b64_tr_b16 a[252:253], v240 offset:12800\n"
"v_exp_f32 v224, v224\n"
"v_mfma_f32_16x16x32_bf16 a[148:151], a[236:239], v[176:179], a[148:151]\n"
"ds_read_b64_tr_b16 a[254:255], v240 offset:14848\n"
"v_add_f32 v247, v247, v224\n"
"v_mfma_f32_16x16x32_bf16 a[136:139], a[232:235], v[168:171], a[136:139]\n"
"v_exp_f32 v225, v225\n"
"v_mfma_f32_16x16x32_bf16 a[140:143], a[236:239], v[168:171], a[140:143]\n"
"s_waitcnt lgkmcnt(4)\n"
"v_add_f32 v247, v247, v225\n"
"v_cvt_pk_bf16_f32 v224, v224, v225\n"
"v_mfma_f32_16x16x32_bf16 a[168:171], a[240:243], v[168:171], a[168:171]\n"
"ds_read_b64_tr_b16 a[232:233], v239 offset:16384\n"
"v_exp_f32 v226, v226\n"
"v_mfma_f32_16x16x32_bf16 a[172:175], a[244:247], v[168:171], a[172:175]\n"
"ds_read_b64_tr_b16 a[234:235], v239 offset:18432\n"
"v_add_f32 v247, v247, v226\n"
"v_mfma_f32_16x16x32_bf16 a[176:179], a[240:243], v[176:179], a[176:179]\n"
"v_exp_f32 v227, v227\n"
"v_exp_f32 v228, v228\n"
"v_mfma_f32_16x16x32_bf16 a[180:183], a[244:247], v[176:179], a[180:183]\n"
"v_add_f32 v247, v247, v227\n"
"v_cvt_pk_bf16_f32 v225, v226, v227\n"
"v_add_f32 v247, v247, v228\n"
"v_mfma_f32_16x16x32_bf16 a[184:187], a[240:243], v[184:187], a[184:187]\n"
"ds_read_b64_tr_b16 a[236:237], v240 offset:16384\n"
"v_exp_f32 v229, v229\n"
"v_mfma_f32_16x16x32_bf16 a[188:191], a[244:247], v[184:187], a[188:191]\n"
"ds_read_b64_tr_b16 a[238:239], v240 offset:18432\n"
"v_add_f32 v247, v247, v229\n"
"v_cvt_pk_bf16_f32 v226, v228, v229\n"
"v_mfma_f32_16x16x32_bf16 a[192:195], a[240:243], v[192:195], a[192:195]\n"
"v_exp_f32 v230, v230\n"
"v_mfma_f32_16x16x32_bf16 a[196:199], a[244:247], v[192:195], a[196:199]\n"
"s_waitcnt lgkmcnt(4)\n"
"v_add_f32 v247, v247, v230\n"
"v_exp_f32 v231, v231\n"
"v_mfma_f32_16x16x32_bf16 a[200:203], a[248:251], v[168:171], a[200:203]\n"
"ds_read_b64_tr_b16 a[240:241], v239 offset:16896\n"
"v_add_f32 v247, v247, v231\n"
"v_cvt_pk_bf16_f32 v227, v230, v231\n"
"v_mfma_f32_16x16x32_bf16 a[204:207], a[252:255], v[168:171], a[204:207]\n"
"ds_read_b64_tr_b16 a[242:243], v239 offset:18944\n"
"v_add_f32 v152, v244, v245\n"
"v_add_f32 v153, v246, v247\n"
"v_mfma_f32_16x16x32_bf16 a[208:211], a[248:251], v[176:179], a[208:211]\n"
"v_add_f32 v152, v152, v153\n"
"v_mfma_f32_16x16x32_bf16 a[212:215], a[252:255], v[176:179], a[212:215]\n"
"v_mfma_f32_16x16x32_bf16 a[216:219], a[248:251], v[184:187], a[216:219]\n"
"ds_read_b64_tr_b16 a[244:245], v240 offset:16896\n"
"v_mfma_f32_16x16x32_bf16 a[220:223], a[252:255], v[184:187], a[220:223]\n"
"ds_read_b64_tr_b16 a[246:247], v240 offset:18944\n"
"v_mfma_f32_16x16x32_bf16 a[224:227], a[248:251], v[192:195], a[224:227]\n"
"v_mfma_f32_16x16x32_bf16 a[228:231], a[252:255], v[192:195], a[228:231]\n"
"s_waitcnt lgkmcnt(4)\n"
"v_cmp_ngt_f32 vcc, 0x4e800000, v152\n"
"s_cbranch_vccnz L_SLOW\n"
"L_BACK:\n"
"v_mfma_f32_16x16x32_bf16 v[0:3], a[232:235], v[200:203], v[0:3]\n"
"ds_read_b64_tr_b16 a[248:249], v239 offset:20480\n"
"s_and_b32 s83, s60, 1\n"
"s_lshl_b32 s84, s83, 14\n"
"v_add_u32 v250, s84, v235\n"
"v_mfma_f32_16x16x32_bf16 v[4:7], a[236:239], v[200:203], v[4:7]\n"
"ds_read_b64_tr_b16 a[250:251], v239 offset:22528\n"
"v_xor_b32 v251, 32, v250\n"
"v_mfma_f32_16x16x32_bf16 v[8:11], a[232:235], v[208:211], v[8:11]\n"
"v_permlane32_swap_b32 v244, v245\n"
"v_mfma_f32_16x16x32_bf16 v[12:15], a[236:239], v[208:211], v[12:15]\n"
"v_permlane32_swap_b32 v246, v247\n"
"v_mfma_f32_16x16x32_bf16 v[16:19], a[232:235], v[216:219], v[16:19]\n"
"ds_read_b64_tr_b16 a[252:253], v240 offset:20480\n"
"v_add_f32 v244, v244, v245\n"
"v_mfma_f32_16x16x32_bf16 v[20:23], a[236:239], v[216:219], v[20:23]\n"
"ds_read_b64_tr_b16 a[254:255], v240 offset:22528\n"
"v_add_f32 v246, v246, v247\n"
"v_mfma_f32_16x16x32_bf16 v[24:27], a[232:235], v[224:227], v[24:27]\n"
"s_nop 1\n"
"v_mfma_f32_16x16x32_bf16 v[28:31], a[236:239], v[224:227], v[28:31]\n"
"s_waitcnt lgkmcnt(4)\n"
"v_permlane16_swap_b32 v244, v246\n"
"v_mfma_f32_16x16x32_bf16 v[32:35], a[240:243], v[200:203], v[32:35]\n"
"ds_read_b64_tr_b16 a[232:233], v239 offset:20992\n"
"v_add_f32 v244, v244, v246\n"
"v_mfma_f32_16x16x32_bf16 v[36:39], a[244:247], v[200:203], v[36:39]\n"
"ds_read_b64_tr_b16 a[234:235], v239 offset:23040\n"
"v_mbcnt_lo_u32_b32 v160, -1, 0\n"
"v_mfma_f32_16x16x32_bf16 v[40:43], a[240:243], v[208:211], v[40:43]\n"
"v_rcp_f32 v244, v244\n"
"v_mfma_f32_16x16x32_bf16 v[44:47], a[244:247], v[208:211], v[44:47]\n"
"s_nop 0\n"
"v_mov_b32 v246, v244\n"
"v_mfma_f32_16x16x32_bf16 v[48:51], a[240:243], v[216:219], v[48:51]\n"
"ds_read_b64_tr_b16 a[236:237], v240 offset:20992\n"
"v_mbcnt_hi_u32_b32 v160, -1, v160\n"
"v_mfma_f32_16x16x32_bf16 v[52:55], a[244:247], v[216:219], v[52:55]\n"
"ds_read_b64_tr_b16 a[238:239], v240 offset:23040\n"
"s_lshl_b32 s84, s60, 10\n"
"s_add_u32 s84, s84, 131072\n"
"v_lshl_add_u32 v160, v160, 4, s84\n"
"v_mfma_f32_16x16x32_bf16 v[56:59], a[240:243], v[224:227], v[56:59]\n"
"s_nop 1\n"
"v_mfma_f32_16x16x32_bf16 v[60:63], a[244:247], v[224:227], v[60:63]\n"
"s_waitcnt lgkmcnt(4)\n"
"v_permlane16_swap_b32 v244, v246\n"
"v_mfma_f32_16x16x32_bf16 v[64:67], a[248:251], v[200:203], v[64:67]\n"
"ds_read_b64_tr_b16 a[240:241], v239 offset:24576\n"
"v_mov_b32 v245, v244\n"
"v_mfma_f32_16x16x32_bf16 v[68:71], a[252:255], v[200:203], v[68:71]\n"
"ds_read_b64_tr_b16 a[242:243], v239 offset:26624\n"
"s_mov_b32 s75, 1839104\n"
"v_mfma_f32_16x16x32_bf16 v[72:75], a[248:251], v[208:211], v[72:75]\n"
"buffer_load_dword v249, v255, s[52:55], s75 offen sc1\n"
"v_mfma_f32_16x16x32_bf16 v[76:79], a[252:255], v[208:211], v[76:79]\n"
"v_mov_b32 v247, v246\n"
"s_nop 0\n"
"v_mfma_f32_16x16x32_bf16 v[80:83], a[248:251], v[216:219], v[80:83]\n"
"ds_read_b64_tr_b16 a[244:245], v240 offset:24576\n"
"v_xor_b32 v161, 1024, v160\n"
"v_mfma_f32_16x16x32_bf16 v[84:87], a[252:255], v[216:219], v[84:87]\n"
"ds_read_b64_tr_b16 a[246:247], v240 offset:26624\n"
"v_cvt_pk_bf16_f32 v0, v0, v1\n"
"v_mfma_f32_16x16x32_bf16 v[88:91], a[248:251], v[224:227], v[88:91]\n"
"v_permlane32_swap_b32 v244, v245\n"
"v_mfma_f32_16x16x32_bf16 v[92:95], a[252:255], v[224:227], v[92:95]\n"
"s_waitcnt lgkmcnt(4)\n"
"v_permlane32_swap_b32 v246, v247\n"
"v_mfma_f32_16x16x32_bf16 v[96:99], a[232:235], v[200:203], v[96:99]\n"
"ds_read_b64_tr_b16 a[248:249], v239 offset:25088\n"
"v_cvt_pk_bf16_f32 v1, v2, v3\n"
"v_mfma_f32_16x16x32_bf16 v[100:103], a[236:239], v[200:203], v[100:103]\n"
"ds_read_b64_tr_b16 a[250:251], v239 offset:27136\n"
"v_cvt_pk_bf16_f32 v2, v4, v5\n"
"v_mfma_f32_16x16x32_bf16 v[104:107], a[232:235], v[208:211], v[104:107]\n"
"s_waitcnt vmcnt(15)\n"
"s_barrier\n"
"v_cvt_pk_bf16_f32 v3, v6, v7\n"
"v_mfma_f32_16x16x32_bf16 v[108:111], a[236:239], v[208:211], v[108:111]\n"
"ds_write_b128 v160, v[244:247]\n"
"v_mfma_f32_16x16x32_bf16 v[112:115], a[232:235], v[216:219], v[112:115]\n"
"ds_read_b64_tr_b16 a[252:253], v240 offset:25088\n"
"v_cvt_pk_bf16_f32 v4, v8, v9\n"
"v_mfma_f32_16x16x32_bf16 v[116:119], a[236:239], v[216:219], v[116:119]\n"
"ds_read_b64_tr_b16 a[254:255], v240 offset:27136\n"
"v_cvt_pk_bf16_f32 v5, v10, v11\n"
"v_mfma_f32_16x16x32_bf16 v[120:123], a[232:235], v[224:227], v[120:123]\n"
"v_cvt_pk_bf16_f32 v6, v12, v13\n"
"v_cvt_pk_bf16_f32 v7, v14, v15\n"
"v_mfma_f32_16x16x32_bf16 v[124:127], a[236:239], v[224:227], v[124:127]\n"
"s_waitcnt lgkmcnt(5)\n"
"v_cvt_pk_bf16_f32 v8, v16, v17\n"
"v_cvt_pk_bf16_f32 v9, v18, v19\n"
"v_mfma_f32_16x16x32_bf16 v[128:131], a[240:243], v[200:203], v[128:131]\n"
"ds_read_b64_tr_b16 a[232:233], v239 offset:28672\n"
"v_cvt_pk_bf16_f32 v10, v20, v21\n"
"v_mfma_f32_16x16x32_bf16 v[132:135], a[244:247], v[200:203], v[132:135]\n"
"ds_read_b64_tr_b16 a[234:235], v239 offset:30720\n"
"v_cvt_pk_bf16_f32 v11, v22, v23\n"
"v_mfma_f32_16x16x32_bf16 v[136:139], a[240:243], v[208:211], v[136:139]\n"
"v_cvt_pk_bf16_f32 v12, v24, v25\n"
"v_cvt_pk_bf16_f32 v13, v26, v27\n"
"v_mfma_f32_16x16x32_bf16 v[140:143], a[244:247], v[208:211], v[140:143]\n"
"v_cvt_pk_bf16_f32 v14, v28, v29\n"
"v_cvt_pk_bf16_f32 v15, v30, v31\n"
"v_mfma_f32_16x16x32_bf16 v[144:147], a[240:243], v[216:219], v[144:147]\n"
"ds_read_b64_tr_b16 a[236:237], v240 offset:28672\n"
"v_cvt_pk_bf16_f32 v16, v32, v33\n"
"v_mfma_f32_16x16x32_bf16 v[148:151], a[244:247], v[216:219], v[148:151]\n"
"ds_read_b64_tr_b16 a[238:239], v240 offset:30720\n"
"v_cvt_pk_bf16_f32 v17, v34, v35\n"
"v_mfma_f32_16x16x32_bf16 a[128:131], a[240:243], v[224:227], a[128:131]\n"
"v_cvt_pk_bf16_f32 v18, v36, v37\n"
"v_cvt_pk_bf16_f32 v19, v38, v39\n"
"v_mfma_f32_16x16x32_bf16 a[132:135], a[244:247], v[224:227], a[132:135]\n"
"s_waitcnt lgkmcnt(4)\n"
"v_cvt_pk_bf16_f32 v20, v40, v41\n"
"v_cvt_pk_bf16_f32 v21, v42, v43\n"
"v_mfma_f32_16x16x32_bf16 a[136:139], a[248:251], v[200:203], a[136:139]\n"
"ds_read_b64_tr_b16 a[240:241], v239 offset:29184\n"
"v_cvt_pk_bf16_f32 v22, v44, v45\n"
"v_mfma_f32_16x16x32_bf16 a[140:143], a[252:255], v[200:203], a[140:143]\n"
"ds_read_b64_tr_b16 a[242:243], v239 offset:31232\n"
"v_cvt_pk_bf16_f32 v23, v46, v47\n"
"v_mfma_f32_16x16x32_bf16 a[144:147], a[248:251], v[208:211], a[144:147]\n"
"v_cvt_pk_bf16_f32 v24, v48, v49\n"
"v_cvt_pk_bf16_f32 v25, v50, v51\n"
"v_mfma_f32_16x16x32_bf16 a[148:151], a[252:255], v[208:211], a[148:151]\n"
"v_cvt_pk_bf16_f32 v26, v52, v53\n"
"v_cvt_pk_bf16_f32 v27, v54, v55\n"
"v_mfma_f32_16x16x32_bf16 a[152:155], a[248:251], v[216:219], a[152:155]\n"
"ds_read_b64_tr_b16 a[244:245], v240 offset:29184\n"
"v_cvt_pk_bf16_f32 v28, v56, v57\n"
"v_mfma_f32_16x16x32_bf16 a[156:159], a[252:255], v[216:219], a[156:159]\n"
"ds_read_b64_tr_b16 a[246:247], v240 offset:31232\n"
"v_cvt_pk_bf16_f32 v29, v58, v59\n"
"v_mfma_f32_16x16x32_bf16 a[160:163], a[248:251], v[224:227], a[160:163]\n"
"v_cvt_pk_bf16_f32 v30, v60, v61\n"
"v_cvt_pk_bf16_f32 v31, v62, v63\n"
"v_mfma_f32_16x16x32_bf16 a[164:167], a[252:255], v[224:227], a[164:167]\n"
"s_waitcnt lgkmcnt(4)\n"
"v_cvt_pk_bf16_f32 v32, v64, v65\n"
"v_cvt_pk_bf16_f32 v33, v66, v67\n"
"v_mfma_f32_16x16x32_bf16 a[168:171], a[232:235], v[200:203], a[168:171]\n"
"ds_read_b64_tr_b16 a[248:249], v250 offset:512\n"
"v_cvt_pk_bf16_f32 v34, v68, v69\n"
"v_mfma_f32_16x16x32_bf16 a[172:175], a[236:239], v[200:203], a[172:175]\n"
"ds_read_b64_tr_b16 a[250:251], v250 offset:2560\n"
"v_cvt_pk_bf16_f32 v35, v70, v71\n"
"v_mfma_f32_16x16x32_bf16 a[176:179], a[232:235], v[208:211], a[176:179]\n"
"v_cvt_pk_bf16_f32 v36, v72, v73\n"
"v_cvt_pk_bf16_f32 v37, v74, v75\n"
"v_mfma_f32_16x16x32_bf16 a[180:183], a[236:239], v[208:211], a[180:183]\n"
"v_cvt_pk_bf16_f32 v38, v76, v77\n"
"v_cvt_pk_bf16_f32 v39, v78, v79\n"
"v_mfma_f32_16x16x32_bf16 a[184:187], a[232:235], v[216:219], a[184:187]\n"
"ds_read_b64_tr_b16 a[252:253], v250 offset:4608\n"
"v_cvt_pk_bf16_f32 v40, v80, v81\n"
"v_mfma_f32_16x16x32_bf16 a[188:191], a[236:239], v[216:219], a[188:191]\n"
"ds_read_b64_tr_b16 a[254:255], v250 offset:6656\n"
"v_cvt_pk_bf16_f32 v41, v82, v83\n"
"v_mfma_f32_16x16x32_bf16 a[192:195], a[232:235], v[224:227], a[192:195]\n"
"v_cvt_pk_bf16_f32 v42, v84, v85\n"
"v_cvt_pk_bf16_f32 v43, v86, v87\n"
"v_mfma_f32_16x16x32_bf16 a[196:199], a[236:239], v[224:227], a[196:199]\n"
"s_waitcnt lgkmcnt(4)\n"
"v_cvt_pk_bf16_f32 v44, v88, v89\n"
"v_cvt_pk_bf16_f32 v45, v90, v91\n"
"v_mfma_f32_16x16x32_bf16 a[200:203], a[240:243], v[200:203], a[200:203]\n"
"ds_read_b64_tr_b16 a[232:233], v250 offset:8704\n"
"v_cvt_pk_bf16_f32 v46, v92, v93\n"
"v_mfma_f32_16x16x32_bf16 a[204:207], a[244:247], v[200:203], a[204:207]\n"
"ds_read_b64_tr_b16 a[234:235], v250 offset:10752\n"
"v_cvt_pk_bf16_f32 v47, v94, v95\n"
"v_mfma_f32_16x16x32_bf16 a[208:211], a[240:243], v[208:211], a[208:211]\n"
"v_cvt_pk_bf16_f32 v48, v96, v97\n"
"v_cvt_pk_bf16_f32 v49, v98, v99\n"
"v_mfma_f32_16x16x32_bf16 a[212:215], a[244:247], v[208:211], a[212:215]\n"
"v_cvt_pk_bf16_f32 v50, v100, v101\n"
"v_cvt_pk_bf16_f32 v51, v102, v103\n"
"v_mfma_f32_16x16x32_bf16 a[216:219], a[240:243], v[216:219], a[216:219]\n"
"ds_read_b64_tr_b16 a[236:237], v250 offset:12800\n"
"v_cvt_pk_bf16_f32 v52, v104, v105\n"
"v_mfma_f32_16x16x32_bf16 a[220:223], a[244:247], v[216:219], a[220:223]\n"
"ds_read_b64_tr_b16 a[238:239], v250 offset:14848\n"
"v_cvt_pk_bf16_f32 v53, v106, v107\n"
"v_mfma_f32_16x16x32_bf16 a[224:227], a[240:243], v[224:227], a[224:227]\n"
"v_cvt_pk_bf16_f32 v54, v108, v109\n"
"v_cvt_pk_bf16_f32 v55, v110, v111\n"
"v_mfma_f32_16x16x32_bf16 a[228:231], a[244:247], v[224:227], a[228:231]\n"
"s_waitcnt lgkmcnt(4)\n"
"v_cvt_pk_bf16_f32 v56, v112, v113\n"
"v_cvt_pk_bf16_f32 v57, v114, v115\n"
"v_mfma_f32_16x16x32_bf16 a[0:3], a[248:251], v[0:3], 0\n"
"s_waitcnt vmcnt(2)\n"
"s_barrier\n"
"ds_read_b64_tr_b16 a[240:241], v251 offset:512\n"
"v_cvt_pk_bf16_f32 v58, v116, v117\n"
"v_mfma_f32_16x16x32_bf16 a[0:3], a[252:255], v[16:19], a[0:3]\n"
"ds_read_b64_tr_b16 a[242:243], v251 offset:2560\n"
"v_cvt_pk_bf16_f32 v59, v118, v119\n"
"v_mov_b32 v230, s72\n"
"s_add_u32 s82, s33, 344064\n"
"v_add_u32 v242, s82, v243\n"
"v_xor_b32 v242, 64, v242\n"
"s_add_u32 m0, s71, 114688\n"
"v_mfma_f32_16x16x32_bf16 a[4:7], a[248:251], v[4:7], 0\n"
"buffer_load_dwordx4 v242, s[48:51], 0 offen offset:0  lds\n"
"ds_read_b128 v[232:235], v161\n"
"v_cvt_pk_bf16_f32 v60, v120, v121\n"
"v_mov_b32 v231, s73\n"
"v_mfma_f32_16x16x32_bf16 a[4:7], a[252:255], v[20:23], a[4:7]\n"
"v_cvt_pk_bf16_f32 v61, v122, v123\n"
"v_cvt_pk_bf16_f32 v62, v124, v125\n"
"v_mbcnt_lo_u32_b32 v238, -1, 0\n"
"v_mfma_f32_16x16x32_bf16 a[8:11], a[248:251], v[8:11], 0\n"
"ds_read_b64_tr_b16 a[244:245], v251 offset:4608\n"
"v_cvt_pk_bf16_f32 v63, v126, v127\n"
"v_mbcnt_hi_u32_b32 v238, -1, v238\n"
"v_mfma_f32_16x16x32_bf16 a[8:11], a[252:255], v[24:27], a[8:11]\n"
"ds_read_b64_tr_b16 a[246:247], v251 offset:6656\n"
"v_cvt_pk_bf16_f32 v64, v128, v129\n"
"v_mfma_f32_16x16x32_bf16 a[12:15], a[248:251], v[12:15], 0\n"
"v_cvt_pk_bf16_f32 v65, v130, v131\n"
"v_cvt_pk_bf16_f32 v66, v132, v133\n"
"v_mfma_f32_16x16x32_bf16 a[12:15], a[252:255], v[28:31], a[12:15]\n"
"s_waitcnt lgkmcnt(5)\n"
"v_cvt_pk_bf16_f32 v67, v134, v135\n"
"v_cvt_pk_bf16_f32 v68, v136, v137\n"
"v_mfma_f32_16x16x32_bf16 a[0:3], a[232:235], v[32:35], a[0:3]\n"
"ds_read_b64_tr_b16 a[248:249], v251 offset:8704\n"
"v_cvt_pk_bf16_f32 v69, v138, v139\n"
"v_mfma_f32_16x16x32_bf16 a[0:3], a[236:239], v[48:51], a[0:3]\n"
"buffer_load_dwordx4 v242, s[48:51], s64 offen offset:1024  lds\n"
"ds_read_b64_tr_b16 a[250:251], v251 offset:10752\n"
"v_cvt_pk_bf16_f32 v70, v140, v141\n"
"v_mfma_f32_16x16x32_bf16 a[4:7], a[232:235], v[36:39], a[4:7]\n"
"v_cvt_pk_bf16_f32 v71, v142, v143\n"
"v_cvt_pk_bf16_f32 v72, v144, v145\n"
"v_mfma_f32_16x16x32_bf16 a[4:7], a[236:239], v[52:55], a[4:7]\n"
"v_cvt_pk_bf16_f32 v73, v146, v147\n"
"v_cvt_pk_bf16_f32 v74, v148, v149\n"
"v_mfma_f32_16x16x32_bf16 a[8:11], a[232:235], v[40:43], a[8:11]\n"
"ds_read_b64_tr_b16 a[252:253], v251 offset:12800\n"
"v_cvt_pk_bf16_f32 v75, v150, v151\n"
"v_mfma_f32_16x16x32_bf16 a[8:11], a[236:239], v[56:59], a[8:11]\n"
"ds_read_b64_tr_b16 a[254:255], v251 offset:14848\n"
"v_accvgpr_read_b32 v152, a128\n"
"v_mfma_f32_16x16x32_bf16 a[12:15], a[232:235], v[44:47], a[12:15]\n"
"v_accvgpr_read_b32 v153, a129\n"
"v_cvt_pk_bf16_f32 v76, v152, v153\n"
"v_mfma_f32_16x16x32_bf16 a[12:15], a[236:239], v[60:63], a[12:15]\n"
"s_waitcnt lgkmcnt(4)\n"
"v_accvgpr_read_b32 v154, a130\n"
"v_accvgpr_read_b32 v155, a131\n"
"v_mfma_f32_16x16x32_bf16 a[16:19], a[240:243], v[0:3], 0\n"
"buffer_load_dwordx4 v242, s[48:51], s65 offen offset:2048  lds\n"
"ds_read_b64_tr_b16 a[232:233], v250 offset:0\n"
"v_cvt_pk_bf16_f32 v77, v154, v155\n"
"v_mfma_f32_16x16x32_bf16 a[16:19], a[244:247], v[16:19], a[16:19]\n"
"ds_read_b64_tr_b16 a[234:235], v250 offset:2048\n"
"v_accvgpr_read_b32 v156, a132\n"
"v_mfma_f32_16x16x32_bf16 a[20:23], a[240:243], v[4:7], 0\n"
"v_accvgpr_read_b32 v157, a133\n"
"v_cvt_pk_bf16_f32 v78, v156, v157\n"
"v_mfma_f32_16x16x32_bf16 a[20:23], a[244:247], v[20:23], a[20:23]\n"
"v_accvgpr_read_b32 v158, a134\n"
"v_accvgpr_read_b32 v159, a135\n"
"v_mfma_f32_16x16x32_bf16 a[24:27], a[240:243], v[8:11], 0\n"
"ds_read_b64_tr_b16 a[236:237], v250 offset:4096\n"
"v_cvt_pk_bf16_f32 v79, v158, v159\n"
"s_lshl_b32 s84, s60, 13\n"
"v_lshl_add_u32 v240, v238, 4, s84\n"
"v_mfma_f32_16x16x32_bf16 a[24:27], a[244:247], v[24:27], a[24:27]\n"
"ds_read_b64_tr_b16 a[238:239], v250 offset:6144\n"
"v_accvgpr_read_b32 v152, a136\n"
"v_mfma_f32_16x16x32_bf16 a[28:31], a[240:243], v[12:15], 0\n"
"L_CW4:\n"
"s_mov_b32 s75, L_CW4-L_PC+3072\n"
"buffer_load_dword a127, v254, s[56:59], s75 offen\n"
"v_accvgpr_read_b32 v153, a137\n"
"v_cvt_pk_bf16_f32 v80, v152, v153\n"
"v_mfma_f32_16x16x32_bf16 a[28:31], a[244:247], v[28:31], a[28:31]\n"
"s_waitcnt lgkmcnt(4)\n"
"buffer_load_dwordx4 v242, s[48:51], s66 offen offset:3072  lds\n"
"v_accvgpr_read_b32 v154, a138\n"
"v_accvgpr_read_b32 v155, a139\n"
"v_mfma_f32_16x16x32_bf16 a[16:19], a[248:251], v[32:35], a[16:19]\n"
"ds_read_b64_tr_b16 a[240:241], v250 offset:8192\n"
"v_cvt_pk_bf16_f32 v81, v154, v155\n"
"v_mfma_f32_16x16x32_bf16 a[16:19], a[252:255], v[48:51], a[16:19]\n"
"ds_read_b64_tr_b16 a[242:243], v250 offset:10240\n"
"v_accvgpr_read_b32 v156, a140\n"
"v_mfma_f32_16x16x32_bf16 a[20:23], a[248:251], v[36:39], a[20:23]\n"
"v_accvgpr_read_b32 v157, a141\n"
"v_cvt_pk_bf16_f32 v82, v156, v157\n"
"v_mfma_f32_16x16x32_bf16 a[20:23], a[252:255], v[52:55], a[20:23]\n"
"v_accvgpr_read_b32 v158, a142\n"
"v_accvgpr_read_b32 v159, a143\n"
"v_mfma_f32_16x16x32_bf16 a[24:27], a[248:251], v[40:43], a[24:27]\n"
"ds_read_b64_tr_b16 a[244:245], v250 offset:12288\n"
"v_cvt_pk_bf16_f32 v83, v158, v159\n"
"v_mfma_f32_16x16x32_bf16 a[24:27], a[252:255], v[56:59], a[24:27]\n"
"ds_read_b64_tr_b16 a[246:247], v250 offset:14336\n"
"v_accvgpr_read_b32 v152, a144\n"
"v_mfma_f32_16x16x32_bf16 a[28:31], a[248:251], v[44:47], a[28:31]\n"
"v_accvgpr_read_b32 v153, a145\n"
"v_cvt_pk_bf16_f32 v84, v152, v153\n"
"v_mfma_f32_16x16x32_bf16 a[28:31], a[252:255], v[60:63], a[28:31]\n"
"s_waitcnt lgkmcnt(4)\n"
"v_accvgpr_read_b32 v154, a146\n"
"v_accvgpr_read_b32 v155, a147\n"
"v_mfma_f32_16x16x32_bf16 v[176:179], a[232:235], v[0:3], 0\n"
"ds_read_b64_tr_b16 a[248:249], v251 offset:0\n"
"v_cvt_pk_bf16_f32 v85, v154, v155\n"
"v_mfma_f32_16x16x32_bf16 v[176:179], a[236:239], v[16:19], v[176:179]\n"
"ds_read_b64_tr_b16 a[250:251], v251 offset:2048\n"
"v_accvgpr_read_b32 v156, a148\n"
"v_mfma_f32_16x16x32_bf16 v[180:183], a[232:235], v[4:7], 0\n"
"v_accvgpr_read_b32 v157, a149\n"
"v_cvt_pk_bf16_f32 v86, v156, v157\n"
"v_mfma_f32_16x16x32_bf16 v[180:183], a[236:239], v[20:23], v[180:183]\n"
"v_accvgpr_read_b32 v158, a150\n"
"v_accvgpr_read_b32 v159, a151\n"
"v_mfma_f32_16x16x32_bf16 v[184:187], a[232:235], v[8:11], 0\n"
"ds_read_b64_tr_b16 a[252:253], v251 offset:4096\n"
"v_cvt_pk_bf16_f32 v87, v158, v159\n"
"v_mfma_f32_16x16x32_bf16 v[184:187], a[236:239], v[24:27], v[184:187]\n"
"ds_read_b64_tr_b16 a[254:255], v251 offset:6144\n"
"v_accvgpr_read_b32 v152, a152\n"
"v_mfma_f32_16x16x32_bf16 v[188:191], a[232:235], v[12:15], 0\n"
"v_accvgpr_read_b32 v153, a153\n"
"v_cvt_pk_bf16_f32 v88, v152, v153\n"
"v_mfma_f32_16x16x32_bf16 v[188:191], a[236:239], v[28:31], v[188:191]\n"
"s_waitcnt lgkmcnt(4)\n"
"v_accvgpr_read_b32 v154, a154\n"
"v_accvgpr_read_b32 v155, a155\n"
"v_mfma_f32_16x16x32_bf16 v[176:179], a[240:243], v[32:35], v[176:179]\n"
"ds_read_b64_tr_b16 a[232:233], v251 offset:8192\n"
"v_cvt_pk_bf16_f32 v89, v154, v155\n"
"s_lshl_b32 s84, %[g], 15\n"
"v_add_u32 v255, s84, v240\n"
"v_mfma_f32_16x16x32_bf16 v[176:179], a[244:247], v[48:51], v[176:179]\n"
"ds_read_b64_tr_b16 a[234:235], v251 offset:10240\n"
"v_accvgpr_read_b32 v156, a156\n"
"v_add_u32 v237, 131072, v240\n"
"v_mfma_f32_16x16x32_bf16 v[180:183], a[240:243], v[36:39], v[180:183]\n"
"v_accvgpr_read_b32 v157, a157\n"
"v_cvt_pk_bf16_f32 v90, v156, v157\n"
"v_xor_b32 v241, 8192, v237\n"
"v_mfma_f32_16x16x32_bf16 v[180:183], a[244:247], v[52:55], v[180:183]\n"
"v_accvgpr_read_b32 v158, a158\n"
"v_accvgpr_read_b32 v159, a159\n"
"v_lshrrev_b32 v239, 2, v238\n"
"v_mfma_f32_16x16x32_bf16 v[184:187], a[240:243], v[40:43], v[184:187]\n"
"ds_read_b64_tr_b16 a[236:237], v251 offset:12288\n"
"v_cvt_pk_bf16_f32 v91, v158, v159\n"
"v_xor_b32 v239, v239, v238\n"
"v_mfma_f32_16x16x32_bf16 v[184:187], a[244:247], v[56:59], v[184:187]\n"
"ds_read_b64_tr_b16 a[238:239], v251 offset:14336\n"
"v_accvgpr_read_b32 v152, a160\n"
"v_bfe_u32 v239, v239, 2, 2\n"
"v_mfma_f32_16x16x32_bf16 v[188:191], a[240:243], v[44:47], v[188:191]\n"
"v_accvgpr_read_b32 v153, a161\n"
"v_cvt_pk_bf16_f32 v92, v152, v153\n"
"v_accvgpr_read_b32 v152, a168\n"
"v_mfma_f32_16x16x32_bf16 v[188:191], a[244:247], v[60:63], v[188:191]\n"
"s_waitcnt lgkmcnt(4)\n"
"v_accvgpr_read_b32 v154, a162\n"
"v_accvgpr_read_b32 v155, a163\n"
"v_accvgpr_read_b32 v153, a169\n"
"v_mfma_f32_16x16x32_bf16 v[160:163], a[248:251], v[0:3], 0\n"
"ds_read_b64_tr_b16 a[240:241], v250 offset:33280\n"
"v_cvt_pk_bf16_f32 v93, v154, v155\n"
"v_cvt_pk_bf16_f32 v96, v152, v153\n"
"v_mfma_f32_16x16x32_bf16 v[160:163], a[252:255], v[16:19], v[160:163]\n"
"ds_read_b64_tr_b16 a[242:243], v250 offset:35328\n"
"v_accvgpr_read_b32 v156, a164\n"
"v_accvgpr_read_b32 v154, a170\n"
"v_mfma_f32_16x16x32_bf16 v[164:167], a[248:251], v[4:7], 0\n"
"v_accvgpr_read_b32 v157, a165\n"
"v_cvt_pk_bf16_f32 v94, v156, v157\n"
"v_accvgpr_read_b32 v155, a171\n"
"v_mfma_f32_16x16x32_bf16 v[164:167], a[252:255], v[20:23], v[164:167]\n"
"v_accvgpr_read_b32 v158, a166\n"
"v_accvgpr_read_b32 v159, a167\n"
"v_cvt_pk_bf16_f32 v97, v154, v155\n"
"v_mfma_f32_16x16x32_bf16 v[168:171], a[248:251], v[8:11], 0\n"
"ds_read_b64_tr_b16 a[244:245], v250 offset:37376\n"
"v_cvt_pk_bf16_f32 v95, v158, v159\n"
"v_accvgpr_read_b32 v156, a172\n"
"v_mfma_f32_16x16x32_bf16 v[168:171], a[252:255], v[24:27], v[168:171]\n"
"ds_read_b64_tr_b16 a[246:247], v250 offset:39424\n"
"v_accvgpr_read_b32 v157, a173\n"
"v_cvt_pk_bf16_f32 v98, v156, v157\n"
"v_mfma_f32_16x16x32_bf16 v[172:175], a[248:251], v[12:15], 0\n"
"v_accvgpr_read_b32 v158, a174\n"
"v_accvgpr_read_b32 v159, a175\n"
"v_cvt_pk_bf16_f32 v99, v158, v159\n"
"v_mfma_f32_16x16x32_bf16 v[172:175], a[252:255], v[28:31], v[172:175]\n"
"s_waitcnt lgkmcnt(4)\n"
"v_accvgpr_read_b32 v152, a176\n"
"v_accvgpr_read_b32 v153, a177\n"
"v_and_b32 v248, 1, v238\n"
"v_mfma_f32_16x16x32_bf16 v[160:163], a[232:235], v[32:35], v[160:163]\n"
"ds_read_b64_tr_b16 a[248:249], v250 offset:41472\n"
"v_cvt_pk_bf16_f32 v100, v152, v153\n"
"v_accvgpr_read_b32 v154, a178\n"
"v_mfma_f32_16x16x32_bf16 v[160:163], a[236:239], v[48:51], v[160:163]\n"
"ds_read_b64_tr_b16 a[250:251], v250 offset:43520\n"
"v_accvgpr_read_b32 v155, a179\n"
"v_cvt_pk_bf16_f32 v101, v154, v155\n"
"v_mfma_f32_16x16x32_bf16 v[164:167], a[232:235], v[36:39], v[164:167]\n"
"v_accvgpr_read_b32 v156, a180\n"
"v_accvgpr_read_b32 v157, a181\n"
"v_cvt_pk_bf16_f32 v102, v156, v157\n"
"v_mfma_f32_16x16x32_bf16 v[164:167], a[236:239], v[52:55], v[164:167]\n"
"v_accvgpr_read_b32 v158, a182\n"
"v_accvgpr_read_b32 v159, a183\n"
"v_cvt_pk_bf16_f32 v103, v158, v159\n"
"v_mfma_f32_16x16x32_bf16 v[168:171], a[232:235], v[40:43], v[168:171]\n"
"s_barrier\n"
"ds_read_b64_tr_b16 a[252:253], v250 offset:45568\n"
"v_accvgpr_read_b32 v152, a184\n"
"v_accvgpr_read_b32 v153, a185\n"
"v_lshlrev_b32 v248, 4, v248\n"
"v_mfma_f32_16x16x32_bf16 v[168:171], a[236:239], v[56:59], v[168:171]\n"
"ds_read_b64_tr_b16 a[254:255], v250 offset:47616\n"
"v_cvt_pk_bf16_f32 v104, v152, v153\n"
"v_accvgpr_read_b32 v154, a186\n"
"s_add_u32 s82, s34, 0\n"
"v_add_u32 v242, s82, v243\n"
"s_add_u32 m0, s71, 0\n"
"v_mfma_f32_16x16x32_bf16 v[172:175], a[232:235], v[44:47], v[172:175]\n"
"buffer_load_dwordx4 v242, s[48:51], 0 offen offset:0  lds\n"
"v_accvgpr_read_b32 v155, a187\n"
"v_cvt_pk_bf16_f32 v105, v154, v155\n"
"v_accvgpr_read_b32 v156, a188\n"
"v_mfma_f32_16x16x32_bf16 v[172:175], a[236:239], v[60:63], v[172:175]\n"
"s_waitcnt lgkmcnt(4)\n"
"v_accvgpr_read_b32 v157, a189\n"
"v_cvt_pk_bf16_f32 v106, v156, v157\n"
"v_accvgpr_read_b32 v158, a190\n"
"v_mfma_f32_16x16x32_bf16 a[0:3], a[240:243], v[64:67], a[0:3]\n"
"ds_read_b64_tr_b16 a[232:233], v251 offset:33280\n"
"v_accvgpr_read_b32 v159, a191\n"
"v_cvt_pk_bf16_f32 v107, v158, v159\n"
"v_mfma_f32_16x16x32_bf16 a[0:3], a[244:247], v[80:83], a[0:3]\n"
"ds_read_b64_tr_b16 a[234:235], v251 offset:35328\n"
"v_accvgpr_read_b32 v152, a192\n"
"v_accvgpr_read_b32 v153, a193\n"
"v_mfma_f32_16x16x32_bf16 a[4:7], a[240:243], v[68:71], a[4:7]\n"
"v_cvt_pk_bf16_f32 v108, v152, v153\n"
"v_accvgpr_read_b32 v154, a194\n"
"v_accvgpr_read_b32 v155, a195\n"
"v_accvgpr_read_b32 v152, a200\n"
"v_accvgpr_read_b32 v153, a201\n"
"v_cvt_pk_bf16_f32 v112, v152, v153\n"
"v_accvgpr_read_b32 v152, a208\n"
"v_accvgpr_read_b32 v153, a209\n"
"v_cvt_pk_bf16_f32 v116, v152, v153\n"
"v_accvgpr_read_b32 v152, a216\n"
"v_accvgpr_read_b32 v153, a217\n"
"v_cvt_pk_bf16_f32 v120, v152, v153\n"
"v_mfma_f32_16x16x32_bf16 a[4:7], a[244:247], v[84:87], a[4:7]\n"
"v_cvt_pk_bf16_f32 v109, v154, v155\n"
"v_accvgpr_read_b32 v156, a196\n"
"v_accvgpr_read_b32 v157, a197\n"
"v_accvgpr_read_b32 v154, a202\n"
"v_accvgpr_read_b32 v155, a203\n"
"v_cvt_pk_bf16_f32 v113, v154, v155\n"
"v_accvgpr_read_b32 v154, a210\n"
"v_accvgpr_read_b32 v155, a211\n"
"v_cvt_pk_bf16_f32 v117, v154, v155\n"
"v_accvgpr_read_b32 v154, a218\n"
"v_accvgpr_read_b32 v155, a219\n"
"v_cvt_pk_bf16_f32 v121, v154, v155\n"
"v_mfma_f32_16x16x32_bf16 a[8:11], a[240:243], v[72:75], a[8:11]\n"
"ds_read_b64_tr_b16 a[236:237], v251 offset:37376\n"
"v_cvt_pk_bf16_f32 v110, v156, v157\n"
"v_accvgpr_read_b32 v158, a198\n"
"v_accvgpr_read_b32 v156, a204\n"
"v_accvgpr_read_b32 v157, a205\n"
"v_cvt_pk_bf16_f32 v114, v156, v157\n"
"v_accvgpr_read_b32 v156, a212\n"
"v_accvgpr_read_b32 v157, a213\n"
"v_cvt_pk_bf16_f32 v118, v156, v157\n"
"v_accvgpr_read_b32 v156, a220\n"
"v_accvgpr_read_b32 v157, a221\n"
"v_cvt_pk_bf16_f32 v122, v156, v157\n"
"v_mfma_f32_16x16x32_bf16 a[8:11], a[244:247], v[88:91], a[8:11]\n"
"buffer_load_dwordx4 v242, s[48:51], s64 offen offset:1024  lds\n"
"ds_read_b64_tr_b16 a[238:239], v251 offset:39424\n"
"v_accvgpr_read_b32 v159, a199\n"
"v_cvt_pk_bf16_f32 v111, v158, v159\n"
"v_accvgpr_read_b32 v158, a206\n"
"v_accvgpr_read_b32 v159, a207\n"
"v_cvt_pk_bf16_f32 v115, v158, v159\n"
"v_accvgpr_read_b32 v158, a214\n"
"v_accvgpr_read_b32 v159, a215\n"
"v_cvt_pk_bf16_f32 v119, v158, v159\n"
"v_accvgpr_read_b32 v158, a222\n"
"v_accvgpr_read_b32 v159, a223\n"
"v_cvt_pk_bf16_f32 v123, v158, v159\n"
"v_mfma_f32_16x16x32_bf16 a[12:15], a[240:243], v[76:79], a[12:15]\n"
"v_accvgpr_read_b32 v152, a224\n"
"v_accvgpr_read_b32 v153, a225\n"
"v_cvt_pk_bf16_f32 v124, v152, v153\n"
"v_accvgpr_read_b32 v154, a226\n"
"v_accvgpr_read_b32 v155, a227\n"
"v_cvt_pk_bf16_f32 v125, v154, v155\n"
"v_accvgpr_read_b32 v156, a228\n"
"v_accvgpr_read_b32 v157, a229\n"
"v_cvt_pk_bf16_f32 v126, v156, v157\n"
"v_accvgpr_read_b32 v158, a230\n"
"v_accvgpr_read_b32 v159, a231\n"
"v_cvt_pk_bf16_f32 v127, v158, v159\n"
"v_mfma_f32_16x16x32_bf16 a[12:15], a[244:247], v[92:95], a[12:15]\n"
"s_waitcnt lgkmcnt(4)\n"
"v_mov_b32 v224, 0x3f80\n"
"v_lshlrev_b32 v248, v248, v224\n"
"v_add_u32 v252, 65536, v250\n"
"v_mfma_f32_16x16x32_bf16 a[0:3], a[248:251], v[96:99], a[0:3]\n"
"ds_read_b64_tr_b16 a[240:241], v251 offset:41472\n"
"v_cmp_eq_u32 vcc, 0, v239\n"
"s_nop 1\n"
"v_cndmask_b32 v248, 0, v248, vcc\n"
"v_mfma_f32_16x16x32_bf16 a[0:3], a[252:255], v[112:115], a[0:3]\n"
"ds_read_b64_tr_b16 a[242:243], v251 offset:43520\n"
"v_bfe_i32 v239, v238, 1, 1\n"
"v_and_b32 v227, v239, v248\n"
"v_mfma_f32_16x16x32_bf16 a[4:7], a[248:251], v[100:103], a[4:7]\n"
"v_xor_b32 v226, v227, v248\n"
"v_mov_b32 v224, 0\n"
"v_mov_b32 v225, 0\n"
"v_mfma_f32_16x16x32_bf16 a[4:7], a[252:255], v[116:119], a[4:7]\n"
"v_mov_b32 v228, 0\n"
"v_mov_b32 v229, 0\n"
"v_add_u32 v253, 65536, v251\n"
"v_mfma_f32_16x16x32_bf16 a[8:11], a[248:251], v[104:107], a[8:11]\n"
"buffer_load_dwordx4 v242, s[48:51], s65 offen offset:2048  lds\n"
"ds_read_b64_tr_b16 a[244:245], v251 offset:45568\n"
"s_lshr_b32 s84, s60, 1\n"
"s_lshl_b32 s84, s84, 6\n"
"v_and_b32 v239, 15, v238\n"
"v_add_u32 v239, s84, v239\n"
"v_mfma_f32_16x16x32_bf16 a[8:11], a[252:255], v[120:123], a[8:11]\n"
"ds_read_b64_tr_b16 a[246:247], v251 offset:47616\n"
"v_mul_u32_u24 v239, 1792, v239\n"
"v_lshrrev_b32 v248, 4, v238\n"
"v_mfma_f32_16x16x32_bf16 a[12:15], a[248:251], v[108:111], a[12:15]\n"
"v_lshl_add_u32 v239, v248, 3, v239\n"
"v_mfma_f32_16x16x32_bf16 a[12:15], a[252:255], v[124:127], a[12:15]\n"
"s_waitcnt lgkmcnt(4)\n"
"s_and_b32 s84, s60, 1\n"
"s_lshl_b32 s84, s84, 6\n"
"v_add_u32 v236, s84, v239\n"
"v_mfma_f32_16x16x32_bf16 a[16:19], a[232:235], v[64:67], a[16:19]\n"
"ds_read_b64_tr_b16 a[248:249], v250 offset:32768\n"
"s_mov_b64 s[40:41], %[out]\n"
"s_and_b32 s41, s41, 0xffff\n"
"s_mov_b32 s42, 228480\n"
"v_mfma_f32_16x16x32_bf16 a[16:19], a[236:239], v[80:83], a[16:19]\n"
"ds_read_b64_tr_b16 a[250:251], v250 offset:34816\n"
"v_mfma_f32_16x16x32_bf16 a[20:23], a[232:235], v[68:71], a[20:23]\n"
"v_rcp_f32 v192, v232\n"
"v_mfma_f32_16x16x32_bf16 a[20:23], a[236:239], v[84:87], a[20:23]\n"
"buffer_load_dwordx4 v242, s[48:51], s66 offen offset:3072  lds\n"
"v_rcp_f32 v193, v233\n"
"v_mfma_f32_16x16x32_bf16 a[24:27], a[232:235], v[72:75], a[24:27]\n"
"ds_read_b64_tr_b16 a[252:253], v250 offset:36864\n"
"v_mul_f32 v244, v244, v192\n"
"v_mfma_f32_16x16x32_bf16 a[24:27], a[236:239], v[88:91], a[24:27]\n"
"ds_read_b64_tr_b16 a[254:255], v250 offset:38912\n"
"v_mul_f32 v245, v245, v193\n"
"v_mfma_f32_16x16x32_bf16 a[28:31], a[232:235], v[76:79], a[28:31]\n"
"v_rcp_f32 v194, v234\n"
"v_mfma_f32_16x16x32_bf16 a[28:31], a[236:239], v[92:95], a[28:31]\n"
"s_waitcnt lgkmcnt(4)\n"
"v_rcp_f32 v195, v235\n"
"v_mfma_f32_16x16x32_bf16 a[16:19], a[240:243], v[96:99], a[16:19]\n"
"ds_read_b64_tr_b16 a[232:233], v250 offset:40960\n"
"v_mul_f32 v246, v246, v194\n"
"v_mfma_f32_16x16x32_bf16 a[16:19], a[244:247], v[112:115], a[16:19]\n"
"ds_read_b64_tr_b16 a[234:235], v250 offset:43008\n"
"v_mul_f32 v247, v247, v195\n"
"s_add_u32 s82, s34, 229376\n"
"v_add_u32 v242, s82, v243\n"
"v_xor_b32 v242, 64, v242\n"
"s_add_u32 m0, s71, 16384\n"
"v_mfma_f32_16x16x32_bf16 a[20:23], a[240:243], v[100:103], a[20:23]\n"
"buffer_load_dwordx4 v242, s[48:51], 0 offen offset:0  lds\n"
"v_mfma_f32_16x16x32_bf16 a[20:23], a[244:247], v[116:119], a[20:23]\n"
"ds_write_b128 v237, a[0:3] offset:0\n"
"v_mfma_f32_16x16x32_bf16 a[24:27], a[240:243], v[104:107], a[24:27]\n"
"ds_read_b64_tr_b16 a[236:237], v250 offset:45056\n"
"v_mfma_f32_16x16x32_bf16 a[24:27], a[244:247], v[120:123], a[24:27]\n"
"ds_read_b64_tr_b16 a[238:239], v250 offset:47104\n"
"ds_write_b128 v237, a[4:7] offset:1024\n"
"v_mfma_f32_16x16x32_bf16 a[28:31], a[240:243], v[108:111], a[28:31]\n"
"ds_write_b128 v237, a[8:11] offset:2048\n"
"v_mfma_f32_16x16x32_bf16 a[28:31], a[244:247], v[124:127], a[28:31]\n"
"s_waitcnt lgkmcnt(7)\n"
"ds_write_b128 v237, a[12:15] offset:3072\n"
"v_mfma_f32_16x16x32_bf16 v[176:179], a[248:251], v[64:67], v[176:179]\n"
"ds_read_b64_tr_b16 a[240:241], v251 offset:32768\n"
"v_mfma_f32_16x16x32_bf16 v[176:179], a[252:255], v[80:83], v[176:179]\n"
"buffer_load_dwordx4 v242, s[48:51], s64 offen offset:1024  lds\n"
"ds_read_b64_tr_b16 a[242:243], v251 offset:34816\n"
"v_mfma_f32_16x16x32_bf16 v[180:183], a[248:251], v[68:71], v[180:183]\n"
"ds_write_b128 v237, a[16:19] offset:4096\n"
"v_mfma_f32_16x16x32_bf16 v[180:183], a[252:255], v[84:87], v[180:183]\n"
"v_mfma_f32_16x16x32_bf16 v[184:187], a[248:251], v[72:75], v[184:187]\n"
"ds_read_b64_tr_b16 a[244:245], v251 offset:36864\n"
"v_mfma_f32_16x16x32_bf16 v[184:187], a[252:255], v[88:91], v[184:187]\n"
"ds_read_b64_tr_b16 a[246:247], v251 offset:38912\n"
"v_mfma_f32_16x16x32_bf16 v[188:191], a[248:251], v[76:79], v[188:191]\n"
"ds_write_b128 v237, a[20:23] offset:5120\n"
"v_mfma_f32_16x16x32_bf16 v[188:191], a[252:255], v[92:95], v[188:191]\n"
"s_waitcnt lgkmcnt(9)\n"
"v_mfma_f32_16x16x32_bf16 v[176:179], a[232:235], v[96:99], v[176:179]\n"
"buffer_load_dwordx4 v242, s[48:51], s65 offen offset:2048  lds\n"
"ds_read_b64_tr_b16 a[248:249], v251 offset:40960\n"
"v_mfma_f32_16x16x32_bf16 v[176:179], a[236:239], v[112:115], v[176:179]\n"
"ds_read_b64_tr_b16 a[250:251], v251 offset:43008\n"
"v_mfma_f32_16x16x32_bf16 v[180:183], a[232:235], v[100:103], v[180:183]\n"
"ds_write_b128 v237, a[24:27] offset:6144\n"
"v_mfma_f32_16x16x32_bf16 v[180:183], a[236:239], v[116:119], v[180:183]\n"
"v_mfma_f32_16x16x32_bf16 v[184:187], a[232:235], v[104:107], v[184:187]\n"
"ds_read_b64_tr_b16 a[252:253], v251 offset:45056\n"
"v_mfma_f32_16x16x32_bf16 v[184:187], a[236:239], v[120:123], v[184:187]\n"
"ds_read_b64_tr_b16 a[254:255], v251 offset:47104\n"
"v_mfma_f32_16x16x32_bf16 v[188:191], a[232:235], v[108:111], v[188:191]\n"
"ds_write_b128 v237, a[28:31] offset:7168\n"
"v_mfma_f32_16x16x32_bf16 v[188:191], a[236:239], v[124:127], v[188:191]\n"
"s_waitcnt lgkmcnt(7)\n"
"buffer_load_dwordx4 v242, s[48:51], s66 offen offset:3072  lds\n"
"v_mfma_f32_16x16x32_bf16 v[160:163], a[240:243], v[64:67], v[160:163]\n"
"ds_read_b64_tr_b16 a[232:233], v252 offset:512\n"
"v_mfma_f32_16x16x32_bf16 v[160:163], a[244:247], v[80:83], v[160:163]\n"
"ds_read_b64_tr_b16 a[234:235], v252 offset:2560\n"
"v_mfma_f32_16x16x32_bf16 v[164:167], a[240:243], v[68:71], v[164:167]\n"
"s_waitcnt lgkmcnt(2)\n"
"s_barrier\n"
"ds_read_b128 v[128:131], v241 offset:0\n"
"v_mfma_f32_16x16x32_bf16 v[164:167], a[244:247], v[84:87], v[164:167]\n"
"ds_read_b128 v[132:135], v241 offset:1024\n"
"v_mfma_f32_16x16x32_bf16 v[168:171], a[240:243], v[72:75], v[168:171]\n"
"ds_read_b64_tr_b16 a[236:237], v252 offset:4608\n"
"v_mfma_f32_16x16x32_bf16 v[168:171], a[244:247], v[88:91], v[168:171]\n"
"ds_read_b64_tr_b16 a[238:239], v252 offset:6656\n"
"v_mfma_f32_16x16x32_bf16 v[172:175], a[240:243], v[76:79], v[172:175]\n"
"ds_read_b128 v[136:139], v241 offset:2048\n"
"v_mfma_f32_16x16x32_bf16 v[172:175], a[244:247], v[92:95], v[172:175]\n"
"ds_read_b128 v[140:143], v241 offset:3072\n"
"v_mfma_f32_16x16x32_bf16 v[160:163], a[248:251], v[96:99], v[160:163]\n"
"ds_read_b64_tr_b16 a[240:241], v252 offset:8704\n"
"v_mfma_f32_16x16x32_bf16 v[160:163], a[252:255], v[112:115], v[160:163]\n"
"ds_read_b64_tr_b16 a[242:243], v252 offset:10752\n"
"v_mfma_f32_16x16x32_bf16 v[164:167], a[248:251], v[100:103], v[164:167]\n"
"ds_read_b128 v[144:147], v241 offset:4096\n"
"v_mfma_f32_16x16x32_bf16 v[164:167], a[252:255], v[116:119], v[164:167]\n"
"ds_read_b128 v[148:151], v241 offset:5120\n"
"v_mfma_f32_16x16x32_bf16 v[168:171], a[248:251], v[104:107], v[168:171]\n"
"ds_read_b64_tr_b16 a[244:245], v252 offset:12800\n"
"s_waitcnt lgkmcnt(5)\n"
"v_mfma_f32_16x16x32_bf16 v[168:171], a[252:255], v[120:123], v[168:171]\n"
"ds_read_b64_tr_b16 a[246:247], v252 offset:14848\n"
"v_fma_f32 v128, v176, v244, v128\n"
"v_mfma_f32_16x16x32_bf16 v[172:175], a[248:251], v[108:111], v[172:175]\n"
"ds_read_b128 v[152:155], v241 offset:6144\n"
"v_fma_f32 v129, v177, v244, v129\n"
"v_mfma_f32_16x16x32_bf16 v[172:175], a[252:255], v[124:127], v[172:175]\n"
"ds_read_b128 v[156:159], v241 offset:7168\n"
"v_fma_f32 v130, v178, v244, v130\n"
"v_mfma_f32_16x16x32_bf16 a[0:3], a[232:235], v[0:3], 0\n"
"s_waitcnt vmcnt(8)\n"
"s_barrier\n"
"ds_read_b64_tr_b16 a[248:249], v253 offset:512\n"
"v_mfma_f32_16x16x32_bf16 a[0:3], a[236:239], v[16:19], a[0:3]\n"
"ds_read_b64_tr_b16 a[250:251], v253 offset:2560\n"
"s_add_u32 s82, s34, 114688\n"
"v_add_u32 v242, s82, v243\n"
"s_add_u32 m0, s71, 32768\n"
"v_mfma_f32_16x16x32_bf16 a[4:7], a[232:235], v[4:7], 0\n"
"buffer_load_dwordx4 v242, s[48:51], 0 offen offset:0  lds\n"
"v_mfma_f32_16x16x32_bf16 a[4:7], a[236:239], v[20:23], a[4:7]\n"
"v_fma_f32 v131, v179, v244, v131\n"
"v_fma_f32 v132, v180, v245, v132\n"
"v_mfma_f32_16x16x32_bf16 a[8:11], a[232:235], v[8:11], 0\n"
"ds_read_b64_tr_b16 a[252:253], v253 offset:4608\n"
"v_fma_f32 v133, v181, v245, v133\n"
"v_mfma_f32_16x16x32_bf16 a[8:11], a[236:239], v[24:27], a[8:11]\n"
"ds_read_b64_tr_b16 a[254:255], v253 offset:6656\n"
"v_fma_f32 v134, v182, v245, v134\n"
"v_mfma_f32_16x16x32_bf16 a[12:15], a[232:235], v[12:15], 0\n"
"v_fma_f32 v135, v183, v245, v135\n"
"v_fma_f32 v136, v184, v246, v136\n"
"v_mfma_f32_16x16x32_bf16 a[12:15], a[236:239], v[28:31], a[12:15]\n"
"s_waitcnt lgkmcnt(6)\n"
"v_fma_f32 v137, v185, v246, v137\n"
"v_fma_f32 v138, v186, v246, v138\n"
"v_mfma_f32_16x16x32_bf16 a[0:3], a[240:243], v[32:35], a[0:3]\n"
"ds_read_b64_tr_b16 a[232:233], v253 offset:8704\n"
"v_fma_f32 v139, v187, v246, v139\n"
"v_mfma_f32_16x16x32_bf16 a[0:3], a[244:247], v[48:51], a[0:3]\n"
"buffer_load_dwordx4 v242, s[48:51], s64 offen offset:1024  lds\n"
"ds_read_b64_tr_b16 a[234:235], v253 offset:10752\n"
"v_mfma_f32_16x16x32_bf16 a[4:7], a[240:243], v[36:39], a[4:7]\n"
"v_fma_f32 v140, v188, v247, v140\n"
"v_fma_f32 v141, v189, v247, v141\n"
"v_mfma_f32_16x16x32_bf16 a[4:7], a[244:247], v[52:55], a[4:7]\n"
"v_fma_f32 v142, v190, v247, v142\n"
"v_fma_f32 v143, v191, v247, v143\n"
"v_mfma_f32_16x16x32_bf16 a[8:11], a[240:243], v[40:43], a[8:11]\n"
"ds_read_b64_tr_b16 a[236:237], v253 offset:12800\n"
"s_waitcnt lgkmcnt(7)\n"
"v_mfma_f32_16x16x32_bf16 a[8:11], a[244:247], v[56:59], a[8:11]\n"
"ds_read_b64_tr_b16 a[238:239], v253 offset:14848\n"
"v_fma_f32 v144, v160, v244, v144\n"
"v_mfma_f32_16x16x32_bf16 a[12:15], a[240:243], v[44:47], a[12:15]\n"
"v_fma_f32 v145, v161, v244, v145\n"
"v_fma_f32 v146, v162, v244, v146\n"
"v_mfma_f32_16x16x32_bf16 a[12:15], a[244:247], v[60:63], a[12:15]\n"
"s_waitcnt lgkmcnt(4)\n"
"v_fma_f32 v147, v163, v244, v147\n"
"v_fma_f32 v148, v164, v245, v148\n"
"v_mfma_f32_16x16x32_bf16 a[16:19], a[248:251], v[0:3], 0\n"
"buffer_load_dwordx4 v242, s[48:51], s65 offen offset:2048  lds\n"
"ds_read_b64_tr_b16 a[240:241], v252 offset:0\n"
"v_mfma_f32_16x16x32_bf16 a[16:19], a[252:255], v[16:19], a[16:19]\n"
"ds_read_b64_tr_b16 a[242:243], v252 offset:2048\n"
"v_fma_f32 v149, v165, v245, v149\n"
"v_mfma_f32_16x16x32_bf16 a[20:23], a[248:251], v[4:7], 0\n"
"v_fma_f32 v150, v166, v245, v150\n"
"v_fma_f32 v151, v167, v245, v151\n"
"v_mfma_f32_16x16x32_bf16 a[20:23], a[252:255], v[20:23], a[20:23]\n"
"v_fma_f32 v152, v168, v246, v152\n"
"v_fma_f32 v153, v169, v246, v153\n"
"v_mfma_f32_16x16x32_bf16 a[24:27], a[248:251], v[8:11], 0\n"
"ds_read_b64_tr_b16 a[244:245], v252 offset:4096\n"
"v_fma_f32 v154, v170, v246, v154\n"
"v_mfma_f32_16x16x32_bf16 a[24:27], a[252:255], v[24:27], a[24:27]\n"
"ds_read_b64_tr_b16 a[246:247], v252 offset:6144\n"
"v_fma_f32 v155, v171, v246, v155\n"
"v_mfma_f32_16x16x32_bf16 a[28:31], a[248:251], v[12:15], 0\n"
"L_CW5:\n"
"s_mov_b32 s75, L_CW5-L_PC+3072\n"
"buffer_load_dword a127, v254, s[56:59], s75 offen\n"
"v_mfma_f32_16x16x32_bf16 a[28:31], a[252:255], v[28:31], a[28:31]\n"
"s_waitcnt lgkmcnt(4)\n"
"buffer_load_dwordx4 v242, s[48:51], s66 offen offset:3072  lds\n"
"v_mfma_f32_16x16x32_bf16 a[16:19], a[232:235], v[32:35], a[16:19]\n"
"ds_read_b64_tr_b16 a[248:249], v252 offset:8192\n"
"v_fma_f32 v156, v172, v247, v156\n"
"v_mfma_f32_16x16x32_bf16 a[16:19], a[236:239], v[48:51], a[16:19]\n"
"ds_read_b64_tr_b16 a[250:251], v252 offset:10240\n"
"v_fma_f32 v157, v173, v247, v157\n"
"v_mfma_f32_16x16x32_bf16 a[20:23], a[232:235], v[36:39], a[20:23]\n"
"v_fma_f32 v158, v174, v247, v158\n"
"v_fma_f32 v159, v175, v247, v159\n"
"v_mfma_f32_16x16x32_bf16 a[20:23], a[236:239], v[52:55], a[20:23]\n"
"s_lshl_b32 s79, s32, 11\n"
"v_mul_f32 v128, v128, v232\n"
"v_mfma_f32_16x16x32_bf16 a[24:27], a[232:235], v[40:43], a[24:27]\n"
"ds_read_b64_tr_b16 a[252:253], v252 offset:12288\n"
"v_mul_f32 v129, v129, v232\n"
"v_mfma_f32_16x16x32_bf16 a[24:27], a[236:239], v[56:59], a[24:27]\n"
"ds_read_b64_tr_b16 a[254:255], v252 offset:14336\n"
"s_add_u32 s82, s34, 344064\n"
"v_add_u32 v242, s82, v243\n"
"v_xor_b32 v242, 64, v242\n"
"s_add_u32 m0, s71, 49152\n"
"v_mfma_f32_16x16x32_bf16 a[28:31], a[232:235], v[44:47], a[28:31]\n"
"buffer_load_dwordx4 v242, s[48:51], 0 offen offset:0  lds\n"
"v_mfma_f32_16x16x32_bf16 a[28:31], a[236:239], v[60:63], a[28:31]\n"
"s_waitcnt lgkmcnt(4)\n"
"v_mul_f32 v130, v130, v232\n"
"v_mul_f32 v131, v131, v232\n"
"v_mfma_f32_16x16x32_bf16 v[176:179], a[240:243], v[0:3], 0\n"
"ds_read_b64_tr_b16 a[232:233], v253 offset:0\n"
"v_mul_f32 v132, v132, v233\n"
"v_mfma_f32_16x16x32_bf16 v[176:179], a[244:247], v[16:19], v[176:179]\n"
"ds_read_b64_tr_b16 a[234:235], v253 offset:2048\n"
"v_mul_f32 v133, v133, v233\n"
"v_mfma_f32_16x16x32_bf16 v[180:183], a[240:243], v[4:7], 0\n"
"v_mul_f32 v134, v134, v233\n"
"v_mul_f32 v135, v135, v233\n"
"v_mfma_f32_16x16x32_bf16 v[180:183], a[244:247], v[20:23], v[180:183]\n"
"v_cvt_pk_bf16_f32 v128, v128, v129\n"
"v_cvt_pk_bf16_f32 v129, v130, v131\n"
"v_mfma_f32_16x16x32_bf16 v[184:187], a[240:243], v[8:11], 0\n"
"ds_read_b64_tr_b16 a[236:237], v253 offset:4096\n"
"v_cvt_pk_bf16_f32 v130, v132, v133\n"
"v_mfma_f32_16x16x32_bf16 v[184:187], a[244:247], v[24:27], v[184:187]\n"
"buffer_load_dwordx4 v242, s[48:51], s64 offen offset:1024  lds\n"
"ds_read_b64_tr_b16 a[238:239], v253 offset:6144\n"
"v_mfma_f32_16x16x32_bf16 v[188:191], a[240:243], v[12:15], 0\n"
"v_cvt_pk_bf16_f32 v131, v134, v135\n"
"v_mul_f32 v136, v136, v234\n"
"v_mfma_f32_16x16x32_bf16 v[188:191], a[244:247], v[28:31], v[188:191]\n"
"s_waitcnt lgkmcnt(4)\n"
"buffer_store_dwordx4 v[128:131], v255, s[52:55], s79 offen offset:0 sc1\n"
"v_mfma_f32_16x16x32_bf16 v[176:179], a[248:251], v[32:35], v[176:179]\n"
"ds_read_b64_tr_b16 a[240:241], v253 offset:8192\n"
"v_mul_f32 v137, v137, v234\n"
"v_mfma_f32_16x16x32_bf16 v[176:179], a[252:255], v[48:51], v[176:179]\n"
"ds_read_b64_tr_b16 a[242:243], v253 offset:10240\n"
"v_mul_f32 v138, v138, v234\n"
"v_mfma_f32_16x16x32_bf16 v[180:183], a[248:251], v[36:39], v[180:183]\n"
"v_mul_f32 v139, v139, v234\n"
"v_mul_f32 v140, v140, v235\n"
"v_mfma_f32_16x16x32_bf16 v[180:183], a[252:255], v[52:55], v[180:183]\n"
"v_mul_f32 v141, v141, v235\n"
"v_mul_f32 v142, v142, v235\n"
"v_mfma_f32_16x16x32_bf16 v[184:187], a[248:251], v[40:43], v[184:187]\n"
"buffer_load_dwordx4 v242, s[48:51], s65 offen offset:2048  lds\n"
"ds_read_b64_tr_b16 a[244:245], v253 offset:12288\n"
"v_mfma_f32_16x16x32_bf16 v[184:187], a[252:255], v[56:59], v[184:187]\n"
"ds_read_b64_tr_b16 a[246:247], v253 offset:14336\n"
"v_mul_f32 v143, v143, v235\n"
"v_mfma_f32_16x16x32_bf16 v[188:191], a[248:251], v[44:47], v[188:191]\n"
"v_cvt_pk_bf16_f32 v136, v136, v137\n"
"v_cvt_pk_bf16_f32 v137, v138, v139\n"
"v_mfma_f32_16x16x32_bf16 v[188:191], a[252:255], v[60:63], v[188:191]\n"
"s_waitcnt lgkmcnt(4)\n"
"v_cvt_pk_bf16_f32 v138, v140, v141\n"
"v_cvt_pk_bf16_f32 v139, v142, v143\n"
"v_mfma_f32_16x16x32_bf16 v[160:163], a[232:235], v[0:3], 0\n"
"ds_read_b64_tr_b16 a[248:249], v252 offset:33280\n"
"v_mul_f32 v144, v144, v232\n"
"v_mfma_f32_16x16x32_bf16 v[160:163], a[236:239], v[16:19], v[160:163]\n"
"ds_read_b64_tr_b16 a[250:251], v252 offset:35328\n"
"v_mul_f32 v145, v145, v232\n"
"v_mfma_f32_16x16x32_bf16 v[164:167], a[232:235], v[4:7], 0\n"
"s_mov_b32 s75, L_TAILX-L_PC+0\n"
"buffer_load_dword a127, v254, s[56:59], s75 offen\n"
"v_mfma_f32_16x16x32_bf16 v[164:167], a[236:239], v[20:23], v[164:167]\n"
"buffer_load_dwordx4 v242, s[48:51], s66 offen offset:3072  lds\n"
"v_mfma_f32_16x16x32_bf16 v[168:171], a[232:235], v[8:11], 0\n"
"ds_read_b64_tr_b16 a[252:253], v252 offset:37376\n"
"v_mul_f32 v146, v146, v232\n"
"v_mfma_f32_16x16x32_bf16 v[168:171], a[236:239], v[24:27], v[168:171]\n"
"ds_read_b64_tr_b16 a[254:255], v252 offset:39424\n"
"v_mul_f32 v147, v147, v232\n"
"v_mfma_f32_16x16x32_bf16 v[172:175], a[232:235], v[12:15], 0\n"
"buffer_store_dwordx4 v[136:139], v255, s[52:55], s79 offen offset:1024 sc1\n"
"v_mfma_f32_16x16x32_bf16 v[172:175], a[236:239], v[28:31], v[172:175]\n"
"s_waitcnt lgkmcnt(4)\n"
"s_lshl_b32 s80, s32, 11\n"
"s_add_u32 s80, s80, 4096\n"
"v_mfma_f32_16x16x32_bf16 v[160:163], a[240:243], v[32:35], v[160:163]\n"
"ds_read_b64_tr_b16 a[232:233], v252 offset:41472\n"
"v_mul_f32 v148, v148, v233\n"
"v_mfma_f32_16x16x32_bf16 v[160:163], a[244:247], v[48:51], v[160:163]\n"
"ds_read_b64_tr_b16 a[234:235], v252 offset:43520\n"
"v_mul_f32 v149, v149, v233\n"
"v_mfma_f32_16x16x32_bf16 v[164:167], a[240:243], v[36:39], v[164:167]\n"
"v_mul_f32 v150, v150, v233\n"
"v_mul_f32 v151, v151, v233\n"
"v_mfma_f32_16x16x32_bf16 v[164:167], a[244:247], v[52:55], v[164:167]\n"
"v_cvt_pk_bf16_f32 v144, v144, v145\n"
"v_cvt_pk_bf16_f32 v145, v146, v147\n"
"v_mfma_f32_16x16x32_bf16 v[168:171], a[240:243], v[40:43], v[168:171]\n"
"s_waitcnt vmcnt(12)\n"
"s_barrier\n"
"ds_read_b64_tr_b16 a[236:237], v252 offset:45568\n"
"v_mfma_f32_16x16x32_bf16 v[168:171], a[244:247], v[56:59], v[168:171]\n"
"ds_read_b64_tr_b16 a[238:239], v252 offset:47616\n"
"s_add_u32 s82, s35, 0\n"
"v_add_u32 v242, s82, v243\n"
"s_add_u32 m0, s71, 65536\n"
"v_mfma_f32_16x16x32_bf16 v[172:175], a[240:243], v[44:47], v[172:175]\n"
"buffer_load_dwordx4 v242, s[48:51], 0 offen offset:0  lds\n"
"v_mfma_f32_16x16x32_bf16 v[172:175], a[244:247], v[60:63], v[172:175]\n"
"s_waitcnt lgkmcnt(4)\n"
"v_cvt_pk_bf16_f32 v146, v148, v149\n"
"v_cvt_pk_bf16_f32 v147, v150, v151\n"
"v_mfma_f32_16x16x32_bf16 a[0:3], a[248:251], v[64:67], a[0:3]\n"
"ds_read_b64_tr_b16 a[240:241], v253 offset:33280\n"
"v_mul_f32 v152, v152, v234\n"
"v_mfma_f32_16x16x32_bf16 a[0:3], a[252:255], v[80:83], a[0:3]\n"
"ds_read_b64_tr_b16 a[242:243], v253 offset:35328\n"
"v_mul_f32 v153, v153, v234\n"
"v_mfma_f32_16x16x32_bf16 a[4:7], a[248:251], v[68:71], a[4:7]\n"
"buffer_store_dwordx4 v[144:147], v255, s[52:55], s80 offen offset:0 sc1\n"
"v_mfma_f32_16x16x32_bf16 a[4:7], a[252:255], v[84:87], a[4:7]\n"
"v_mul_f32 v154, v154, v234\n"
"v_mul_f32 v155, v155, v234\n"
"v_mfma_f32_16x16x32_bf16 a[8:11], a[248:251], v[72:75], a[8:11]\n"
"ds_read_b64_tr_b16 a[244:245], v253 offset:37376\n"
"v_mul_f32 v156, v156, v235\n"
"v_mfma_f32_16x16x32_bf16 a[8:11], a[252:255], v[88:91], a[8:11]\n"
"buffer_load_dwordx4 v242, s[48:51], s64 offen offset:1024  lds\n"
"ds_read_b64_tr_b16 a[246:247], v253 offset:39424\n"
"v_mfma_f32_16x16x32_bf16 a[12:15], a[248:251], v[76:79], a[12:15]\n"
"v_mul_f32 v157, v157, v235\n"
"v_mul_f32 v158, v158, v235\n"
"v_mfma_f32_16x16x32_bf16 a[12:15], a[252:255], v[92:95], a[12:15]\n"
"s_waitcnt lgkmcnt(4)\n"
"v_mul_f32 v159, v159, v235\n"
"v_cvt_pk_bf16_f32 v152, v152, v153\n"
"v_mfma_f32_16x16x32_bf16 a[0:3], a[232:235], v[96:99], a[0:3]\n"
"ds_read_b64_tr_b16 a[248:249], v253 offset:41472\n"
"v_cvt_pk_bf16_f32 v153, v154, v155\n"
"v_mfma_f32_16x16x32_bf16 a[0:3], a[236:239], v[112:115], a[0:3]\n"
"ds_read_b64_tr_b16 a[250:251], v253 offset:43520\n"
"v_cvt_pk_bf16_f32 v154, v156, v157\n"
"v_mfma_f32_16x16x32_bf16 a[4:7], a[232:235], v[100:103], a[4:7]\n"
"v_cvt_pk_bf16_f32 v155, v158, v159\n"
"v_mfma_f32_16x16x32_bf16 a[4:7], a[236:239], v[116:119], a[4:7]\n"
"buffer_store_dwordx4 v[152:155], v255, s[52:55], s80 offen offset:1024 sc1\n"
"v_mfma_f32_16x16x32_bf16 a[8:11], a[232:235], v[104:107], a[8:11]\n"
"buffer_load_dwordx4 v242, s[48:51], s65 offen offset:2048  lds\n"
"ds_read_b64_tr_b16 a[252:253], v253 offset:45568\n"
"v_mfma_f32_16x16x32_bf16 a[8:11], a[236:239], v[120:123], a[8:11]\n"
"ds_read_b64_tr_b16 a[254:255], v253 offset:47616\n"
"v_mfma_f32_16x16x32_bf16 a[12:15], a[232:235], v[108:111], a[12:15]\n"
"v_mfma_f32_16x16x32_bf16 a[12:15], a[236:239], v[124:127], a[12:15]\n"
"s_waitcnt lgkmcnt(4)\n"
"v_mfma_f32_16x16x32_bf16 a[16:19], a[240:243], v[64:67], a[16:19]\n"
"ds_read_b64_tr_b16 a[232:233], v252 offset:32768\n"
"v_mfma_f32_16x16x32_bf16 a[16:19], a[244:247], v[80:83], a[16:19]\n"
"ds_read_b64_tr_b16 a[234:235], v252 offset:34816\n"
"v_mfma_f32_16x16x32_bf16 a[20:23], a[240:243], v[68:71], a[20:23]\n"
"ds_write_b128 v237, a[0:3] offset:0\n"
"v_mfma_f32_16x16x32_bf16 a[20:23], a[244:247], v[84:87], a[20:23]\n"
"buffer_load_dwordx4 v242, s[48:51], s66 offen offset:3072  lds\n"
"v_mfma_f32_16x16x32_bf16 a[24:27], a[240:243], v[72:75], a[24:27]\n"
"ds_read_b64_tr_b16 a[236:237], v252 offset:36864\n"
"v_mfma_f32_16x16x32_bf16 a[24:27], a[244:247], v[88:91], a[24:27]\n"
"ds_read_b64_tr_b16 a[238:239], v252 offset:38912\n"
"v_mfma_f32_16x16x32_bf16 a[28:31], a[240:243], v[76:79], a[28:31]\n"
"ds_write_b128 v237, a[4:7] offset:1024\n"
"v_mfma_f32_16x16x32_bf16 a[28:31], a[244:247], v[92:95], a[28:31]\n"
"s_waitcnt lgkmcnt(6)\n"
"v_mfma_f32_16x16x32_bf16 a[16:19], a[248:251], v[96:99], a[16:19]\n"
"ds_read_b64_tr_b16 a[240:241], v252 offset:40960\n"
"v_mfma_f32_16x16x32_bf16 a[16:19], a[252:255], v[112:115], a[16:19]\n"
"ds_read_b64_tr_b16 a[242:243], v252 offset:43008\n"
"s_add_u32 s82, s35, 229376\n"
"v_add_u32 v242, s82, v243\n"
"v_xor_b32 v242, 64, v242\n"
"s_add_u32 m0, s71, 81920\n"
"v_mfma_f32_16x16x32_bf16 a[20:23], a[248:251], v[100:103], a[20:23]\n"
"buffer_load_dwordx4 v242, s[48:51], 0 offen offset:0  lds\n"
"v_mfma_f32_16x16x32_bf16 a[20:23], a[252:255], v[116:119], a[20:23]\n"
"ds_write_b128 v237, a[8:11] offset:2048\n"
"v_mfma_f32_16x16x32_bf16 a[24:27], a[248:251], v[104:107], a[24:27]\n"
"ds_read_b64_tr_b16 a[244:245], v252 offset:45056\n"
"v_mfma_f32_16x16x32_bf16 a[24:27], a[252:255], v[120:123], a[24:27]\n"
"ds_read_b64_tr_b16 a[246:247], v252 offset:47104\n"
"v_mfma_f32_16x16x32_bf16 a[28:31], a[248:251], v[108:111], a[28:31]\n"
"ds_write_b128 v237, a[12:15] offset:3072\n"
"v_mfma_f32_16x16x32_bf16 a[28:31], a[252:255], v[124:127], a[28:31]\n"
"s_waitcnt lgkmcnt(7)\n"
"v_mfma_f32_16x16x32_bf16 v[176:179], a[232:235], v[64:67], v[176:179]\n"
"ds_read_b64_tr_b16 a[248:249], v253 offset:32768\n"
"v_mfma_f32_16x16x32_bf16 v[176:179], a[236:239], v[80:83], v[176:179]\n"
"buffer_load_dwordx4 v242, s[48:51], s64 offen offset:1024  lds\n"
"ds_read_b64_tr_b16 a[250:251], v253 offset:34816\n"
"v_mfma_f32_16x16x32_bf16 v[180:183], a[232:235], v[68:71], v[180:183]\n"
"ds_write_b128 v237, a[16:19] offset:4096\n"
"v_mfma_f32_16x16x32_bf16 v[180:183], a[236:239], v[84:87], v[180:183]\n"
"v_mfma_f32_16x16x32_bf16 v[184:187], a[232:235], v[72:75], v[184:187]\n"
"ds_read_b64_tr_b16 a[252:253], v253 offset:36864\n"
"v_mfma_f32_16x16x32_bf16 v[184:187], a[236:239], v[88:91], v[184:187]\n"
"ds_read_b64_tr_b16 a[254:255], v253 offset:38912\n"
"v_mfma_f32_16x16x32_bf16 v[188:191], a[232:235], v[76:79], v[188:191]\n"
"ds_write_b128 v237, a[20:23] offset:5120\n"
"v_mfma_f32_16x16x32_bf16 v[188:191], a[236:239], v[92:95], v[188:191]\n"
"s_waitcnt lgkmcnt(7)\n"
"v_mfma_f32_16x16x32_bf16 v[176:179], a[240:243], v[96:99], v[176:179]\n"
"buffer_load_dwordx4 v242, s[48:51], s65 offen offset:2048  lds\n"
"ds_read_b64_tr_b16 a[232:233], v253 offset:40960\n"
"v_mfma_f32_16x16x32_bf16 v[176:179], a[244:247], v[112:115], v[176:179]\n"
"ds_read_b64_tr_b16 a[234:235], v253 offset:43008\n"
"v_mfma_f32_16x16x32_bf16 v[180:183], a[240:243], v[100:103], v[180:183]\n"
"ds_write_b128 v237, a[24:27] offset:6144\n"
"v_mfma_f32_16x16x32_bf16 v[180:183], a[244:247], v[116:119], v[180:183]\n"
"v_mfma_f32_16x16x32_bf16 v[184:187], a[240:243], v[104:107], v[184:187]\n"
"ds_read_b64_tr_b16 a[236:237], v253 offset:45056\n"
"v_mfma_f32_16x16x32_bf16 v[184:187], a[244:247], v[120:123], v[184:187]\n"
"ds_read_b64_tr_b16 a[238:239], v253 offset:47104\n"
"ds_write_b128 v237, a[28:31] offset:7168\n"
"v_mfma_f32_16x16x32_bf16 v[188:191], a[240:243], v[108:111], v[188:191]\n"
"s_waitcnt vmcnt(5)\n"
"s_lshl_b32 s83, s32, 1\n"
"s_add_u32 s83, s83, s39\n"
"buffer_store_dwordx2 v[230:231], off, s[52:55], s83 sc1\n"
"v_mfma_f32_16x16x32_bf16 v[188:191], a[244:247], v[124:127], v[188:191]\n"
"s_waitcnt lgkmcnt(7)\n"
"buffer_load_dwordx4 v242, s[48:51], s66 offen offset:3072  lds\n"
"v_mfma_f32_16x16x32_bf16 v[160:163], a[248:251], v[64:67], v[160:163]\n"
"ds_read_b64_tr_b16 a[240:241], v250 offset:512\n"
"v_mfma_f32_16x16x32_bf16 v[160:163], a[252:255], v[80:83], v[160:163]\n"
"ds_read_b64_tr_b16 a[242:243], v250 offset:2560\n"
"v_mfma_f32_16x16x32_bf16 v[164:167], a[248:251], v[68:71], v[164:167]\n"
"s_waitcnt lgkmcnt(2)\n"
"s_barrier\n"
"ds_read_b128 v[128:131], v241 offset:0\n"
"v_mfma_f32_16x16x32_bf16 v[164:167], a[252:255], v[84:87], v[164:167]\n"
"ds_read_b128 v[132:135], v241 offset:1024\n"
"v_mfma_f32_16x16x32_bf16 v[168:171], a[248:251], v[72:75], v[168:171]\n"
"ds_read_b64_tr_b16 a[244:245], v250 offset:4608\n"
"v_mfma_f32_16x16x32_bf16 v[168:171], a[252:255], v[88:91], v[168:171]\n"
"ds_read_b64_tr_b16 a[246:247], v250 offset:6656\n"
"v_mfma_f32_16x16x32_bf16 v[172:175], a[248:251], v[76:79], v[172:175]\n"
"ds_read_b128 v[136:139], v241 offset:2048\n"
"v_mfma_f32_16x16x32_bf16 v[172:175], a[252:255], v[92:95], v[172:175]\n"
"ds_read_b128 v[140:143], v241 offset:3072\n"
"v_mfma_f32_16x16x32_bf16 v[160:163], a[232:235], v[96:99], v[160:163]\n"
"ds_read_b64_tr_b16 a[248:249], v250 offset:8704\n"
"v_mfma_f32_16x16x32_bf16 v[160:163], a[236:239], v[112:115], v[160:163]\n"
"ds_read_b64_tr_b16 a[250:251], v250 offset:10752\n"
"v_mfma_f32_16x16x32_bf16 v[164:167], a[232:235], v[100:103], v[164:167]\n"
"ds_read_b128 v[144:147], v241 offset:4096\n"
"v_mfma_f32_16x16x32_bf16 v[164:167], a[236:239], v[116:119], v[164:167]\n"
"ds_read_b128 v[148:151], v241 offset:5120\n"
"v_mfma_f32_16x16x32_bf16 v[168:171], a[232:235], v[104:107], v[168:171]\n"
"ds_read_b64_tr_b16 a[252:253], v250 offset:12800\n"
"s_waitcnt lgkmcnt(5)\n"
"v_mfma_f32_16x16x32_bf16 v[168:171], a[236:239], v[120:123], v[168:171]\n"
"ds_read_b64_tr_b16 a[254:255], v250 offset:14848\n"
"v_fma_f32 v128, v176, v244, v128\n"
"v_mfma_f32_16x16x32_bf16 v[172:175], a[232:235], v[108:111], v[172:175]\n"
"ds_read_b128 v[152:155], v241 offset:6144\n"
"v_fma_f32 v129, v177, v244, v129\n"
"v_mfma_f32_16x16x32_bf16 v[172:175], a[236:239], v[124:127], v[172:175]\n"
"ds_read_b128 v[156:159], v241 offset:7168\n"
"v_fma_f32 v130, v178, v244, v130\n"
"v_mfma_f32_16x16x32_bf16 a[0:3], a[240:243], v[0:3], 0\n"
"s_barrier\n"
"ds_read_b64_tr_b16 a[232:233], v251 offset:512\n"
"v_mfma_f32_16x16x32_bf16 a[0:3], a[244:247], v[16:19], a[0:3]\n"
"ds_read_b64_tr_b16 a[234:235], v251 offset:2560\n"
"s_add_u32 s82, s35, 114688\n"
"v_add_u32 v242, s82, v243\n"
"s_add_u32 m0, s71, 98304\n"
"v_mfma_f32_16x16x32_bf16 a[4:7], a[240:243], v[4:7], 0\n"
"buffer_load_dwordx4 v242, s[48:51], 0 offen offset:0  lds\n"
"v_mfma_f32_16x16x32_bf16 a[4:7], a[244:247], v[20:23], a[4:7]\n"
"v_fma_f32 v131, v179, v244, v131\n"
"v_fma_f32 v132, v180, v245, v132\n"
"v_mfma_f32_16x16x32_bf16 a[8:11], a[240:243], v[8:11], 0\n"
"ds_read_b64_tr_b16 a[236:237], v251 offset:4608\n"
"v_fma_f32 v133, v181, v245, v133\n"
"v_mfma_f32_16x16x32_bf16 a[8:11], a[244:247], v[24:27], a[8:11]\n"
"ds_read_b64_tr_b16 a[238:239], v251 offset:6656\n"
"v_fma_f32 v134, v182, v245, v134\n"
"v_mfma_f32_16x16x32_bf16 a[12:15], a[240:243], v[12:15], 0\n"
"v_fma_f32 v135, v183, v245, v135\n"
"v_fma_f32 v136, v184, v246, v136\n"
"v_mfma_f32_16x16x32_bf16 a[12:15], a[244:247], v[28:31], a[12:15]\n"
"s_waitcnt lgkmcnt(6)\n"
"v_fma_f32 v137, v185, v246, v137\n"
"v_fma_f32 v138, v186, v246, v138\n"
"v_mfma_f32_16x16x32_bf16 a[0:3], a[248:251], v[32:35], a[0:3]\n"
"ds_read_b64_tr_b16 a[240:241], v251 offset:8704\n"
"v_fma_f32 v139, v187, v246, v139\n"
"v_mfma_f32_16x16x32_bf16 a[0:3], a[252:255], v[48:51], a[0:3]\n"
"buffer_load_dwordx4 v242, s[48:51], s64 offen offset:1024  lds\n"
"ds_read_b64_tr_b16 a[242:243], v251 offset:10752\n"
"v_mfma_f32_16x16x32_bf16 a[4:7], a[248:251], v[36:39], a[4:7]\n"
"v_fma_f32 v140, v188, v247, v140\n"
"v_fma_f32 v141, v189, v247, v141\n"
"v_mfma_f32_16x16x32_bf16 a[4:7], a[252:255], v[52:55], a[4:7]\n"
"v_fma_f32 v142, v190, v247, v142\n"
"v_fma_f32 v143, v191, v247, v143\n"
"v_mfma_f32_16x16x32_bf16 a[8:11], a[248:251], v[40:43], a[8:11]\n"
"ds_read_b64_tr_b16 a[244:245], v251 offset:12800\n"
"s_waitcnt lgkmcnt(7)\n"
"v_mfma_f32_16x16x32_bf16 a[8:11], a[252:255], v[56:59], a[8:11]\n"
"ds_read_b64_tr_b16 a[246:247], v251 offset:14848\n"
"v_fma_f32 v144, v160, v244, v144\n"
"v_mfma_f32_16x16x32_bf16 a[12:15], a[248:251], v[44:47], a[12:15]\n"
"v_fma_f32 v145, v161, v244, v145\n"
"v_fma_f32 v146, v162, v244, v146\n"
"v_mfma_f32_16x16x32_bf16 a[12:15], a[252:255], v[60:63], a[12:15]\n"
"s_waitcnt lgkmcnt(4)\n"
"v_fma_f32 v147, v163, v244, v147\n"
"v_fma_f32 v148, v164, v245, v148\n"
"v_mfma_f32_16x16x32_bf16 a[16:19], a[232:235], v[0:3], 0\n"
"buffer_load_dwordx4 v242, s[48:51], s65 offen offset:2048  lds\n"
"ds_read_b64_tr_b16 a[248:249], v250 offset:0\n"
"v_mfma_f32_16x16x32_bf16 a[16:19], a[236:239], v[16:19], a[16:19]\n"
"ds_read_b64_tr_b16 a[250:251], v250 offset:2048\n"
"v_fma_f32 v149, v165, v245, v149\n"
"v_mfma_f32_16x16x32_bf16 a[20:23], a[232:235], v[4:7], 0\n"
"v_fma_f32 v150, v166, v245, v150\n"
"v_fma_f32 v151, v167, v245, v151\n"
"v_mfma_f32_16x16x32_bf16 a[20:23], a[236:239], v[20:23], a[20:23]\n"
"v_fma_f32 v152, v168, v246, v152\n"
"v_fma_f32 v153, v169, v246, v153\n"
"v_mfma_f32_16x16x32_bf16 a[24:27], a[232:235], v[8:11], 0\n"
"ds_read_b64_tr_b16 a[252:253], v250 offset:4096\n"
"v_fma_f32 v154, v170, v246, v154\n"
"v_mfma_f32_16x16x32_bf16 a[24:27], a[236:239], v[24:27], a[24:27]\n"
"ds_read_b64_tr_b16 a[254:255], v250 offset:6144\n"
"v_fma_f32 v155, v171, v246, v155\n"
"v_mfma_f32_16x16x32_bf16 a[28:31], a[232:235], v[12:15], 0\n"
"L_CW7:\n"
"s_mov_b32 s75, L_CW7-L_PC+3072\n"
"buffer_load_dword a127, v254, s[56:59], s75 offen\n"
"v_mfma_f32_16x16x32_bf16 a[28:31], a[236:239], v[28:31], a[28:31]\n"
"s_waitcnt lgkmcnt(4)\n"
"buffer_load_dwordx4 v242, s[48:51], s66 offen offset:3072  lds\n"
"v_mfma_f32_16x16x32_bf16 a[16:19], a[240:243], v[32:35], a[16:19]\n"
"ds_read_b64_tr_b16 a[232:233], v250 offset:8192\n"
"v_fma_f32 v156, v172, v247, v156\n"
"v_mfma_f32_16x16x32_bf16 a[16:19], a[244:247], v[48:51], a[16:19]\n"
"ds_read_b64_tr_b16 a[234:235], v250 offset:10240\n"
"v_fma_f32 v157, v173, v247, v157\n"
"v_mfma_f32_16x16x32_bf16 a[20:23], a[240:243], v[36:39], a[20:23]\n"
"v_fma_f32 v158, v174, v247, v158\n"
"v_fma_f32 v159, v175, v247, v159\n"
"v_mfma_f32_16x16x32_bf16 a[20:23], a[244:247], v[52:55], a[20:23]\n"
"s_lshl_b32 s79, s33, 11\n"
"v_mul_f32 v128, v128, v232\n"
"v_mfma_f32_16x16x32_bf16 a[24:27], a[240:243], v[40:43], a[24:27]\n"
"ds_read_b64_tr_b16 a[236:237], v250 offset:12288\n"
"v_mul_f32 v129, v129, v232\n"
"v_mfma_f32_16x16x32_bf16 a[24:27], a[244:247], v[56:59], a[24:27]\n"
"ds_read_b64_tr_b16 a[238:239], v250 offset:14336\n"
"s_add_u32 s82, s35, 344064\n"
"v_add_u32 v242, s82, v243\n"
"v_xor_b32 v242, 64, v242\n"
"s_add_u32 m0, s71, 114688\n"
"v_mfma_f32_16x16x32_bf16 a[28:31], a[240:243], v[44:47], a[28:31]\n"
"buffer_load_dwordx4 v242, s[48:51], 0 offen offset:0  lds\n"
"v_mfma_f32_16x16x32_bf16 a[28:31], a[244:247], v[60:63], a[28:31]\n"
"s_waitcnt lgkmcnt(4)\n"
"v_mul_f32 v130, v130, v232\n"
"v_mul_f32 v131, v131, v232\n"
"v_mfma_f32_16x16x32_bf16 v[176:179], a[248:251], v[0:3], 0\n"
"ds_read_b64_tr_b16 a[240:241], v251 offset:0\n"
"v_mul_f32 v132, v132, v233\n"
"v_mfma_f32_16x16x32_bf16 v[176:179], a[252:255], v[16:19], v[176:179]\n"
"ds_read_b64_tr_b16 a[242:243], v251 offset:2048\n"
"v_mul_f32 v133, v133, v233\n"
"v_mfma_f32_16x16x32_bf16 v[180:183], a[248:251], v[4:7], 0\n"
"v_mul_f32 v134, v134, v233\n"
"v_mul_f32 v135, v135, v233\n"
"v_mfma_f32_16x16x32_bf16 v[180:183], a[252:255], v[20:23], v[180:183]\n"
"v_cvt_pk_bf16_f32 v128, v128, v129\n"
"v_cvt_pk_bf16_f32 v129, v130, v131\n"
"v_mfma_f32_16x16x32_bf16 v[184:187], a[248:251], v[8:11], 0\n"
"ds_read_b64_tr_b16 a[244:245], v251 offset:4096\n"
"v_cvt_pk_bf16_f32 v130, v132, v133\n"
"v_mfma_f32_16x16x32_bf16 v[184:187], a[252:255], v[24:27], v[184:187]\n"
"buffer_load_dwordx4 v242, s[48:51], s64 offen offset:1024  lds\n"
"ds_read_b64_tr_b16 a[246:247], v251 offset:6144\n"
"v_mfma_f32_16x16x32_bf16 v[188:191], a[248:251], v[12:15], 0\n"
"v_cvt_pk_bf16_f32 v131, v134, v135\n"
"v_mul_f32 v136, v136, v234\n"
"v_mfma_f32_16x16x32_bf16 v[188:191], a[252:255], v[28:31], v[188:191]\n"
"s_waitcnt lgkmcnt(4)\n"
"s_lshl_b32 s83, s37, 4\n"
"s_add_u32 s83, s83, s67\n"
"v_mfma_f32_16x16x32_bf16 v[176:179], a[232:235], v[32:35], v[176:179]\n"
"ds_read_b64_tr_b16 a[248:249], v251 offset:8192\n"
"buffer_load_dwordx2 v[238:239], off, s[52:55], s83 sc1\n"
"v_mfma_f32_16x16x32_bf16 v[176:179], a[236:239], v[48:51], v[176:179]\n"
"ds_read_b64_tr_b16 a[250:251], v251 offset:10240\n"
"v_mul_f32 v137, v137, v234\n"
"v_mfma_f32_16x16x32_bf16 v[180:183], a[232:235], v[36:39], v[180:183]\n"
"buffer_store_dwordx4 v[128:131], v255, s[52:55], s79 offen offset:0 sc1\n"
"v_mfma_f32_16x16x32_bf16 v[180:183], a[236:239], v[52:55], v[180:183]\n"
"v_mul_f32 v138, v138, v234\n"
"v_mul_f32 v139, v139, v234\n"
"v_mfma_f32_16x16x32_bf16 v[184:187], a[232:235], v[40:43], v[184:187]\n"
"buffer_load_dwordx4 v242, s[48:51], s65 offen offset:2048  lds\n"
"ds_read_b64_tr_b16 a[252:253], v251 offset:12288\n"
"v_mfma_f32_16x16x32_bf16 v[184:187], a[236:239], v[56:59], v[184:187]\n"
"ds_read_b64_tr_b16 a[254:255], v251 offset:14336\n"
"v_mul_f32 v140, v140, v235\n"
"v_mfma_f32_16x16x32_bf16 v[188:191], a[232:235], v[44:47], v[188:191]\n"
"v_mul_f32 v141, v141, v235\n"
"v_mul_f32 v142, v142, v235\n"
"v_mfma_f32_16x16x32_bf16 v[188:191], a[236:239], v[60:63], v[188:191]\n"
"s_waitcnt lgkmcnt(4)\n"
"v_mul_f32 v143, v143, v235\n"
"v_cvt_pk_bf16_f32 v136, v136, v137\n"
"v_mfma_f32_16x16x32_bf16 v[160:163], a[240:243], v[0:3], 0\n"
"ds_read_b64_tr_b16 a[232:233], v250 offset:33280\n"
"v_cvt_pk_bf16_f32 v137, v138, v139\n"
"v_mfma_f32_16x16x32_bf16 v[160:163], a[244:247], v[16:19], v[160:163]\n"
"ds_read_b64_tr_b16 a[234:235], v250 offset:35328\n"
"v_cvt_pk_bf16_f32 v138, v140, v141\n"
"v_mfma_f32_16x16x32_bf16 v[164:167], a[240:243], v[4:7], 0\n"
"v_cvt_pk_bf16_f32 v139, v142, v143\n"
"v_mul_f32 v144, v144, v232\n"
"v_mfma_f32_16x16x32_bf16 v[164:167], a[244:247], v[20:23], v[164:167]\n"
"buffer_load_dwordx4 v242, s[48:51], s66 offen offset:3072  lds\n"
"v_mfma_f32_16x16x32_bf16 v[168:171], a[240:243], v[8:11], 0\n"
"ds_read_b64_tr_b16 a[236:237], v250 offset:37376\n"
"v_mul_f32 v145, v145, v232\n"
"v_mfma_f32_16x16x32_bf16 v[168:171], a[244:247], v[24:27], v[168:171]\n"
"ds_read_b64_tr_b16 a[238:239], v250 offset:39424\n"
"v_mul_f32 v146, v146, v232\n"
"v_mfma_f32_16x16x32_bf16 v[172:175], a[240:243], v[12:15], 0\n"
"buffer_store_dwordx4 v[136:139], v255, s[52:55], s79 offen offset:1024 sc1\n"
"v_mfma_f32_16x16x32_bf16 v[172:175], a[244:247], v[28:31], v[172:175]\n"
"s_waitcnt lgkmcnt(4)\n"
"s_lshl_b32 s80, s33, 11\n"
"s_add_u32 s80, s80, 4096\n"
"v_mfma_f32_16x16x32_bf16 v[160:163], a[248:251], v[32:35], v[160:163]\n"
"ds_read_b64_tr_b16 a[240:241], v250 offset:41472\n"
"v_mul_f32 v147, v147, v232\n"
"v_mfma_f32_16x16x32_bf16 v[160:163], a[252:255], v[48:51], v[160:163]\n"
"ds_read_b64_tr_b16 a[242:243], v250 offset:43520\n"
"v_mul_f32 v148, v148, v233\n"
"v_mfma_f32_16x16x32_bf16 v[164:167], a[248:251], v[36:39], v[164:167]\n"
"v_mul_f32 v149, v149, v233\n"
"v_mul_f32 v150, v150, v233\n"
"v_mfma_f32_16x16x32_bf16 v[164:167], a[252:255], v[52:55], v[164:167]\n"
"v_mul_f32 v151, v151, v233\n"
"v_cvt_pk_bf16_f32 v144, v144, v145\n"
"v_mfma_f32_16x16x32_bf16 v[168:171], a[248:251], v[40:43], v[168:171]\n"
"s_waitcnt vmcnt(12)\n"
"s_barrier\n"
"ds_read_b64_tr_b16 a[244:245], v250 offset:45568\n"
"v_mfma_f32_16x16x32_bf16 v[168:171], a[252:255], v[56:59], v[168:171]\n"
"ds_read_b64_tr_b16 a[246:247], v250 offset:47616\n"
"s_add_u32 s82, s36, 0\n"
"v_add_u32 v242, s82, v243\n"
"s_add_u32 m0, s71, 0\n"
"v_mfma_f32_16x16x32_bf16 v[172:175], a[248:251], v[44:47], v[172:175]\n"
"buffer_load_dwordx4 v242, s[48:51], 0 offen offset:0  lds\n"
"v_mfma_f32_16x16x32_bf16 v[172:175], a[252:255], v[60:63], v[172:175]\n"
"s_waitcnt lgkmcnt(4)\n"
"v_cvt_pk_bf16_f32 v145, v146, v147\n"
"v_cvt_pk_bf16_f32 v146, v148, v149\n"
"v_mfma_f32_16x16x32_bf16 a[0:3], a[232:235], v[64:67], a[0:3]\n"
"ds_read_b64_tr_b16 a[248:249], v251 offset:33280\n"
"v_cvt_pk_bf16_f32 v147, v150, v151\n"
"v_mfma_f32_16x16x32_bf16 a[0:3], a[236:239], v[80:83], a[0:3]\n"
"ds_read_b64_tr_b16 a[250:251], v251 offset:35328\n"
"v_mul_f32 v152, v152, v234\n"
"v_mfma_f32_16x16x32_bf16 a[4:7], a[232:235], v[68:71], a[4:7]\n"
"buffer_store_dwordx4 v[144:147], v255, s[52:55], s80 offen offset:0 sc1\n"
"v_mfma_f32_16x16x32_bf16 a[4:7], a[236:239], v[84:87], a[4:7]\n"
"v_mul_f32 v153, v153, v234\n"
"v_mul_f32 v154, v154, v234\n"
"v_mfma_f32_16x16x32_bf16 a[8:11], a[232:235], v[72:75], a[8:11]\n"
"ds_read_b64_tr_b16 a[252:253], v251 offset:37376\n"
"v_mul_f32 v155, v155, v234\n"
"v_mfma_f32_16x16x32_bf16 a[8:11], a[236:239], v[88:91], a[8:11]\n"
"buffer_load_dwordx4 v242, s[48:51], s64 offen offset:1024  lds\n"
"ds_read_b64_tr_b16 a[254:255], v251 offset:39424\n"
"v_mfma_f32_16x16x32_bf16 a[12:15], a[232:235], v[76:79], a[12:15]\n"
"v_mul_f32 v156, v156, v235\n"
"v_mul_f32 v157, v157, v235\n"
"v_mfma_f32_16x16x32_bf16 a[12:15], a[236:239], v[92:95], a[12:15]\n"
"s_waitcnt lgkmcnt(4)\n"
"v_mul_f32 v158, v158, v235\n"
"v_mul_f32 v159, v159, v235\n"
"v_mfma_f32_16x16x32_bf16 a[0:3], a[240:243], v[96:99], a[0:3]\n"
"ds_read_b64_tr_b16 a[232:233], v251 offset:41472\n"
"v_cvt_pk_bf16_f32 v152, v152, v153\n"
"v_mfma_f32_16x16x32_bf16 a[0:3], a[244:247], v[112:115], a[0:3]\n"
"ds_read_b64_tr_b16 a[234:235], v251 offset:43520\n"
"v_cvt_pk_bf16_f32 v153, v154, v155\n"
"v_mfma_f32_16x16x32_bf16 a[4:7], a[240:243], v[100:103], a[4:7]\n"
"v_cvt_pk_bf16_f32 v154, v156, v157\n"
"v_cvt_pk_bf16_f32 v155, v158, v159\n"
"v_mfma_f32_16x16x32_bf16 a[4:7], a[244:247], v[116:119], a[4:7]\n"
"buffer_store_dwordx4 v[152:155], v255, s[52:55], s80 offen offset:1024 sc1\n"
"v_mfma_f32_16x16x32_bf16 a[8:11], a[240:243], v[104:107], a[8:11]\n"
"buffer_load_dwordx4 v242, s[48:51], s65 offen offset:2048  lds\n"
"ds_read_b64_tr_b16 a[236:237], v251 offset:45568\n"
"v_mfma_f32_16x16x32_bf16 a[8:11], a[244:247], v[120:123], a[8:11]\n"
"ds_read_b64_tr_b16 a[238:239], v251 offset:47616\n"
"v_mfma_f32_16x16x32_bf16 a[12:15], a[240:243], v[108:111], a[12:15]\n"
"v_mfma_f32_16x16x32_bf16 a[12:15], a[244:247], v[124:127], a[12:15]\n"
"s_waitcnt lgkmcnt(4)\n"
"v_mfma_f32_16x16x32_bf16 a[16:19], a[248:251], v[64:67], a[16:19]\n"
"ds_read_b64_tr_b16 a[240:241], v250 offset:32768\n"
"v_mfma_f32_16x16x32_bf16 a[16:19], a[252:255], v[80:83], a[16:19]\n"
"ds_read_b64_tr_b16 a[242:243], v250 offset:34816\n"
"v_mfma_f32_16x16x32_bf16 a[20:23], a[248:251], v[68:71], a[20:23]\n"
"ds_write_b128 v237, a[0:3] offset:0\n"
"v_mfma_f32_16x16x32_bf16 a[20:23], a[252:255], v[84:87], a[20:23]\n"
"buffer_load_dwordx4 v242, s[48:51], s66 offen offset:3072  lds\n"
"v_mfma_f32_16x16x32_bf16 a[24:27], a[248:251], v[72:75], a[24:27]\n"
"ds_read_b64_tr_b16 a[244:245], v250 offset:36864\n"
"v_mfma_f32_16x16x32_bf16 a[24:27], a[252:255], v[88:91], a[24:27]\n"
"ds_read_b64_tr_b16 a[246:247], v250 offset:38912\n"
"v_mfma_f32_16x16x32_bf16 a[28:31], a[248:251], v[76:79], a[28:31]\n"
"ds_write_b128 v237, a[4:7] offset:1024\n"
"v_mfma_f32_16x16x32_bf16 a[28:31], a[252:255], v[92:95], a[28:31]\n"
"s_waitcnt lgkmcnt(6)\n"
"v_mfma_f32_16x16x32_bf16 a[16:19], a[232:235], v[96:99], a[16:19]\n"
"ds_read_b64_tr_b16 a[248:249], v250 offset:40960\n"
"v_mfma_f32_16x16x32_bf16 a[16:19], a[236:239], v[112:115], a[16:19]\n"
"ds_read_b64_tr_b16 a[250:251], v250 offset:43008\n"
"s_add_u32 s82, s36, 229376\n"
"v_add_u32 v242, s82, v243\n"
"v_xor_b32 v242, 64, v242\n"
"s_add_u32 m0, s71, 16384\n"
"v_mfma_f32_16x16x32_bf16 a[20:23], a[232:235], v[100:103], a[20:23]\n"
"buffer_load_dwordx4 v242, s[48:51], 0 offen offset:0  lds\n"
"s_waitcnt vmcnt(11)\n"
"v_cmp_ne_u64 vcc, v[230:231], v[238:239]\n"
"s_cbranch_vccnz L_FS0\n"
"L_FOK0:\n"
"v_mfma_f32_16x16x32_bf16 a[20:23], a[236:239], v[116:119], a[20:23]\n"
"ds_write_b128 v237, a[8:11] offset:2048\n"
"v_mfma_f32_16x16x32_bf16 a[24:27], a[232:235], v[104:107], a[24:27]\n"
"s_lshl_b32 s84, s37, 8\n"
"s_add_u32 s84, s84, s68\n"
"buffer_load_dwordx4 a[32:35], v240, s[52:55], s84 offen offset:0 sc1\n"
"ds_read_b64_tr_b16 a[252:253], v250 offset:45056\n"
"v_mfma_f32_16x16x32_bf16 a[24:27], a[236:239], v[120:123], a[24:27]\n"
"ds_read_b64_tr_b16 a[254:255], v250 offset:47104\n"
"v_mfma_f32_16x16x32_bf16 a[28:31], a[232:235], v[108:111], a[28:31]\n"
"ds_write_b128 v237, a[12:15] offset:3072\n"
"v_mfma_f32_16x16x32_bf16 a[28:31], a[236:239], v[124:127], a[28:31]\n"
"s_waitcnt lgkmcnt(7)\n"
"v_mfma_f32_16x16x32_bf16 v[176:179], a[240:243], v[64:67], v[176:179]\n"
"ds_read_b64_tr_b16 a[232:233], v251 offset:32768\n"
"v_mfma_f32_16x16x32_bf16 v[176:179], a[244:247], v[80:83], v[176:179]\n"
"buffer_load_dwordx4 v242, s[48:51], s64 offen offset:1024  lds\n"
"ds_read_b64_tr_b16 a[234:235], v251 offset:34816\n"
"v_mfma_f32_16x16x32_bf16 v[180:183], a[240:243], v[68:71], v[180:183]\n"
"ds_write_b128 v237, a[16:19] offset:4096\n"
"v_mfma_f32_16x16x32_bf16 v[180:183], a[244:247], v[84:87], v[180:183]\n"
"v_mfma_f32_16x16x32_bf16 v[184:187], a[240:243], v[72:75], v[184:187]\n"
"s_lshl_b32 s84, s37, 8\n"
"s_add_u32 s84, s84, s68\n"
"buffer_load_dwordx4 a[36:39], v240, s[52:55], s84 offen offset:1024 sc1\n"
"ds_read_b64_tr_b16 a[236:237], v251 offset:36864\n"
"v_mfma_f32_16x16x32_bf16 v[184:187], a[244:247], v[88:91], v[184:187]\n"
"ds_read_b64_tr_b16 a[238:239], v251 offset:38912\n"
"v_mfma_f32_16x16x32_bf16 v[188:191], a[240:243], v[76:79], v[188:191]\n"
"ds_write_b128 v237, a[20:23] offset:5120\n"
"v_mfma_f32_16x16x32_bf16 v[188:191], a[244:247], v[92:95], v[188:191]\n"
"s_waitcnt lgkmcnt(7)\n"
"v_mfma_f32_16x16x32_bf16 v[176:179], a[248:251], v[96:99], v[176:179]\n"
"buffer_load_dwordx4 v242, s[48:51], s65 offen offset:2048  lds\n"
"ds_read_b64_tr_b16 a[240:241], v251 offset:40960\n"
"v_mfma_f32_16x16x32_bf16 v[176:179], a[252:255], v[112:115], v[176:179]\n"
"ds_read_b64_tr_b16 a[242:243], v251 offset:43008\n"
"v_mfma_f32_16x16x32_bf16 v[180:183], a[248:251], v[100:103], v[180:183]\n"
"ds_write_b128 v237, a[24:27] offset:6144\n"
"v_mfma_f32_16x16x32_bf16 v[180:183], a[252:255], v[116:119], v[180:183]\n"
"v_mfma_f32_16x16x32_bf16 v[184:187], a[248:251], v[104:107], v[184:187]\n"
"s_lshl_b32 s84, s37, 8\n"
"s_add_u32 s84, s84, s69\n"
"buffer_load_dwordx4 a[40:43], v240, s[52:55], s84 offen offset:0 sc1\n"
"ds_read_b64_tr_b16 a[244:245], v251 offset:45056\n"
"v_mfma_f32_16x16x32_bf16 v[184:187], a[252:255], v[120:123], v[184:187]\n"
"ds_read_b64_tr_b16 a[246:247], v251 offset:47104\n"
"ds_write_b128 v237, a[28:31] offset:7168\n"
"v_mfma_f32_16x16x32_bf16 v[188:191], a[248:251], v[108:111], v[188:191]\n"
"s_waitcnt vmcnt(8)\n"
"s_lshl_b32 s83, s33, 1\n"
"s_add_u32 s83, s83, s39\n"
"buffer_store_dwordx2 v[230:231], off, s[52:55], s83 sc1\n"
"v_mfma_f32_16x16x32_bf16 v[188:191], a[252:255], v[124:127], v[188:191]\n"
"s_waitcnt lgkmcnt(7)\n"
"buffer_load_dwordx4 v242, s[48:51], s66 offen offset:3072  lds\n"
"v_mfma_f32_16x16x32_bf16 v[160:163], a[232:235], v[64:67], v[160:163]\n"
"ds_read_b64_tr_b16 a[248:249], v252 offset:512\n"
"v_mfma_f32_16x16x32_bf16 v[160:163], a[236:239], v[80:83], v[160:163]\n"
"ds_read_b64_tr_b16 a[250:251], v252 offset:2560\n"
"v_mfma_f32_16x16x32_bf16 v[164:167], a[232:235], v[68:71], v[164:167]\n"
"s_waitcnt lgkmcnt(2)\n"
"s_barrier\n"
"ds_read_b128 v[128:131], v241 offset:0\n"
"v_mfma_f32_16x16x32_bf16 v[164:167], a[236:239], v[84:87], v[164:167]\n"
"ds_read_b128 v[132:135], v241 offset:1024\n"
"v_mfma_f32_16x16x32_bf16 v[168:171], a[232:235], v[72:75], v[168:171]\n"
"s_lshl_b32 s84, s37, 8\n"
"s_add_u32 s84, s84, s69\n"
"buffer_load_dwordx4 a[44:47], v240, s[52:55], s84 offen offset:1024 sc1\n"
"ds_read_b64_tr_b16 a[252:253], v252 offset:4608\n"
"v_mfma_f32_16x16x32_bf16 v[168:171], a[236:239], v[88:91], v[168:171]\n"
"ds_read_b64_tr_b16 a[254:255], v252 offset:6656\n"
"v_mfma_f32_16x16x32_bf16 v[172:175], a[232:235], v[76:79], v[172:175]\n"
"ds_read_b128 v[136:139], v241 offset:2048\n"
"v_mfma_f32_16x16x32_bf16 v[172:175], a[236:239], v[92:95], v[172:175]\n"
"ds_read_b128 v[140:143], v241 offset:3072\n"
"v_mfma_f32_16x16x32_bf16 v[160:163], a[240:243], v[96:99], v[160:163]\n"
"ds_read_b64_tr_b16 a[232:233], v252 offset:8704\n"
"v_mfma_f32_16x16x32_bf16 v[160:163], a[244:247], v[112:115], v[160:163]\n"
"ds_read_b64_tr_b16 a[234:235], v252 offset:10752\n"
"v_mfma_f32_16x16x32_bf16 v[164:167], a[240:243], v[100:103], v[164:167]\n"
"ds_read_b128 v[144:147], v241 offset:4096\n"
"v_mfma_f32_16x16x32_bf16 v[164:167], a[244:247], v[116:119], v[164:167]\n"
"ds_read_b128 v[148:151], v241 offset:5120\n"
"v_mfma_f32_16x16x32_bf16 v[168:171], a[240:243], v[104:107], v[168:171]\n"
"ds_read_b64_tr_b16 a[236:237], v252 offset:12800\n"
"s_waitcnt lgkmcnt(5)\n"
"v_mfma_f32_16x16x32_bf16 v[168:171], a[244:247], v[120:123], v[168:171]\n"
"ds_read_b64_tr_b16 a[238:239], v252 offset:14848\n"
"v_fma_f32 v128, v176, v244, v128\n"
"v_mfma_f32_16x16x32_bf16 v[172:175], a[240:243], v[108:111], v[172:175]\n"
"ds_read_b128 v[152:155], v241 offset:6144\n"
"v_fma_f32 v129, v177, v244, v129\n"
"v_mfma_f32_16x16x32_bf16 v[172:175], a[244:247], v[124:127], v[172:175]\n"
"ds_read_b128 v[156:159], v241 offset:7168\n"
"v_fma_f32 v130, v178, v244, v130\n"
"v_mfma_f32_16x16x32_bf16 a[0:3], a[248:251], v[0:3], 0\n"
"s_barrier\n"
"ds_read_b64_tr_b16 a[240:241], v253 offset:512\n"
"v_mfma_f32_16x16x32_bf16 a[0:3], a[252:255], v[16:19], a[0:3]\n"
"ds_read_b64_tr_b16 a[242:243], v253 offset:2560\n"
"s_add_u32 s82, s36, 114688\n"
"v_add_u32 v242, s82, v243\n"
"s_add_u32 m0, s71, 32768\n"
"v_mfma_f32_16x16x32_bf16 a[4:7], a[248:251], v[4:7], 0\n"
"buffer_load_dwordx4 v242, s[48:51], 0 offen offset:0  lds\n"
"v_mfma_f32_16x16x32_bf16 a[4:7], a[252:255], v[20:23], a[4:7]\n"
"v_fma_f32 v131, v179, v244, v131\n"
"v_fma_f32 v132, v180, v245, v132\n"
"v_mfma_f32_16x16x32_bf16 a[8:11], a[248:251], v[8:11], 0\n"
"ds_read_b64_tr_b16 a[244:245], v253 offset:4608\n"
"v_fma_f32 v133, v181, v245, v133\n"
"v_mfma_f32_16x16x32_bf16 a[8:11], a[252:255], v[24:27], a[8:11]\n"
"ds_read_b64_tr_b16 a[246:247], v253 offset:6656\n"
"v_fma_f32 v134, v182, v245, v134\n"
"v_mfma_f32_16x16x32_bf16 a[12:15], a[248:251], v[12:15], 0\n"
"v_fma_f32 v135, v183, v245, v135\n"
"v_fma_f32 v136, v184, v246, v136\n"
"v_mfma_f32_16x16x32_bf16 a[12:15], a[252:255], v[28:31], a[12:15]\n"
"s_waitcnt lgkmcnt(6)\n"
"v_fma_f32 v137, v185, v246, v137\n"
"v_fma_f32 v138, v186, v246, v138\n"
"v_mfma_f32_16x16x32_bf16 a[0:3], a[232:235], v[32:35], a[0:3]\n"
"ds_read_b64_tr_b16 a[248:249], v253 offset:8704\n"
"v_fma_f32 v139, v187, v246, v139\n"
"v_mfma_f32_16x16x32_bf16 a[0:3], a[236:239], v[48:51], a[0:3]\n"
"buffer_load_dwordx4 v242, s[48:51], s64 offen offset:1024  lds\n"
"ds_read_b64_tr_b16 a[250:251], v253 offset:10752\n"
"v_mfma_f32_16x16x32_bf16 a[4:7], a[232:235], v[36:39], a[4:7]\n"
"v_fma_f32 v140, v188, v247, v140\n"
"v_fma_f32 v141, v189, v247, v141\n"
"v_mfma_f32_16x16x32_bf16 a[4:7], a[236:239], v[52:55], a[4:7]\n"
"v_fma_f32 v142, v190, v247, v142\n"
"v_fma_f32 v143, v191, v247, v143\n"
"v_mfma_f32_16x16x32_bf16 a[8:11], a[232:235], v[40:43], a[8:11]\n"
"ds_read_b64_tr_b16 a[252:253], v253 offset:12800\n"
"s_waitcnt lgkmcnt(7)\n"
"v_mfma_f32_16x16x32_bf16 a[8:11], a[236:239], v[56:59], a[8:11]\n"
"ds_read_b64_tr_b16 a[254:255], v253 offset:14848\n"
"v_fma_f32 v144, v160, v244, v144\n"
"v_mfma_f32_16x16x32_bf16 a[12:15], a[232:235], v[44:47], a[12:15]\n"
"v_fma_f32 v145, v161, v244, v145\n"
"v_fma_f32 v146, v162, v244, v146\n"
"v_mfma_f32_16x16x32_bf16 a[12:15], a[236:239], v[60:63], a[12:15]\n"
"s_waitcnt lgkmcnt(4)\n"
"v_fma_f32 v147, v163, v244, v147\n"
"v_fma_f32 v148, v164, v245, v148\n"
"v_mfma_f32_16x16x32_bf16 a[16:19], a[240:243], v[0:3], 0\n"
"buffer_load_dwordx4 v242, s[48:51], s65 offen offset:2048  lds\n"
"ds_read_b64_tr_b16 a[232:233], v252 offset:0\n"
"v_mfma_f32_16x16x32_bf16 a[16:19], a[244:247], v[16:19], a[16:19]\n"
"ds_read_b64_tr_b16 a[234:235], v252 offset:2048\n"
"v_fma_f32 v149, v165, v245, v149\n"
"v_mfma_f32_16x16x32_bf16 a[20:23], a[240:243], v[4:7], 0\n"
"v_fma_f32 v150, v166, v245, v150\n"
"v_fma_f32 v151, v167, v245, v151\n"
"v_mfma_f32_16x16x32_bf16 a[20:23], a[244:247], v[20:23], a[20:23]\n"
"v_fma_f32 v152, v168, v246, v152\n"
"v_fma_f32 v153, v169, v246, v153\n"
"s_lshl_b32 s79, s34, 11\n"
"v_mfma_f32_16x16x32_bf16 a[24:27], a[240:243], v[8:11], 0\n"
"ds_read_b64_tr_b16 a[236:237], v252 offset:4096\n"
"v_fma_f32 v154, v170, v246, v154\n"
"v_mfma_f32_16x16x32_bf16 a[24:27], a[244:247], v[24:27], a[24:27]\n"
"ds_read_b64_tr_b16 a[238:239], v252 offset:6144\n"
"v_fma_f32 v155, v171, v246, v155\n"
"v_mfma_f32_16x16x32_bf16 a[28:31], a[240:243], v[12:15], 0\n"
"L_CW8:\n"
"s_mov_b32 s75, L_CW8-L_PC+3072\n"
"buffer_load_dword a127, v254, s[56:59], s75 offen\n"
"v_mfma_f32_16x16x32_bf16 a[28:31], a[244:247], v[28:31], a[28:31]\n"
"s_waitcnt lgkmcnt(4)\n"
"buffer_load_dwordx4 v242, s[48:51], s66 offen offset:3072  lds\n"
"v_mfma_f32_16x16x32_bf16 a[16:19], a[248:251], v[32:35], a[16:19]\n"
"ds_read_b64_tr_b16 a[240:241], v252 offset:8192\n"
"v_fma_f32 v156, v172, v247, v156\n"
"v_mfma_f32_16x16x32_bf16 a[16:19], a[252:255], v[48:51], a[16:19]\n"
"ds_read_b64_tr_b16 a[242:243], v252 offset:10240\n"
"v_fma_f32 v157, v173, v247, v157\n"
"v_mfma_f32_16x16x32_bf16 a[20:23], a[248:251], v[36:39], a[20:23]\n"
"v_fma_f32 v158, v174, v247, v158\n"
"v_fma_f32 v159, v175, v247, v159\n"
"v_mfma_f32_16x16x32_bf16 a[20:23], a[252:255], v[52:55], a[20:23]\n"
"v_mul_f32 v128, v128, v232\n"
"v_mul_f32 v129, v129, v232\n"
"v_mfma_f32_16x16x32_bf16 a[24:27], a[248:251], v[40:43], a[24:27]\n"
"ds_read_b64_tr_b16 a[244:245], v252 offset:12288\n"
"v_mul_f32 v130, v130, v232\n"
"v_mfma_f32_16x16x32_bf16 a[24:27], a[252:255], v[56:59], a[24:27]\n"
"ds_read_b64_tr_b16 a[246:247], v252 offset:14336\n"
"s_add_u32 s82, s36, 344064\n"
"v_add_u32 v242, s82, v243\n"
"v_xor_b32 v242, 64, v242\n"
"s_add_u32 m0, s71, 49152\n"
"v_mfma_f32_16x16x32_bf16 a[28:31], a[248:251], v[44:47], a[28:31]\n"
"buffer_load_dwordx4 v242, s[48:51], 0 offen offset:0  lds\n"
"v_mfma_f32_16x16x32_bf16 a[28:31], a[252:255], v[60:63], a[28:31]\n"
"s_waitcnt lgkmcnt(4)\n"
"v_mul_f32 v131, v131, v232\n"
"v_mul_f32 v132, v132, v233\n"
"v_mfma_f32_16x16x32_bf16 v[176:179], a[232:235], v[0:3], 0\n"
"ds_read_b64_tr_b16 a[248:249], v253 offset:0\n"
"v_mul_f32 v133, v133, v233\n"
"v_mfma_f32_16x16x32_bf16 v[176:179], a[236:239], v[16:19], v[176:179]\n"
"ds_read_b64_tr_b16 a[250:251], v253 offset:2048\n"
"v_mul_f32 v134, v134, v233\n"
"v_mfma_f32_16x16x32_bf16 v[180:183], a[232:235], v[4:7], 0\n"
"v_mul_f32 v135, v135, v233\n"
"v_cvt_pk_bf16_f32 v128, v128, v129\n"
"v_mfma_f32_16x16x32_bf16 v[180:183], a[236:239], v[20:23], v[180:183]\n"
"v_cvt_pk_bf16_f32 v129, v130, v131\n"
"v_cvt_pk_bf16_f32 v130, v132, v133\n"
"v_mfma_f32_16x16x32_bf16 v[184:187], a[232:235], v[8:11], 0\n"
"ds_read_b64_tr_b16 a[252:253], v253 offset:4096\n"
"v_cvt_pk_bf16_f32 v131, v134, v135\n"
"v_mfma_f32_16x16x32_bf16 v[184:187], a[236:239], v[24:27], v[184:187]\n"
"buffer_load_dwordx4 v242, s[48:51], s64 offen offset:1024  lds\n"
"ds_read_b64_tr_b16 a[254:255], v253 offset:6144\n"
"v_mfma_f32_16x16x32_bf16 v[188:191], a[232:235], v[12:15], 0\n"
"buffer_store_dwordx4 v[128:131], v255, s[52:55], s79 offen offset:0 sc1\n"
"v_mfma_f32_16x16x32_bf16 v[188:191], a[236:239], v[28:31], v[188:191]\n"
"s_waitcnt lgkmcnt(4)\n"
"s_lshl_b32 s83, s36, 4\n"
"s_add_u32 s83, s83, s67\n"
"v_mul_f32 v136, v136, v234\n"
"v_mfma_f32_16x16x32_bf16 v[176:179], a[240:243], v[32:35], v[176:179]\n"
"ds_read_b64_tr_b16 a[232:233], v253 offset:8192\n"
"buffer_load_dwordx2 v[238:239], off, s[52:55], s83 sc1\n"
"v_mfma_f32_16x16x32_bf16 v[176:179], a[244:247], v[48:51], v[176:179]\n"
"ds_read_b64_tr_b16 a[234:235], v253 offset:10240\n"
"v_mul_f32 v137, v137, v234\n"
"v_mfma_f32_16x16x32_bf16 v[180:183], a[240:243], v[36:39], v[180:183]\n"
"v_mul_f32 v138, v138, v234\n"
"v_mul_f32 v139, v139, v234\n"
"v_mfma_f32_16x16x32_bf16 v[180:183], a[244:247], v[52:55], v[180:183]\n"
"v_mul_f32 v140, v140, v235\n"
"v_mul_f32 v141, v141, v235\n"
"v_mfma_f32_16x16x32_bf16 v[184:187], a[240:243], v[40:43], v[184:187]\n"
"buffer_load_dwordx4 v242, s[48:51], s65 offen offset:2048  lds\n"
"ds_read_b64_tr_b16 a[236:237], v253 offset:12288\n"
"v_mfma_f32_16x16x32_bf16 v[184:187], a[244:247], v[56:59], v[184:187]\n"
"ds_read_b64_tr_b16 a[238:239], v253 offset:14336\n"
"v_mul_f32 v142, v142, v235\n"
"v_mfma_f32_16x16x32_bf16 v[188:191], a[240:243], v[44:47], v[188:191]\n"
"v_mul_f32 v143, v143, v235\n"
"v_cvt_pk_bf16_f32 v136, v136, v137\n"
"v_mfma_f32_16x16x32_bf16 v[188:191], a[244:247], v[60:63], v[188:191]\n"
"s_waitcnt lgkmcnt(4)\n"
"v_cvt_pk_bf16_f32 v137, v138, v139\n"
"v_cvt_pk_bf16_f32 v138, v140, v141\n"
"v_mfma_f32_16x16x32_bf16 v[160:163], a[248:251], v[0:3], 0\n"
"ds_read_b64_tr_b16 a[240:241], v252 offset:33280\n"
"v_cvt_pk_bf16_f32 v139, v142, v143\n"
"v_mfma_f32_16x16x32_bf16 v[160:163], a[252:255], v[16:19], v[160:163]\n"
"ds_read_b64_tr_b16 a[242:243], v252 offset:35328\n"
"v_mul_f32 v144, v144, v232\n"
"v_mfma_f32_16x16x32_bf16 v[164:167], a[248:251], v[4:7], 0\n"
"s_mov_b32 s75, L_TAILX-L_PC+0\n"
"buffer_load_dword a127, v254, s[56:59], s75 offen\n"
"v_mfma_f32_16x16x32_bf16 v[164:167], a[252:255], v[20:23], v[164:167]\n"
"buffer_load_dwordx4 v242, s[48:51], s66 offen offset:3072  lds\n"
"v_mfma_f32_16x16x32_bf16 v[168:171], a[248:251], v[8:11], 0\n"
"ds_read_b64_tr_b16 a[244:245], v252 offset:37376\n"
"v_mul_f32 v145, v145, v232\n"
"v_mfma_f32_16x16x32_bf16 v[168:171], a[252:255], v[24:27], v[168:171]\n"
"ds_read_b64_tr_b16 a[246:247], v252 offset:39424\n"
"v_mul_f32 v146, v146, v232\n"
"v_mfma_f32_16x16x32_bf16 v[172:175], a[248:251], v[12:15], 0\n"
"buffer_store_dwordx4 v[136:139], v255, s[52:55], s79 offen offset:1024 sc1\n"
"v_mfma_f32_16x16x32_bf16 v[172:175], a[252:255], v[28:31], v[172:175]\n"
"s_waitcnt lgkmcnt(4)\n"
"s_lshl_b32 s80, s34, 11\n"
"s_add_u32 s80, s80, 4096\n"
"v_mfma_f32_16x16x32_bf16 v[160:163], a[232:235], v[32:35], v[160:163]\n"
"ds_read_b64_tr_b16 a[248:249], v252 offset:41472\n"
"v_mul_f32 v147, v147, v232\n"
"v_mfma_f32_16x16x32_bf16 v[160:163], a[236:239], v[48:51], v[160:163]\n"
"ds_read_b64_tr_b16 a[250:251], v252 offset:43520\n"
"v_mul_f32 v148, v148, v233\n"
"v_mfma_f32_16x16x32_bf16 v[164:167], a[232:235], v[36:39], v[164:167]\n"
"v_mul_f32 v149, v149, v233\n"
"v_mul_f32 v150, v150, v233\n"
"v_mfma_f32_16x16x32_bf16 v[164:167], a[236:239], v[52:55], v[164:167]\n"
"v_mul_f32 v151, v151, v233\n"
"v_cvt_pk_bf16_f32 v144, v144, v145\n"
"v_cvt_pk_bf16_f32 v145, v146, v147\n"
"v_mfma_f32_16x16x32_bf16 v[168:171], a[232:235], v[40:43], v[168:171]\n"
"s_waitcnt vmcnt(14)\n"
"s_barrier\n"
"ds_read_b64_tr_b16 a[252:253], v252 offset:45568\n"
"v_mfma_f32_16x16x32_bf16 v[168:171], a[236:239], v[56:59], v[168:171]\n"
"ds_read_b64_tr_b16 a[254:255], v252 offset:47616\n"
"s_add_u32 s82, s37, 0\n"
"v_add_u32 v242, s82, v243\n"
"s_add_u32 m0, s71, 65536\n"
"v_mfma_f32_16x16x32_bf16 v[172:175], a[232:235], v[44:47], v[172:175]\n"
"buffer_load_dwordx4 v242, s[48:51], 0 offen offset:0  lds\n"
"v_mfma_f32_16x16x32_bf16 v[172:175], a[236:239], v[60:63], v[172:175]\n"
"s_waitcnt lgkmcnt(4)\n"
"v_cvt_pk_bf16_f32 v146, v148, v149\n"
"v_cvt_pk_bf16_f32 v147, v150, v151\n"
"v_mfma_f32_16x16x32_bf16 a[0:3], a[240:243], v[64:67], a[0:3]\n"
"ds_read_b64_tr_b16 a[232:233], v253 offset:33280\n"
"v_mul_f32 v152, v152, v234\n"
"v_mfma_f32_16x16x32_bf16 a[0:3], a[244:247], v[80:83], a[0:3]\n"
"ds_read_b64_tr_b16 a[234:235], v253 offset:35328\n"
"v_mul_f32 v153, v153, v234\n"
"v_mfma_f32_16x16x32_bf16 a[4:7], a[240:243], v[68:71], a[4:7]\n"
"buffer_store_dwordx4 v[144:147], v255, s[52:55], s80 offen offset:0 sc1\n"
"v_mfma_f32_16x16x32_bf16 a[4:7], a[244:247], v[84:87], a[4:7]\n"
"v_mul_f32 v154, v154, v234\n"
"v_mul_f32 v155, v155, v234\n"
"v_mfma_f32_16x16x32_bf16 a[8:11], a[240:243], v[72:75], a[8:11]\n"
"ds_read_b64_tr_b16 a[236:237], v253 offset:37376\n"
"v_mul_f32 v156, v156, v235\n"
"v_mfma_f32_16x16x32_bf16 a[8:11], a[244:247], v[88:91], a[8:11]\n"
"buffer_load_dwordx4 v242, s[48:51], s64 offen offset:1024  lds\n"
"ds_read_b64_tr_b16 a[238:239], v253 offset:39424\n"
"v_mfma_f32_16x16x32_bf16 a[12:15], a[240:243], v[76:79], a[12:15]\n"
"v_mul_f32 v157, v157, v235\n"
"v_mul_f32 v158, v158, v235\n"
"v_mfma_f32_16x16x32_bf16 a[12:15], a[244:247], v[92:95], a[12:15]\n"
"s_waitcnt lgkmcnt(4)\n"
"v_mul_f32 v159, v159, v235\n"
"v_cvt_pk_bf16_f32 v152, v152, v153\n"
"v_mfma_f32_16x16x32_bf16 a[0:3], a[248:251], v[96:99], a[0:3]\n"
"ds_read_b64_tr_b16 a[240:241], v253 offset:41472\n"
"v_cvt_pk_bf16_f32 v153, v154, v155\n"
"v_mfma_f32_16x16x32_bf16 a[0:3], a[252:255], v[112:115], a[0:3]\n"
"ds_read_b64_tr_b16 a[242:243], v253 offset:43520\n"
"v_cvt_pk_bf16_f32 v154, v156, v157\n"
"v_mfma_f32_16x16x32_bf16 a[4:7], a[248:251], v[100:103], a[4:7]\n"
"v_cvt_pk_bf16_f32 v155, v158, v159\n"
"v_mfma_f32_16x16x32_bf16 a[4:7], a[252:255], v[116:119], a[4:7]\n"
"buffer_store_dwordx4 v[152:155], v255, s[52:55], s80 offen offset:1024 sc1\n"
"v_mfma_f32_16x16x32_bf16 a[8:11], a[248:251], v[104:107], a[8:11]\n"
"buffer_load_dwordx4 v242, s[48:51], s65 offen offset:2048  lds\n"
"ds_read_b64_tr_b16 a[244:245], v253 offset:45568\n"
"v_mfma_f32_16x16x32_bf16 a[8:11], a[252:255], v[120:123], a[8:11]\n"
"ds_read_b64_tr_b16 a[246:247], v253 offset:47616\n"
"v_mfma_f32_16x16x32_bf16 a[12:15], a[248:251], v[108:111], a[12:15]\n"
"v_mfma_f32_16x16x32_bf16 a[12:15], a[252:255], v[124:127], a[12:15]\n"
"s_waitcnt vmcnt(18)\n"
"v_mfma_f32_16x16x32_bf16 v[192:195], v[226:229], a[32:35], 0\n"
"v_mfma_f32_16x16x32_bf16 v[196:199], v[224:227], a[32:35], 0\n"
"v_mfma_f32_16x16x32_bf16 v[200:203], v[226:229], a[36:39], 0\n"
"v_mfma_f32_16x16x32_bf16 v[204:207], v[224:227], a[36:39], 0\n"
"buffer_load_dwordx4 v242, s[48:51], s66 offen offset:3072  lds\n"
"v_mfma_f32_16x16x32_bf16 v[208:211], v[226:229], a[40:43], 0\n"
"v_mfma_f32_16x16x32_bf16 v[212:215], v[224:227], a[40:43], 0\n"
"v_mfma_f32_16x16x32_bf16 v[216:219], v[226:229], a[44:47], 0\n"
"v_mfma_f32_16x16x32_bf16 v[220:223], v[224:227], a[44:47], 0\n"
"s_waitcnt lgkmcnt(4)\n"
"v_mfma_f32_16x16x32_bf16 a[16:19], a[232:235], v[64:67], a[16:19]\n"
"ds_read_b64_tr_b16 a[248:249], v252 offset:32768\n"
"v_mfma_f32_16x16x32_bf16 a[16:19], a[236:239], v[80:83], a[16:19]\n"
"ds_read_b64_tr_b16 a[250:251], v252 offset:34816\n"
"s_add_u32 s82, s37, 229376\n"
"v_add_u32 v242, s82, v243\n"
"v_xor_b32 v242, 64, v242\n"
"s_add_u32 m0, s71, 81920\n"
"v_mfma_f32_16x16x32_bf16 a[20:23], a[232:235], v[68:71], a[20:23]\n"
"buffer_load_dwordx4 v242, s[48:51], 0 offen offset:0  lds\n"
"v_mfma_f32_16x16x32_bf16 a[20:23], a[236:239], v[84:87], a[20:23]\n"
"ds_write_b128 v237, a[0:3] offset:0\n"
"v_mfma_f32_16x16x32_bf16 a[24:27], a[232:235], v[72:75], a[24:27]\n"
"ds_read_b64_tr_b16 a[252:253], v252 offset:36864\n"
"v_mfma_f32_16x16x32_bf16 a[24:27], a[236:239], v[88:91], a[24:27]\n"
"ds_read_b64_tr_b16 a[254:255], v252 offset:38912\n"
"v_mfma_f32_16x16x32_bf16 a[28:31], a[232:235], v[76:79], a[28:31]\n"
"ds_write_b128 v237, a[4:7] offset:1024\n"
"v_mfma_f32_16x16x32_bf16 a[28:31], a[236:239], v[92:95], a[28:31]\n"
"s_waitcnt lgkmcnt(6)\n"
"v_mfma_f32_16x16x32_bf16 a[16:19], a[240:243], v[96:99], a[16:19]\n"
"ds_read_b64_tr_b16 a[232:233], v252 offset:40960\n"
"v_mfma_f32_16x16x32_bf16 a[16:19], a[244:247], v[112:115], a[16:19]\n"
"buffer_load_dwordx4 v242, s[48:51], s64 offen offset:1024  lds\n"
"ds_read_b64_tr_b16 a[234:235], v252 offset:43008\n"
"v_mfma_f32_16x16x32_bf16 a[20:23], a[240:243], v[100:103], a[20:23]\n"
"s_waitcnt vmcnt(12)\n"
"v_cmp_ne_u64 vcc, v[230:231], v[238:239]\n"
"s_cbranch_vccnz L_FS1\n"
"L_FOK1:\n"
"v_mfma_f32_16x16x32_bf16 a[20:23], a[244:247], v[116:119], a[20:23]\n"
"ds_write_b128 v237, a[8:11] offset:2048\n"
"v_mfma_f32_16x16x32_bf16 a[24:27], a[240:243], v[104:107], a[24:27]\n"
"s_lshl_b32 s84, s36, 8\n"
"s_add_u32 s84, s84, s68\n"
"buffer_load_dwordx4 a[48:51], v240, s[52:55], s84 offen offset:0 sc1\n"
"ds_read_b64_tr_b16 a[236:237], v252 offset:45056\n"
"v_mfma_f32_16x16x32_bf16 a[24:27], a[244:247], v[120:123], a[24:27]\n"
"ds_read_b64_tr_b16 a[238:239], v252 offset:47104\n"
"v_mfma_f32_16x16x32_bf16 a[28:31], a[240:243], v[108:111], a[28:31]\n"
"ds_write_b128 v237, a[12:15] offset:3072\n"
"v_mfma_f32_16x16x32_bf16 a[28:31], a[244:247], v[124:127], a[28:31]\n"
"s_waitcnt lgkmcnt(7)\n"
"v_mfma_f32_16x16x32_bf16 v[176:179], a[248:251], v[64:67], v[176:179]\n"
"buffer_load_dwordx4 v242, s[48:51], s65 offen offset:2048  lds\n"
"ds_read_b64_tr_b16 a[240:241], v253 offset:32768\n"
"v_mfma_f32_16x16x32_bf16 v[176:179], a[252:255], v[80:83], v[176:179]\n"
"ds_read_b64_tr_b16 a[242:243], v253 offset:34816\n"
"v_mfma_f32_16x16x32_bf16 v[180:183], a[248:251], v[68:71], v[180:183]\n"
"ds_write_b128 v237, a[16:19] offset:4096\n"
"v_mfma_f32_16x16x32_bf16 v[180:183], a[252:255], v[84:87], v[180:183]\n"
"v_mfma_f32_16x16x32_bf16 v[184:187], a[248:251], v[72:75], v[184:187]\n"
"s_lshl_b32 s84, s36, 8\n"
"s_add_u32 s84, s84, s68\n"
"buffer_load_dwordx4 a[52:55], v240, s[52:55], s84 offen offset:1024 sc1\n"
"ds_read_b64_tr_b16 a[244:245], v253 offset:36864\n"
"v_mfma_f32_16x16x32_bf16 v[184:187], a[252:255], v[88:91], v[184:187]\n"
"ds_read_b64_tr_b16 a[246:247], v253 offset:38912\n"
"v_mfma_f32_16x16x32_bf16 v[188:191], a[248:251], v[76:79], v[188:191]\n"
"ds_write_b128 v237, a[20:23] offset:5120\n"
"v_mfma_f32_16x16x32_bf16 v[188:191], a[252:255], v[92:95], v[188:191]\n"
"s_waitcnt lgkmcnt(7)\n"
"buffer_load_dwordx4 v242, s[48:51], s66 offen offset:3072  lds\n"
"v_mfma_f32_16x16x32_bf16 v[176:179], a[232:235], v[96:99], v[176:179]\n"
"ds_read_b64_tr_b16 a[248:249], v253 offset:40960\n"
"v_mfma_f32_16x16x32_bf16 v[176:179], a[236:239], v[112:115], v[176:179]\n"
"ds_read_b64_tr_b16 a[250:251], v253 offset:43008\n"
"v_mfma_f32_16x16x32_bf16 v[180:183], a[232:235], v[100:103], v[180:183]\n"
"ds_write_b128 v237, a[24:27] offset:6144\n"
"v_mfma_f32_16x16x32_bf16 v[180:183], a[236:239], v[116:119], v[180:183]\n"
"v_mfma_f32_16x16x32_bf16 v[184:187], a[232:235], v[104:107], v[184:187]\n"
"s_lshl_b32 s84, s36, 8\n"
"s_add_u32 s84, s84, s69\n"
"buffer_load_dwordx4 a[56:59], v240, s[52:55], s84 offen offset:0 sc1\n"
"ds_read_b64_tr_b16 a[252:253], v253 offset:45056\n"
"v_mfma_f32_16x16x32_bf16 v[184:187], a[236:239], v[120:123], v[184:187]\n"
"ds_read_b64_tr_b16 a[254:255], v253 offset:47104\n"
"v_mfma_f32_16x16x32_bf16 v[188:191], a[232:235], v[108:111], v[188:191]\n"
"s_waitcnt vmcnt(9)\n"
"s_lshl_b32 s83, s34, 1\n"
"s_add_u32 s83, s83, s39\n"
"buffer_store_dwordx2 v[230:231], off, s[52:55], s83 sc1\n"
"v_mfma_f32_16x16x32_bf16 v[188:191], a[236:239], v[124:127], v[188:191]\n"
"s_waitcnt lgkmcnt(6)\n"
"ds_write_b128 v237, a[28:31] offset:7168\n"
"v_mfma_f32_16x16x32_bf16 v[160:163], a[240:243], v[64:67], v[160:163]\n"
"ds_read_b64_tr_b16 a[232:233], v250 offset:512\n"
"v_mfma_f32_16x16x32_bf16 v[160:163], a[244:247], v[80:83], v[160:163]\n"
"ds_read_b64_tr_b16 a[234:235], v250 offset:2560\n"
"v_mfma_f32_16x16x32_bf16 v[164:167], a[240:243], v[68:71], v[164:167]\n"
"s_waitcnt lgkmcnt(2)\n"
"s_barrier\n"
"ds_read_b128 v[128:131], v241 offset:0\n"
"v_mfma_f32_16x16x32_bf16 v[164:167], a[244:247], v[84:87], v[164:167]\n"
"ds_read_b128 v[132:135], v241 offset:1024\n"
"v_mfma_f32_16x16x32_bf16 v[168:171], a[240:243], v[72:75], v[168:171]\n"
"s_lshl_b32 s84, s36, 8\n"
"s_add_u32 s84, s84, s69\n"
"buffer_load_dwordx4 a[60:63], v240, s[52:55], s84 offen offset:1024 sc1\n"
"ds_read_b64_tr_b16 a[236:237], v250 offset:4608\n"
"v_mfma_f32_16x16x32_bf16 v[168:171], a[244:247], v[88:91], v[168:171]\n"
"ds_read_b64_tr_b16 a[238:239], v250 offset:6656\n"
"v_mfma_f32_16x16x32_bf16 v[172:175], a[240:243], v[76:79], v[172:175]\n"
"ds_read_b128 v[136:139], v241 offset:2048\n"
"v_mfma_f32_16x16x32_bf16 v[172:175], a[244:247], v[92:95], v[172:175]\n"
"ds_read_b128 v[140:143], v241 offset:3072\n"
"v_mfma_f32_16x16x32_bf16 v[160:163], a[248:251], v[96:99], v[160:163]\n"
"ds_read_b64_tr_b16 a[240:241], v250 offset:8704\n"
"v_mfma_f32_16x16x32_bf16 v[160:163], a[252:255], v[112:115], v[160:163]\n"
"ds_read_b64_tr_b16 a[242:243], v250 offset:10752\n"
"v_mfma_f32_16x16x32_bf16 v[164:167], a[248:251], v[100:103], v[164:167]\n"
"ds_read_b128 v[144:147], v241 offset:4096\n"
"v_mfma_f32_16x16x32_bf16 v[164:167], a[252:255], v[116:119], v[164:167]\n"
"ds_read_b128 v[148:151], v241 offset:5120\n"
"v_mfma_f32_16x16x32_bf16 v[168:171], a[248:251], v[104:107], v[168:171]\n"
"ds_read_b64_tr_b16 a[244:245], v250 offset:12800\n"
"s_waitcnt lgkmcnt(5)\n"
"v_mfma_f32_16x16x32_bf16 v[168:171], a[252:255], v[120:123], v[168:171]\n"
"ds_read_b64_tr_b16 a[246:247], v250 offset:14848\n"
"v_fma_f32 v128, v176, v244, v128\n"
"v_mfma_f32_16x16x32_bf16 v[172:175], a[248:251], v[108:111], v[172:175]\n"
"ds_read_b128 v[152:155], v241 offset:6144\n"
"v_fma_f32 v129, v177, v244, v129\n"
"v_mfma_f32_16x16x32_bf16 v[172:175], a[252:255], v[124:127], v[172:175]\n"
"ds_read_b128 v[156:159], v241 offset:7168\n"
"v_fma_f32 v130, v178, v244, v130\n"
"v_mfma_f32_16x16x32_bf16 a[0:3], a[232:235], v[0:3], 0\n"
"s_barrier\n"
"ds_read_b64_tr_b16 a[248:249], v251 offset:512\n"
"v_mfma_f32_16x16x32_bf16 a[0:3], a[236:239], v[16:19], a[0:3]\n"
"ds_read_b64_tr_b16 a[250:251], v251 offset:2560\n"
"s_add_u32 s82, s37, 114688\n"
"v_add_u32 v242, s82, v243\n"
"s_add_u32 m0, s71, 98304\n"
"v_mfma_f32_16x16x32_bf16 a[4:7], a[232:235], v[4:7], 0\n"
"buffer_load_dwordx4 v242, s[48:51], 0 offen offset:0  lds\n"
"v_mfma_f32_16x16x32_bf16 a[4:7], a[236:239], v[20:23], a[4:7]\n"
"v_fma_f32 v131, v179, v244, v131\n"
"v_fma_f32 v132, v180, v245, v132\n"
"v_mfma_f32_16x16x32_bf16 a[8:11], a[232:235], v[8:11], 0\n"
"ds_read_b64_tr_b16 a[252:253], v251 offset:4608\n"
"v_fma_f32 v133, v181, v245, v133\n"
"v_mfma_f32_16x16x32_bf16 a[8:11], a[236:239], v[24:27], a[8:11]\n"
"ds_read_b64_tr_b16 a[254:255], v251 offset:6656\n"
"v_fma_f32 v134, v182, v245, v134\n"
"v_mfma_f32_16x16x32_bf16 a[12:15], a[232:235], v[12:15], 0\n"
"v_fma_f32 v135, v183, v245, v135\n"
"v_fma_f32 v136, v184, v246, v136\n"
"v_mfma_f32_16x16x32_bf16 a[12:15], a[236:239], v[28:31], a[12:15]\n"
"s_waitcnt lgkmcnt(6)\n"
"v_fma_f32 v137, v185, v246, v137\n"
"v_fma_f32 v138, v186, v246, v138\n"
"v_mfma_f32_16x16x32_bf16 a[0:3], a[240:243], v[32:35], a[0:3]\n"
"ds_read_b64_tr_b16 a[232:233], v251 offset:8704\n"
"v_fma_f32 v139, v187, v246, v139\n"
"v_mfma_f32_16x16x32_bf16 a[0:3], a[244:247], v[48:51], a[0:3]\n"
"buffer_load_dwordx4 v242, s[48:51], s64 offen offset:1024  lds\n"
"ds_read_b64_tr_b16 a[234:235], v251 offset:10752\n"
"v_mfma_f32_16x16x32_bf16 a[4:7], a[240:243], v[36:39], a[4:7]\n"
"v_fma_f32 v140, v188, v247, v140\n"
"v_fma_f32 v141, v189, v247, v141\n"
"v_mfma_f32_16x16x32_bf16 a[4:7], a[244:247], v[52:55], a[4:7]\n"
"v_fma_f32 v142, v190, v247, v142\n"
"v_fma_f32 v143, v191, v247, v143\n"
"v_mfma_f32_16x16x32_bf16 a[8:11], a[240:243], v[40:43], a[8:11]\n"
"ds_read_b64_tr_b16 a[236:237], v251 offset:12800\n"
"s_waitcnt lgkmcnt(7)\n"
"v_mfma_f32_16x16x32_bf16 a[8:11], a[244:247], v[56:59], a[8:11]\n"
"ds_read_b64_tr_b16 a[238:239], v251 offset:14848\n"
"v_fma_f32 v144, v160, v244, v144\n"
"v_mfma_f32_16x16x32_bf16 a[12:15], a[240:243], v[44:47], a[12:15]\n"
"v_fma_f32 v145, v161, v244, v145\n"
"v_fma_f32 v146, v162, v244, v146\n"
"v_mfma_f32_16x16x32_bf16 a[12:15], a[244:247], v[60:63], a[12:15]\n"
"s_waitcnt lgkmcnt(4)\n"
"v_fma_f32 v147, v163, v244, v147\n"
"v_fma_f32 v148, v164, v245, v148\n"
"v_mfma_f32_16x16x32_bf16 a[16:19], a[248:251], v[0:3], 0\n"
"buffer_load_dwordx4 v242, s[48:51], s65 offen offset:2048  lds\n"
"ds_read_b64_tr_b16 a[240:241], v250 offset:0\n"
"v_mfma_f32_16x16x32_bf16 a[16:19], a[252:255], v[16:19], a[16:19]\n"
"ds_read_b64_tr_b16 a[242:243], v250 offset:2048\n"
"v_fma_f32 v149, v165, v245, v149\n"
"v_mfma_f32_16x16x32_bf16 a[20:23], a[248:251], v[4:7], 0\n"
"v_fma_f32 v150, v166, v245, v150\n"
"v_fma_f32 v151, v167, v245, v151\n"
"v_mfma_f32_16x16x32_bf16 a[20:23], a[252:255], v[20:23], a[20:23]\n"
"v_fma_f32 v152, v168, v246, v152\n"
"v_fma_f32 v153, v169, v246, v153\n"
"v_mfma_f32_16x16x32_bf16 a[24:27], a[248:251], v[8:11], 0\n"
"ds_read_b64_tr_b16 a[244:245], v250 offset:4096\n"
"v_fma_f32 v154, v170, v246, v154\n"
"v_mfma_f32_16x16x32_bf16 a[24:27], a[252:255], v[24:27], a[24:27]\n"
"ds_read_b64_tr_b16 a[246:247], v250 offset:6144\n"
"v_fma_f32 v155, v171, v246, v155\n"
"v_mfma_f32_16x16x32_bf16 a[28:31], a[248:251], v[12:15], 0\n"
"L_CW10:\n"
"s_mov_b32 s75, L_CW10-L_PC+3072\n"
"buffer_load_dword a127, v254, s[56:59], s75 offen\n"
"v_mfma_f32_16x16x32_bf16 a[28:31], a[252:255], v[28:31], a[28:31]\n"
"s_waitcnt lgkmcnt(4)\n"
"buffer_load_dwordx4 v242, s[48:51], s66 offen offset:3072  lds\n"
"v_mfma_f32_16x16x32_bf16 a[16:19], a[232:235], v[32:35], a[16:19]\n"
"ds_read_b64_tr_b16 a[248:249], v250 offset:8192\n"
"v_fma_f32 v156, v172, v247, v156\n"
"v_mfma_f32_16x16x32_bf16 a[16:19], a[236:239], v[48:51], a[16:19]\n"
"ds_read_b64_tr_b16 a[250:251], v250 offset:10240\n"
"v_fma_f32 v157, v173, v247, v157\n"
"v_mfma_f32_16x16x32_bf16 a[20:23], a[232:235], v[36:39], a[20:23]\n"
"v_fma_f32 v158, v174, v247, v158\n"
"v_fma_f32 v159, v175, v247, v159\n"
"v_mfma_f32_16x16x32_bf16 a[20:23], a[236:239], v[52:55], a[20:23]\n"
"s_lshl_b32 s79, s35, 11\n"
"v_mul_f32 v128, v128, v232\n"
"v_mfma_f32_16x16x32_bf16 a[24:27], a[232:235], v[40:43], a[24:27]\n"
"ds_read_b64_tr_b16 a[252:253], v250 offset:12288\n"
"v_mul_f32 v129, v129, v232\n"
"v_mfma_f32_16x16x32_bf16 a[24:27], a[236:239], v[56:59], a[24:27]\n"
"ds_read_b64_tr_b16 a[254:255], v250 offset:14336\n"
"s_add_u32 s82, s37, 344064\n"
"v_add_u32 v242, s82, v243\n"
"v_xor_b32 v242, 64, v242\n"
"s_add_u32 m0, s71, 114688\n"
"v_mfma_f32_16x16x32_bf16 a[28:31], a[232:235], v[44:47], a[28:31]\n"
"buffer_load_dwordx4 v242, s[48:51], 0 offen offset:0  lds\n"
"v_mfma_f32_16x16x32_bf16 a[28:31], a[236:239], v[60:63], a[28:31]\n"
"s_waitcnt lgkmcnt(4)\n"
"v_mul_f32 v130, v130, v232\n"
"v_mul_f32 v131, v131, v232\n"
"v_mfma_f32_16x16x32_bf16 v[176:179], a[240:243], v[0:3], 0\n"
"ds_read_b64_tr_b16 a[232:233], v251 offset:0\n"
"v_mul_f32 v132, v132, v233\n"
"v_mfma_f32_16x16x32_bf16 v[176:179], a[244:247], v[16:19], v[176:179]\n"
"ds_read_b64_tr_b16 a[234:235], v251 offset:2048\n"
"v_mul_f32 v133, v133, v233\n"
"v_mfma_f32_16x16x32_bf16 v[180:183], a[240:243], v[4:7], 0\n"
"v_mul_f32 v134, v134, v233\n"
"v_mul_f32 v135, v135, v233\n"
"v_mfma_f32_16x16x32_bf16 v[180:183], a[244:247], v[20:23], v[180:183]\n"
"v_cvt_pk_bf16_f32 v128, v128, v129\n"
"v_cvt_pk_bf16_f32 v129, v130, v131\n"
"v_mfma_f32_16x16x32_bf16 v[184:187], a[240:243], v[8:11], 0\n"
"ds_read_b64_tr_b16 a[236:237], v251 offset:4096\n"
"v_cvt_pk_bf16_f32 v130, v132, v133\n"
"v_mfma_f32_16x16x32_bf16 v[184:187], a[244:247], v[24:27], v[184:187]\n"
"buffer_load_dwordx4 v242, s[48:51], s64 offen offset:1024  lds\n"
"ds_read_b64_tr_b16 a[238:239], v251 offset:6144\n"
"v_mfma_f32_16x16x32_bf16 v[188:191], a[240:243], v[12:15], 0\n"
"v_cvt_pk_bf16_f32 v131, v134, v135\n"
"v_mul_f32 v136, v136, v234\n"
"v_mfma_f32_16x16x32_bf16 v[188:191], a[244:247], v[28:31], v[188:191]\n"
"s_waitcnt lgkmcnt(4)\n"
"s_lshl_b32 s83, s35, 4\n"
"s_add_u32 s83, s83, s67\n"
"v_mfma_f32_16x16x32_bf16 v[176:179], a[248:251], v[32:35], v[176:179]\n"
"ds_read_b64_tr_b16 a[240:241], v251 offset:8192\n"
"buffer_load_dwordx2 v[238:239], off, s[52:55], s83 sc1\n"
"v_mfma_f32_16x16x32_bf16 v[176:179], a[252:255], v[48:51], v[176:179]\n"
"ds_read_b64_tr_b16 a[242:243], v251 offset:10240\n"
"v_mul_f32 v137, v137, v234\n"
"v_mfma_f32_16x16x32_bf16 v[180:183], a[248:251], v[36:39], v[180:183]\n"
"buffer_store_dwordx4 v[128:131], v255, s[52:55], s79 offen offset:0 sc1\n"
"v_mfma_f32_16x16x32_bf16 v[180:183], a[252:255], v[52:55], v[180:183]\n"
"v_mul_f32 v138, v138, v234\n"
"v_mul_f32 v139, v139, v234\n"
"v_mfma_f32_16x16x32_bf16 v[184:187], a[248:251], v[40:43], v[184:187]\n"
"buffer_load_dwordx4 v242, s[48:51], s65 offen offset:2048  lds\n"
"ds_read_b64_tr_b16 a[244:245], v251 offset:12288\n"
"v_mfma_f32_16x16x32_bf16 v[184:187], a[252:255], v[56:59], v[184:187]\n"
"ds_read_b64_tr_b16 a[246:247], v251 offset:14336\n"
"v_mul_f32 v140, v140, v235\n"
"v_mfma_f32_16x16x32_bf16 v[188:191], a[248:251], v[44:47], v[188:191]\n"
"v_mul_f32 v141, v141, v235\n"
"v_mul_f32 v142, v142, v235\n"
"v_mfma_f32_16x16x32_bf16 v[188:191], a[252:255], v[60:63], v[188:191]\n"
"s_waitcnt lgkmcnt(4)\n"
"v_mul_f32 v143, v143, v235\n"
"v_cvt_pk_bf16_f32 v136, v136, v137\n"
"v_mfma_f32_16x16x32_bf16 v[160:163], a[232:235], v[0:3], 0\n"
"ds_read_b64_tr_b16 a[248:249], v250 offset:33280\n"
"v_cvt_pk_bf16_f32 v137, v138, v139\n"
"v_mfma_f32_16x16x32_bf16 v[160:163], a[236:239], v[16:19], v[160:163]\n"
"ds_read_b64_tr_b16 a[250:251], v250 offset:35328\n"
"v_cvt_pk_bf16_f32 v138, v140, v141\n"
"v_mfma_f32_16x16x32_bf16 v[164:167], a[232:235], v[4:7], 0\n"
"v_cvt_pk_bf16_f32 v139, v142, v143\n"
"v_mul_f32 v144, v144, v232\n"
"v_mfma_f32_16x16x32_bf16 v[164:167], a[236:239], v[20:23], v[164:167]\n"
"buffer_load_dwordx4 v242, s[48:51], s66 offen offset:3072  lds\n"
"v_mfma_f32_16x16x32_bf16 v[168:171], a[232:235], v[8:11], 0\n"
"ds_read_b64_tr_b16 a[252:253], v250 offset:37376\n"
"v_mul_f32 v145, v145, v232\n"
"v_mfma_f32_16x16x32_bf16 v[168:171], a[236:239], v[24:27], v[168:171]\n"
"ds_read_b64_tr_b16 a[254:255], v250 offset:39424\n"
"v_mul_f32 v146, v146, v232\n"
"v_mfma_f32_16x16x32_bf16 v[172:175], a[232:235], v[12:15], 0\n"
"buffer_store_dwordx4 v[136:139], v255, s[52:55], s79 offen offset:1024 sc1\n"
"v_mfma_f32_16x16x32_bf16 v[172:175], a[236:239], v[28:31], v[172:175]\n"
"s_waitcnt lgkmcnt(4)\n"
"s_lshl_b32 s80, s35, 11\n"
"s_add_u32 s80, s80, 4096\n"
"v_mfma_f32_16x16x32_bf16 v[160:163], a[240:243], v[32:35], v[160:163]\n"
"ds_read_b64_tr_b16 a[232:233], v250 offset:41472\n"
"v_mul_f32 v147, v147, v232\n"
"v_mfma_f32_16x16x32_bf16 v[160:163], a[244:247], v[48:51], v[160:163]\n"
"ds_read_b64_tr_b16 a[234:235], v250 offset:43520\n"
"v_mul_f32 v148, v148, v233\n"
"v_mfma_f32_16x16x32_bf16 v[164:167], a[240:243], v[36:39], v[164:167]\n"
"v_mul_f32 v149, v149, v233\n"
"v_mul_f32 v150, v150, v233\n"
"v_mfma_f32_16x16x32_bf16 v[164:167], a[244:247], v[52:55], v[164:167]\n"
"v_mul_f32 v151, v151, v233\n"
"v_cvt_pk_bf16_f32 v144, v144, v145\n"
"v_mfma_f32_16x16x32_bf16 v[168:171], a[240:243], v[40:43], v[168:171]\n"
"s_waitcnt vmcnt(15)\n"
"s_barrier\n"
"ds_read_b64_tr_b16 a[236:237], v250 offset:45568\n"
"v_mfma_f32_16x16x32_bf16 v[168:171], a[244:247], v[56:59], v[168:171]\n"
"ds_read_b64_tr_b16 a[238:239], v250 offset:47616\n"
"v_xor_b32 v243, s85, v243\n"
"s_add_u32 s82, s38, 0\n"
"v_add_u32 v242, s82, v243\n"
"s_add_u32 m0, s71, 0\n"
"v_mfma_f32_16x16x32_bf16 v[172:175], a[240:243], v[44:47], v[172:175]\n"
"buffer_load_dwordx4 v242, s[48:51], 0 offen offset:0  lds\n"
"v_mfma_f32_16x16x32_bf16 v[172:175], a[244:247], v[60:63], v[172:175]\n"
"s_waitcnt lgkmcnt(4)\n"
"v_cvt_pk_bf16_f32 v145, v146, v147\n"
"v_cvt_pk_bf16_f32 v146, v148, v149\n"
"v_mfma_f32_16x16x32_bf16 a[0:3], a[248:251], v[64:67], a[0:3]\n"
"ds_read_b64_tr_b16 a[240:241], v251 offset:33280\n"
"v_cvt_pk_bf16_f32 v147, v150, v151\n"
"v_mfma_f32_16x16x32_bf16 a[0:3], a[252:255], v[80:83], a[0:3]\n"
"ds_read_b64_tr_b16 a[242:243], v251 offset:35328\n"
"v_mul_f32 v152, v152, v234\n"
"v_mfma_f32_16x16x32_bf16 a[4:7], a[248:251], v[68:71], a[4:7]\n"
"buffer_store_dwordx4 v[144:147], v255, s[52:55], s80 offen offset:0 sc1\n"
"v_mfma_f32_16x16x32_bf16 a[4:7], a[252:255], v[84:87], a[4:7]\n"
"v_mul_f32 v153, v153, v234\n"
"v_mul_f32 v154, v154, v234\n"
"v_mfma_f32_16x16x32_bf16 a[8:11], a[248:251], v[72:75], a[8:11]\n"
"ds_read_b64_tr_b16 a[244:245], v251 offset:37376\n"
"v_mul_f32 v155, v155, v234\n"
"v_mfma_f32_16x16x32_bf16 a[8:11], a[252:255], v[88:91], a[8:11]\n"
"buffer_load_dwordx4 v242, s[48:51], s64 offen offset:1024  lds\n"
"ds_read_b64_tr_b16 a[246:247], v251 offset:39424\n"
"v_mfma_f32_16x16x32_bf16 a[12:15], a[248:251], v[76:79], a[12:15]\n"
"v_mul_f32 v156, v156, v235\n"
"v_mul_f32 v157, v157, v235\n"
"v_mfma_f32_16x16x32_bf16 a[12:15], a[252:255], v[92:95], a[12:15]\n"
"s_waitcnt lgkmcnt(4)\n"
"v_mul_f32 v158, v158, v235\n"
"v_mul_f32 v159, v159, v235\n"
"v_mfma_f32_16x16x32_bf16 a[0:3], a[232:235], v[96:99], a[0:3]\n"
"ds_read_b64_tr_b16 a[248:249], v251 offset:41472\n"
"v_cvt_pk_bf16_f32 v152, v152, v153\n"
"v_mfma_f32_16x16x32_bf16 a[0:3], a[236:239], v[112:115], a[0:3]\n"
"ds_read_b64_tr_b16 a[250:251], v251 offset:43520\n"
"v_cvt_pk_bf16_f32 v153, v154, v155\n"
"v_mfma_f32_16x16x32_bf16 a[4:7], a[232:235], v[100:103], a[4:7]\n"
"v_cvt_pk_bf16_f32 v154, v156, v157\n"
"v_cvt_pk_bf16_f32 v155, v158, v159\n"
"v_mfma_f32_16x16x32_bf16 a[4:7], a[236:239], v[116:119], a[4:7]\n"
"buffer_store_dwordx4 v[152:155], v255, s[52:55], s80 offen offset:1024 sc1\n"
"v_mfma_f32_16x16x32_bf16 a[8:11], a[232:235], v[104:107], a[8:11]\n"
"buffer_load_dwordx4 v242, s[48:51], s65 offen offset:2048  lds\n"
"ds_read_b64_tr_b16 a[252:253], v251 offset:45568\n"
"v_mfma_f32_16x16x32_bf16 a[8:11], a[236:239], v[120:123], a[8:11]\n"
"ds_read_b64_tr_b16 a[254:255], v251 offset:47616\n"
"v_mfma_f32_16x16x32_bf16 a[12:15], a[232:235], v[108:111], a[12:15]\n"
"v_mfma_f32_16x16x32_bf16 a[12:15], a[236:239], v[124:127], a[12:15]\n"
"s_waitcnt vmcnt(17)\n"
"v_mfma_f32_16x16x32_bf16 v[192:195], v[226:229], a[48:51], v[192:195]\n"
"v_mfma_f32_16x16x32_bf16 v[196:199], v[224:227], a[48:51], v[196:199]\n"
"v_mfma_f32_16x16x32_bf16 v[200:203], v[226:229], a[52:55], v[200:203]\n"
"v_mfma_f32_16x16x32_bf16 v[204:207], v[224:227], a[52:55], v[204:207]\n"
"buffer_load_dwordx4 v242, s[48:51], s66 offen offset:3072  lds\n"
"v_mfma_f32_16x16x32_bf16 v[208:211], v[226:229], a[56:59], v[208:211]\n"
"v_mfma_f32_16x16x32_bf16 v[212:215], v[224:227], a[56:59], v[212:215]\n"
"v_mfma_f32_16x16x32_bf16 v[216:219], v[226:229], a[60:63], v[216:219]\n"
"v_mfma_f32_16x16x32_bf16 v[220:223], v[224:227], a[60:63], v[220:223]\n"
"s_waitcnt lgkmcnt(4)\n"
"v_mfma_f32_16x16x32_bf16 a[16:19], a[240:243], v[64:67], a[16:19]\n"
"ds_read_b64_tr_b16 a[232:233], v250 offset:32768\n"
"v_mfma_f32_16x16x32_bf16 a[16:19], a[244:247], v[80:83], a[16:19]\n"
"ds_read_b64_tr_b16 a[234:235], v250 offset:34816\n"
"s_add_u32 s82, s38, 229376\n"
"v_add_u32 v242, s82, v243\n"
"v_xor_b32 v242, 64, v242\n"
"s_add_u32 m0, s71, 16384\n"
"v_mfma_f32_16x16x32_bf16 a[20:23], a[240:243], v[68:71], a[20:23]\n"
"buffer_load_dwordx4 v242, s[48:51], 0 offen offset:0  lds\n"
"v_mfma_f32_16x16x32_bf16 a[20:23], a[244:247], v[84:87], a[20:23]\n"
"ds_write_b128 v237, a[0:3] offset:0\n"
"v_mfma_f32_16x16x32_bf16 a[24:27], a[240:243], v[72:75], a[24:27]\n"
"ds_read_b64_tr_b16 a[236:237], v250 offset:36864\n"
"v_mfma_f32_16x16x32_bf16 a[24:27], a[244:247], v[88:91], a[24:27]\n"
"ds_read_b64_tr_b16 a[238:239], v250 offset:38912\n"
"v_mfma_f32_16x16x32_bf16 a[28:31], a[240:243], v[76:79], a[28:31]\n"
"ds_write_b128 v237, a[4:7] offset:1024\n"
"v_mfma_f32_16x16x32_bf16 a[28:31], a[244:247], v[92:95], a[28:31]\n"
"s_waitcnt lgkmcnt(6)\n"
"v_mfma_f32_16x16x32_bf16 a[16:19], a[248:251], v[96:99], a[16:19]\n"
"ds_read_b64_tr_b16 a[240:241], v250 offset:40960\n"
"v_mfma_f32_16x16x32_bf16 a[16:19], a[252:255], v[112:115], a[16:19]\n"
"buffer_load_dwordx4 v242, s[48:51], s64 offen offset:1024  lds\n"
"ds_read_b64_tr_b16 a[242:243], v250 offset:43008\n"
"v_mfma_f32_16x16x32_bf16 a[20:23], a[248:251], v[100:103], a[20:23]\n"
"s_waitcnt vmcnt(12)\n"
"v_cmp_ne_u64 vcc, v[230:231], v[238:239]\n"
"s_cbranch_vccnz L_FS2\n"
"L_FOK2:\n"
"v_mfma_f32_16x16x32_bf16 a[20:23], a[252:255], v[116:119], a[20:23]\n"
"ds_write_b128 v237, a[8:11] offset:2048\n"
"v_mfma_f32_16x16x32_bf16 a[24:27], a[248:251], v[104:107], a[24:27]\n"
"s_lshl_b32 s84, s35, 8\n"
"s_add_u32 s84, s84, s68\n"
"buffer_load_dwordx4 a[32:35], v240, s[52:55], s84 offen offset:0 sc1\n"
"ds_read_b64_tr_b16 a[244:245], v250 offset:45056\n"
"v_mfma_f32_16x16x32_bf16 a[24:27], a[252:255], v[120:123], a[24:27]\n"
"ds_read_b64_tr_b16 a[246:247], v250 offset:47104\n"
"v_mfma_f32_16x16x32_bf16 a[28:31], a[248:251], v[108:111], a[28:31]\n"
"ds_write_b128 v237, a[12:15] offset:3072\n"
"v_mfma_f32_16x16x32_bf16 a[28:31], a[252:255], v[124:127], a[28:31]\n"
"s_waitcnt lgkmcnt(7)\n"
"v_mfma_f32_16x16x32_bf16 v[176:179], a[232:235], v[64:67], v[176:179]\n"
"buffer_load_dwordx4 v242, s[48:51], s65 offen offset:2048  lds\n"
"ds_read_b64_tr_b16 a[248:249], v251 offset:32768\n"
"v_mfma_f32_16x16x32_bf16 v[176:179], a[236:239], v[80:83], v[176:179]\n"
"ds_read_b64_tr_b16 a[250:251], v251 offset:34816\n"
"v_mfma_f32_16x16x32_bf16 v[180:183], a[232:235], v[68:71], v[180:183]\n"
"ds_write_b128 v237, a[16:19] offset:4096\n"
"v_mfma_f32_16x16x32_bf16 v[180:183], a[236:239], v[84:87], v[180:183]\n"
"v_mfma_f32_16x16x32_bf16 v[184:187], a[232:235], v[72:75], v[184:187]\n"
"s_lshl_b32 s84, s35, 8\n"
"s_add_u32 s84, s84, s68\n"
"buffer_load_dwordx4 a[36:39], v240, s[52:55], s84 offen offset:1024 sc1\n"
"ds_read_b64_tr_b16 a[252:253], v251 offset:36864\n"
"v_mfma_f32_16x16x32_bf16 v[184:187], a[236:239], v[88:91], v[184:187]\n"
"ds_read_b64_tr_b16 a[254:255], v251 offset:38912\n"
"v_mfma_f32_16x16x32_bf16 v[188:191], a[232:235], v[76:79], v[188:191]\n"
"ds_write_b128 v237, a[20:23] offset:5120\n"
"v_mfma_f32_16x16x32_bf16 v[188:191], a[236:239], v[92:95], v[188:191]\n"
"s_waitcnt lgkmcnt(7)\n"
"buffer_load_dwordx4 v242, s[48:51], s66 offen offset:3072  lds\n"
"v_mfma_f32_16x16x32_bf16 v[176:179], a[240:243], v[96:99], v[176:179]\n"
"ds_read_b64_tr_b16 a[232:233], v251 offset:40960\n"
"v_mfma_f32_16x16x32_bf16 v[176:179], a[244:247], v[112:115], v[176:179]\n"
"ds_read_b64_tr_b16 a[234:235], v251 offset:43008\n"
"v_mfma_f32_16x16x32_bf16 v[180:183], a[240:243], v[100:103], v[180:183]\n"
"ds_write_b128 v237, a[24:27] offset:6144\n"
"v_mfma_f32_16x16x32_bf16 v[180:183], a[244:247], v[116:119], v[180:183]\n"
"v_mfma_f32_16x16x32_bf16 v[184:187], a[240:243], v[104:107], v[184:187]\n"
"s_lshl_b32 s84, s35, 8\n"
"s_add_u32 s84, s84, s69\n"
"buffer_load_dwordx4 a[40:43], v240, s[52:55], s84 offen offset:0 sc1\n"
"ds_read_b64_tr_b16 a[236:237], v251 offset:45056\n"
"v_mfma_f32_16x16x32_bf16 v[184:187], a[244:247], v[120:123], v[184:187]\n"
"ds_read_b64_tr_b16 a[238:239], v251 offset:47104\n"
"v_mfma_f32_16x16x32_bf16 v[188:191], a[240:243], v[108:111], v[188:191]\n"
"s_waitcnt vmcnt(9)\n"
"s_lshl_b32 s83, s35, 1\n"
"s_add_u32 s83, s83, s39\n"
"buffer_store_dwordx2 v[230:231], off, s[52:55], s83 sc1\n"
"v_mfma_f32_16x16x32_bf16 v[188:191], a[244:247], v[124:127], v[188:191]\n"
"s_waitcnt lgkmcnt(6)\n"
"ds_write_b128 v237, a[28:31] offset:7168\n"
"v_mfma_f32_16x16x32_bf16 v[160:163], a[248:251], v[64:67], v[160:163]\n"
"ds_read_b64_tr_b16 a[240:241], v252 offset:512\n"
"v_mfma_f32_16x16x32_bf16 v[160:163], a[252:255], v[80:83], v[160:163]\n"
"ds_read_b64_tr_b16 a[242:243], v252 offset:2560\n"
"v_mfma_f32_16x16x32_bf16 v[164:167], a[248:251], v[68:71], v[164:167]\n"
"s_waitcnt lgkmcnt(2)\n"
"s_barrier\n"
"ds_read_b128 v[128:131], v241 offset:0\n"
"v_mfma_f32_16x16x32_bf16 v[164:167], a[252:255], v[84:87], v[164:167]\n"
"ds_read_b128 v[132:135], v241 offset:1024\n"
"v_mfma_f32_16x16x32_bf16 v[168:171], a[248:251], v[72:75], v[168:171]\n"
"s_lshl_b32 s84, s35, 8\n"
"s_add_u32 s84, s84, s69\n"
"buffer_load_dwordx4 a[44:47], v240, s[52:55], s84 offen offset:1024 sc1\n"
"ds_read_b64_tr_b16 a[244:245], v252 offset:4608\n"
"v_mfma_f32_16x16x32_bf16 v[168:171], a[252:255], v[88:91], v[168:171]\n"
"ds_read_b64_tr_b16 a[246:247], v252 offset:6656\n"
"v_mfma_f32_16x16x32_bf16 v[172:175], a[248:251], v[76:79], v[172:175]\n"
"ds_read_b128 v[136:139], v241 offset:2048\n"
"v_mfma_f32_16x16x32_bf16 v[172:175], a[252:255], v[92:95], v[172:175]\n"
"ds_read_b128 v[140:143], v241 offset:3072\n"
"v_mfma_f32_16x16x32_bf16 v[160:163], a[232:235], v[96:99], v[160:163]\n"
"ds_read_b64_tr_b16 a[248:249], v252 offset:8704\n"
"v_mfma_f32_16x16x32_bf16 v[160:163], a[236:239], v[112:115], v[160:163]\n"
"ds_read_b64_tr_b16 a[250:251], v252 offset:10752\n"
"v_mfma_f32_16x16x32_bf16 v[164:167], a[232:235], v[100:103], v[164:167]\n"
"ds_read_b128 v[144:147], v241 offset:4096\n"
"v_mfma_f32_16x16x32_bf16 v[164:167], a[236:239], v[116:119], v[164:167]\n"
"ds_read_b128 v[148:151], v241 offset:5120\n"
"v_mfma_f32_16x16x32_bf16 v[168:171], a[232:235], v[104:107], v[168:171]\n"
"ds_read_b64_tr_b16 a[252:253], v252 offset:12800\n"
"s_waitcnt lgkmcnt(5)\n"
"v_mfma_f32_16x16x32_bf16 v[168:171], a[236:239], v[120:123], v[168:171]\n"
"ds_read_b64_tr_b16 a[254:255], v252 offset:14848\n"
"v_fma_f32 v128, v176, v244, v128\n"
"v_mfma_f32_16x16x32_bf16 v[172:175], a[232:235], v[108:111], v[172:175]\n"
"ds_read_b128 v[152:155], v241 offset:6144\n"
"v_fma_f32 v129, v177, v244, v129\n"
"v_mfma_f32_16x16x32_bf16 v[172:175], a[236:239], v[124:127], v[172:175]\n"
"ds_read_b128 v[156:159], v241 offset:7168\n"
"v_fma_f32 v130, v178, v244, v130\n"
"v_mfma_f32_16x16x32_bf16 a[0:3], a[240:243], v[0:3], 0\n"
"s_barrier\n"
"ds_read_b64_tr_b16 a[232:233], v252 offset:33280\n"
"v_mfma_f32_16x16x32_bf16 a[0:3], a[244:247], v[16:19], a[0:3]\n"
"ds_read_b64_tr_b16 a[234:235], v252 offset:35328\n"
"s_add_u32 s82, s38, 114688\n"
"v_add_u32 v242, s82, v243\n"
"s_add_u32 m0, s71, 32768\n"
"v_mfma_f32_16x16x32_bf16 a[4:7], a[240:243], v[4:7], 0\n"
"buffer_load_dwordx4 v242, s[48:51], 0 offen offset:0  lds\n"
"s_lshl_b32 s79, s36, 11\n"
"v_mfma_f32_16x16x32_bf16 a[4:7], a[244:247], v[20:23], a[4:7]\n"
"v_fma_f32 v131, v179, v244, v131\n"
"v_fma_f32 v132, v180, v245, v132\n"
"v_mfma_f32_16x16x32_bf16 a[8:11], a[240:243], v[8:11], 0\n"
"ds_read_b64_tr_b16 a[236:237], v252 offset:37376\n"
"v_fma_f32 v133, v181, v245, v133\n"
"v_mul_f32 v128, v128, v232\n"
"v_mfma_f32_16x16x32_bf16 a[8:11], a[244:247], v[24:27], a[8:11]\n"
"ds_read_b64_tr_b16 a[238:239], v252 offset:39424\n"
"v_fma_f32 v134, v182, v245, v134\n"
"v_mfma_f32_16x16x32_bf16 a[12:15], a[240:243], v[12:15], 0\n"
"v_fma_f32 v135, v183, v245, v135\n"
"v_fma_f32 v136, v184, v246, v136\n"
"v_mfma_f32_16x16x32_bf16 a[12:15], a[244:247], v[28:31], a[12:15]\n"
"s_waitcnt lgkmcnt(6)\n"
"v_fma_f32 v137, v185, v246, v137\n"
"v_fma_f32 v138, v186, v246, v138\n"
"v_mul_f32 v129, v129, v232\n"
"v_mfma_f32_16x16x32_bf16 a[0:3], a[248:251], v[32:35], a[0:3]\n"
"ds_read_b64_tr_b16 a[240:241], v252 offset:41472\n"
"v_fma_f32 v139, v187, v246, v139\n"
"v_mfma_f32_16x16x32_bf16 a[0:3], a[252:255], v[48:51], a[0:3]\n"
"buffer_load_dwordx4 v242, s[48:51], s64 offen offset:1024  lds\n"
"ds_read_b64_tr_b16 a[242:243], v252 offset:43520\n"
"v_mfma_f32_16x16x32_bf16 a[4:7], a[248:251], v[36:39], a[4:7]\n"
"v_fma_f32 v140, v188, v247, v140\n"
"v_fma_f32 v141, v189, v247, v141\n"
"v_mfma_f32_16x16x32_bf16 a[4:7], a[252:255], v[52:55], a[4:7]\n"
"v_fma_f32 v142, v190, v247, v142\n"
"v_fma_f32 v143, v191, v247, v143\n"
"v_mfma_f32_16x16x32_bf16 a[8:11], a[248:251], v[40:43], a[8:11]\n"
"ds_read_b64_tr_b16 a[244:245], v252 offset:45568\n"
"s_waitcnt lgkmcnt(7)\n"
"v_mul_f32 v130, v130, v232\n"
"v_mfma_f32_16x16x32_bf16 a[8:11], a[252:255], v[56:59], a[8:11]\n"
"ds_read_b64_tr_b16 a[246:247], v252 offset:47616\n"
"v_fma_f32 v144, v160, v244, v144\n"
"v_mfma_f32_16x16x32_bf16 a[12:15], a[248:251], v[44:47], a[12:15]\n"
"v_fma_f32 v145, v161, v244, v145\n"
"v_fma_f32 v146, v162, v244, v146\n"
"v_mul_f32 v131, v131, v232\n"
"v_mfma_f32_16x16x32_bf16 a[12:15], a[252:255], v[60:63], a[12:15]\n"
"s_waitcnt lgkmcnt(4)\n"
"v_fma_f32 v147, v163, v244, v147\n"
"v_fma_f32 v148, v164, v245, v148\n"
"v_mfma_f32_16x16x32_bf16 a[0:3], a[232:235], v[64:67], a[0:3]\n"
"buffer_load_dwordx4 v242, s[48:51], s65 offen offset:2048  lds\n"
"ds_read_b64_tr_b16 a[248:249], v253 offset:512\n"
"v_mfma_f32_16x16x32_bf16 a[0:3], a[236:239], v[80:83], a[0:3]\n"
"ds_read_b64_tr_b16 a[250:251], v253 offset:2560\n"
"v_fma_f32 v149, v165, v245, v149\n"
"v_mul_f32 v132, v132, v233\n"
"v_mfma_f32_16x16x32_bf16 a[4:7], a[232:235], v[68:71], a[4:7]\n"
"v_fma_f32 v150, v166, v245, v150\n"
"v_fma_f32 v151, v167, v245, v151\n"
"v_mfma_f32_16x16x32_bf16 a[4:7], a[236:239], v[84:87], a[4:7]\n"
"v_fma_f32 v152, v168, v246, v152\n"
"v_fma_f32 v153, v169, v246, v153\n"
"v_mul_f32 v133, v133, v233\n"
"v_mfma_f32_16x16x32_bf16 a[8:11], a[232:235], v[72:75], a[8:11]\n"
"ds_read_b64_tr_b16 a[252:253], v253 offset:4608\n"
"v_fma_f32 v154, v170, v246, v154\n"
"v_mfma_f32_16x16x32_bf16 a[8:11], a[236:239], v[88:91], a[8:11]\n"
"ds_read_b64_tr_b16 a[254:255], v253 offset:6656\n"
"v_fma_f32 v155, v171, v246, v155\n"
"v_mfma_f32_16x16x32_bf16 a[12:15], a[232:235], v[76:79], a[12:15]\n"
"L_CW11:\n"
"s_mov_b32 s75, L_CW11-L_PC+3072\n"
"buffer_load_dword a127, v254, s[56:59], s75 offen\n"
"v_mul_f32 v134, v134, v233\n"
"v_mfma_f32_16x16x32_bf16 a[12:15], a[236:239], v[92:95], a[12:15]\n"
"s_waitcnt lgkmcnt(4)\n"
"buffer_load_dwordx4 v242, s[48:51], s66 offen offset:3072  lds\n"
"v_mfma_f32_16x16x32_bf16 a[0:3], a[240:243], v[96:99], a[0:3]\n"
"ds_read_b64_tr_b16 a[232:233], v253 offset:8704\n"
"v_fma_f32 v156, v172, v247, v156\n"
"v_mul_f32 v135, v135, v233\n"
"v_mfma_f32_16x16x32_bf16 a[0:3], a[244:247], v[112:115], a[0:3]\n"
"ds_read_b64_tr_b16 a[234:235], v253 offset:10752\n"
"v_fma_f32 v157, v173, v247, v157\n"
"v_mfma_f32_16x16x32_bf16 a[4:7], a[240:243], v[100:103], a[4:7]\n"
"v_fma_f32 v158, v174, v247, v158\n"
"v_fma_f32 v159, v175, v247, v159\n"
"v_mfma_f32_16x16x32_bf16 a[4:7], a[244:247], v[116:119], a[4:7]\n"
"v_cvt_pk_bf16_f32 v128, v128, v129\n"
"v_cvt_pk_bf16_f32 v129, v130, v131\n"
"v_cvt_pk_bf16_f32 v130, v132, v133\n"
"v_mfma_f32_16x16x32_bf16 a[8:11], a[240:243], v[104:107], a[8:11]\n"
"ds_read_b64_tr_b16 a[236:237], v253 offset:12800\n"
"v_cvt_pk_bf16_f32 v131, v134, v135\n"
"v_mfma_f32_16x16x32_bf16 a[8:11], a[244:247], v[120:123], a[8:11]\n"
"ds_read_b64_tr_b16 a[238:239], v253 offset:14848\n"
"s_add_u32 s82, s38, 344064\n"
"v_add_u32 v242, s82, v243\n"
"v_xor_b32 v242, 64, v242\n"
"s_add_u32 m0, s71, 49152\n"
"v_mfma_f32_16x16x32_bf16 a[12:15], a[240:243], v[108:111], a[12:15]\n"
"s_barrier\n"
"buffer_load_dwordx4 v242, s[48:51], 0 offen offset:0  lds\n"
"v_mfma_f32_16x16x32_bf16 a[12:15], a[244:247], v[124:127], a[12:15]\n"
"s_waitcnt lgkmcnt(4)\n"
"buffer_store_dwordx4 v[128:131], v255, s[52:55], s79 offen offset:0 sc1\n"
"v_mfma_f32_16x16x32_bf16 a[16:19], a[248:251], v[0:3], 0\n"
"ds_read_b64_tr_b16 a[240:241], v253 offset:33280\n"
"v_mul_f32 v136, v136, v234\n"
"v_mul_f32 v137, v137, v234\n"
"v_mfma_f32_16x16x32_bf16 a[16:19], a[252:255], v[16:19], a[16:19]\n"
"ds_read_b64_tr_b16 a[242:243], v253 offset:35328\n"
"v_mul_f32 v138, v138, v234\n"
"v_mfma_f32_16x16x32_bf16 a[20:23], a[248:251], v[4:7], 0\n"
"ds_write_b128 v237, a[0:3] offset:0\n"
"v_mul_f32 v139, v139, v234\n"
"v_mfma_f32_16x16x32_bf16 a[20:23], a[252:255], v[20:23], a[20:23]\n"
"v_mul_f32 v140, v140, v235\n"
"v_mul_f32 v141, v141, v235\n"
"v_mfma_f32_16x16x32_bf16 a[24:27], a[248:251], v[8:11], 0\n"
"ds_read_b64_tr_b16 a[244:245], v253 offset:37376\n"
"v_mul_f32 v142, v142, v235\n"
"v_mfma_f32_16x16x32_bf16 a[24:27], a[252:255], v[24:27], a[24:27]\n"
"buffer_load_dwordx4 v242, s[48:51], s64 offen offset:1024  lds\n"
"ds_read_b64_tr_b16 a[246:247], v253 offset:39424\n"
"v_mfma_f32_16x16x32_bf16 a[28:31], a[248:251], v[12:15], 0\n"
"ds_write_b128 v237, a[4:7] offset:1024\n"
"v_mfma_f32_16x16x32_bf16 a[28:31], a[252:255], v[28:31], a[28:31]\n"
"s_waitcnt lgkmcnt(6)\n"
"s_lshl_b32 s83, s34, 4\n"
"s_add_u32 s83, s83, s67\n"
"v_mul_f32 v143, v143, v235\n"
"v_mfma_f32_16x16x32_bf16 a[16:19], a[232:235], v[32:35], a[16:19]\n"
"ds_read_b64_tr_b16 a[248:249], v253 offset:41472\n"
"buffer_load_dwordx2 v[238:239], off, s[52:55], s83 sc1\n"
"v_mfma_f32_16x16x32_bf16 a[16:19], a[236:239], v[48:51], a[16:19]\n"
"ds_read_b64_tr_b16 a[250:251], v253 offset:43520\n"
"v_cvt_pk_bf16_f32 v136, v136, v137\n"
"v_mfma_f32_16x16x32_bf16 a[20:23], a[232:235], v[36:39], a[20:23]\n"
"ds_write_b128 v237, a[8:11] offset:2048\n"
"v_cvt_pk_bf16_f32 v137, v138, v139\n"
"v_mfma_f32_16x16x32_bf16 a[20:23], a[236:239], v[52:55], a[20:23]\n"
"v_cvt_pk_bf16_f32 v138, v140, v141\n"
"v_cvt_pk_bf16_f32 v139, v142, v143\n"
"v_mfma_f32_16x16x32_bf16 a[24:27], a[232:235], v[40:43], a[24:27]\n"
"buffer_load_dwordx4 v242, s[48:51], s65 offen offset:2048  lds\n"
"ds_read_b64_tr_b16 a[252:253], v253 offset:45568\n"
"v_mfma_f32_16x16x32_bf16 a[24:27], a[236:239], v[56:59], a[24:27]\n"
"ds_read_b64_tr_b16 a[254:255], v253 offset:47616\n"
"v_mul_f32 v144, v144, v232\n"
"v_mfma_f32_16x16x32_bf16 a[28:31], a[232:235], v[44:47], a[28:31]\n"
"ds_write_b128 v237, a[12:15] offset:3072\n"
"v_mfma_f32_16x16x32_bf16 a[28:31], a[236:239], v[60:63], a[28:31]\n"
"s_waitcnt lgkmcnt(7)\n"
"buffer_store_dwordx4 v[136:139], v255, s[52:55], s79 offen offset:1024 sc1\n"
"v_mul_f32 v145, v145, v232\n"
"v_mfma_f32_16x16x32_bf16 a[16:19], a[240:243], v[64:67], a[16:19]\n"
"ds_read_b64_tr_b16 a[232:233], v252 offset:0\n"
"v_mul_f32 v146, v146, v232\n"
"v_mfma_f32_16x16x32_bf16 a[16:19], a[244:247], v[80:83], a[16:19]\n"
"ds_read_b64_tr_b16 a[234:235], v252 offset:2048\n"
"s_lshl_b32 s80, s36, 11\n"
"s_add_u32 s80, s80, 4096\n"
"v_mfma_f32_16x16x32_bf16 a[20:23], a[240:243], v[68:71], a[20:23]\n"
"v_mul_f32 v147, v147, v232\n"
"v_mul_f32 v148, v148, v233\n"
"v_mfma_f32_16x16x32_bf16 a[20:23], a[244:247], v[84:87], a[20:23]\n"
"buffer_load_dwordx4 v242, s[48:51], s66 offen offset:3072  lds\n"
"v_mfma_f32_16x16x32_bf16 a[24:27], a[240:243], v[72:75], a[24:27]\n"
"ds_read_b64_tr_b16 a[236:237], v252 offset:4096\n"
"v_mul_f32 v149, v149, v233\n"
"v_mul_f32 v150, v150, v233\n"
"v_mfma_f32_16x16x32_bf16 a[24:27], a[244:247], v[88:91], a[24:27]\n"
"ds_read_b64_tr_b16 a[238:239], v252 offset:6144\n"
"v_mul_f32 v151, v151, v233\n"
"v_mfma_f32_16x16x32_bf16 a[28:31], a[240:243], v[76:79], a[28:31]\n"
"v_cvt_pk_bf16_f32 v144, v144, v145\n"
"v_cvt_pk_bf16_f32 v145, v146, v147\n"
"v_cvt_pk_bf16_f32 v146, v148, v149\n"
"v_mfma_f32_16x16x32_bf16 a[28:31], a[244:247], v[92:95], a[28:31]\n"
"s_waitcnt lgkmcnt(5)\n"
"v_cvt_pk_bf16_f32 v147, v150, v151\n"
"v_mul_f32 v152, v152, v234\n"
"v_mfma_f32_16x16x32_bf16 a[16:19], a[248:251], v[96:99], a[16:19]\n"
"ds_read_b64_tr_b16 a[240:241], v252 offset:8192\n"
"v_mul_f32 v153, v153, v234\n"
"v_mfma_f32_16x16x32_bf16 a[16:19], a[252:255], v[112:115], a[16:19]\n"
"ds_read_b64_tr_b16 a[242:243], v252 offset:10240\n"
"buffer_store_dwordx4 v[144:147], v255, s[52:55], s80 offen offset:0 sc1\n"
"v_mfma_f32_16x16x32_bf16 a[20:23], a[248:251], v[100:103], a[20:23]\n"
"v_mul_f32 v154, v154, v234\n"
"v_mul_f32 v155, v155, v234\n"
"v_mfma_f32_16x16x32_bf16 a[20:23], a[252:255], v[116:119], a[20:23]\n"
"v_mul_f32 v156, v156, v235\n"
"v_mul_f32 v157, v157, v235\n"
"v_mul_f32 v158, v158, v235\n"
"v_mfma_f32_16x16x32_bf16 a[24:27], a[248:251], v[104:107], a[24:27]\n"
"ds_read_b64_tr_b16 a[244:245], v252 offset:12288\n"
"v_mul_f32 v159, v159, v235\n"
"v_mfma_f32_16x16x32_bf16 a[24:27], a[252:255], v[120:123], a[24:27]\n"
"ds_read_b64_tr_b16 a[246:247], v252 offset:14336\n"
"v_cvt_pk_bf16_f32 v152, v152, v153\n"
"v_mfma_f32_16x16x32_bf16 a[28:31], a[248:251], v[108:111], a[28:31]\n"
"v_cvt_pk_bf16_f32 v153, v154, v155\n"
"v_cvt_pk_bf16_f32 v154, v156, v157\n"
"v_cvt_pk_bf16_f32 v155, v158, v159\n"
"v_mfma_f32_16x16x32_bf16 a[28:31], a[252:255], v[124:127], a[28:31]\n"
"s_waitcnt lgkmcnt(4)\n"
"buffer_store_dwordx4 v[152:155], v255, s[52:55], s80 offen offset:1024 sc1\n"
"v_mfma_f32_16x16x32_bf16 v[176:179], a[232:235], v[0:3], 0\n"
"ds_read_b64_tr_b16 a[248:249], v252 offset:32768\n"
"v_mfma_f32_16x16x32_bf16 v[176:179], a[236:239], v[16:19], v[176:179]\n"
"ds_read_b64_tr_b16 a[250:251], v252 offset:34816\n"
"v_mfma_f32_16x16x32_bf16 v[180:183], a[232:235], v[4:7], 0\n"
"ds_write_b128 v237, a[16:19] offset:4096\n"
"v_mfma_f32_16x16x32_bf16 v[180:183], a[236:239], v[20:23], v[180:183]\n"
"v_mfma_f32_16x16x32_bf16 v[184:187], a[232:235], v[8:11], 0\n"
"ds_read_b64_tr_b16 a[252:253], v252 offset:36864\n"
"v_mfma_f32_16x16x32_bf16 v[184:187], a[236:239], v[24:27], v[184:187]\n"
"ds_read_b64_tr_b16 a[254:255], v252 offset:38912\n"
"v_mfma_f32_16x16x32_bf16 v[188:191], a[232:235], v[12:15], 0\n"
"ds_write_b128 v237, a[20:23] offset:5120\n"
"v_mfma_f32_16x16x32_bf16 v[188:191], a[236:239], v[28:31], v[188:191]\n"
"s_waitcnt lgkmcnt(6)\n"
"v_mfma_f32_16x16x32_bf16 v[176:179], a[240:243], v[32:35], v[176:179]\n"
"ds_read_b64_tr_b16 a[232:233], v252 offset:40960\n"
"v_mfma_f32_16x16x32_bf16 v[176:179], a[244:247], v[48:51], v[176:179]\n"
"ds_read_b64_tr_b16 a[234:235], v252 offset:43008\n"
"v_mfma_f32_16x16x32_bf16 v[180:183], a[240:243], v[36:39], v[180:183]\n"
"ds_write_b128 v237, a[24:27] offset:6144\n"
"v_mfma_f32_16x16x32_bf16 v[180:183], a[244:247], v[52:55], v[180:183]\n"
"v_mfma_f32_16x16x32_bf16 v[184:187], a[240:243], v[40:43], v[184:187]\n"
"ds_read_b64_tr_b16 a[236:237], v252 offset:45056\n"
"v_mfma_f32_16x16x32_bf16 v[184:187], a[244:247], v[56:59], v[184:187]\n"
"ds_read_b64_tr_b16 a[238:239], v252 offset:47104\n"
"v_mfma_f32_16x16x32_bf16 v[188:191], a[240:243], v[44:47], v[188:191]\n"
"ds_write_b128 v237, a[28:31] offset:7168\n"
"v_mfma_f32_16x16x32_bf16 v[188:191], a[244:247], v[60:63], v[188:191]\n"
"s_waitcnt vmcnt(14)\n"
"v_mfma_f32_16x16x32_bf16 v[192:195], v[226:229], a[32:35], v[192:195]\n"
"v_mfma_f32_16x16x32_bf16 v[196:199], v[224:227], a[32:35], v[196:199]\n"
"v_mfma_f32_16x16x32_bf16 v[200:203], v[226:229], a[36:39], v[200:203]\n"
"v_mfma_f32_16x16x32_bf16 v[204:207], v[224:227], a[36:39], v[204:207]\n"
"v_mfma_f32_16x16x32_bf16 v[208:211], v[226:229], a[40:43], v[208:211]\n"
"v_mfma_f32_16x16x32_bf16 v[212:215], v[224:227], a[40:43], v[212:215]\n"
"v_mfma_f32_16x16x32_bf16 v[216:219], v[226:229], a[44:47], v[216:219]\n"
"v_mfma_f32_16x16x32_bf16 v[220:223], v[224:227], a[44:47], v[220:223]\n"
"s_waitcnt lgkmcnt(7)\n"
"v_mfma_f32_16x16x32_bf16 v[176:179], a[248:251], v[64:67], v[176:179]\n"
"ds_read_b64_tr_b16 a[240:241], v253 offset:0\n"
"v_mfma_f32_16x16x32_bf16 v[176:179], a[252:255], v[80:83], v[176:179]\n"
"ds_read_b64_tr_b16 a[242:243], v253 offset:2048\n"
"v_mfma_f32_16x16x32_bf16 v[180:183], a[248:251], v[68:71], v[180:183]\n"
"s_waitcnt lgkmcnt(2)\n"
"s_barrier\n"
"ds_read_b128 v[128:131], v241 offset:0\n"
"v_mfma_f32_16x16x32_bf16 v[180:183], a[252:255], v[84:87], v[180:183]\n"
"ds_read_b128 v[132:135], v241 offset:1024\n"
"v_mfma_f32_16x16x32_bf16 v[184:187], a[248:251], v[72:75], v[184:187]\n"
"ds_read_b64_tr_b16 a[244:245], v253 offset:4096\n"
"v_mfma_f32_16x16x32_bf16 v[184:187], a[252:255], v[88:91], v[184:187]\n"
"ds_read_b64_tr_b16 a[246:247], v253 offset:6144\n"
"v_mfma_f32_16x16x32_bf16 v[188:191], a[248:251], v[76:79], v[188:191]\n"
"ds_read_b128 v[136:139], v241 offset:2048\n"
"v_mfma_f32_16x16x32_bf16 v[188:191], a[252:255], v[92:95], v[188:191]\n"
"ds_read_b128 v[140:143], v241 offset:3072\n"
"v_mfma_f32_16x16x32_bf16 v[176:179], a[232:235], v[96:99], v[176:179]\n"
"ds_read_b64_tr_b16 a[248:249], v253 offset:8192\n"
"v_mfma_f32_16x16x32_bf16 v[176:179], a[236:239], v[112:115], v[176:179]\n"
"ds_read_b64_tr_b16 a[250:251], v253 offset:10240\n"
"v_mfma_f32_16x16x32_bf16 v[180:183], a[232:235], v[100:103], v[180:183]\n"
"s_waitcnt vmcnt(0)\n"
"s_lshl_b32 s83, s36, 1\n"
"s_add_u32 s83, s83, s39\n"
"buffer_store_dwordx2 v[230:231], off, s[52:55], s83 sc1\n"
"ds_read_b128 v[144:147], v241 offset:4096\n"
"v_cmp_ne_u64 vcc, v[230:231], v[238:239]\n"
"s_cbranch_vccnz L_FS3\n"
"L_FOK3:\n"
"v_mfma_f32_16x16x32_bf16 v[180:183], a[236:239], v[116:119], v[180:183]\n"
"ds_read_b128 v[148:151], v241 offset:5120\n"
"v_mfma_f32_16x16x32_bf16 v[184:187], a[232:235], v[104:107], v[184:187]\n"
"s_lshl_b32 s84, s34, 8\n"
"s_add_u32 s84, s84, s68\n"
"buffer_load_dwordx4 a[48:51], v240, s[52:55], s84 offen offset:0 sc1\n"
"ds_read_b64_tr_b16 a[252:253], v253 offset:12288\n"
"v_mfma_f32_16x16x32_bf16 v[184:187], a[236:239], v[120:123], v[184:187]\n"
"ds_read_b64_tr_b16 a[254:255], v253 offset:14336\n"
"s_waitcnt lgkmcnt(6)\n"
"v_mfma_f32_16x16x32_bf16 v[188:191], a[232:235], v[108:111], v[188:191]\n"
"ds_read_b128 v[152:155], v241 offset:6144\n"
"s_lshl_b32 s79, s37, 11\n"
"v_mfma_f32_16x16x32_bf16 v[188:191], a[236:239], v[124:127], v[188:191]\n"
"ds_read_b128 v[156:159], v241 offset:7168\n"
"v_mfma_f32_16x16x32_bf16 v[160:163], a[240:243], v[0:3], 0\n"
"ds_read_b64_tr_b16 a[232:233], v253 offset:32768\n"
"v_mfma_f32_16x16x32_bf16 v[160:163], a[244:247], v[16:19], v[160:163]\n"
"ds_read_b64_tr_b16 a[234:235], v253 offset:34816\n"
"v_fma_f32 v128, v176, v244, v128\n"
"v_mfma_f32_16x16x32_bf16 v[164:167], a[240:243], v[4:7], 0\n"
"v_fma_f32 v129, v177, v244, v129\n"
"v_fma_f32 v130, v178, v244, v130\n"
"v_mfma_f32_16x16x32_bf16 v[164:167], a[244:247], v[20:23], v[164:167]\n"
"v_fma_f32 v131, v179, v244, v131\n"
"v_fma_f32 v132, v180, v245, v132\n"
"v_fma_f32 v133, v181, v245, v133\n"
"v_mfma_f32_16x16x32_bf16 v[168:171], a[240:243], v[8:11], 0\n"
"s_lshl_b32 s84, s34, 8\n"
"s_add_u32 s84, s84, s68\n"
"buffer_load_dwordx4 a[52:55], v240, s[52:55], s84 offen offset:1024 sc1\n"
"ds_read_b64_tr_b16 a[236:237], v253 offset:36864\n"
"v_mfma_f32_16x16x32_bf16 v[168:171], a[244:247], v[24:27], v[168:171]\n"
"ds_read_b64_tr_b16 a[238:239], v253 offset:38912\n"
"v_fma_f32 v134, v182, v245, v134\n"
"v_fma_f32 v135, v183, v245, v135\n"
"v_mfma_f32_16x16x32_bf16 v[172:175], a[240:243], v[12:15], 0\n"
"v_fma_f32 v136, v184, v246, v136\n"
"v_fma_f32 v137, v185, v246, v137\n"
"v_mfma_f32_16x16x32_bf16 v[172:175], a[244:247], v[28:31], v[172:175]\n"
"s_waitcnt lgkmcnt(6)\n"
"v_fma_f32 v138, v186, v246, v138\n"
"v_fma_f32 v139, v187, v246, v139\n"
"s_waitcnt lgkmcnt(4)\n"
"v_mfma_f32_16x16x32_bf16 v[160:163], a[248:251], v[32:35], v[160:163]\n"
"ds_read_b64_tr_b16 a[240:241], v253 offset:40960\n"
"v_fma_f32 v140, v188, v247, v140\n"
"v_fma_f32 v141, v189, v247, v141\n"
"v_mfma_f32_16x16x32_bf16 v[160:163], a[252:255], v[48:51], v[160:163]\n"
"ds_read_b64_tr_b16 a[242:243], v253 offset:43008\n"
"v_fma_f32 v142, v190, v247, v142\n"
"v_mfma_f32_16x16x32_bf16 v[164:167], a[248:251], v[36:39], v[164:167]\n"
"v_fma_f32 v143, v191, v247, v143\n"
"v_mul_f32 v128, v128, v232\n"
"v_mul_f32 v129, v129, v232\n"
"v_mfma_f32_16x16x32_bf16 v[164:167], a[252:255], v[52:55], v[164:167]\n"
"v_mul_f32 v130, v130, v232\n"
"v_mul_f32 v131, v131, v232\n"
"v_mfma_f32_16x16x32_bf16 v[168:171], a[248:251], v[40:43], v[168:171]\n"
"s_lshl_b32 s84, s34, 8\n"
"s_add_u32 s84, s84, s69\n"
"buffer_load_dwordx4 a[56:59], v240, s[52:55], s84 offen offset:0 sc1\n"
"ds_read_b64_tr_b16 a[244:245], v253 offset:45056\n"
"v_mfma_f32_16x16x32_bf16 v[168:171], a[252:255], v[56:59], v[168:171]\n"
"ds_read_b64_tr_b16 a[246:247], v253 offset:47104\n"
"v_mul_f32 v132, v132, v233\n"
"v_mul_f32 v133, v133, v233\n"
"v_mfma_f32_16x16x32_bf16 v[172:175], a[248:251], v[44:47], v[172:175]\n"
"v_mul_f32 v134, v134, v233\n"
"v_mul_f32 v135, v135, v233\n"
"v_mfma_f32_16x16x32_bf16 v[172:175], a[252:255], v[60:63], v[172:175]\n"
"s_waitcnt lgkmcnt(4)\n"
"v_cvt_pk_bf16_f32 v131, v130, v131\n"
"v_cvt_pk_bf16_f32 v130, v128, v129\n"
"v_mov_b64 v[128:129], v[230:231]\n"
"v_mfma_f32_16x16x32_bf16 v[160:163], a[232:235], v[64:67], v[160:163]\n"
"ds_read_b64_tr_b16 a[248:249], v250 offset:512\n"
"v_cvt_pk_bf16_f32 v132, v132, v133\n"
"v_mfma_f32_16x16x32_bf16 v[160:163], a[236:239], v[80:83], v[160:163]\n"
"ds_read_b64_tr_b16 a[250:251], v250 offset:2560\n"
"v_cvt_pk_bf16_f32 v133, v134, v135\n"
"v_mfma_f32_16x16x32_bf16 v[164:167], a[232:235], v[68:71], v[164:167]\n"
"buffer_store_dwordx4 v[128:131], v255, s[52:55], s79 offen offset:0 sc1\n"
"v_mov_b64 v[134:135], v[230:231]\n"
"v_mfma_f32_16x16x32_bf16 v[164:167], a[236:239], v[84:87], v[164:167]\n"
"buffer_store_dwordx4 v[132:135], v255, s[52:55], s79 offen offset:1024 sc1\n"
"v_mfma_f32_16x16x32_bf16 v[168:171], a[232:235], v[72:75], v[168:171]\n"
"s_lshl_b32 s84, s34, 8\n"
"s_add_u32 s84, s84, s69\n"
"buffer_load_dwordx4 a[60:63], v240, s[52:55], s84 offen offset:1024 sc1\n"
"ds_read_b64_tr_b16 a[252:253], v250 offset:4608\n"
"v_mfma_f32_16x16x32_bf16 v[168:171], a[236:239], v[88:91], v[168:171]\n"
"ds_read_b64_tr_b16 a[254:255], v250 offset:6656\n"
"v_mul_f32 v136, v136, v234\n"
"v_mfma_f32_16x16x32_bf16 v[172:175], a[232:235], v[76:79], v[172:175]\n"
"v_mul_f32 v137, v137, v234\n"
"v_mul_f32 v138, v138, v234\n"
"v_mfma_f32_16x16x32_bf16 v[172:175], a[236:239], v[92:95], v[172:175]\n"
"s_waitcnt lgkmcnt(4)\n"
"v_mul_f32 v139, v139, v234\n"
"v_mul_f32 v140, v140, v235\n"
"v_mul_f32 v141, v141, v235\n"
"v_mfma_f32_16x16x32_bf16 v[160:163], a[240:243], v[96:99], v[160:163]\n"
"ds_read_b64_tr_b16 a[232:233], v250 offset:8704\n"
"v_mul_f32 v142, v142, v235\n"
"v_mfma_f32_16x16x32_bf16 v[160:163], a[244:247], v[112:115], v[160:163]\n"
"ds_read_b64_tr_b16 a[234:235], v250 offset:10752\n"
"v_mul_f32 v143, v143, v235\n"
"v_cvt_pk_bf16_f32 v139, v138, v139\n"
"v_mfma_f32_16x16x32_bf16 v[164:167], a[240:243], v[100:103], v[164:167]\n"
"v_cvt_pk_bf16_f32 v138, v136, v137\n"
"v_mov_b64 v[136:137], v[230:231]\n"
"v_mfma_f32_16x16x32_bf16 v[164:167], a[244:247], v[116:119], v[164:167]\n"
"buffer_store_dwordx4 v[136:139], v255, s[52:55], s79 offen offset:2048 sc1\n"
"v_mfma_f32_16x16x32_bf16 v[168:171], a[240:243], v[104:107], v[168:171]\n"
"ds_read_b64_tr_b16 a[236:237], v250 offset:12800\n"
"v_cvt_pk_bf16_f32 v140, v140, v141\n"
"v_cvt_pk_bf16_f32 v141, v142, v143\n"
"v_mfma_f32_16x16x32_bf16 v[168:171], a[244:247], v[120:123], v[168:171]\n"
"ds_read_b64_tr_b16 a[238:239], v250 offset:14848\n"
"v_mov_b64 v[142:143], v[230:231]\n"
"v_mfma_f32_16x16x32_bf16 v[172:175], a[240:243], v[108:111], v[172:175]\n"
"buffer_store_dwordx4 v[140:143], v255, s[52:55], s79 offen offset:3072 sc1\n"
"v_mfma_f32_16x16x32_bf16 v[172:175], a[244:247], v[124:127], v[172:175]\n"
"s_waitcnt lgkmcnt(4)\n"
"s_lshl_b32 s80, s37, 11\n"
"s_add_u32 s80, s80, 4096\n"
"v_mfma_f32_16x16x32_bf16 a[0:3], a[248:251], v[0:3], 0\n"
"s_barrier\n"
"ds_read_b64_tr_b16 a[240:241], v250 offset:33280\n"
"v_mfma_f32_16x16x32_bf16 a[0:3], a[252:255], v[16:19], a[0:3]\n"
"ds_read_b64_tr_b16 a[242:243], v250 offset:35328\n"
"v_fma_f32 v144, v160, v244, v144\n"
"v_fma_f32 v145, v161, v244, v145\n"
"v_mfma_f32_16x16x32_bf16 a[4:7], a[248:251], v[4:7], 0\n"
"v_fma_f32 v146, v162, v244, v146\n"
"v_fma_f32 v147, v163, v244, v147\n"
"v_mfma_f32_16x16x32_bf16 a[4:7], a[252:255], v[20:23], a[4:7]\n"
"v_fma_f32 v148, v164, v245, v148\n"
"v_fma_f32 v149, v165, v245, v149\n"
"v_mfma_f32_16x16x32_bf16 a[8:11], a[248:251], v[8:11], 0\n"
"ds_read_b64_tr_b16 a[244:245], v250 offset:37376\n"
"v_fma_f32 v150, v166, v245, v150\n"
"v_fma_f32 v151, v167, v245, v151\n"
"v_mfma_f32_16x16x32_bf16 a[8:11], a[252:255], v[24:27], a[8:11]\n"
"ds_read_b64_tr_b16 a[246:247], v250 offset:39424\n"
"v_fma_f32 v152, v168, v246, v152\n"
"v_mfma_f32_16x16x32_bf16 a[12:15], a[248:251], v[12:15], 0\n"
"v_fma_f32 v153, v169, v246, v153\n"
"v_fma_f32 v154, v170, v246, v154\n"
"v_fma_f32 v155, v171, v246, v155\n"
"v_mfma_f32_16x16x32_bf16 a[12:15], a[252:255], v[28:31], a[12:15]\n"
"s_waitcnt lgkmcnt(4)\n"
"v_fma_f32 v156, v172, v247, v156\n"
"v_fma_f32 v157, v173, v247, v157\n"
"v_mfma_f32_16x16x32_bf16 a[0:3], a[232:235], v[32:35], a[0:3]\n"
"ds_read_b64_tr_b16 a[248:249], v250 offset:41472\n"
"v_fma_f32 v158, v174, v247, v158\n"
"v_mfma_f32_16x16x32_bf16 a[0:3], a[236:239], v[48:51], a[0:3]\n"
"ds_read_b64_tr_b16 a[250:251], v250 offset:43520\n"
"v_fma_f32 v159, v175, v247, v159\n"
"v_mul_f32 v144, v144, v232\n"
"v_mfma_f32_16x16x32_bf16 a[4:7], a[232:235], v[36:39], a[4:7]\n"
"v_mul_f32 v145, v145, v232\n"
"v_mul_f32 v146, v146, v232\n"
"v_mfma_f32_16x16x32_bf16 a[4:7], a[236:239], v[52:55], a[4:7]\n"
"v_mul_f32 v147, v147, v232\n"
"v_mul_f32 v148, v148, v233\n"
"v_mfma_f32_16x16x32_bf16 a[8:11], a[232:235], v[40:43], a[8:11]\n"
"ds_read_b64_tr_b16 a[252:253], v250 offset:45568\n"
"v_mul_f32 v149, v149, v233\n"
"v_mul_f32 v150, v150, v233\n"
"v_mfma_f32_16x16x32_bf16 a[8:11], a[236:239], v[56:59], a[8:11]\n"
"ds_read_b64_tr_b16 a[254:255], v250 offset:47616\n"
"v_mul_f32 v151, v151, v233\n"
"v_mfma_f32_16x16x32_bf16 a[12:15], a[232:235], v[44:47], a[12:15]\n"
"v_cvt_pk_bf16_f32 v147, v146, v147\n"
"v_cvt_pk_bf16_f32 v146, v144, v145\n"
"v_mfma_f32_16x16x32_bf16 a[12:15], a[236:239], v[60:63], a[12:15]\n"
"s_waitcnt lgkmcnt(4)\n"
"v_mov_b64 v[144:145], v[230:231]\n"
"v_cvt_pk_bf16_f32 v148, v148, v149\n"
"v_cvt_pk_bf16_f32 v149, v150, v151\n"
"v_mfma_f32_16x16x32_bf16 a[0:3], a[240:243], v[64:67], a[0:3]\n"
"ds_read_b64_tr_b16 a[232:233], v251 offset:512\n"
"v_mov_b64 v[150:151], v[230:231]\n"
"v_mfma_f32_16x16x32_bf16 a[0:3], a[244:247], v[80:83], a[0:3]\n"
"ds_read_b64_tr_b16 a[234:235], v251 offset:2560\n"
"v_mul_f32 v152, v152, v234\n"
"v_mfma_f32_16x16x32_bf16 a[4:7], a[240:243], v[68:71], a[4:7]\n"
"buffer_store_dwordx4 v[144:147], v255, s[52:55], s80 offen offset:0 sc1\n"
"v_mul_f32 v153, v153, v234\n"
"v_mfma_f32_16x16x32_bf16 a[4:7], a[244:247], v[84:87], a[4:7]\n"
"s_lshl_b32 s83, s33, 4\n"
"s_add_u32 s83, s83, s67\n"
"v_mfma_f32_16x16x32_bf16 a[8:11], a[240:243], v[72:75], a[8:11]\n"
"ds_read_b64_tr_b16 a[236:237], v251 offset:4608\n"
"buffer_load_dwordx2 v[238:239], off, s[52:55], s83 sc1\n"
"buffer_load_dwordx2 v[248:249], off, s[52:55], s77 sc1\n"
"v_mfma_f32_16x16x32_bf16 a[8:11], a[244:247], v[88:91], a[8:11]\n"
"ds_read_b64_tr_b16 a[238:239], v251 offset:6656\n"
"buffer_store_dwordx4 v[148:151], v255, s[52:55], s80 offen offset:1024 sc1\n"
"v_mfma_f32_16x16x32_bf16 a[12:15], a[240:243], v[76:79], a[12:15]\n"
"L_CW12:\n"
"s_mov_b32 s75, L_CW12-L_PC+3072\n"
"buffer_load_dword a127, v254, s[56:59], s75 offen\n"
"v_mfma_f32_16x16x32_bf16 a[12:15], a[244:247], v[92:95], a[12:15]\n"
"s_waitcnt lgkmcnt(4)\n"
"v_mul_f32 v154, v154, v234\n"
"v_mul_f32 v155, v155, v234\n"
"v_mfma_f32_16x16x32_bf16 a[0:3], a[248:251], v[96:99], a[0:3]\n"
"ds_read_b64_tr_b16 a[240:241], v251 offset:8704\n"
"v_mul_f32 v156, v156, v235\n"
"v_mul_f32 v157, v157, v235\n"
"v_mfma_f32_16x16x32_bf16 a[0:3], a[252:255], v[112:115], a[0:3]\n"
"ds_read_b64_tr_b16 a[242:243], v251 offset:10752\n"
"v_mul_f32 v158, v158, v235\n"
"v_mfma_f32_16x16x32_bf16 a[4:7], a[248:251], v[100:103], a[4:7]\n"
"v_mul_f32 v159, v159, v235\n"
"v_cvt_pk_bf16_f32 v155, v154, v155\n"
"v_cvt_pk_bf16_f32 v154, v152, v153\n"
"v_mfma_f32_16x16x32_bf16 a[4:7], a[252:255], v[116:119], a[4:7]\n"
"v_mov_b64 v[152:153], v[230:231]\n"
"v_cvt_pk_bf16_f32 v156, v156, v157\n"
"v_mfma_f32_16x16x32_bf16 a[8:11], a[248:251], v[104:107], a[8:11]\n"
"ds_read_b64_tr_b16 a[244:245], v251 offset:12800\n"
"v_cvt_pk_bf16_f32 v157, v158, v159\n"
"v_mfma_f32_16x16x32_bf16 a[8:11], a[252:255], v[120:123], a[8:11]\n"
"ds_read_b64_tr_b16 a[246:247], v251 offset:14848\n"
"buffer_store_dwordx4 v[152:155], v255, s[52:55], s80 offen offset:2048 sc1\n"
"v_mfma_f32_16x16x32_bf16 a[12:15], a[248:251], v[108:111], a[12:15]\n"
"v_mov_b64 v[158:159], v[230:231]\n"
"v_mfma_f32_16x16x32_bf16 a[12:15], a[252:255], v[124:127], a[12:15]\n"
"s_waitcnt lgkmcnt(4)\n"
"buffer_store_dwordx4 v[156:159], v255, s[52:55], s80 offen offset:3072 sc1\n"
"v_mfma_f32_16x16x32_bf16 a[16:19], a[232:235], v[0:3], 0\n"
"ds_read_b64_tr_b16 a[248:249], v251 offset:33280\n"
"v_mfma_f32_16x16x32_bf16 a[16:19], a[236:239], v[16:19], a[16:19]\n"
"ds_read_b64_tr_b16 a[250:251], v251 offset:35328\n"
"v_mfma_f32_16x16x32_bf16 a[20:23], a[232:235], v[4:7], 0\n"
"ds_write_b128 v237, a[0:3] offset:0\n"
"v_mfma_f32_16x16x32_bf16 a[20:23], a[236:239], v[20:23], a[20:23]\n"
"v_mfma_f32_16x16x32_bf16 a[24:27], a[232:235], v[8:11], 0\n"
"ds_read_b64_tr_b16 a[252:253], v251 offset:37376\n"
"v_mfma_f32_16x16x32_bf16 a[24:27], a[236:239], v[24:27], a[24:27]\n"
"ds_read_b64_tr_b16 a[254:255], v251 offset:39424\n"
"v_mfma_f32_16x16x32_bf16 a[28:31], a[232:235], v[12:15], 0\n"
"ds_write_b128 v237, a[4:7] offset:1024\n"
"v_mfma_f32_16x16x32_bf16 a[28:31], a[236:239], v[28:31], a[28:31]\n"
"s_waitcnt lgkmcnt(6)\n"
"v_mfma_f32_16x16x32_bf16 a[16:19], a[240:243], v[32:35], a[16:19]\n"
"ds_read_b64_tr_b16 a[232:233], v251 offset:41472\n"
"v_mfma_f32_16x16x32_bf16 a[16:19], a[244:247], v[48:51], a[16:19]\n"
"ds_read_b64_tr_b16 a[234:235], v251 offset:43520\n"
"v_mfma_f32_16x16x32_bf16 a[20:23], a[240:243], v[36:39], a[20:23]\n"
"s_lshl_b32 s83, s32, 8\n"
"s_add_u32 s83, s83, s68\n"
"buffer_load_dwordx4 a[80:83], v240, s[52:55], s83 offen offset:0 sc1\n"
"v_mfma_f32_16x16x32_bf16 a[20:23], a[244:247], v[52:55], a[20:23]\n"
"buffer_load_dwordx4 a[84:87], v240, s[52:55], s83 offen offset:1024 sc1\n"
"v_mfma_f32_16x16x32_bf16 a[24:27], a[240:243], v[40:43], a[24:27]\n"
"ds_read_b64_tr_b16 a[236:237], v251 offset:45568\n"
"v_mfma_f32_16x16x32_bf16 a[24:27], a[244:247], v[56:59], a[24:27]\n"
"ds_read_b64_tr_b16 a[238:239], v251 offset:47616\n"
"v_mfma_f32_16x16x32_bf16 a[28:31], a[240:243], v[44:47], a[28:31]\n"
"buffer_load_dwordx4 a[88:91], v240, s[52:55], s83 offen offset:2048 sc1\n"
"v_mfma_f32_16x16x32_bf16 a[28:31], a[244:247], v[60:63], a[28:31]\n"
"s_waitcnt lgkmcnt(5)\n"
"buffer_load_dwordx4 a[92:95], v240, s[52:55], s83 offen offset:3072 sc1\n"
"v_mfma_f32_16x16x32_bf16 a[16:19], a[248:251], v[64:67], a[16:19]\n"
"ds_read_b64_tr_b16 a[240:241], v250 offset:0\n"
"v_mfma_f32_16x16x32_bf16 a[16:19], a[252:255], v[80:83], a[16:19]\n"
"ds_read_b64_tr_b16 a[242:243], v250 offset:2048\n"
"v_mfma_f32_16x16x32_bf16 a[20:23], a[248:251], v[68:71], a[20:23]\n"
"ds_write_b128 v237, a[8:11] offset:2048\n"
"v_mfma_f32_16x16x32_bf16 a[20:23], a[252:255], v[84:87], a[20:23]\n"
"ds_write_b128 v237, a[12:15] offset:3072\n"
"v_mfma_f32_16x16x32_bf16 a[24:27], a[248:251], v[72:75], a[24:27]\n"
"ds_read_b64_tr_b16 a[244:245], v250 offset:4096\n"
"v_mfma_f32_16x16x32_bf16 a[24:27], a[252:255], v[88:91], a[24:27]\n"
"ds_read_b64_tr_b16 a[246:247], v250 offset:6144\n"
"v_mfma_f32_16x16x32_bf16 a[28:31], a[248:251], v[76:79], a[28:31]\n"
"v_mfma_f32_16x16x32_bf16 a[28:31], a[252:255], v[92:95], a[28:31]\n"
"s_waitcnt lgkmcnt(6)\n"
"v_mfma_f32_16x16x32_bf16 a[16:19], a[232:235], v[96:99], a[16:19]\n"
"ds_read_b64_tr_b16 a[248:249], v250 offset:8192\n"
"v_mfma_f32_16x16x32_bf16 a[16:19], a[236:239], v[112:115], a[16:19]\n"
"ds_read_b64_tr_b16 a[250:251], v250 offset:10240\n"
"v_mfma_f32_16x16x32_bf16 a[20:23], a[232:235], v[100:103], a[20:23]\n"
"v_mfma_f32_16x16x32_bf16 a[20:23], a[236:239], v[116:119], a[20:23]\n"
"v_mfma_f32_16x16x32_bf16 a[24:27], a[232:235], v[104:107], a[24:27]\n"
"ds_read_b64_tr_b16 a[252:253], v250 offset:12288\n"
"v_mfma_f32_16x16x32_bf16 a[24:27], a[236:239], v[120:123], a[24:27]\n"
"ds_read_b64_tr_b16 a[254:255], v250 offset:14336\n"
"v_mfma_f32_16x16x32_bf16 a[28:31], a[232:235], v[108:111], a[28:31]\n"
"v_mfma_f32_16x16x32_bf16 a[28:31], a[236:239], v[124:127], a[28:31]\n"
"s_waitcnt lgkmcnt(4)\n"
"v_mfma_f32_16x16x32_bf16 v[176:179], a[240:243], v[0:3], 0\n"
"ds_read_b64_tr_b16 a[232:233], v250 offset:32768\n"
"v_mfma_f32_16x16x32_bf16 v[176:179], a[244:247], v[16:19], v[176:179]\n"
"ds_read_b64_tr_b16 a[234:235], v250 offset:34816\n"
"v_mfma_f32_16x16x32_bf16 v[180:183], a[240:243], v[4:7], 0\n"
"ds_write_b128 v237, a[16:19] offset:4096\n"
"v_mfma_f32_16x16x32_bf16 v[180:183], a[244:247], v[20:23], v[180:183]\n"
"v_mfma_f32_16x16x32_bf16 v[184:187], a[240:243], v[8:11], 0\n"
"ds_read_b64_tr_b16 a[236:237], v250 offset:36864\n"
"v_mfma_f32_16x16x32_bf16 v[184:187], a[244:247], v[24:27], v[184:187]\n"
"ds_read_b64_tr_b16 a[238:239], v250 offset:38912\n"
"v_mfma_f32_16x16x32_bf16 v[188:191], a[240:243], v[12:15], 0\n"
"s_waitcnt vmcnt(9)\n"
"v_cmp_ne_u64 vcc, v[230:231], v[238:239]\n"
"s_cbranch_vccnz L_FS4\n"
"L_FOK4:\n"
"s_waitcnt vmcnt(8)\n"
"v_cmp_ne_u64 vcc, v[230:231], v[248:249]\n"
"s_cbranch_vccnz L_FSx\n"
"L_FOKx:\n"
"v_mfma_f32_16x16x32_bf16 v[188:191], a[244:247], v[28:31], v[188:191]\n"
"s_waitcnt lgkmcnt(5)\n"
"ds_write_b128 v237, a[20:23] offset:5120\n"
"v_mfma_f32_16x16x32_bf16 v[176:179], a[248:251], v[32:35], v[176:179]\n"
"s_lshl_b32 s84, s33, 8\n"
"s_add_u32 s84, s84, s68\n"
"buffer_load_dwordx4 a[32:35], v240, s[52:55], s84 offen offset:0 sc1\n"
"ds_read_b64_tr_b16 a[240:241], v250 offset:40960\n"
"v_mfma_f32_16x16x32_bf16 v[176:179], a[252:255], v[48:51], v[176:179]\n"
"ds_read_b64_tr_b16 a[242:243], v250 offset:43008\n"
"v_mfma_f32_16x16x32_bf16 v[180:183], a[248:251], v[36:39], v[180:183]\n"
"ds_write_b128 v237, a[24:27] offset:6144\n"
"v_mfma_f32_16x16x32_bf16 v[180:183], a[252:255], v[52:55], v[180:183]\n"
"v_mfma_f32_16x16x32_bf16 v[184:187], a[248:251], v[40:43], v[184:187]\n"
"s_lshl_b32 s84, s33, 8\n"
"s_add_u32 s84, s84, s68\n"
"buffer_load_dwordx4 a[36:39], v240, s[52:55], s84 offen offset:1024 sc1\n"
"ds_read_b64_tr_b16 a[244:245], v250 offset:45056\n"
"v_mfma_f32_16x16x32_bf16 v[184:187], a[252:255], v[56:59], v[184:187]\n"
"ds_read_b64_tr_b16 a[246:247], v250 offset:47104\n"
"v_mfma_f32_16x16x32_bf16 v[188:191], a[248:251], v[44:47], v[188:191]\n"
"ds_write_b128 v237, a[28:31] offset:7168\n"
"v_mfma_f32_16x16x32_bf16 v[188:191], a[252:255], v[60:63], v[188:191]\n"
"v_mfma_f32_16x16x32_bf16 v[192:195], v[226:229], a[48:51], v[192:195]\n"
"v_mfma_f32_16x16x32_bf16 v[196:199], v[224:227], a[48:51], v[196:199]\n"
"v_mfma_f32_16x16x32_bf16 v[200:203], v[226:229], a[52:55], v[200:203]\n"
"v_mfma_f32_16x16x32_bf16 v[204:207], v[224:227], a[52:55], v[204:207]\n"
"v_mfma_f32_16x16x32_bf16 v[208:211], v[226:229], a[56:59], v[208:211]\n"
"v_mfma_f32_16x16x32_bf16 v[212:215], v[224:227], a[56:59], v[212:215]\n"
"v_mfma_f32_16x16x32_bf16 v[216:219], v[226:229], a[60:63], v[216:219]\n"
"v_mfma_f32_16x16x32_bf16 v[220:223], v[224:227], a[60:63], v[220:223]\n"
"s_waitcnt lgkmcnt(7)\n"
"v_mfma_f32_16x16x32_bf16 v[176:179], a[232:235], v[64:67], v[176:179]\n"
"s_lshl_b32 s84, s33, 8\n"
"s_add_u32 s84, s84, s69\n"
"buffer_load_dwordx4 a[40:43], v240, s[52:55], s84 offen offset:0 sc1\n"
"ds_read_b64_tr_b16 a[248:249], v251 offset:0\n"
"v_mfma_f32_16x16x32_bf16 v[176:179], a[236:239], v[80:83], v[176:179]\n"
"ds_read_b64_tr_b16 a[250:251], v251 offset:2048\n"
"v_mfma_f32_16x16x32_bf16 v[180:183], a[232:235], v[68:71], v[180:183]\n"
"s_waitcnt lgkmcnt(2)\n"
"s_barrier\n"
"ds_read_b128 v[128:131], v241 offset:0\n"
"v_mfma_f32_16x16x32_bf16 v[180:183], a[236:239], v[84:87], v[180:183]\n"
"ds_read_b128 v[132:135], v241 offset:1024\n"
"v_mfma_f32_16x16x32_bf16 v[184:187], a[232:235], v[72:75], v[184:187]\n"
"s_lshl_b32 s84, s33, 8\n"
"s_add_u32 s84, s84, s69\n"
"buffer_load_dwordx4 a[44:47], v240, s[52:55], s84 offen offset:1024 sc1\n"
"ds_read_b64_tr_b16 a[252:253], v251 offset:4096\n"
"v_mfma_f32_16x16x32_bf16 v[184:187], a[236:239], v[88:91], v[184:187]\n"
"ds_read_b64_tr_b16 a[254:255], v251 offset:6144\n"
"v_mfma_f32_16x16x32_bf16 v[188:191], a[232:235], v[76:79], v[188:191]\n"
"ds_read_b128 v[136:139], v241 offset:2048\n"
"v_mfma_f32_16x16x32_bf16 v[188:191], a[236:239], v[92:95], v[188:191]\n"
"ds_read_b128 v[140:143], v241 offset:3072\n"
"v_mfma_f32_16x16x32_bf16 v[176:179], a[240:243], v[96:99], v[176:179]\n"
"s_add_u32 s84, s76, s68\n"
"buffer_load_dwordx4 a[64:67], v240, s[52:55], s84 offen offset:0 sc1\n"
"ds_read_b64_tr_b16 a[232:233], v251 offset:8192\n"
"v_mfma_f32_16x16x32_bf16 v[176:179], a[244:247], v[112:115], v[176:179]\n"
"ds_read_b64_tr_b16 a[234:235], v251 offset:10240\n"
"v_mfma_f32_16x16x32_bf16 v[180:183], a[240:243], v[100:103], v[180:183]\n"
"ds_read_b128 v[144:147], v241 offset:4096\n"
"v_mfma_f32_16x16x32_bf16 v[180:183], a[244:247], v[116:119], v[180:183]\n"
"ds_read_b128 v[148:151], v241 offset:5120\n"
"v_mfma_f32_16x16x32_bf16 v[184:187], a[240:243], v[104:107], v[184:187]\n"
"s_add_u32 s84, s76, s68\n"
"buffer_load_dwordx4 a[68:71], v240, s[52:55], s84 offen offset:1024 sc1\n"
"ds_read_b64_tr_b16 a[236:237], v251 offset:12288\n"
"v_mfma_f32_16x16x32_bf16 v[184:187], a[244:247], v[120:123], v[184:187]\n"
"ds_read_b64_tr_b16 a[238:239], v251 offset:14336\n"
"s_waitcnt lgkmcnt(6)\n"
"v_mfma_f32_16x16x32_bf16 v[188:191], a[240:243], v[108:111], v[188:191]\n"
"ds_read_b128 v[152:155], v241 offset:6144\n"
"v_mfma_f32_16x16x32_bf16 v[188:191], a[244:247], v[124:127], v[188:191]\n"
"ds_read_b128 v[156:159], v241 offset:7168\n"
"v_mfma_f32_16x16x32_bf16 v[160:163], a[248:251], v[0:3], 0\n"
"s_add_u32 s84, s76, s69\n"
"buffer_load_dwordx4 a[72:75], v240, s[52:55], s84 offen offset:0 sc1\n"
"ds_read_b64_tr_b16 a[240:241], v251 offset:32768\n"
"v_mfma_f32_16x16x32_bf16 v[160:163], a[252:255], v[16:19], v[160:163]\n"
"ds_read_b64_tr_b16 a[242:243], v251 offset:34816\n"
"v_fma_f32 v128, v176, v244, v128\n"
"v_mfma_f32_16x16x32_bf16 v[164:167], a[248:251], v[4:7], 0\n"
"v_fma_f32 v129, v177, v244, v129\n"
"v_fma_f32 v130, v178, v244, v130\n"
"v_fma_f32 v131, v179, v244, v131\n"
"v_mfma_f32_16x16x32_bf16 v[164:167], a[252:255], v[20:23], v[164:167]\n"
"v_fma_f32 v132, v180, v245, v132\n"
"v_fma_f32 v133, v181, v245, v133\n"
"v_mfma_f32_16x16x32_bf16 v[168:171], a[248:251], v[8:11], 0\n"
"s_add_u32 s84, s76, s69\n"
"buffer_load_dwordx4 a[76:79], v240, s[52:55], s84 offen offset:1024 sc1\n"
"ds_read_b64_tr_b16 a[244:245], v251 offset:36864\n"
"v_mfma_f32_16x16x32_bf16 v[168:171], a[252:255], v[24:27], v[168:171]\n"
"ds_read_b64_tr_b16 a[246:247], v251 offset:38912\n"
"v_fma_f32 v134, v182, v245, v134\n"
"v_mfma_f32_16x16x32_bf16 v[172:175], a[248:251], v[12:15], 0\n"
"s_lshl_b32 s83, s32, 8\n"
"s_add_u32 s83, s83, s69\n"
"buffer_load_dwordx4 a[96:99], v240, s[52:55], s83 offen offset:0 sc1\n"
"v_mfma_f32_16x16x32_bf16 v[172:175], a[252:255], v[28:31], v[172:175]\n"
"s_waitcnt lgkmcnt(6)\n"
"buffer_load_dwordx4 a[100:103], v240, s[52:55], s83 offen offset:1024 sc1\n"
"v_mfma_f32_16x16x32_bf16 v[160:163], a[232:235], v[32:35], v[160:163]\n"
"ds_read_b64_tr_b16 a[248:249], v251 offset:40960\n"
"v_fma_f32 v135, v183, v245, v135\n"
"v_mfma_f32_16x16x32_bf16 v[160:163], a[236:239], v[48:51], v[160:163]\n"
"ds_read_b64_tr_b16 a[250:251], v251 offset:43008\n"
"v_fma_f32 v136, v184, v246, v136\n"
"v_fma_f32 v137, v185, v246, v137\n"
"v_mfma_f32_16x16x32_bf16 v[164:167], a[232:235], v[36:39], v[164:167]\n"
"buffer_load_dwordx4 a[104:107], v240, s[52:55], s83 offen offset:2048 sc1\n"
"v_mfma_f32_16x16x32_bf16 v[164:167], a[236:239], v[52:55], v[164:167]\n"
"buffer_load_dwordx4 a[108:111], v240, s[52:55], s83 offen offset:3072 sc1\n"
"v_mfma_f32_16x16x32_bf16 v[168:171], a[232:235], v[40:43], v[168:171]\n"
"ds_read_b64_tr_b16 a[252:253], v251 offset:45056\n"
"v_fma_f32 v138, v186, v246, v138\n"
"v_fma_f32 v139, v187, v246, v139\n"
"v_mfma_f32_16x16x32_bf16 v[168:171], a[236:239], v[56:59], v[168:171]\n"
"ds_read_b64_tr_b16 a[254:255], v251 offset:47104\n"
"v_fma_f32 v140, v188, v247, v140\n"
"v_mfma_f32_16x16x32_bf16 v[172:175], a[232:235], v[44:47], v[172:175]\n"
"v_fma_f32 v141, v189, v247, v141\n"
"v_fma_f32 v142, v190, v247, v142\n"
"v_mfma_f32_16x16x32_bf16 v[172:175], a[236:239], v[60:63], v[172:175]\n"
"s_waitcnt lgkmcnt(4)\n"
"v_fma_f32 v143, v191, v247, v143\n"
"v_mul_f32 v128, v128, v232\n"
"v_mfma_f32_16x16x32_bf16 v[160:163], a[240:243], v[64:67], v[160:163]\n"
"v_mul_f32 v129, v129, v232\n"
"v_mul_f32 v130, v130, v232\n"
"v_mul_f32 v131, v131, v232\n"
"v_mfma_f32_16x16x32_bf16 v[160:163], a[244:247], v[80:83], v[160:163]\n"
"v_mul_f32 v132, v132, v233\n"
"v_mul_f32 v133, v133, v233\n"
"v_mfma_f32_16x16x32_bf16 v[164:167], a[240:243], v[68:71], v[164:167]\n"
"v_mul_f32 v134, v134, v233\n"
"v_mul_f32 v135, v135, v233\n"
"v_mfma_f32_16x16x32_bf16 v[164:167], a[244:247], v[84:87], v[164:167]\n"
"v_mov_b64 v[176:177], v[230:231]\n"
"v_cvt_pk_bf16_f32 v178, v128, v129\n"
"v_cvt_pk_bf16_f32 v179, v130, v131\n"
"v_mfma_f32_16x16x32_bf16 v[168:171], a[240:243], v[72:75], v[168:171]\n"
"v_cvt_pk_bf16_f32 v180, v132, v133\n"
"v_cvt_pk_bf16_f32 v181, v134, v135\n"
"v_mfma_f32_16x16x32_bf16 v[168:171], a[244:247], v[88:91], v[168:171]\n"
"v_mov_b64 v[182:183], v[230:231]\n"
"v_mul_f32 v136, v136, v234\n"
"v_mfma_f32_16x16x32_bf16 v[172:175], a[240:243], v[76:79], v[172:175]\n"
"buffer_store_dwordx4 v[176:179], v255, s[52:55], s70 offen offset:0 sc1\n"
"v_mul_f32 v137, v137, v234\n"
"v_mfma_f32_16x16x32_bf16 v[172:175], a[244:247], v[92:95], v[172:175]\n"
"s_waitcnt lgkmcnt(0)\n"
"buffer_store_dwordx4 v[180:183], v255, s[52:55], s70 offen offset:1024 sc1\n"
"v_mfma_f32_16x16x32_bf16 v[160:163], a[248:251], v[96:99], v[160:163]\n"
"v_mul_f32 v138, v138, v234\n"
"v_mul_f32 v139, v139, v234\n"
"v_mfma_f32_16x16x32_bf16 v[160:163], a[252:255], v[112:115], v[160:163]\n"
"v_mul_f32 v140, v140, v235\n"
"v_mul_f32 v141, v141, v235\n"
"v_mfma_f32_16x16x32_bf16 v[164:167], a[248:251], v[100:103], v[164:167]\n"
"v_mul_f32 v142, v142, v235\n"
"v_mul_f32 v143, v143, v235\n"
"v_mov_b64 v[184:185], v[230:231]\n"
"v_mfma_f32_16x16x32_bf16 v[164:167], a[252:255], v[116:119], v[164:167]\n"
"v_cvt_pk_bf16_f32 v186, v136, v137\n"
"v_cvt_pk_bf16_f32 v187, v138, v139\n"
"v_mfma_f32_16x16x32_bf16 v[168:171], a[248:251], v[104:107], v[168:171]\n"
"v_cvt_pk_bf16_f32 v188, v140, v141\n"
"v_cvt_pk_bf16_f32 v189, v142, v143\n"
"v_mfma_f32_16x16x32_bf16 v[168:171], a[252:255], v[120:123], v[168:171]\n"
"buffer_store_dwordx4 v[184:187], v255, s[52:55], s70 offen offset:2048 sc1\n"
"v_mov_b64 v[190:191], v[230:231]\n"
"v_mfma_f32_16x16x32_bf16 v[172:175], a[248:251], v[108:111], v[172:175]\n"
"buffer_store_dwordx4 v[188:191], v255, s[52:55], s70 offen offset:3072 sc1\n"
"v_mfma_f32_16x16x32_bf16 v[172:175], a[252:255], v[124:127], v[172:175]\n"
"s_cmp_lt_u32 %[g], 5\n"
"s_cbranch_scc1 L_TAILY\n"
"L_TAILX:\n"
"s_cmp_eq_u32 %[g], 5\n"
"s_cbranch_scc1 L_TAILX5\n"
"s_add_u32 s84, s88, s69\n"
"buffer_load_dwordx4 a[144:147], v240, s[52:55], s84 offen offset:0 sc1\n"
"buffer_load_dwordx4 a[148:151], v240, s[52:55], s84 offen offset:1024 sc1\n"
"buffer_load_dwordx4 a[152:155], v240, s[52:55], s84 offen offset:2048 sc1\n"
"buffer_load_dwordx4 a[156:159], v240, s[52:55], s84 offen offset:3072 sc1\n"
"s_nop 3\n"
"v_fma_f32 v144, v160, v244, v144\n"
"v_fma_f32 v145, v161, v244, v145\n"
"v_fma_f32 v146, v162, v244, v146\n"
"v_fma_f32 v147, v163, v244, v147\n"
"v_fma_f32 v148, v164, v245, v148\n"
"v_fma_f32 v149, v165, v245, v149\n"
"v_fma_f32 v150, v166, v245, v150\n"
"v_fma_f32 v151, v167, v245, v151\n"
"v_fma_f32 v152, v168, v246, v152\n"
"v_fma_f32 v153, v169, v246, v153\n"
"v_fma_f32 v154, v170, v246, v154\n"
"v_fma_f32 v155, v171, v246, v155\n"
"v_fma_f32 v156, v172, v247, v156\n"
"v_fma_f32 v157, v173, v247, v157\n"
"v_fma_f32 v158, v174, v247, v158\n"
"v_fma_f32 v159, v175, v247, v159\n"
"s_waitcnt vmcnt(16)\n"
"v_mfma_f32_16x16x32_bf16 v[208:211], v[226:229], a[40:43], v[208:211]\n"
"v_mfma_f32_16x16x32_bf16 v[212:215], v[224:227], a[40:43], v[212:215]\n"
"v_mfma_f32_16x16x32_bf16 v[216:219], v[226:229], a[44:47], v[216:219]\n"
"v_mfma_f32_16x16x32_bf16 v[220:223], v[224:227], a[44:47], v[220:223]\n"
"s_waitcnt vmcnt(8)\n"
"v_accvgpr_read_b32 v0, a96\n"
"v_accvgpr_read_b32 v1, a97\n"
"v_accvgpr_read_b32 v2, a102\n"
"v_accvgpr_read_b32 v3, a103\n"
"v_accvgpr_read_b32 v4, a104\n"
"v_accvgpr_read_b32 v5, a105\n"
"v_accvgpr_read_b32 v6, a110\n"
"v_accvgpr_read_b32 v7, a111\n"
"v_cmp_ne_u64 s[44:45], v[230:231], v[0:1]\n"
"v_cmp_ne_u64 s[82:83], v[230:231], v[2:3]\n"
"v_cmp_ne_u64 vcc, v[230:231], v[4:5]\n"
"s_or_b64 s[44:45], s[44:45], s[82:83]\n"
"v_cmp_ne_u64 s[82:83], v[230:231], v[6:7]\n"
"s_or_b64 s[44:45], s[44:45], vcc\n"
"s_or_b64 s[44:45], s[44:45], s[82:83]\n"
"s_cbranch_scc1 L_LS4Z\n"
"L_LOK4Z:\n"
"v_mfma_f32_16x16x32_bf16 v[208:211], v[226:229], a[98:101], v[208:211]\n"
"v_mfma_f32_16x16x32_bf16 v[212:215], v[224:227], a[98:101], v[212:215]\n"
"v_mfma_f32_16x16x32_bf16 v[216:219], v[226:229], a[106:109], v[216:219]\n"
"v_mfma_f32_16x16x32_bf16 v[220:223], v[224:227], a[106:109], v[220:223]\n"
"s_waitcnt vmcnt(0)\n"
"v_accvgpr_read_b32 v0, a144\n"
"v_accvgpr_read_b32 v1, a145\n"
"v_accvgpr_read_b32 v2, a150\n"
"v_accvgpr_read_b32 v3, a151\n"
"v_accvgpr_read_b32 v4, a152\n"
"v_accvgpr_read_b32 v5, a153\n"
"v_accvgpr_read_b32 v6, a158\n"
"v_accvgpr_read_b32 v7, a159\n"
"v_cmp_ne_u64 s[44:45], v[230:231], v[0:1]\n"
"v_cmp_ne_u64 s[82:83], v[230:231], v[2:3]\n"
"v_cmp_ne_u64 vcc, v[230:231], v[4:5]\n"
"s_or_b64 s[44:45], s[44:45], s[82:83]\n"
"v_cmp_ne_u64 s[82:83], v[230:231], v[6:7]\n"
"s_or_b64 s[44:45], s[44:45], vcc\n"
"s_or_b64 s[44:45], s[44:45], s[82:83]\n"
"s_cbranch_scc1 L_LS6Z\n"
"L_LOK6Z:\n"
"v_mfma_f32_16x16x32_bf16 v[208:211], v[226:229], a[146:149], v[208:211]\n"
"v_mfma_f32_16x16x32_bf16 v[212:215], v[224:227], a[146:149], v[212:215]\n"
"v_mfma_f32_16x16x32_bf16 v[216:219], v[226:229], a[154:157], v[216:219]\n"
"v_mfma_f32_16x16x32_bf16 v[220:223], v[224:227], a[154:157], v[220:223]\n"
"s_nop 4\n"
"v_fma_f32 v208, v144, v232, v208\n"
"v_fma_f32 v209, v145, v232, v209\n"
"v_fma_f32 v210, v146, v232, v210\n"
"v_fma_f32 v211, v147, v232, v211\n"
"v_cvt_pk_bf16_f32 v208, v208, v209\n"
"v_cvt_pk_bf16_f32 v209, v210, v211\n"
"buffer_store_dwordx2 v[208:209], v236, s[40:43], s74 offen\n"
"v_fma_f32 v212, v148, v233, v212\n"
"v_fma_f32 v213, v149, v233, v213\n"
"v_fma_f32 v214, v150, v233, v214\n"
"v_fma_f32 v215, v151, v233, v215\n"
"v_cvt_pk_bf16_f32 v212, v212, v213\n"
"v_cvt_pk_bf16_f32 v213, v214, v215\n"
"s_add_u32 s84, s74, 28672\n"
"buffer_store_dwordx2 v[212:213], v236, s[40:43], s84 offen\n"
"v_fma_f32 v216, v152, v234, v216\n"
"v_fma_f32 v217, v153, v234, v217\n"
"v_fma_f32 v218, v154, v234, v218\n"
"v_fma_f32 v219, v155, v234, v219\n"
"v_cvt_pk_bf16_f32 v216, v216, v217\n"
"v_cvt_pk_bf16_f32 v217, v218, v219\n"
"s_add_u32 s84, s74, 57344\n"
"buffer_store_dwordx2 v[216:217], v236, s[40:43], s84 offen\n"
"v_fma_f32 v220, v156, v235, v220\n"
"v_fma_f32 v221, v157, v235, v221\n"
"v_fma_f32 v222, v158, v235, v222\n"
"v_fma_f32 v223, v159, v235, v223\n"
"v_cvt_pk_bf16_f32 v220, v220, v221\n"
"v_cvt_pk_bf16_f32 v221, v222, v223\n"
"s_add_u32 s84, s74, 86016\n"
"buffer_store_dwordx2 v[220:221], v236, s[40:43], s84 offen\n"
"s_branch L_END\n"
"L_LS4Z:\n"
"s_mov_b32 s78, 0\n"
"L_LL4Z:\n"
"s_sleep 1\n"
"s_lshl_b32 s84, s32, 8\n"
"s_add_u32 s84, s84, s69\n"
"buffer_load_dwordx4 a[96:99], v240, s[52:55], s84 offen offset:0 sc1\n"
"buffer_load_dwordx4 a[100:103], v240, s[52:55], s84 offen offset:1024 sc1\n"
"buffer_load_dwordx4 a[104:107], v240, s[52:55], s84 offen offset:2048 sc1\n"
"buffer_load_dwordx4 a[108:111], v240, s[52:55], s84 offen offset:3072 sc1\n"
"s_add_u32 s84, s88, s69\n"
"buffer_load_dwordx4 a[144:147], v240, s[52:55], s84 offen offset:0 sc1\n"
"buffer_load_dwordx4 a[148:151], v240, s[52:55], s84 offen offset:1024 sc1\n"
"buffer_load_dwordx4 a[152:155], v240, s[52:55], s84 offen offset:2048 sc1\n"
"buffer_load_dwordx4 a[156:159], v240, s[52:55], s84 offen offset:3072 sc1\n"
"s_waitcnt vmcnt(0)\n"
"s_add_u32 s78, s78, 1\n"
"v_accvgpr_read_b32 v0, a96\n"
"v_accvgpr_read_b32 v1, a97\n"
"v_accvgpr_read_b32 v2, a102\n"
"v_accvgpr_read_b32 v3, a103\n"
"v_accvgpr_read_b32 v4, a104\n"
"v_accvgpr_read_b32 v5, a105\n"
"v_accvgpr_read_b32 v6, a110\n"
"v_accvgpr_read_b32 v7, a111\n"
"v_cmp_ne_u64 s[44:45], v[230:231], v[0:1]\n"
"v_cmp_ne_u64 s[82:83], v[230:231], v[2:3]\n"
"v_cmp_ne_u64 vcc, v[230:231], v[4:5]\n"
"s_or_b64 s[44:45], s[44:45], s[82:83]\n"
"v_cmp_ne_u64 s[82:83], v[230:231], v[6:7]\n"
"s_or_b64 s[44:45], s[44:45], vcc\n"
"s_or_b64 s[44:45], s[44:45], s[82:83]\n"
"s_cbranch_scc0 L_LOK4Z\n"
"s_cmp_lt_u32 s78, 524288\n"
"s_cbranch_scc1 L_LL4Z\n"
"s_branch L_LOK4Z\n"
"L_LS6Z:\n"
"s_mov_b32 s78, 0\n"
"L_LL6Z:\n"
"s_sleep 1\n"
"s_add_u32 s84, s88, s69\n"
"buffer_load_dwordx4 a[144:147], v240, s[52:55], s84 offen offset:0 sc1\n"
"buffer_load_dwordx4 a[148:151], v240, s[52:55], s84 offen offset:1024 sc1\n"
"buffer_load_dwordx4 a[152:155], v240, s[52:55], s84 offen offset:2048 sc1\n"
"buffer_load_dwordx4 a[156:159], v240, s[52:55], s84 offen offset:3072 sc1\n"
"s_waitcnt vmcnt(0)\n"
"s_add_u32 s78, s78, 1\n"
"v_accvgpr_read_b32 v0, a144\n"
"v_accvgpr_read_b32 v1, a145\n"
"v_accvgpr_read_b32 v2, a150\n"
"v_accvgpr_read_b32 v3, a151\n"
"v_accvgpr_read_b32 v4, a152\n"
"v_accvgpr_read_b32 v5, a153\n"
"v_accvgpr_read_b32 v6, a158\n"
"v_accvgpr_read_b32 v7, a159\n"
"v_cmp_ne_u64 s[44:45], v[230:231], v[0:1]\n"
"v_cmp_ne_u64 s[82:83], v[230:231], v[2:3]\n"
"v_cmp_ne_u64 vcc, v[230:231], v[4:5]\n"
"s_or_b64 s[44:45], s[44:45], s[82:83]\n"
"v_cmp_ne_u64 s[82:83], v[230:231], v[6:7]\n"
"s_or_b64 s[44:45], s[44:45], vcc\n"
"s_or_b64 s[44:45], s[44:45], s[82:83]\n"
"s_cbranch_scc0 L_LOK6Z\n"
"s_cmp_lt_u32 s78, 524288\n"
"s_cbranch_scc1 L_LL6Z\n"
"s_branch L_LOK6Z\n"
"L_TAILX5:\n"
"s_nop 7\n"
"s_add_u32 s84, s88, s68\n"
"buffer_load_dwordx4 a[128:131], v240, s[52:55], s84 offen offset:0 sc1\n"
"buffer_load_dwordx4 a[132:135], v240, s[52:55], s84 offen offset:1024 sc1\n"
"buffer_load_dwordx4 a[136:139], v240, s[52:55], s84 offen offset:2048 sc1\n"
"buffer_load_dwordx4 a[140:143], v240, s[52:55], s84 offen offset:3072 sc1\n"
"s_add_u32 s84, s88, s69\n"
"buffer_load_dwordx4 a[144:147], v240, s[52:55], s84 offen offset:0 sc1\n"
"buffer_load_dwordx4 a[148:151], v240, s[52:55], s84 offen offset:1024 sc1\n"
"buffer_load_dwordx4 a[152:155], v240, s[52:55], s84 offen offset:2048 sc1\n"
"buffer_load_dwordx4 a[156:159], v240, s[52:55], s84 offen offset:3072 sc1\n"
"s_nop 3\n"
"v_fma_f32 v144, v160, v244, v144\n"
"v_fma_f32 v145, v161, v244, v145\n"
"v_fma_f32 v146, v162, v244, v146\n"
"v_fma_f32 v147, v163, v244, v147\n"
"v_fma_f32 v148, v164, v245, v148\n"
"v_fma_f32 v149, v165, v245, v149\n"
"v_fma_f32 v150, v166, v245, v150\n"
"v_fma_f32 v151, v167, v245, v151\n"
"v_fma_f32 v152, v168, v246, v152\n"
"v_fma_f32 v153, v169, v246, v153\n"
"v_fma_f32 v154, v170, v246, v154\n"
"v_fma_f32 v155, v171, v246, v155\n"
"v_fma_f32 v156, v172, v247, v156\n"
"v_fma_f32 v157, v173, v247, v157\n"
"v_fma_f32 v158, v174, v247, v158\n"
"v_fma_f32 v159, v175, v247, v159\n"
"s_waitcnt vmcnt(24)\n"
"v_accvgpr_read_b32 v0, a80\n"
"v_accvgpr_read_b32 v1, a81\n"
"v_accvgpr_read_b32 v2, a86\n"
"v_accvgpr_read_b32 v3, a87\n"
"v_accvgpr_read_b32 v4, a88\n"
"v_accvgpr_read_b32 v5, a89\n"
"v_accvgpr_read_b32 v6, a94\n"
"v_accvgpr_read_b32 v7, a95\n"
"v_cmp_ne_u64 s[44:45], v[230:231], v[0:1]\n"
"v_cmp_ne_u64 s[82:83], v[230:231], v[2:3]\n"
"v_cmp_ne_u64 vcc, v[230:231], v[4:5]\n"
"s_or_b64 s[44:45], s[44:45], s[82:83]\n"
"v_cmp_ne_u64 s[82:83], v[230:231], v[6:7]\n"
"s_or_b64 s[44:45], s[44:45], vcc\n"
"s_or_b64 s[44:45], s[44:45], s[82:83]\n"
"s_cbranch_scc1 L_LS3X\n"
"L_LOK3X:\n"
"v_mfma_f32_16x16x32_bf16 v[192:195], v[226:229], a[82:85], v[192:195]\n"
"v_mfma_f32_16x16x32_bf16 v[196:199], v[224:227], a[82:85], v[196:199]\n"
"v_mfma_f32_16x16x32_bf16 v[200:203], v[226:229], a[90:93], v[200:203]\n"
"v_mfma_f32_16x16x32_bf16 v[204:207], v[224:227], a[90:93], v[204:207]\n"
"s_waitcnt vmcnt(20)\n"
"v_mfma_f32_16x16x32_bf16 v[192:195], v[226:229], a[32:35], v[192:195]\n"
"v_mfma_f32_16x16x32_bf16 v[196:199], v[224:227], a[32:35], v[196:199]\n"
"v_mfma_f32_16x16x32_bf16 v[200:203], v[226:229], a[36:39], v[200:203]\n"
"v_mfma_f32_16x16x32_bf16 v[204:207], v[224:227], a[36:39], v[204:207]\n"
"v_mfma_f32_16x16x32_bf16 v[208:211], v[226:229], a[40:43], v[208:211]\n"
"v_mfma_f32_16x16x32_bf16 v[212:215], v[224:227], a[40:43], v[212:215]\n"
"v_mfma_f32_16x16x32_bf16 v[216:219], v[226:229], a[44:47], v[216:219]\n"
"v_mfma_f32_16x16x32_bf16 v[220:223], v[224:227], a[44:47], v[220:223]\n"
"s_waitcnt vmcnt(4)\n"
"v_accvgpr_read_b32 v0, a128\n"
"v_accvgpr_read_b32 v1, a129\n"
"v_accvgpr_read_b32 v2, a134\n"
"v_accvgpr_read_b32 v3, a135\n"
"v_accvgpr_read_b32 v4, a136\n"
"v_accvgpr_read_b32 v5, a137\n"
"v_accvgpr_read_b32 v6, a142\n"
"v_accvgpr_read_b32 v7, a143\n"
"v_cmp_ne_u64 s[44:45], v[230:231], v[0:1]\n"
"v_cmp_ne_u64 s[82:83], v[230:231], v[2:3]\n"
"v_cmp_ne_u64 vcc, v[230:231], v[4:5]\n"
"s_or_b64 s[44:45], s[44:45], s[82:83]\n"
"v_cmp_ne_u64 s[82:83], v[230:231], v[6:7]\n"
"s_or_b64 s[44:45], s[44:45], vcc\n"
"s_or_b64 s[44:45], s[44:45], s[82:83]\n"
"s_cbranch_scc1 L_LS5X\n"
"L_LOK5X:\n"
"v_mfma_f32_16x16x32_bf16 v[192:195], v[226:229], a[130:133], v[192:195]\n"
"v_mfma_f32_16x16x32_bf16 v[196:199], v[224:227], a[130:133], v[196:199]\n"
"v_mfma_f32_16x16x32_bf16 v[200:203], v[226:229], a[138:141], v[200:203]\n"
"v_mfma_f32_16x16x32_bf16 v[204:207], v[224:227], a[138:141], v[204:207]\n"
"v_accvgpr_read_b32 v0, a96\n"
"v_accvgpr_read_b32 v1, a97\n"
"v_accvgpr_read_b32 v2, a102\n"
"v_accvgpr_read_b32 v3, a103\n"
"v_accvgpr_read_b32 v4, a104\n"
"v_accvgpr_read_b32 v5, a105\n"
"v_accvgpr_read_b32 v6, a110\n"
"v_accvgpr_read_b32 v7, a111\n"
"v_cmp_ne_u64 s[44:45], v[230:231], v[0:1]\n"
"v_cmp_ne_u64 s[82:83], v[230:231], v[2:3]\n"
"v_cmp_ne_u64 vcc, v[230:231], v[4:5]\n"
"s_or_b64 s[44:45], s[44:45], s[82:83]\n"
"v_cmp_ne_u64 s[82:83], v[230:231], v[6:7]\n"
"s_or_b64 s[44:45], s[44:45], vcc\n"
"s_or_b64 s[44:45], s[44:45], s[82:83]\n"
"s_cbranch_scc1 L_LS4X\n"
"L_LOK4X:\n"
"v_mfma_f32_16x16x32_bf16 v[208:211], v[226:229], a[98:101], v[208:211]\n"
"v_mfma_f32_16x16x32_bf16 v[212:215], v[224:227], a[98:101], v[212:215]\n"
"v_mfma_f32_16x16x32_bf16 v[216:219], v[226:229], a[106:109], v[216:219]\n"
"v_mfma_f32_16x16x32_bf16 v[220:223], v[224:227], a[106:109], v[220:223]\n"
"v_add_f32 v192, v192, v128\n"
"v_add_f32 v193, v193, v129\n"
"v_add_f32 v194, v194, v130\n"
"v_add_f32 v195, v195, v131\n"
"v_cvt_pk_bf16_f32 v192, v192, v193\n"
"v_cvt_pk_bf16_f32 v193, v194, v195\n"
"buffer_store_dwordx2 v[192:193], v236, s[40:43], s89 offen\n"
"v_add_f32 v196, v196, v132\n"
"v_add_f32 v197, v197, v133\n"
"v_add_f32 v198, v198, v134\n"
"v_add_f32 v199, v199, v135\n"
"v_cvt_pk_bf16_f32 v196, v196, v197\n"
"v_cvt_pk_bf16_f32 v197, v198, v199\n"
"s_add_u32 s84, s89, 28672\n"
"buffer_store_dwordx2 v[196:197], v236, s[40:43], s84 offen\n"
"v_add_f32 v200, v200, v136\n"
"v_add_f32 v201, v201, v137\n"
"v_add_f32 v202, v202, v138\n"
"v_add_f32 v203, v203, v139\n"
"v_cvt_pk_bf16_f32 v200, v200, v201\n"
"v_cvt_pk_bf16_f32 v201, v202, v203\n"
"s_add_u32 s84, s89, 57344\n"
"buffer_store_dwordx2 v[200:201], v236, s[40:43], s84 offen\n"
"v_add_f32 v204, v204, v140\n"
"v_add_f32 v205, v205, v141\n"
"v_add_f32 v206, v206, v142\n"
"v_add_f32 v207, v207, v143\n"
"v_cvt_pk_bf16_f32 v204, v204, v205\n"
"v_cvt_pk_bf16_f32 v205, v206, v207\n"
"s_add_u32 s84, s89, 86016\n"
"buffer_store_dwordx2 v[204:205], v236, s[40:43], s84 offen\n"
"s_waitcnt vmcnt(4)\n"
"v_accvgpr_read_b32 v0, a144\n"
"v_accvgpr_read_b32 v1, a145\n"
"v_accvgpr_read_b32 v2, a150\n"
"v_accvgpr_read_b32 v3, a151\n"
"v_accvgpr_read_b32 v4, a152\n"
"v_accvgpr_read_b32 v5, a153\n"
"v_accvgpr_read_b32 v6, a158\n"
"v_accvgpr_read_b32 v7, a159\n"
"v_cmp_ne_u64 s[44:45], v[230:231], v[0:1]\n"
"v_cmp_ne_u64 s[82:83], v[230:231], v[2:3]\n"
"v_cmp_ne_u64 vcc, v[230:231], v[4:5]\n"
"s_or_b64 s[44:45], s[44:45], s[82:83]\n"
"v_cmp_ne_u64 s[82:83], v[230:231], v[6:7]\n"
"s_or_b64 s[44:45], s[44:45], vcc\n"
"s_or_b64 s[44:45], s[44:45], s[82:83]\n"
"s_cbranch_scc1 L_LS6X\n"
"L_LOK6X:\n"
"v_mfma_f32_16x16x32_bf16 v[208:211], v[226:229], a[146:149], v[208:211]\n"
"v_mfma_f32_16x16x32_bf16 v[212:215], v[224:227], a[146:149], v[212:215]\n"
"v_mfma_f32_16x16x32_bf16 v[216:219], v[226:229], a[154:157], v[216:219]\n"
"v_mfma_f32_16x16x32_bf16 v[220:223], v[224:227], a[154:157], v[220:223]\n"
"s_nop 4\n"
"v_fma_f32 v208, v144, v232, v208\n"
"v_fma_f32 v209, v145, v232, v209\n"
"v_fma_f32 v210, v146, v232, v210\n"
"v_fma_f32 v211, v147, v232, v211\n"
"v_cvt_pk_bf16_f32 v208, v208, v209\n"
"v_cvt_pk_bf16_f32 v209, v210, v211\n"
"buffer_store_dwordx2 v[208:209], v236, s[40:43], s74 offen\n"
"v_fma_f32 v212, v148, v233, v212\n"
"v_fma_f32 v213, v149, v233, v213\n"
"v_fma_f32 v214, v150, v233, v214\n"
"v_fma_f32 v215, v151, v233, v215\n"
"v_cvt_pk_bf16_f32 v212, v212, v213\n"
"v_cvt_pk_bf16_f32 v213, v214, v215\n"
"s_add_u32 s84, s74, 28672\n"
"buffer_store_dwordx2 v[212:213], v236, s[40:43], s84 offen\n"
"v_fma_f32 v216, v152, v234, v216\n"
"v_fma_f32 v217, v153, v234, v217\n"
"v_fma_f32 v218, v154, v234, v218\n"
"v_fma_f32 v219, v155, v234, v219\n"
"v_cvt_pk_bf16_f32 v216, v216, v217\n"
"v_cvt_pk_bf16_f32 v217, v218, v219\n"
"s_add_u32 s84, s74, 57344\n"
"buffer_store_dwordx2 v[216:217], v236, s[40:43], s84 offen\n"
"v_fma_f32 v220, v156, v235, v220\n"
"v_fma_f32 v221, v157, v235, v221\n"
"v_fma_f32 v222, v158, v235, v222\n"
"v_fma_f32 v223, v159, v235, v223\n"
"v_cvt_pk_bf16_f32 v220, v220, v221\n"
"v_cvt_pk_bf16_f32 v221, v222, v223\n"
"s_add_u32 s84, s74, 86016\n"
"buffer_store_dwordx2 v[220:221], v236, s[40:43], s84 offen\n"
"s_branch L_END\n"
"L_LS3X:\n"
"s_mov_b32 s78, 0\n"
"L_LL3X:\n"
"s_sleep 1\n"
"s_lshl_b32 s84, s32, 8\n"
"s_add_u32 s84, s84, s68\n"
"buffer_load_dwordx4 a[80:83], v240, s[52:55], s84 offen offset:0 sc1\n"
"buffer_load_dwordx4 a[84:87], v240, s[52:55], s84 offen offset:1024 sc1\n"
"buffer_load_dwordx4 a[88:91], v240, s[52:55], s84 offen offset:2048 sc1\n"
"buffer_load_dwordx4 a[92:95], v240, s[52:55], s84 offen offset:3072 sc1\n"
"s_add_u32 s84, s88, s68\n"
"buffer_load_dwordx4 a[128:131], v240, s[52:55], s84 offen offset:0 sc1\n"
"buffer_load_dwordx4 a[132:135], v240, s[52:55], s84 offen offset:1024 sc1\n"
"buffer_load_dwordx4 a[136:139], v240, s[52:55], s84 offen offset:2048 sc1\n"
"buffer_load_dwordx4 a[140:143], v240, s[52:55], s84 offen offset:3072 sc1\n"
"s_lshl_b32 s84, s32, 8\n"
"s_add_u32 s84, s84, s69\n"
"buffer_load_dwordx4 a[96:99], v240, s[52:55], s84 offen offset:0 sc1\n"
"buffer_load_dwordx4 a[100:103], v240, s[52:55], s84 offen offset:1024 sc1\n"
"buffer_load_dwordx4 a[104:107], v240, s[52:55], s84 offen offset:2048 sc1\n"
"buffer_load_dwordx4 a[108:111], v240, s[52:55], s84 offen offset:3072 sc1\n"
"s_add_u32 s84, s88, s69\n"
"buffer_load_dwordx4 a[144:147], v240, s[52:55], s84 offen offset:0 sc1\n"
"buffer_load_dwordx4 a[148:151], v240, s[52:55], s84 offen offset:1024 sc1\n"
"buffer_load_dwordx4 a[152:155], v240, s[52:55], s84 offen offset:2048 sc1\n"
"buffer_load_dwordx4 a[156:159], v240, s[52:55], s84 offen offset:3072 sc1\n"
"s_waitcnt vmcnt(0)\n"
"s_add_u32 s78, s78, 1\n"
"v_accvgpr_read_b32 v0, a80\n"
"v_accvgpr_read_b32 v1, a81\n"
"v_accvgpr_read_b32 v2, a86\n"
"v_accvgpr_read_b32 v3, a87\n"
"v_accvgpr_read_b32 v4, a88\n"
"v_accvgpr_read_b32 v5, a89\n"
"v_accvgpr_read_b32 v6, a94\n"
"v_accvgpr_read_b32 v7, a95\n"
"v_cmp_ne_u64 s[44:45], v[230:231], v[0:1]\n"
"v_cmp_ne_u64 s[82:83], v[230:231], v[2:3]\n"
"v_cmp_ne_u64 vcc, v[230:231], v[4:5]\n"
"s_or_b64 s[44:45], s[44:45], s[82:83]\n"
"v_cmp_ne_u64 s[82:83], v[230:231], v[6:7]\n"
"s_or_b64 s[44:45], s[44:45], vcc\n"
"s_or_b64 s[44:45], s[44:45], s[82:83]\n"
"s_cbranch_scc0 L_LOK3X\n"
"s_cmp_lt_u32 s78, 524288\n"
"s_cbranch_scc1 L_LL3X\n"
"s_branch L_LOK3X\n"
"L_LS4X:\n"
"s_mov_b32 s78, 0\n"
"L_LL4X:\n"
"s_sleep 1\n"
"s_lshl_b32 s84, s32, 8\n"
"s_add_u32 s84, s84, s69\n"
"buffer_load_dwordx4 a[96:99], v240, s[52:55], s84 offen offset:0 sc1\n"
"buffer_load_dwordx4 a[100:103], v240, s[52:55], s84 offen offset:1024 sc1\n"
"buffer_load_dwordx4 a[104:107], v240, s[52:55], s84 offen offset:2048 sc1\n"
"buffer_load_dwordx4 a[108:111], v240, s[52:55], s84 offen offset:3072 sc1\n"
"s_add_u32 s84, s88, s69\n"
"buffer_load_dwordx4 a[144:147], v240, s[52:55], s84 offen offset:0 sc1\n"
"buffer_load_dwordx4 a[148:151], v240, s[52:55], s84 offen offset:1024 sc1\n"
"buffer_load_dwordx4 a[152:155], v240, s[52:55], s84 offen offset:2048 sc1\n"
"buffer_load_dwordx4 a[156:159], v240, s[52:55], s84 offen offset:3072 sc1\n"
"s_waitcnt vmcnt(0)\n"
"s_add_u32 s78, s78, 1\n"
"v_accvgpr_read_b32 v0, a96\n"
"v_accvgpr_read_b32 v1, a97\n"
"v_accvgpr_read_b32 v2, a102\n"
"v_accvgpr_read_b32 v3, a103\n"
"v_accvgpr_read_b32 v4, a104\n"
"v_accvgpr_read_b32 v5, a105\n"
"v_accvgpr_read_b32 v6, a110\n"
"v_accvgpr_read_b32 v7, a111\n"
"v_cmp_ne_u64 s[44:45], v[230:231], v[0:1]\n"
"v_cmp_ne_u64 s[82:83], v[230:231], v[2:3]\n"
"v_cmp_ne_u64 vcc, v[230:231], v[4:5]\n"
"s_or_b64 s[44:45], s[44:45], s[82:83]\n"
"v_cmp_ne_u64 s[82:83], v[230:231], v[6:7]\n"
"s_or_b64 s[44:45], s[44:45], vcc\n"
"s_or_b64 s[44:45], s[44:45], s[82:83]\n"
"s_cbranch_scc0 L_LOK4X\n"
"s_cmp_lt_u32 s78, 524288\n"
"s_cbranch_scc1 L_LL4X\n"
"s_branch L_LOK4X\n"
"L_LS5X:\n"
"s_mov_b32 s78, 0\n"
"L_LL5X:\n"
"s_sleep 1\n"
"s_add_u32 s84, s88, s68\n"
"buffer_load_dwordx4 a[128:131], v240, s[52:55], s84 offen offset:0 sc1\n"
"buffer_load_dwordx4 a[132:135], v240, s[52:55], s84 offen offset:1024 sc1\n"
"buffer_load_dwordx4 a[136:139], v240, s[52:55], s84 offen offset:2048 sc1\n"
"buffer_load_dwordx4 a[140:143], v240, s[52:55], s84 offen offset:3072 sc1\n"
"s_lshl_b32 s84, s32, 8\n"
"s_add_u32 s84, s84, s69\n"
"buffer_load_dwordx4 a[96:99], v240, s[52:55], s84 offen offset:0 sc1\n"
"buffer_load_dwordx4 a[100:103], v240, s[52:55], s84 offen offset:1024 sc1\n"
"buffer_load_dwordx4 a[104:107], v240, s[52:55], s84 offen offset:2048 sc1\n"
"buffer_load_dwordx4 a[108:111], v240, s[52:55], s84 offen offset:3072 sc1\n"
"s_add_u32 s84, s88, s69\n"
"buffer_load_dwordx4 a[144:147], v240, s[52:55], s84 offen offset:0 sc1\n"
"buffer_load_dwordx4 a[148:151], v240, s[52:55], s84 offen offset:1024 sc1\n"
"buffer_load_dwordx4 a[152:155], v240, s[52:55], s84 offen offset:2048 sc1\n"
"buffer_load_dwordx4 a[156:159], v240, s[52:55], s84 offen offset:3072 sc1\n"
"s_waitcnt vmcnt(0)\n"
"s_add_u32 s78, s78, 1\n"
"v_accvgpr_read_b32 v0, a128\n"
"v_accvgpr_read_b32 v1, a129\n"
"v_accvgpr_read_b32 v2, a134\n"
"v_accvgpr_read_b32 v3, a135\n"
"v_accvgpr_read_b32 v4, a136\n"
"v_accvgpr_read_b32 v5, a137\n"
"v_accvgpr_read_b32 v6, a142\n"
"v_accvgpr_read_b32 v7, a143\n"
"v_cmp_ne_u64 s[44:45], v[230:231], v[0:1]\n"
"v_cmp_ne_u64 s[82:83], v[230:231], v[2:3]\n"
"v_cmp_ne_u64 vcc, v[230:231], v[4:5]\n"
"s_or_b64 s[44:45], s[44:45], s[82:83]\n"
"v_cmp_ne_u64 s[82:83], v[230:231], v[6:7]\n"
"s_or_b64 s[44:45], s[44:45], vcc\n"
"s_or_b64 s[44:45], s[44:45], s[82:83]\n"
"s_cbranch_scc0 L_LOK5X\n"
"s_cmp_lt_u32 s78, 524288\n"
"s_cbranch_scc1 L_LL5X\n"
"s_branch L_LOK5X\n"
"L_LS6X:\n"
"s_mov_b32 s78, 0\n"
"L_LL6X:\n"
"s_sleep 1\n"
"s_add_u32 s84, s88, s69\n"
"buffer_load_dwordx4 a[144:147], v240, s[52:55], s84 offen offset:0 sc1\n"
"buffer_load_dwordx4 a[148:151], v240, s[52:55], s84 offen offset:1024 sc1\n"
"buffer_load_dwordx4 a[152:155], v240, s[52:55], s84 offen offset:2048 sc1\n"
"buffer_load_dwordx4 a[156:159], v240, s[52:55], s84 offen offset:3072 sc1\n"
"s_waitcnt vmcnt(0)\n"
"s_add_u32 s78, s78, 1\n"
"v_accvgpr_read_b32 v0, a144\n"
"v_accvgpr_read_b32 v1, a145\n"
"v_accvgpr_read_b32 v2, a150\n"
"v_accvgpr_read_b32 v3, a151\n"
"v_accvgpr_read_b32 v4, a152\n"
"v_accvgpr_read_b32 v5, a153\n"
"v_accvgpr_read_b32 v6, a158\n"
"v_accvgpr_read_b32 v7, a159\n"
"v_cmp_ne_u64 s[44:45], v[230:231], v[0:1]\n"
"v_cmp_ne_u64 s[82:83], v[230:231], v[2:3]\n"
"v_cmp_ne_u64 vcc, v[230:231], v[4:5]\n"
"s_or_b64 s[44:45], s[44:45], s[82:83]\n"
"v_cmp_ne_u64 s[82:83], v[230:231], v[6:7]\n"
"s_or_b64 s[44:45], s[44:45], vcc\n"
"s_or_b64 s[44:45], s[44:45], s[82:83]\n"
"s_cbranch_scc0 L_LOK6X\n"
"s_cmp_lt_u32 s78, 524288\n"
"s_cbranch_scc1 L_LL6X\n"
"s_branch L_LOK6X\n"
"L_FS0:\n"
"s_mov_b32 s78, 0\n"
"L_FL0:\n"
"s_sleep 1\n"
"s_lshl_b32 s83, s37, 4\n"
"s_add_u32 s83, s83, s67\n"
"buffer_load_dwordx2 v[238:239], off, s[52:55], s83 sc1\n"
"s_waitcnt vmcnt(0)\n"
"v_cmp_ne_u64 vcc, v[230:231], v[238:239]\n"
"s_add_u32 s78, s78, 1\n"
"s_cbranch_vccz L_FOK0\n"
"s_cmp_lt_u32 s78, 524288\n"
"s_cbranch_scc1 L_FL0\n"
"s_branch L_FOK0\n"
"L_FS1:\n"
"s_mov_b32 s78, 0\n"
"L_FL1:\n"
"s_sleep 1\n"
"s_lshl_b32 s83, s36, 4\n"
"s_add_u32 s83, s83, s67\n"
"buffer_load_dwordx2 v[238:239], off, s[52:55], s83 sc1\n"
"s_waitcnt vmcnt(0)\n"
"v_cmp_ne_u64 vcc, v[230:231], v[238:239]\n"
"s_add_u32 s78, s78, 1\n"
"s_cbranch_vccz L_FOK1\n"
"s_cmp_lt_u32 s78, 524288\n"
"s_cbranch_scc1 L_FL1\n"
"s_branch L_FOK1\n"
"L_FS2:\n"
"s_mov_b32 s78, 0\n"
"L_FL2:\n"
"s_sleep 1\n"
"s_lshl_b32 s83, s35, 4\n"
"s_add_u32 s83, s83, s67\n"
"buffer_load_dwordx2 v[238:239], off, s[52:55], s83 sc1\n"
"s_waitcnt vmcnt(0)\n"
"v_cmp_ne_u64 vcc, v[230:231], v[238:239]\n"
"s_add_u32 s78, s78, 1\n"
"s_cbranch_vccz L_FOK2\n"
"s_cmp_lt_u32 s78, 524288\n"
"s_cbranch_scc1 L_FL2\n"
"s_branch L_FOK2\n"
"L_FS3:\n"
"s_mov_b32 s78, 0\n"
"L_FL3:\n"
"s_sleep 1\n"
"s_lshl_b32 s83, s34, 4\n"
"s_add_u32 s83, s83, s67\n"
"buffer_load_dwordx2 v[238:239], off, s[52:55], s83 sc1\n"
"s_waitcnt vmcnt(0)\n"
"v_cmp_ne_u64 vcc, v[230:231], v[238:239]\n"
"s_add_u32 s78, s78, 1\n"
"s_cbranch_vccz L_FOK3\n"
"s_cmp_lt_u32 s78, 524288\n"
"s_cbranch_scc1 L_FL3\n"
"s_branch L_FOK3\n"
"L_FS4:\n"
"s_mov_b32 s78, 0\n"
"L_FL4:\n"
"s_sleep 1\n"
"s_lshl_b32 s83, s33, 4\n"
"s_add_u32 s83, s83, s67\n"
"buffer_load_dwordx2 v[238:239], off, s[52:55], s83 sc1\n"
"s_waitcnt vmcnt(0)\n"
"v_cmp_ne_u64 vcc, v[230:231], v[238:239]\n"
"s_add_u32 s78, s78, 1\n"
"s_cbranch_vccz L_FOK4\n"
"s_cmp_lt_u32 s78, 524288\n"
"s_cbranch_scc1 L_FL4\n"
"s_branch L_FOK4\n"
"L_FSx:\n"
"s_mov_b32 s78, 0\n"
"L_FLx:\n"
"s_sleep 1\n"
"buffer_load_dwordx2 v[248:249], off, s[52:55], s77 sc1\n"
"s_waitcnt vmcnt(0)\n"
"v_cmp_ne_u64 vcc, v[230:231], v[248:249]\n"
"s_add_u32 s78, s78, 1\n"
"s_cbranch_vccz L_FOKx\n"
"s_cmp_lt_u32 s78, 524288\n"
"s_cbranch_scc1 L_FLx\n"
"s_branch L_FOKx\n"
"L_LS3Y:\n"
"s_mov_b32 s78, 0\n"
"L_LL3Y:\n"
"s_sleep 1\n"
"s_lshl_b32 s84, s32, 8\n"
"s_add_u32 s84, s84, s68\n"
"buffer_load_dwordx4 a[80:83], v240, s[52:55], s84 offen offset:0 sc1\n"
"buffer_load_dwordx4 a[84:87], v240, s[52:55], s84 offen offset:1024 sc1\n"
"buffer_load_dwordx4 a[88:91], v240, s[52:55], s84 offen offset:2048 sc1\n"
"buffer_load_dwordx4 a[92:95], v240, s[52:55], s84 offen offset:3072 sc1\n"
"s_lshl_b32 s84, s32, 8\n"
"s_add_u32 s84, s84, s69\n"
"buffer_load_dwordx4 a[96:99], v240, s[52:55], s84 offen offset:0 sc1\n"
"buffer_load_dwordx4 a[100:103], v240, s[52:55], s84 offen offset:1024 sc1\n"
"buffer_load_dwordx4 a[104:107], v240, s[52:55], s84 offen offset:2048 sc1\n"
"buffer_load_dwordx4 a[108:111], v240, s[52:55], s84 offen offset:3072 sc1\n"
"s_waitcnt vmcnt(0)\n"
"s_add_u32 s78, s78, 1\n"
"v_accvgpr_read_b32 v0, a80\n"
"v_accvgpr_read_b32 v1, a81\n"
"v_accvgpr_read_b32 v2, a86\n"
"v_accvgpr_read_b32 v3, a87\n"
"v_accvgpr_read_b32 v4, a88\n"
"v_accvgpr_read_b32 v5, a89\n"
"v_accvgpr_read_b32 v6, a94\n"
"v_accvgpr_read_b32 v7, a95\n"
"v_cmp_ne_u64 s[44:45], v[230:231], v[0:1]\n"
"v_cmp_ne_u64 s[82:83], v[230:231], v[2:3]\n"
"v_cmp_ne_u64 vcc, v[230:231], v[4:5]\n"
"s_or_b64 s[44:45], s[44:45], s[82:83]\n"
"v_cmp_ne_u64 s[82:83], v[230:231], v[6:7]\n"
"s_or_b64 s[44:45], s[44:45], vcc\n"
"s_or_b64 s[44:45], s[44:45], s[82:83]\n"
"s_cbranch_scc0 L_LOK3Y\n"
"s_cmp_lt_u32 s78, 524288\n"
"s_cbranch_scc1 L_LL3Y\n"
"s_branch L_LOK3Y\n"
"L_LS4Y:\n"
"s_mov_b32 s78, 0\n"
"L_LL4Y:\n"
"s_sleep 1\n"
"s_lshl_b32 s84, s32, 8\n"
"s_add_u32 s84, s84, s69\n"
"buffer_load_dwordx4 a[96:99], v240, s[52:55], s84 offen offset:0 sc1\n"
"buffer_load_dwordx4 a[100:103], v240, s[52:55], s84 offen offset:1024 sc1\n"
"buffer_load_dwordx4 a[104:107], v240, s[52:55], s84 offen offset:2048 sc1\n"
"buffer_load_dwordx4 a[108:111], v240, s[52:55], s84 offen offset:3072 sc1\n"
"s_waitcnt vmcnt(0)\n"
"s_add_u32 s78, s78, 1\n"
"v_accvgpr_read_b32 v0, a96\n"
"v_accvgpr_read_b32 v1, a97\n"
"v_accvgpr_read_b32 v2, a102\n"
"v_accvgpr_read_b32 v3, a103\n"
"v_accvgpr_read_b32 v4, a104\n"
"v_accvgpr_read_b32 v5, a105\n"
"v_accvgpr_read_b32 v6, a110\n"
"v_accvgpr_read_b32 v7, a111\n"
"v_cmp_ne_u64 s[44:45], v[230:231], v[0:1]\n"
"v_cmp_ne_u64 s[82:83], v[230:231], v[2:3]\n"
"v_cmp_ne_u64 vcc, v[230:231], v[4:5]\n"
"s_or_b64 s[44:45], s[44:45], s[82:83]\n"
"v_cmp_ne_u64 s[82:83], v[230:231], v[6:7]\n"
"s_or_b64 s[44:45], s[44:45], vcc\n"
"s_or_b64 s[44:45], s[44:45], s[82:83]\n"
"s_cbranch_scc0 L_LOK4Y\n"
"s_cmp_lt_u32 s78, 524288\n"
"s_cbranch_scc1 L_LL4Y\n"
"s_branch L_LOK4Y\n"
"L_TAILY:\n"
"s_nop 7\n"
"s_nop 3\n"
"v_fma_f32 v144, v160, v244, v144\n"
"v_fma_f32 v145, v161, v244, v145\n"
"v_fma_f32 v146, v162, v244, v146\n"
"v_fma_f32 v147, v163, v244, v147\n"
"v_fma_f32 v148, v164, v245, v148\n"
"v_fma_f32 v149, v165, v245, v149\n"
"v_fma_f32 v150, v166, v245, v150\n"
"v_fma_f32 v151, v167, v245, v151\n"
"v_fma_f32 v152, v168, v246, v152\n"
"v_fma_f32 v153, v169, v246, v153\n"
"v_fma_f32 v154, v170, v246, v154\n"
"v_fma_f32 v155, v171, v246, v155\n"
"v_fma_f32 v156, v172, v247, v156\n"
"v_fma_f32 v157, v173, v247, v157\n"
"v_fma_f32 v158, v174, v247, v158\n"
"v_fma_f32 v159, v175, v247, v159\n"
"s_waitcnt vmcnt(16)\n"
"v_accvgpr_read_b32 v0, a80\n"
"v_accvgpr_read_b32 v1, a81\n"
"v_accvgpr_read_b32 v2, a86\n"
"v_accvgpr_read_b32 v3, a87\n"
"v_accvgpr_read_b32 v4, a88\n"
"v_accvgpr_read_b32 v5, a89\n"
"v_accvgpr_read_b32 v6, a94\n"
"v_accvgpr_read_b32 v7, a95\n"
"v_cmp_ne_u64 s[44:45], v[230:231], v[0:1]\n"
"v_cmp_ne_u64 s[82:83], v[230:231], v[2:3]\n"
"v_cmp_ne_u64 vcc, v[230:231], v[4:5]\n"
"s_or_b64 s[44:45], s[44:45], s[82:83]\n"
"v_cmp_ne_u64 s[82:83], v[230:231], v[6:7]\n"
"s_or_b64 s[44:45], s[44:45], vcc\n"
"s_or_b64 s[44:45], s[44:45], s[82:83]\n"
"s_cbranch_scc1 L_LS3Y\n"
"L_LOK3Y:\n"
"v_mfma_f32_16x16x32_bf16 v[192:195], v[226:229], a[82:85], v[192:195]\n"
"v_mfma_f32_16x16x32_bf16 v[196:199], v[224:227], a[82:85], v[196:199]\n"
"v_mfma_f32_16x16x32_bf16 v[200:203], v[226:229], a[90:93], v[200:203]\n"
"v_mfma_f32_16x16x32_bf16 v[204:207], v[224:227], a[90:93], v[204:207]\n"
"s_waitcnt vmcnt(12)\n"
"v_mfma_f32_16x16x32_bf16 v[192:195], v[226:229], a[32:35], v[192:195]\n"
"v_mfma_f32_16x16x32_bf16 v[196:199], v[224:227], a[32:35], v[196:199]\n"
"v_mfma_f32_16x16x32_bf16 v[200:203], v[226:229], a[36:39], v[200:203]\n"
"v_mfma_f32_16x16x32_bf16 v[204:207], v[224:227], a[36:39], v[204:207]\n"
"v_mfma_f32_16x16x32_bf16 v[208:211], v[226:229], a[40:43], v[208:211]\n"
"v_mfma_f32_16x16x32_bf16 v[212:215], v[224:227], a[40:43], v[212:215]\n"
"v_mfma_f32_16x16x32_bf16 v[216:219], v[226:229], a[44:47], v[216:219]\n"
"v_mfma_f32_16x16x32_bf16 v[220:223], v[224:227], a[44:47], v[220:223]\n"
"s_waitcnt vmcnt(8)\n"
"v_mfma_f32_16x16x32_bf16 v[192:195], v[226:229], a[64:67], v[192:195]\n"
"v_mfma_f32_16x16x32_bf16 v[196:199], v[224:227], a[64:67], v[196:199]\n"
"v_mfma_f32_16x16x32_bf16 v[200:203], v[226:229], a[68:71], v[200:203]\n"
"v_mfma_f32_16x16x32_bf16 v[204:207], v[224:227], a[68:71], v[204:207]\n"
"v_mfma_f32_16x16x32_bf16 v[208:211], v[226:229], a[72:75], v[208:211]\n"
"v_mfma_f32_16x16x32_bf16 v[212:215], v[224:227], a[72:75], v[212:215]\n"
"v_mfma_f32_16x16x32_bf16 v[216:219], v[226:229], a[76:79], v[216:219]\n"
"v_mfma_f32_16x16x32_bf16 v[220:223], v[224:227], a[76:79], v[220:223]\n"
"s_nop 0\n"
"v_add_f32 v192, v192, v128\n"
"v_add_f32 v193, v193, v129\n"
"v_add_f32 v194, v194, v130\n"
"v_add_f32 v195, v195, v131\n"
"v_cvt_pk_bf16_f32 v192, v192, v193\n"
"v_cvt_pk_bf16_f32 v193, v194, v195\n"
"buffer_store_dwordx2 v[192:193], v236, s[40:43], s89 offen\n"
"v_add_f32 v196, v196, v132\n"
"v_add_f32 v197, v197, v133\n"
"v_add_f32 v198, v198, v134\n"
"v_add_f32 v199, v199, v135\n"
"v_cvt_pk_bf16_f32 v196, v196, v197\n"
"v_cvt_pk_bf16_f32 v197, v198, v199\n"
"s_add_u32 s84, s89, 28672\n"
"buffer_store_dwordx2 v[196:197], v236, s[40:43], s84 offen\n"
"v_add_f32 v200, v200, v136\n"
"v_add_f32 v201, v201, v137\n"
"v_add_f32 v202, v202, v138\n"
"v_add_f32 v203, v203, v139\n"
"v_cvt_pk_bf16_f32 v200, v200, v201\n"
"v_cvt_pk_bf16_f32 v201, v202, v203\n"
"s_add_u32 s84, s89, 57344\n"
"buffer_store_dwordx2 v[200:201], v236, s[40:43], s84 offen\n"
"v_add_f32 v204, v204, v140\n"
"v_add_f32 v205, v205, v141\n"
"v_add_f32 v206, v206, v142\n"
"v_add_f32 v207, v207, v143\n"
"v_cvt_pk_bf16_f32 v204, v204, v205\n"
"v_cvt_pk_bf16_f32 v205, v206, v207\n"
"s_add_u32 s84, s89, 86016\n"
"buffer_store_dwordx2 v[204:205], v236, s[40:43], s84 offen\n"
"s_waitcnt vmcnt(8)\n"
"v_accvgpr_read_b32 v0, a96\n"
"v_accvgpr_read_b32 v1, a97\n"
"v_accvgpr_read_b32 v2, a102\n"
"v_accvgpr_read_b32 v3, a103\n"
"v_accvgpr_read_b32 v4, a104\n"
"v_accvgpr_read_b32 v5, a105\n"
"v_accvgpr_read_b32 v6, a110\n"
"v_accvgpr_read_b32 v7, a111\n"
"v_cmp_ne_u64 s[44:45], v[230:231], v[0:1]\n"
"v_cmp_ne_u64 s[82:83], v[230:231], v[2:3]\n"
"v_cmp_ne_u64 vcc, v[230:231], v[4:5]\n"
"s_or_b64 s[44:45], s[44:45], s[82:83]\n"
"v_cmp_ne_u64 s[82:83], v[230:231], v[6:7]\n"
"s_or_b64 s[44:45], s[44:45], vcc\n"
"s_or_b64 s[44:45], s[44:45], s[82:83]\n"
"s_cbranch_scc1 L_LS4Y\n"
"L_LOK4Y:\n"
"v_mfma_f32_16x16x32_bf16 v[208:211], v[226:229], a[98:101], v[208:211]\n"
"v_mfma_f32_16x16x32_bf16 v[212:215], v[224:227], a[98:101], v[212:215]\n"
"v_mfma_f32_16x16x32_bf16 v[216:219], v[226:229], a[106:109], v[216:219]\n"
"v_mfma_f32_16x16x32_bf16 v[220:223], v[224:227], a[106:109], v[220:223]\n"
"s_nop 4\n"
"v_fma_f32 v208, v144, v232, v208\n"
"v_fma_f32 v209, v145, v232, v209\n"
"v_fma_f32 v210, v146, v232, v210\n"
"v_fma_f32 v211, v147, v232, v211\n"
"v_cvt_pk_bf16_f32 v208, v208, v209\n"
"v_cvt_pk_bf16_f32 v209, v210, v211\n"
"buffer_store_dwordx2 v[208:209], v236, s[40:43], s74 offen\n"
"v_fma_f32 v212, v148, v233, v212\n"
"v_fma_f32 v213, v149, v233, v213\n"
"v_fma_f32 v214, v150, v233, v214\n"
"v_fma_f32 v215, v151, v233, v215\n"
"v_cvt_pk_bf16_f32 v212, v212, v213\n"
"v_cvt_pk_bf16_f32 v213, v214, v215\n"
"s_add_u32 s84, s74, 28672\n"
"buffer_store_dwordx2 v[212:213], v236, s[40:43], s84 offen\n"
"v_fma_f32 v216, v152, v234, v216\n"
"v_fma_f32 v217, v153, v234, v217\n"
"v_fma_f32 v218, v154, v234, v218\n"
"v_fma_f32 v219, v155, v234, v219\n"
"v_cvt_pk_bf16_f32 v216, v216, v217\n"
"v_cvt_pk_bf16_f32 v217, v218, v219\n"
"s_add_u32 s84, s74, 57344\n"
"buffer_store_dwordx2 v[216:217], v236, s[40:43], s84 offen\n"
"v_fma_f32 v220, v156, v235, v220\n"
"v_fma_f32 v221, v157, v235, v221\n"
"v_fma_f32 v222, v158, v235, v222\n"
"v_fma_f32 v223, v159, v235, v223\n"
"v_cvt_pk_bf16_f32 v220, v220, v221\n"
"v_cvt_pk_bf16_f32 v221, v222, v223\n"
"s_add_u32 s84, s74, 86016\n"
"buffer_store_dwordx2 v[220:221], v236, s[40:43], s84 offen\n"
"s_cmp_lg_u32 %[g], 0\n"
"s_cbranch_scc1 L_END\n"
"s_mov_b32 s84, 1835008\n"
"buffer_store_dwordx2 v[230:231], off, s[52:55], s84 sc1\n"
"L_END:\n"
"s_waitcnt vmcnt(0) lgkmcnt(0)\n"
  : [tid] "+v"(tid)
  : [x] "s"(x), [k] "s"(k), [v] "s"(v), [wq] "s"(wq), [wo] "s"(wo), [ws] "s"(wsp), [g] "s"(g), [b] "s"(b), [out] "s"(op), [lds] "s"((unsigned)(size_t)lds)
  : "v1","v2","v3","v4","v5","v6","v7","v8","v9","v10","v11","v12","v13","v14","v15","v16","v17","v18","v19","v20","v21","v22","v23","v24","v25","v26","v27","v28","v29","v30","v31","v32","v33","v34","v35","v36","v37","v38","v39","v40","v41","v42","v43","v44","v45","v46","v47","v48","v49","v50","v51","v52","v53","v54","v55","v56","v57","v58","v59","v60","v61","v62","v63","v64","v65","v66","v67","v68","v69","v70","v71","v72","v73","v74","v75","v76","v77","v78","v79","v80","v81","v82","v83","v84","v85","v86","v87","v88","v89","v90","v91","v92","v93","v94","v95","v96","v97","v98","v99","v100","v101","v102","v103","v104","v105","v106","v107","v108","v109","v110","v111","v112","v113","v114","v115","v116","v117","v118","v119","v120","v121","v122","v123","v124","v125","v126","v127","v128","v129","v130","v131","v132","v133","v134","v135","v136","v137","v138","v139","v140","v141","v142","v143","v144","v145","v146","v147","v148","v149","v150","v151","v152","v153","v154","v155","v156","v157","v158","v159","v160","v161","v162","v163","v164","v165","v166","v167","v168","v169","v170","v171","v172","v173","v174","v175","v176","v177","v178","v179","v180","v181","v182","v183","v184","v185","v186","v187","v188","v189","v190","v191","v192","v193","v194","v195","v196","v197","v198","v199","v200","v201","v202","v203","v204","v205","v206","v207","v208","v209","v210","v211","v212","v213","v214","v215","v216","v217","v218","v219","v220","v221","v222","v223","v224","v225","v226","v227","v228","v229","v230","v231","v232","v233","v234","v235","v236","v237","v238","v239","v240","v241","v242","v243","v244","v245","v246","v247","v248","v249","v250","v251","v252","v253","v254","v255","a0","a1","a2","a3","a4","a5","a6","a7","a8","a9","a10","a11","a12","a13","a14","a15","a16","a17","a18","a19","a20","a21","a22","a23","a24","a25","a26","a27","a28","a29","a30","a31","a32","a33","a34","a35","a36","a37","a38","a39","a40","a41","a42","a43","a44","a45","a46","a47","a48","a49","a50","a51","a52","a53","a54","a55","a56","a57","a58","a59","a60","a61","a62","a63","a64","a65","a66","a67","a68","a69","a70","a71","a72","a73","a74","a75","a76","a77","a78","a79","a80","a81","a82","a83","a84","a85","a86","a87","a88","a89","a90","a91","a92","a93","a94","a95","a96","a97","a98","a99","a100","a101","a102","a103","a104","a105","a106","a107","a108","a109","a110","a111","a112","a113","a114","a115","a116","a117","a118","a119","a120","a121","a122","a123","a124","a125","a126","a127","a128","a129","a130","a131","a132","a133","a134","a135","a136","a137","a138","a139","a140","a141","a142","a143","a144","a145","a146","a147","a148","a149","a150","a151","a152","a153","a154","a155","a156","a157","a158","a159","a160","a161","a162","a163","a164","a165","a166","a167","a168","a169","a170","a171","a172","a173","a174","a175","a176","a177","a178","a179","a180","a181","a182","a183","a184","a185","a186","a187","a188","a189","a190","a191","a192","a193","a194","a195","a196","a197","a198","a199","a200","a201","a202","a203","a204","a205","a206","a207","a208","a209","a210","a211","a212","a213","a214","a215","a216","a217","a218","a219","a220","a221","a222","a223","a224","a225","a226","a227","a228","a229","a230","a231","a232","a233","a234","a235","a236","a237","a238","a239","a240","a241","a242","a243","a244","a245","a246","a247","a248","a249","a250","a251","a252","a253","a254","a255","s32","s33","s34","s35","s36","s37","s38","s39","s40","s41","s42","s43","s44","s45","s46","s47","s48","s49","s50","s51","s52","s53","s54","s55","s56","s57","s58","s59","s60","s61","s62","s63","s64","s65","s66","s67","s68","s69","s70","s71","s72","s73","s74","s75","s76","s77","s78","s79","s80","s81","s82","s83","s84","s85","s86","s87","s88","s89","s90","s91","s92","s93","s94","s95","s96","s97","s98","s99","s100","s101","vcc","scc","memory");
}

extern "C" void kernel_launch(void* const* d_in, const int* in_sizes, int n_in, void* d_out, int out_size, void* d_ws, size_t ws_size,
                              hipStream_t stream) {
  fused_kernel<<<256, 256, 0, stream>>>((const char*)d_in[0], (const char*)d_in[1], (const char*)d_in[2], (const char*)d_in[3], (const char*)d_in[4],
                                        (const char*)d_in[5], (const char*)d_in[6], (const char*)d_in[7], (const char*)d_in[8], (const char*)d_in[9],
                                        (char*)d_ws, (char*)d_out);

}
```
